```python
import torch

def _softmax(x, axis=-1):
    return torch.softmax(x, dim=axis)
def _gelu(x):
    return torch.nn.functional.gelu(x, approximate="tanh")
def _silu(x):
    return torch.nn.functional.silu(x)
def _sigmoid(x):
    return torch.sigmoid(x)
def _layernorm(x, axis=-1, eps=1e-5):
    m = x.mean(dim=axis, keepdim=True)
    v = x.var(dim=axis, keepdim=True, unbiased=False)
    return (x - m) / torch.sqrt(v + eps)
def _scatter_add(tgt, idx, src, axis=0):
    return tgt.clone().index_add_(axis, idx, src)
def _segment_sum(src, seg, n):
    out = torch.zeros((n,) + src.shape[1:], dtype=src.dtype)
    return out.index_add_(0, seg, src)
def _pad(x, pw):
    flat = tuple(v for pair in reversed(pw) for v in pair)
    return torch.nn.functional.pad(x, flat)

kernel_name = 'sequential_preLN__linear_kernel_hedgehog__dense_activation_relu__prompt'

def setup_inputs():

    _w = (lambda a: (lambda t: t.to(torch.bfloat16) if t.is_floating_point() else t)(torch.as_tensor(a)))
    def _A_setup():
        B, S, D, H = 1, 2048, 1024, 32
        Dh = D // H
        return {
            "x": _w((lambda *s: torch.randn(*s, dtype=torch.bfloat16) * 0.05)(B, S, D)),
            "wqkv": _w((lambda *s: torch.randn(*s, dtype=torch.bfloat16) * 0.2)(D, 3 * D)),
            "w_phi": _w((lambda *s: torch.randn(*s, dtype=torch.bfloat16) * 0.2)(H, Dh, Dh) * (Dh ** -0.5)),
            "wo": _w((lambda *s: torch.randn(*s, dtype=torch.bfloat16) * 0.05)(D, D)),
        }
    _A_in = _A_setup()

    def _B_setup():
        B, S, D, Dff = 1, 2048, 1024, 3584
        return {
            "x": _w((lambda *s: torch.randn(*s, dtype=torch.bfloat16) * 0.05)(B, S, D)),
            "w1": _w((lambda *s: torch.randn(*s, dtype=torch.bfloat16) * 0.05)(D, Dff)),
            "w2": _w((lambda *s: torch.randn(*s, dtype=torch.bfloat16) * 0.05)(Dff, D)),
        }
    _B_in = _B_setup()

    return {
        "A_x": _A_in["x"],
        "A_wqkv": _A_in["wqkv"],
        "A_w_phi": _A_in["w_phi"],
        "A_wo": _A_in["wo"],
        "B_x": _B_in["x"],
        "B_w1": _B_in["w1"],
        "B_w2": _B_in["w2"],
    }

def reference(A_x, A_wqkv, A_w_phi, A_wo, B_x, B_w1, B_w2):
    def _A_ref(x, wqkv, w_phi, wo):
        B, S, D = x.shape
        H = 32; Dh = D // H
        qkv = (lambda x, *d: x.permute(*d))((x @ wqkv).reshape(B, S, 3, H, Dh), 2, 0, 3, 1, 4)
        q, k, v = qkv[0], qkv[1], qkv[2]
        pq = torch.einsum("bhsd,hdf->bhsf", q, w_phi)
        pk = torch.einsum("bhsd,hdf->bhsf", k, w_phi)
        phi_q = torch.concatenate([_softmax(pq, axis=-1), _softmax(-pq, axis=-1)], axis=-1)
        phi_k = torch.concatenate([_softmax(pk, axis=-1), _softmax(-pk, axis=-1)], axis=-1)
        kv = torch.einsum("bhsf,bhsd->bhfd", phi_k, v)
        z  = torch.einsum("bhsf->bhf", phi_k)
        num = torch.einsum("bhqf,bhfd->bhqd", phi_q, kv)
        den = torch.einsum("bhqf,bhf->bhq", phi_q, z)[..., None] + 1e-6
        o = num / den
        return (lambda x, *d: x.permute(*d))(o, 0, 2, 1, 3).reshape(B, S, D) @ wo

    def _B_ref(x, w1, w2):
        h = torch.einsum("bsd,df->bsf", x, w1)
        zero = torch.zeros_like(h)
        h = torch.maximum(h, zero)
        return torch.einsum("bsf,fd->bsd", h, w2)

    x = A_x
    h = x + _A_ref(_layernorm(x, axis=-1), A_wqkv, A_w_phi, A_wo)
    h2 = h + _B_ref(_layernorm(h, axis=-1), B_w1, B_w2)
    return h2
```

```cpp
#include <hip/hip_runtime.h>
#include <hip/hip_bf16.h>
namespace naive {
typedef __hip_bfloat16 bf16;
constexpr int S=2048, D=1024, H=32, DH=32, FF=3584;
__device__ __forceinline__ float rb(float x){ return __bfloat162float(__float2bfloat16(x)); }
__global__ void ln_k(const bf16* x, bf16* y){
  int row=blockIdx.x; __shared__ float red[256];
  float s=0; for(int i=threadIdx.x;i<D;i+=256) s+=__bfloat162float(x[row*D+i]);
  red[threadIdx.x]=s; __syncthreads();
  for(int o=128;o>0;o>>=1){ if(threadIdx.x<o) red[threadIdx.x]+=red[threadIdx.x+o]; __syncthreads(); }
  float m=red[0]/D; __syncthreads();
  float v=0; for(int i=threadIdx.x;i<D;i+=256){ float d=__bfloat162float(x[row*D+i])-m; v+=d*d; }
  red[threadIdx.x]=v; __syncthreads();
  for(int o=128;o>0;o>>=1){ if(threadIdx.x<o) red[threadIdx.x]+=red[threadIdx.x+o]; __syncthreads(); }
  float var=rb(red[0]/D); float mb=rb(m);
  float sd=rb(sqrtf(rb(var+1e-5f)));
  for(int i=threadIdx.x;i<D;i+=256) y[row*D+i]=__float2bfloat16(rb(__bfloat162float(x[row*D+i])-mb)/sd);
}
__global__ void gemm_k(const bf16* A,const bf16* B,bf16* C,const bf16* res,int M,int N,int K,int mode){
  __shared__ float As[32][65], Bs[32][64];
  int tx=threadIdx.x%16, ty=threadIdx.x/16; int bm=blockIdx.y*64, bn=blockIdx.x*64;
  float acc[4][4]={};
  for(int k0=0;k0<K;k0+=32){
    for(int i=threadIdx.x;i<64*32;i+=256){ int r=i/32,c=i%32; As[c][r]=__bfloat162float(A[(size_t)(bm+r)*K+k0+c]); }
    for(int i=threadIdx.x;i<64*32;i+=256){ int r=i/64,c=i%64; Bs[r][c]=__bfloat162float(B[(size_t)(k0+r)*N+bn+c]); }
    __syncthreads();
    for(int k=0;k<32;++k){ float a[4],b[4];
      for(int i=0;i<4;++i) a[i]=As[k][ty*4+i];
      for(int j=0;j<4;++j) b[j]=Bs[k][tx*4+j];
      for(int i=0;i<4;++i) for(int j=0;j<4;++j) acc[i][j]+=a[i]*b[j]; }
    __syncthreads();
  }
  for(int i=0;i<4;++i) for(int j=0;j<4;++j){ size_t o=(size_t)(bm+ty*4+i)*N+bn+tx*4+j; float v=rb(acc[i][j]);
    if(mode==1) v=fmaxf(v,0.f); if(mode==2) v=v+__bfloat162float(res[o]); C[o]=__float2bfloat16(v); }
}
__global__ void phi_k(const bf16* qkv,const bf16* wphi,bf16* phi){
  int s=blockIdx.x, which=blockIdx.y; int h=threadIdx.x/32, f=threadIdx.x%32;
  float p=0; for(int d=0;d<32;++d) p+=__bfloat162float(qkv[(size_t)s*3*D+which*D+h*32+d])*__bfloat162float(wphi[(h*32+d)*32+f]);
  p=rb(p);
  float mx=p,mn=p; for(int o=16;o>0;o>>=1){ mx=fmaxf(mx,__shfl_xor(mx,o)); mn=fminf(mn,__shfl_xor(mn,o)); }
  float e1=expf(p-mx), e2=expf(mn-p); float s1=e1,s2=e2;
  for(int o=16;o>0;o>>=1){ s1+=__shfl_xor(s1,o); s2+=__shfl_xor(s2,o); }
  size_t base=(((size_t)which*H+h)*S+s)*64;
  phi[base+f]=__float2bfloat16(e1/s1); phi[base+32+f]=__float2bfloat16(e2/s2);
}
__global__ void kv_k(const bf16* phi,const bf16* qkv,float* kv){
  int h=blockIdx.x, f=blockIdx.y; int d=threadIdx.x;
  if(d>32) return; float a=0; const bf16* pk=phi+((size_t)H+h)*S*64;
  for(int s=0;s<S;++s){ float v= d<32? __bfloat162float(qkv[(size_t)s*3*D+2*D+h*32+d]) : 1.f; a+=__bfloat162float(pk[(size_t)s*64+f])*v; }
  kv[(h*64+f)*33+d]=rb(a);
}
__global__ void o_k(const bf16* phi,const float* kv,bf16* o){
  int s=blockIdx.x; int h=threadIdx.x/32,d=threadIdx.x%32; const bf16* pq=phi+((size_t)h*S+s)*64;
  float n=0,de=0; for(int f=0;f<64;++f){ float p=__bfloat162float(pq[f]); n+=p*kv[(h*64+f)*33+d]; de+=p*kv[(h*64+f)*33+32]; }
  o[(size_t)s*D+h*32+d]=__float2bfloat16(rb(n)/rb(rb(de)+1e-6f));
}
struct Bufs { bf16 *xn,*qkv,*phi,*o,*h,*hn,*f; float* kv; };
inline Bufs carve(void* ws){ char* p=(char*)ws; Bufs b; b.xn=(bf16*)p; p+=(size_t)S*D*2; b.qkv=(bf16*)p; p+=(size_t)S*3*D*2; b.phi=(bf16*)p; p+=(size_t)2*H*S*64*2;
  b.o=(bf16*)p; p+=(size_t)S*D*2; b.h=(bf16*)p; p+=(size_t)S*D*2; b.hn=(bf16*)p; p+=(size_t)S*D*2; b.f=(bf16*)p; p+=(size_t)S*FF*2; b.kv=(float*)p; return b; }
inline void run(void* const* d_in, void* d_out, void* d_ws, hipStream_t st){
  const bf16* x=(const bf16*)d_in[0]; const bf16* wqkv=(const bf16*)d_in[1]; const bf16* wphi=(const bf16*)d_in[2]; const bf16* wo=(const bf16*)d_in[3];
  const bf16* w1=(const bf16*)d_in[5]; const bf16* w2=(const bf16*)d_in[6];
  Bufs b=carve(d_ws);
  ln_k<<<S,256,0,st>>>(x,b.xn);
  gemm_k<<<dim3(3*D/64,S/64),256,0,st>>>(b.xn,wqkv,b.qkv,nullptr,S,3*D,D,0);
  phi_k<<<dim3(S,2),1024,0,st>>>(b.qkv,wphi,b.phi);
  kv_k<<<dim3(H,64),64,0,st>>>(b.phi,b.qkv,b.kv);
  o_k<<<S,1024,0,st>>>(b.phi,b.kv,b.o);
  gemm_k<<<dim3(D/64,S/64),256,0,st>>>(b.o,wo,b.h,x,S,D,D,2);
  ln_k<<<S,256,0,st>>>(b.h,b.hn);
  gemm_k<<<dim3(FF/64,S/64),256,0,st>>>(b.hn,w1,b.f,nullptr,S,FF,D,1);
  gemm_k<<<dim3(D/64,S/64),256,0,st>>>(b.f,w2,(bf16*)d_out,b.h,S,D,FF,2);
}
}
extern "C" void kernel_launch(void* const* d_in, const int* in_sizes, int n_in, void* d_out, int out_size, void* d_ws, size_t ws_size, hipStream_t stream) {
  naive::run(d_in,d_out,d_ws,stream);
}
```

```cpp
#include <hip/hip_runtime.h>
#include <hip/hip_bf16.h>

#define LAS __attribute__((address_space(3)))
#define DI __device__ __forceinline__
typedef __bf16 bf16x8 __attribute__((ext_vector_type(8)));
typedef __bf16 bf16x2 __attribute__((ext_vector_type(2)));
typedef short s16x4 __attribute__((ext_vector_type(4)));
typedef float f32x2 __attribute__((ext_vector_type(2)));
typedef float f32x4 __attribute__((ext_vector_type(4)));
typedef unsigned u32x2 __attribute__((ext_vector_type(2)));
typedef unsigned u32x4 __attribute__((ext_vector_type(4)));
typedef LAS char* lds_ptr;
typedef __amdgpu_buffer_rsrc_t rsrc_t;

constexpr int D = 1024, FF = 3584;
constexpr unsigned WS_CTL = 0;
constexpr unsigned WS_FLG = 4096;
constexpr unsigned WS_ST1 = 1u << 20;
constexpr unsigned WS_CSQ = WS_ST1 + (64u << 10);
constexpr unsigned WS_CS1 = WS_CSQ + (128u << 10);
constexpr unsigned WS_ZP = WS_CS1 + (128u << 10);
constexpr unsigned WS_ST2 = WS_ZP + (128u << 10);
constexpr unsigned WS_KVP = 2u << 20;
constexpr unsigned WS_O = 4u << 20;
constexpr unsigned WS_H = 8u << 20;
constexpr unsigned WS_XB = 12u << 20;
constexpr unsigned WS_F = 20u << 20;
constexpr unsigned WS_END = 36u << 20;
enum { FL_P0 = 0, FL_KV = 1, FL_O = 2, FL_H = 3, FL_F = 4, FL_X = 5, FL_CS = 6 };

#define TS(I) do {} while (0)

#define DBG(k) nullptr
#define CLK(v)
struct Params { const char* x; const char* wqkv; const char* wphi; const char* wo; const char* w1; const char* w2; char* ws; char* out; };

DI unsigned pk2(float a, float b) { f32x2 v = {a, b}; return __builtin_bit_cast(unsigned, __builtin_convertvector(v, bf16x2)); }
DI float bflo(unsigned u) { return __uint_as_float(u << 16); }
DI float bfhi(unsigned u) { return __uint_as_float(u & 0xffff0000u); }
DI u32x4 pack8(const f32x4& a, const f32x4& b) { return (u32x4){pk2(a[0], a[1]), pk2(a[2], a[3]), pk2(b[0], b[1]), pk2(b[2], b[3])}; }
DI bf16x8 asbf(const u32x4& v) { return __builtin_bit_cast(bf16x8, v); }
#define SB() __builtin_amdgcn_sched_barrier(0)
DI void bar() { asm volatile("" ::: "memory"); __builtin_amdgcn_s_barrier(); asm volatile("" ::: "memory"); }
DI void wait_lgkm0() { __builtin_amdgcn_s_waitcnt(0xC07F); asm volatile("" ::: "memory"); }
template <int N> DI void wait_vm() { asm volatile("s_waitcnt vmcnt(%0)" ::"i"(N) : "memory"); }
template <bool SC1> DI void dma(unsigned voff, const char* sbase, unsigned lds) {
  if (SC1) asm volatile("s_mov_b32 m0, %2\n\ts_nop 0\n\tglobal_load_lds_dwordx4 %0, %1 sc1" ::"v"(voff), "s"(sbase), "s"(lds) : "memory");
  else asm volatile("s_mov_b32 m0, %2\n\ts_nop 0\n\tglobal_load_lds_dwordx4 %0, %1" ::"v"(voff), "s"(sbase), "s"(lds) : "memory");
}
DI u32x4 lds128(lds_ptr p) { return *(const LAS u32x4*)p; }
DI u32x4 lds_tr2(lds_ptr p0, lds_ptr p1) {
  s16x4 a = __builtin_amdgcn_ds_read_tr16_b64_v4i16((LAS s16x4*)p0);
  s16x4 b = __builtin_amdgcn_ds_read_tr16_b64_v4i16((LAS s16x4*)p1);
  return __builtin_bit_cast(u32x4, __builtin_shufflevector(a, b, 0, 1, 2, 3, 4, 5, 6, 7));
}
DI u32x2 lds_tr(lds_ptr p) { return __builtin_bit_cast(u32x2, __builtin_amdgcn_ds_read_tr16_b64_v4i16((LAS s16x4*)p)); }
DI f32x4 mfma(const u32x4& a, const u32x4& b, const f32x4& c) { return __builtin_amdgcn_mfma_f32_16x16x32_bf16(asbf(a), asbf(b), c, 0, 0, 0); }
DI void mfma_ip(f32x4& acc, const u32x4& a, const u32x4& b) { asm volatile("v_mfma_f32_16x16x32_bf16 %0, %1, %2, %0" : "+v"(acc) : "v"(a), "v"(b)); }
DI u32x4 ld16(rsrc_t r, unsigned voff, unsigned soff) { return __builtin_amdgcn_raw_buffer_load_b128(r, voff, soff, 16); }
DI void st16(rsrc_t r, const u32x4& v, unsigned voff, unsigned soff) { __builtin_amdgcn_raw_buffer_store_b128(v, r, voff + soff, 0, 16);
}
DI void st8(rsrc_t r, const u32x2& v, unsigned voff, unsigned soff) { __builtin_amdgcn_raw_buffer_store_b64(v, r, voff, soff, 16); }
DI void st4(rsrc_t r, unsigned v, unsigned voff, unsigned soff) { __builtin_amdgcn_raw_buffer_store_b32(v, r, voff, soff, 16); }
DI float fb(unsigned u) { return __uint_as_float(u); }
DI unsigned bu(float f) { return __float_as_uint(f); }
DI float xsum16(float x) { auto r = __builtin_amdgcn_permlane16_swap(bu(x), bu(x), false, false); return fb(r[0]) + fb(r[1]); }
DI float xsum32(float x) { auto r = __builtin_amdgcn_permlane32_swap(bu(x), bu(x), false, false); return fb(r[0]) + fb(r[1]); }
template <int CTRL> DI float dpp(float x) { return __builtin_bit_cast(float, __builtin_amdgcn_update_dpp(0, __builtin_bit_cast(int, x), CTRL, 0xf, 0xf, true)); }
DI float row16_sum(float x) { x += dpp<0xB1>(x); x += dpp<0x4E>(x); x += dpp<0x141>(x); x += dpp<0x140>(x); return x; }

struct Sync { rsrc_t r; unsigned tag; };
DI void publish(const Sync& y, int phase, int idx, int w, int lane) {
  wait_vm<0>();
  if (lane == 0) st4(y.r, y.tag, 0, WS_FLG + (phase * 256 + idx) * 128 + w * 4);
}
DI void await(const Sync& y, unsigned rec, int n, int one, int lane) {
  const unsigned voff = WS_FLG + rec * 128;
  const unsigned long long t0 = __builtin_amdgcn_s_memrealtime();
  for (unsigned it = 0;; ++it) {
    const u32x4 v = ld16(y.r, voff, 0);
    const bool ok = lane >= n || (one < 0 ? (v[0] == y.tag && v[1] == y.tag && v[2] == y.tag && v[3] == y.tag)
                                          : ((one == 0 ? v[0] : one == 1 ? v[1] : one == 2 ? v[2] : v[3]) == y.tag));
    if (__all(ok)) break;
    if ((it & 63) == 63) {
      if (__builtin_amdgcn_readfirstlane(__builtin_amdgcn_raw_buffer_load_b32(y.r, 0, WS_CTL + 128, 16)) == y.tag) break;
      if (__builtin_amdgcn_s_memrealtime() - t0 > 4000000ull) { if (lane == 0) st4(y.r, y.tag, 0, WS_CTL + 128); break; }
    }
    __builtin_amdgcn_s_sleep(1);
  }
  asm volatile("" ::: "memory");
}

template <int NK> DI int kmap(int t, int c) {
  if (t == 0) return c;
  const int u = t - 1;
  if (u < NK - 4) { const int b = u >> 2, q = u & 3; return (q == c) ? 4 * (b + 1) + c : 4 * b + ((c + 1 + q - (q > c)) & 3); }
  return NK - 4 + ((c + 1 + (u - (NK - 4))) & 3);
}
enum { ORD_NAT = 0, ORD_PAIR = 1, ORD_F = 2 };
template <int NSUB_, bool HALF_, int NSLOT_, int NK_, int ORD_, int WSTRIDE_, bool XROW_> struct Cfg {
  static constexpr int NSUB = NSUB_, NSLOT = NSLOT_, NK = NK_, ORD = ORD_, WSTRIDE = WSTRIDE_;
  static constexpr bool HALF = HALF_, XROW = XROW_;
  static constexpr int STAGE = 16384 + NSUB * 8192 + (HALF ? 4096 : 0), PW = 4 + 2 * NSUB + (HALF ? 1 : 0), RING = STAGE * NSLOT;
};
typedef Cfg<3, false, 3, 16, ORD_NAT, 3 * D * 2, true> C1;
typedef Cfg<1, false, 5, 16, ORD_PAIR, D * 2, false> C3;
typedef Cfg<3, true, 3, 16, ORD_PAIR, FF * 2, false> C4;
typedef Cfg<2, false, 4, 28, ORD_F, D * 2, false> C5;

template <class C> struct Ld {
  const char* A; const char* W; int sub0, substep, halfcol, lw, c, kt0; unsigned vA, vW, vH;
  DI void init(int lane) {
    vA = C::XROW ? (lane >> 3) * 2048 + (((lane & 7) ^ (lane >> 4)) << 4) : lane * 16;
    const int rl = (C::ORD == ORD_NAT) ? 8 * (lane >> 5) + ((lane >> 3) & 3) : (lane >> 3);
    vW = rl * C::WSTRIDE + (((lane & 7) ^ (((lane >> 4) & 3) << 1)) << 4);
    vH = (lane >> 2) * C::WSTRIDE + (((lane & 3) ^ (((lane >> 4) & 1) << 1)) << 4);
  }
  DI void issueA(int s, unsigned base) const {
    const int kt = kt0 + kmap<C::NK>(s, c);
#pragma unroll
    for (int i = 0; i < 4; ++i) {
      if (C::XROW) dma<false>((i & 1) ? (vA ^ 64u) : vA, A + (32 * lw + 8 * i) * 2048 + kt * 128, base + lw * 4096 + i * 1024);
      else dma<true>(vA, A + (size_t)kt * 16384 + lw * 4096 + i * 1024, base + lw * 4096 + i * 1024);
    }
  }
  DI void issueW(int s, unsigned base) const {
    const int kt = kt0 + kmap<C::NK>(s, c), kb = lw >> 1, t = lw & 1;
#pragma unroll
    for (int i = 0; i < 2; ++i) {
      int ur;
      if (C::ORD == ORD_NAT) ur = 32 * kb + 16 * i + 4 * t;
      else if (C::ORD == ORD_F) ur = ((kt % 7) == 3) ? 16 * lw + 8 * i : 16 * kb + 32 * t + 8 * i;
      else ur = 16 * lw + 8 * i;
      const char* w = W + (size_t)(kt * 64 + ur) * C::WSTRIDE + sub0;
#pragma unroll
      for (int j = 0; j < C::NSUB; ++j) dma<false>(vW, w + j * substep, base + 16384 + j * 8192 + (2 * lw + i) * 1024);
    }
    if (C::HALF) dma<false>(vH, W + (size_t)(kt * 64 + 16 * lw) * C::WSTRIDE + halfcol, base + 16384 + C::NSUB * 8192 + lw * 1024);
  }
};
template <class C> DI void load_preW(const Ld<C>& L, unsigned ring) {
#pragma unroll
  for (int s = 0; s < C::NSLOT; ++s) L.issueW(s, ring + s * C::STAGE);
}
template <class C, bool SPLIT, int S> DI void load_early(const Ld<C>& L, unsigned ring) {
  if constexpr (S < C::NSLOT) {
    wait_vm<SPLIT ? 4 * (C::NSLOT - 1 - S) + C::PW * (S - 1) : C::PW * (C::NSLOT - 2)>();
    bar();
    L.issueW(S - 1 + C::NSLOT, ring + (S - 1) * C::STAGE); L.issueA(S - 1 + C::NSLOT, ring + (S - 1) * C::STAGE);
    load_early<C, SPLIT, S + 1>(L, ring);
  }
}
template <class C, bool SPLIT> DI void load_main(const Ld<C>& L, unsigned ring, unsigned long long* dbg = nullptr) {
#pragma unroll
  for (int s = 0; s < C::NSLOT; ++s) { if (!SPLIT) L.issueW(s, ring + s * C::STAGE); L.issueA(s, ring + s * C::STAGE); }
  wait_vm<(SPLIT ? 4 : C::PW) * (C::NSLOT - 1)>();
  bar();
  load_early<C, SPLIT, 1>(L, ring);
  int slot = C::NSLOT - 1;
#pragma unroll 1
  for (int s = C::NSLOT; s <= C::NK - C::NSLOT; ++s) {
    CLK(t0);
    wait_vm<(C::NSLOT - 2) * C::PW>();
    CLK(t1);
    bar();
    CLK(t2);
    const unsigned b = ring + slot * C::STAGE;
    L.issueW(s - 1 + C::NSLOT, b); L.issueA(s - 1 + C::NSLOT, b);
    slot = (slot + 1 == C::NSLOT) ? 0 : slot + 1;
    CLK(t3);
  }
}
template <class C> DI void load_tail() {
  if (C::NSLOT >= 5) { wait_vm<3 * C::PW>(); bar(); }
  if (C::NSLOT >= 4) { wait_vm<2 * C::PW>(); bar(); }
  wait_vm<C::PW>(); bar();
  wait_vm<0>(); bar();
  bar();
}

template <int MT, int NWF> struct Fr { u32x4 a[MT]; u32x2 w[NWF][2]; };
DI u32x4 cat(const u32x2 (&w)[2]) { return (u32x4){w[0][0], w[0][1], w[1][0], w[1][1]}; }
struct Adr { unsigned a[2]; unsigned w[4]; };
template <int KIND> struct Shape;
template <> struct Shape<1> { static constexpr int MT = 4, NWF = 6, NMF = 24, NACC = 24; };
template <> struct Shape<3> { static constexpr int MT = 2, NWF = 4, NMF = 8, NACC = 8; };
template <> struct Shape<4> { static constexpr int MT = 4, NWF = 8, NMF = 28, NACC = 28; };
template <> struct Shape<5> { static constexpr int MT = 4, NWF = 4, NMF = 16, NACC = 16; };
template <int KIND, int J> DI void rd1(Fr<Shape<KIND>::MT, Shape<KIND>::NWF>& f, lds_ptr sl, const Adr& ad, int kb) {
  constexpr int NWF = Shape<KIND>::NWF, MT = Shape<KIND>::MT;
  if constexpr (J < 2 * NWF) {
    constexpr int n = J >> 1, t = J & 1;
    if constexpr (KIND == 4 && n >= 6) f.w[n][t] = lds_tr(sl + ad.w[2 + (n - 6)] + kb * 2048 + t * 1024);
    else if constexpr (KIND == 1 || KIND == 4) f.w[n][t] = lds_tr(sl + ad.w[n & 1] + (n >> 1) * 8192 + kb * 4096 + t * 2048);
    else f.w[n][t] = lds_tr(sl + ad.w[n] + kb * 4096 + t * 2048);
  } else if constexpr (J < 2 * NWF + MT) {
    constexpr int m = J - 2 * NWF;
    if constexpr (KIND == 1) f.a[m] = lds128(sl + ad.a[kb] + m * 2048);
    else f.a[m] = lds128(sl + ad.a[m >> 1] + (m & 1) * 2048 + kb * 1024);
  }
}
template <int KIND, int I> DI void mm1(f32x4 (&acc)[Shape<KIND>::NACC], const Fr<Shape<KIND>::MT, Shape<KIND>::NWF>& f) {
  if constexpr (KIND == 1) { constexpr int m = I / 6, n = I % 6; if constexpr (n < 4) mfma_ip(acc[I], cat(f.w[n]), f.a[m]); else mfma_ip(acc[I], f.a[m], cat(f.w[n])); }
  else if constexpr (KIND == 4) { if constexpr (I < 24) mfma_ip(acc[I], cat(f.w[I % 6]), f.a[I / 6]); else mfma_ip(acc[I], cat(f.w[6 + ((I - 24) & 1)]), f.a[(I - 24) >> 1]); }
  else mfma_ip(acc[I], cat(f.w[I % 4]), f.a[I / 4]);
}
template <int KIND, int RPG, bool READ, int I = 0>
DI void step(f32x4 (&acc)[Shape<KIND>::NACC], const Fr<Shape<KIND>::MT, Shape<KIND>::NWF>& cur, Fr<Shape<KIND>::MT, Shape<KIND>::NWF>& nxt, lds_ptr sl, const Adr& ad, int kb) {
  if constexpr (I < Shape<KIND>::NMF) {
    if constexpr (READ) {
      rd1<KIND, I * RPG>(nxt, sl, ad, kb);
      if constexpr (RPG > 1) rd1<KIND, I * RPG + 1>(nxt, sl, ad, kb);
      if constexpr (RPG > 2) rd1<KIND, I * RPG + 2>(nxt, sl, ad, kb);
    }
    mm1<KIND, I>(acc, cur);
    SB();
    step<KIND, RPG, READ, I + 1>(acc, cur, nxt, sl, ad, kb);
  }
}
template <int KIND, int J = 0> DI void rdall(Fr<Shape<KIND>::MT, Shape<KIND>::NWF>& f, lds_ptr sl, const Adr& ad, int kb) {
  if constexpr (J < 2 * Shape<KIND>::NWF + Shape<KIND>::MT) { rd1<KIND, J>(f, sl, ad, kb); rdall<KIND, J + 1>(f, sl, ad, kb); }
}
template <class C, int KIND, int RPG, class Hook> DI void consume(f32x4 (&acc)[Shape<KIND>::NACC], lds_ptr ring, const Adr& ad, const Hook& hook) {
  Fr<Shape<KIND>::MT, Shape<KIND>::NWF> f0, f1;
#pragma unroll
  for (int i = 0; i < Shape<KIND>::NACC; ++i) acc[i] = (f32x4){0.f, 0.f, 0.f, 0.f};
  bar();
  rdall<KIND>(f0, ring, ad, 0);
  hook(-1);
  wait_lgkm0();
  int slot = 0;
#pragma unroll 1
  for (int s = 0; s < C::NK - 1; ++s) {
    lds_ptr sl = ring + slot * C::STAGE;
    slot = (slot + 1 == C::NSLOT) ? 0 : slot + 1;
    SB();
    step<KIND, RPG, true>(acc, f0, f1, sl, ad, 1);
    wait_lgkm0();
    bar();
    SB();
    step<KIND, RPG, true>(acc, f1, f0, ring + slot * C::STAGE, ad, 0);
    hook(s);
    wait_lgkm0();
  }
  SB();
  step<KIND, RPG, true>(acc, f0, f1, ring + slot * C::STAGE, ad, 1);
  wait_lgkm0();
  bar();
  SB();
  step<KIND, RPG, false>(acc, f1, f0, ring, ad, 0);
  asm volatile("s_nop 7\n\ts_nop 3");
  SB();
}
template <class C, int KIND, class Hook> DI void consume_s(f32x4 (&acc)[Shape<KIND>::NACC], lds_ptr ring, const Adr& ad, const Hook& hook) {
  Fr<Shape<KIND>::MT, Shape<KIND>::NWF> f0, f1, g0, g1;
#pragma unroll
  for (int i = 0; i < Shape<KIND>::NACC; ++i) acc[i] = (f32x4){0.f, 0.f, 0.f, 0.f};
  bar();
  rdall<KIND>(f0, ring, ad, 0); rdall<KIND>(f1, ring, ad, 1);
  hook(-1);
  int slot = 0;
#pragma unroll 1
  for (int s = 0; s < C::NK - 2; s += 2) {
    slot = (slot + 1 == C::NSLOT) ? 0 : slot + 1;
    wait_lgkm0(); bar(); SB();
    step<KIND, 2, true>(acc, f0, g0, ring + slot * C::STAGE, ad, 0);
    step<KIND, 2, true>(acc, f1, g1, ring + slot * C::STAGE, ad, 1);
    slot = (slot + 1 == C::NSLOT) ? 0 : slot + 1;
    wait_lgkm0(); bar(); SB();
    step<KIND, 2, true>(acc, g0, f0, ring + slot * C::STAGE, ad, 0);
    step<KIND, 2, true>(acc, g1, f1, ring + slot * C::STAGE, ad, 1);
  }
  slot = (slot + 1 == C::NSLOT) ? 0 : slot + 1;
  wait_lgkm0(); bar(); SB();
  step<KIND, 2, true>(acc, f0, g0, ring + slot * C::STAGE, ad, 0);
  step<KIND, 2, true>(acc, f1, g1, ring + slot * C::STAGE, ad, 1);
  wait_lgkm0(); bar(); SB();
  step<KIND, 2, false>(acc, g0, f0, ring, ad, 0);
  step<KIND, 2, false>(acc, g1, f1, ring, ad, 1);
  asm volatile("s_nop 7\n\ts_nop 3");
  SB();
}
DI unsigned wlane(int lane, int nt) {
  const int g = lane >> 4, q = (lane >> 2) & 3, p = lane & 3, sw = (2 * g + (q >> 1)) & 3;
  return (4 * g + q) * 128 + ((nt ^ sw) << 5) + p * 8;
}
DI unsigned hlane(int lane, int nt) {
  const int g = lane >> 4, q = (lane >> 2) & 3, p = lane & 3;
  return (4 * g + q) * 64 + ((nt ^ (g & 1)) << 5) + p * 8;
}

template <int OP> DI float op2(float a, float b) { return OP == 0 ? a + b : OP == 1 ? fmaxf(a, b) : fminf(a, b); }
template <int OP> DI float red4(float x0, float x1, float x2, float x3) {
  auto a = __builtin_amdgcn_permlane16_swap(bu(x0), bu(x1), false, false);
  auto b = __builtin_amdgcn_permlane16_swap(bu(x2), bu(x3), false, false);
  const float y0 = op2<OP>(fb(a[0]), fb(a[1])), y1 = op2<OP>(fb(b[0]), fb(b[1]));
  auto c = __builtin_amdgcn_permlane32_swap(bu(y0), bu(y1), false, false);
  return op2<OP>(fb(c[0]), fb(c[1]));
}
DI void spread(float z, float (&o)[4]) {
  auto a = __builtin_amdgcn_permlane32_swap(bu(z), bu(z), false, false);
  auto b = __builtin_amdgcn_permlane16_swap(a[0], a[0], false, false);
  auto c = __builtin_amdgcn_permlane16_swap(a[1], a[1], false, false);
  o[0] = fb(b[0]); o[1] = fb(b[1]); o[2] = fb(c[0]); o[3] = fb(c[1]);
}
DI void dual_softmax4(const f32x4 (&p)[4][2], u32x4 (&pp)[4], u32x4 (&pn)[4]) {
  constexpr float L2E = 1.4426950408889634f;
  float mx[4], mn[4];
#pragma unroll
  for (int m = 0; m < 4; ++m) {
    mx[m] = fmaxf(fmaxf(fmaxf(p[m][0][0], p[m][0][1]), fmaxf(p[m][0][2], p[m][0][3])), fmaxf(fmaxf(p[m][1][0], p[m][1][1]), fmaxf(p[m][1][2], p[m][1][3])));
    mn[m] = fminf(fminf(fminf(p[m][0][0], p[m][0][1]), fminf(p[m][0][2], p[m][0][3])), fminf(fminf(p[m][1][0], p[m][1][1]), fminf(p[m][1][2], p[m][1][3])));
  }
  spread(-L2E * red4<1>(mx[0], mx[1], mx[2], mx[3]), mx);
  spread(L2E * red4<2>(mn[0], mn[1], mn[2], mn[3]), mn);
  f32x4 ep[4][2], en[4][2];
  float sp[4], sn[4];
#pragma unroll
  for (int m = 0; m < 4; ++m) {
#pragma unroll
    for (int F = 0; F < 2; ++F) {
      const f32x4 a = p[m][F] * L2E + mx[m], b = p[m][F] * (-L2E) + mn[m];
#pragma unroll
      for (int r = 0; r < 4; ++r) { ep[m][F][r] = __builtin_amdgcn_exp2f(a[r]); en[m][F][r] = __builtin_amdgcn_exp2f(b[r]); }
    }
    const f32x4 a = ep[m][0] + ep[m][1], b = en[m][0] + en[m][1];
    sp[m] = (a[0] + a[1]) + (a[2] + a[3]); sn[m] = (b[0] + b[1]) + (b[2] + b[3]);
  }
  spread(__builtin_amdgcn_rcpf(red4<0>(sp[0], sp[1], sp[2], sp[3])), sp);
  spread(__builtin_amdgcn_rcpf(red4<0>(sn[0], sn[1], sn[2], sn[3])), sn);
#pragma unroll
  for (int m = 0; m < 4; ++m) { pp[m] = pack8(ep[m][0] * sp[m], ep[m][1] * sp[m]); pn[m] = pack8(en[m][0] * sn[m], en[m][1] * sn[m]); }
}

DI void colsum128(const char* w, int stride, rsrc_t r, unsigned dst, int lane) {
  u32x4 v[16];
#pragma unroll
  for (int i = 0; i < 16; ++i) v[i] = *(const u32x4*)(w + (size_t)(8 * i + (lane >> 3)) * stride + (lane & 7) * 16);
  float s[8];
#pragma unroll
  for (int j = 0; j < 8; ++j) s[j] = 0.f;
#pragma unroll
  for (int i = 0; i < 16; ++i)
#pragma unroll
    for (int j = 0; j < 4; ++j) { s[2 * j] += bflo(v[i][j]); s[2 * j + 1] += bfhi(v[i][j]); }
#pragma unroll
  for (int j = 0; j < 8; ++j) { s[j] += dpp<0x128>(s[j]); s[j] = xsum32(xsum16(s[j])); }
  if (lane < 8) {
    st16(r, (u32x4){bu(s[0]), bu(s[1]), bu(s[2]), bu(s[3])}, lane * 32, dst);
    st16(r, (u32x4){bu(s[4]), bu(s[5]), bu(s[6]), bu(s[7])}, lane * 32 + 16, dst);
  }
}

template <int XN, int TM, int TN> DI void tile_of(int bid, int& tm, int& tn, int& c) {
  const int x = bid & 7, r = bid >> 3, li = r % TM, lj = r / TM;
  tm = (x / XN) * TM + li; tn = (x % XN) * TN + lj; c = (li + lj) & 3;
}

constexpr int R1 = 40960, R3 = 40960, R4 = 0, R5 = 0;
constexpr int S1_ST = 0, S1_CSR = 1024, S1_CSF = 7168, S1_XF = 7936, S1_WP = 8192, S1_IMG = 12288, S1_KV = 29696;
constexpr int S4_STR = C4::RING, S4_CSR = S4_STR + 16384, S4_STF = S4_CSR + 8192, S4_CSF = S4_STF + 1024;
constexpr int LDS_BYTES = 163840;
static_assert(S4_CSF + 1024 <= LDS_BYTES && S1_KV + 8448 <= R1 && R1 + C1::RING <= LDS_BYTES && R3 + C3::RING <= LDS_BYTES && C5::RING <= S4_STR, "lds");

__global__ __launch_bounds__(512, 2) void fused(Params P) {
  extern __shared__ __attribute__((aligned(16))) char smem_[];
  lds_ptr smem = (lds_ptr)smem_;
  const unsigned lds0 = (unsigned)(size_t)smem;
  const int wid = __builtin_amdgcn_readfirstlane(threadIdx.x >> 6), lane = threadIdx.x & 63, bid = blockIdx.x;
  int tm1, tn1, c1, tm3, tn3, c3;
  tile_of<4, 8, 4>(bid, tm1, tn1, c1);
  tile_of<2, 4, 8>(bid, tm3, tn3, c3);
  const int kh = bid & 1, tm5 = ((bid & 7) >> 1) * 4 + ((bid >> 3) & 3), tn5 = bid >> 5, c5 = ((bid >> 3) + tn5) & 3;
  const int par = tn1 & 1, base7 = 7 * (tn1 >> 1), ch0 = base7 + 4 * par;
  const int g = lane >> 4, m16 = lane & 15;
  Sync y;
  y.r = __builtin_amdgcn_make_buffer_rsrc(P.ws, 0, WS_END, 0x00020000);
  asm volatile("s_load_dword %0, %1, 0x0\n\ts_waitcnt lgkmcnt(0)" : "=s"(y.tag) : "s"(P.ws) : "memory");
  y.tag += 1u;
  if (wid == 0) TS(0);

  if (wid >= 4) {
    __builtin_amdgcn_s_setprio(3);
    const int lw = wid - 4, lt = threadIdx.x - 256;
    {
      Ld<C1> L; L.A = P.x + (size_t)tm1 * 128 * 2048; L.W = P.wqkv; L.sub0 = tn1 * 128; L.substep = 2048; L.halfcol = 0; L.lw = lw; L.c = c1; L.kt0 = 0; L.init(lane);
      load_main<C1, false>(L, lds0 + R1, DBG(0));
      await(y, FL_P0 * 256 + (lane < 16 ? tm1 * 16 + lane : lane < 28 ? ((lane - 16) / 3) * 48 + ((lane - 16) % 3) * 16 + tn1 : 0), 28, -1, lane);
      if (lw == 3) dma<true>(lane * 16, P.ws + WS_ST1 + tm1 * 1024, lds0 + S1_ST);
      else {
#pragma unroll
        for (int i = 0; i < 2; ++i)
          dma<true>((lane >> 4) * 12288 + (lane & 15) * 16, P.ws + WS_CSQ + i * 49152 + lw * 4096 + tn1 * 256, lds0 + S1_CSR + lw * 2048 + i * 1024);
      }
      dma<false>(lane * 16, P.wphi + tn1 * 4096 + lw * 1024, lds0 + S1_WP + lw * 1024);
      wait_vm<0>(); bar();
      if (lt < 192) {
        const LAS float* rawp = (const LAS float*)(smem + S1_CSR) + (lt >> 6) * 512 + (lt & 63);
        float s = 0.f;
#pragma unroll
        for (int i = 0; i < 8; ++i) s += rawp[i * 64];
        ((LAS float*)(smem + S1_CSF))[lt] = s;
      }
      wait_lgkm0(); bar();
      bar();
    }
    if (wid == 4) TS(16);
    {
      Ld<C3> L; L.A = P.ws + WS_O + (size_t)tm3 * 16 * 16384; L.W = P.wo; L.sub0 = tn3 * 128; L.substep = 0; L.halfcol = 0; L.lw = lw; L.c = c3; L.kt0 = 0; L.init(lane);
      load_preW<C3>(L, lds0 + R3);
      if (lw >= 2 && bid < 224)
        colsum128(P.w1 + (size_t)((bid / 56) * 256 + (lw - 2) * 128) * (FF * 2) + (bid % 56) * 128, FF * 2, y.r, WS_CS1 + (((bid / 56) * 2 + lw - 2) * FF + (bid % 56) * 64) * 4, lane);
      publish(y, FL_CS, bid, lw, lane);
      await(y, FL_KV * 256 + m16 * 16 + tn1, 16, -1, lane);
      if (wid == 4) TS(17);
      {
        u32x4 v[2][16]; unsigned zz[16];
#pragma unroll
        for (int h = 0; h < 2; ++h)
#pragma unroll
          for (int r = 0; r < 16; ++r) v[h][r] = ld16(y.r, lt * 16, WS_KVP + ((2 * tn1 + h) * 16 + r) * 4096);
#pragma unroll
        for (int r = 0; r < 16; ++r) zz[r] = __builtin_amdgcn_raw_buffer_load_b32(y.r, lane * 4, WS_ZP + ((2 * tn1 + (lw & 1)) * 16 + r) * 256, 16);
#pragma unroll
        for (int h = 0; h < 2; ++h) {
          float s[8];
#pragma unroll
          for (int j = 0; j < 8; ++j) s[j] = 0.f;
#pragma unroll
          for (int r = 0; r < 16; ++r)
#pragma unroll
            for (int j = 0; j < 4; ++j) { s[2 * j] += bflo(v[h][r][j]); s[2 * j + 1] += bfhi(v[h][r][j]); }
          *(LAS u32x4*)(smem + S1_KV + h * 4096 + lt * 16) = (u32x4){pk2(s[0], s[1]), pk2(s[2], s[3]), pk2(s[4], s[5]), pk2(s[6], s[7])};
        }
        float z = 0.f;
#pragma unroll
        for (int r = 0; r < 16; ++r) z += fb(zz[r]);
        if (lw < 2) *(LAS __bf16*)(smem + S1_KV + 8192 + lw * 128 + lane * 2) = (__bf16)z;
      }
      wait_lgkm0(); bar();
      await(y, FL_O * 256 + tm3 * 16 + m16, 16, -1, lane);
      if (wid == 4) TS(18);
      load_main<C3, true>(L, lds0 + R3, DBG(1));
      load_tail<C3>();
    }
    if (wid == 4) TS(19);
    {
      Ld<C4> L; L.A = P.ws + WS_H + (size_t)tm1 * 16 * 16384; L.W = P.w1; L.sub0 = ch0 * 128; L.substep = 128; L.halfcol = (base7 + 3) * 128 + par * 64; L.lw = lw; L.c = c1; L.kt0 = 0; L.init(lane);
      load_preW<C4>(L, lds0 + R4);
      await(y, FL_H * 256 + tm1 * 16 + m16, 16, -1, lane);
      if (wid == 4) TS(20);
      load_main<C4, true>(L, lds0 + R4, DBG(2));
      await(y, FL_CS * 256 + (lane >> 2) * 56 + base7 + 3 * par + (lane & 3), 16, -1, lane);
#pragma unroll
      for (int i = 0; i < 4; ++i) dma<true>(lane * 16, P.ws + WS_ST2 + (tm1 * 16 + lw * 4 + i) * 1024, lds0 + S4_STR + (lw * 4 + i) * 1024);
#pragma unroll
      for (int i = 0; i < 2; ++i) dma<true>(lane * 16, P.ws + WS_CS1 + ((lw * 2 + i) * FF + (base7 + 3 * par) * 64) * 4, lds0 + S4_CSR + (lw * 2 + i) * 1024);
      wait_vm<0>(); bar();
      {
        const LAS float* rawp = (const LAS float*)(smem + S4_CSR) + lt;
        float s = 0.f;
#pragma unroll
        for (int i = 0; i < 8; ++i) s += rawp[i * 256];
        ((LAS float*)(smem + S4_CSF))[lt] = s;
        if (lt < 128) {
          f32x2 st[16]; float mu = 0.f, m2 = 0.f;
#pragma unroll
          for (int i = 0; i < 16; ++i) { st[i] = *(const LAS f32x2*)(smem + S4_STR + i * 1024 + lt * 8); mu += st[i][0]; }
          mu *= (1.f / 16.f);
#pragma unroll
          for (int i = 0; i < 16; ++i) { const float d = st[i][0] - mu; m2 += st[i][1] + 64.f * d * d; }
          *(LAS f32x2*)(smem + S4_STF + lt * 8) = (f32x2){mu, 1.f / sqrtf(m2 * (1.f / D) + 1e-5f)};
        }
      }
      wait_lgkm0(); bar();
      bar();
    }
    if (wid == 4) TS(21);
    {
      Ld<C5> L; L.A = P.ws + WS_F + (size_t)tm5 * 56 * 16384; L.W = P.w2; L.sub0 = tn5 * 256; L.substep = 128; L.halfcol = 0; L.lw = lw; L.c = c5; L.kt0 = 28 * kh; L.init(lane);
      load_preW<C5>(L, lds0 + R5);
      await(y, FL_F * 256 + tm5 * 16 + 8 * kh + (lane & 7), 8, -1, lane);
      if (wid == 4) TS(22);
      load_main<C5, true>(L, lds0 + R5, DBG(3));
      load_tail<C5>();
    }
    if (wid == 4) TS(23);
    return;
  }

  const int wm = wid >> 1, wn = wid & 1;
  if (lane == 0) ((LAS unsigned*)(smem + S1_XF))[wid] = 0u;
  if (wid < 2) {
    const int row = bid * 8 + wid * 4 + g;
    u32x4 v[8];
#pragma unroll
    for (int i = 0; i < 8; ++i) v[i] = *(const u32x4*)(P.x + (size_t)row * 2048 + (m16 + 16 * i) * 16);
    float s = 0.f;
#pragma unroll
    for (int i = 0; i < 8; ++i)
#pragma unroll
      for (int j = 0; j < 4; ++j) s += bflo(v[i][j]) + bfhi(v[i][j]);
    const float mean = row16_sum(s) * (1.f / D);
    float q = 0.f;
#pragma unroll
    for (int i = 0; i < 8; ++i)
#pragma unroll
      for (int j = 0; j < 4; ++j) { const float a = bflo(v[i][j]) - mean, b = bfhi(v[i][j]) - mean; q += a * a + b * b; }
    const float rstd = 1.f / sqrtf(row16_sum(q) * (1.f / D) + 1e-5f);
    if (m16 == 0) st8(y.r, (u32x2){bu(mean), bu(rstd)}, row * 8, WS_ST1);
  } else if (bid < 192) {
    colsum128(P.wqkv + (size_t)((bid / 48) * 256 + (wid - 2) * 128) * (3 * D * 2) + (bid % 48) * 128, 3 * D * 2, y.r, WS_CSQ + (((bid / 48) * 2 + wid - 2) * 3 * D + (bid % 48) * 64) * 4, lane);
  }
  {
    f32x4 acc[24];
    Adr ad;
    ad.a[0] = (wm * 64 + m16) * 128 + ((g ^ (m16 >> 1)) << 4); ad.a[1] = ad.a[0] ^ 64u;
    ad.w[0] = 16384 + wlane(lane, 2 * wn); ad.w[1] = 16384 + wlane(lane, 2 * wn + 1); ad.w[2] = 0; ad.w[3] = 0;
    consume<C1, 1, 1>(acc, smem + R1, ad, [&](int s) { if (s == 1) publish(y, FL_P0, bid, wid, lane); if (s < 0 && wid == 0) TS(1); });
    if (wid == 0) TS(2);
    const int head = 2 * tn1 + wn;
    f32x4 cs[4]; float csv[2];
#pragma unroll
    for (int n = 0; n < 4; ++n) cs[n] = *(const LAS f32x4*)(smem + S1_CSF + ((n >> 1) * 64 + wn * 32 + (n & 1) * 16 + 4 * g) * 4);
#pragma unroll
    for (int n = 0; n < 2; ++n) csv[n] = *(const LAS float*)(smem + S1_CSF + (128 + wn * 32 + n * 16 + m16) * 4);
    u32x4 wp[2];
#pragma unroll
    for (int F = 0; F < 2; ++F) { lds_ptr a0 = smem + S1_WP + wn * 2048 + (4 * g + (m16 >> 2)) * 64 + F * 32 + (lane & 3) * 8; wp[F] = lds_tr2(a0, a0 + 1024); }
    u32x4 qb[4], vb[2][2];
    f32x4 p[4][2];
    const f32x4 zero = {0.f, 0.f, 0.f, 0.f};
    {
      f32x4 vv[4][2];
#pragma unroll
      for (int m = 0; m < 4; ++m) {
        const f32x2 st = *(const LAS f32x2*)(smem + S1_ST + (wm * 64 + m * 16 + m16) * 8);
        const f32x4 s0 = *(const LAS f32x4*)(smem + S1_ST + (wm * 64 + m * 16 + 4 * g) * 8), s1 = *(const LAS f32x4*)(smem + S1_ST + (wm * 64 + m * 16 + 4 * g) * 8 + 16);
        const f32x4 mu = {s0[0], s0[2], s1[0], s1[2]}, rs = {s0[1], s0[3], s1[1], s1[3]};
        qb[m] = pack8((acc[m * 6] - cs[0] * st[0]) * st[1], (acc[m * 6 + 1] - cs[1] * st[0]) * st[1]);
        const u32x4 kb = pack8((acc[m * 6 + 2] - cs[2] * st[0]) * st[1], (acc[m * 6 + 3] - cs[3] * st[0]) * st[1]);
        p[m][0] = mfma(wp[0], kb, zero); p[m][1] = mfma(wp[1], kb, zero);
        vv[m][0] = (acc[m * 6 + 4] - mu * csv[0]) * rs; vv[m][1] = (acc[m * 6 + 5] - mu * csv[1]) * rs;
      }
#pragma unroll
      for (int u = 0; u < 2; ++u) { vb[u][0] = pack8(vv[2 * u][0], vv[2 * u + 1][0]); vb[u][1] = pack8(vv[2 * u][1], vv[2 * u + 1][1]); }
    }
    u32x4 pp[4], pn[4];
    dual_softmax4(p, pp, pn);
    lds_ptr img = smem + S1_IMG + wid * 4352;
    f32x4 kv[4][2], z[4];
#pragma unroll
    for (int F = 0; F < 4; ++F) { kv[F][0] = zero; kv[F][1] = zero; z[F] = zero; }
    const u32x4 ones = {0x3f803f80u, 0x3f803f80u, 0x3f803f80u, 0x3f803f80u};
#pragma unroll
    for (int u = 0; u < 2; ++u) {
#pragma unroll
      for (int t = 0; t < 2; ++t) {
        lds_ptr w = img + (16 * t + m16) * 136 + 8 * g;
        *(LAS u32x2*)(w) = (u32x2){pp[2 * u + t][0], pp[2 * u + t][1]}; *(LAS u32x2*)(w + 32) = (u32x2){pp[2 * u + t][2], pp[2 * u + t][3]};
        *(LAS u32x2*)(w + 64) = (u32x2){pn[2 * u + t][0], pn[2 * u + t][1]}; *(LAS u32x2*)(w + 96) = (u32x2){pn[2 * u + t][2], pn[2 * u + t][3]};
      }
      wait_lgkm0();
#pragma unroll
      for (int F = 0; F < 4; ++F) {
        lds_ptr a0 = img + (4 * g + (m16 >> 2)) * 136 + F * 32 + (lane & 3) * 8;
        const u32x4 pa = lds_tr2(a0, a0 + 16 * 136);
        kv[F][0] = mfma(pa, vb[u][0], kv[F][0]); kv[F][1] = mfma(pa, vb[u][1], kv[F][1]); z[F] = mfma(pa, ones, z[F]);
      }
      wait_lgkm0();
    }
    {
      lds_ptr xi = smem + S1_IMG + (wid ^ 2) * 4352;
#pragma unroll
      for (int i = 0; i < 4; ++i) *(LAS f32x4*)(img + i * 1024 + lane * 16) = wm ? kv[i >> 1][i & 1] : kv[2 + (i >> 1)][i & 1];
      if (m16 == 0) { *(LAS f32x4*)(img + 4096 + g * 32) = wm ? z[0] : z[2]; *(LAS f32x4*)(img + 4096 + g * 32 + 16) = wm ? z[1] : z[3]; }
      wait_lgkm0();
      volatile LAS unsigned* xf = (volatile LAS unsigned*)(smem + S1_XF);
      if (lane == 0) xf[wid] = 1u;
      while (xf[wid ^ 2] == 0u) __builtin_amdgcn_s_sleep(0);
      asm volatile("" ::: "memory");
      f32x4 o[4];
#pragma unroll
      for (int i = 0; i < 4; ++i) o[i] = *(const LAS f32x4*)(xi + i * 1024 + lane * 16) + (wm ? kv[2 + (i >> 1)][i & 1] : kv[i >> 1][i & 1]);
      const unsigned kd = WS_KVP + (head * 16 + tm1) * 4096 + wm * 2048;
      st16(y.r, pack8(o[0], o[2]), lane * 16, kd);
      st16(y.r, pack8(o[1], o[3]), lane * 16, kd + 1024);
      if (m16 == 0) {
        const f32x4 za = *(const LAS f32x4*)(xi + 4096 + g * 32) + (wm ? z[2] : z[0]), zb = *(const LAS f32x4*)(xi + 4096 + g * 32 + 16) + (wm ? z[3] : z[1]);
        st16(y.r, __builtin_bit_cast(u32x4, za), g * 32, WS_ZP + (head * 16 + tm1) * 256 + wm * 128);
        st16(y.r, __builtin_bit_cast(u32x4, zb), g * 32 + 16, WS_ZP + (head * 16 + tm1) * 256 + wm * 128);
      }
      publish(y, FL_KV, tm1 * 16 + tn1, wid, lane);
    }
    if (wid == 0) TS(3);
#pragma unroll
    for (int m = 0; m < 4; ++m) { p[m][0] = mfma(wp[0], qb[m], zero); p[m][1] = mfma(wp[1], qb[m], zero); }
    dual_softmax4(p, pp, pn);
    if (wid == 0) TS(4);
    bar();
    if (wid == 0) TS(5);
    u32x4 ka[2][2], za[2];
#pragma unroll
    for (int ks = 0; ks < 2; ++ks) {
      ka[ks][0] = lds128(smem + S1_KV + wn * 4096 + ks * 2048 + lane * 16); ka[ks][1] = lds128(smem + S1_KV + wn * 4096 + ks * 2048 + 1024 + lane * 16);
      za[ks] = lds128(smem + S1_KV + 8192 + wn * 128 + ks * 64 + g * 16);
    }
#pragma unroll
    for (int m = 0; m < 4; ++m) {
      f32x4 n0 = mfma(ka[0][0], pp[m], zero), n1 = mfma(ka[0][1], pp[m], zero), dn = mfma(za[0], pp[m], zero);
      n0 = mfma(ka[1][0], pn[m], n0); n1 = mfma(ka[1][1], pn[m], n1); dn = mfma(za[1], pn[m], dn);
      const float rd = 1.f / (dn[0] + 1e-6f);
      st16(y.r, pack8(n0 * rd, n1 * rd), lane * 16, WS_O + (((tm1 * 16 + tn1) * 8 + wm * 4 + m) * 2 + wn) * 1024);
    }
    publish(y, FL_O, tm1 * 16 + tn1, wid, lane);
    if (wid == 0) TS(6);
  }
  {
    u32x4 xr[2][2];
#pragma unroll
    for (int m = 0; m < 2; ++m)
#pragma unroll
      for (int Q = 0; Q < 2; ++Q)
        xr[m][Q] = *(const u32x4*)(P.x + (size_t)(tm3 * 128 + wid * 32 + m * 16 + m16) * 2048 + tn3 * 128 + Q * 64 + (g & 1) * 32 + (g >> 1) * 16);
    f32x4 acc[8];
    Adr ad; ad.a[0] = wid * 4096 + lane * 16; ad.a[1] = 0;
#pragma unroll
    for (int n = 0; n < 4; ++n) ad.w[n] = 16384 + wlane(lane, n);
    consume_s<C3, 3>(acc, smem + R3, ad, [&](int) { if (wid == 0) TS(7); });
    if (wid == 0) TS(8);
#pragma unroll
    for (int m = 0; m < 2; ++m) {
      float hv[16];
#pragma unroll
      for (int Q = 0; Q < 2; ++Q) {
        auto r0 = __builtin_amdgcn_permlane16_swap(xr[m][Q][0], xr[m][Q][2], false, false);
        auto r1 = __builtin_amdgcn_permlane16_swap(xr[m][Q][1], xr[m][Q][3], false, false);
        const f32x4 a = acc[m * 4 + 2 * Q], b = acc[m * 4 + 2 * Q + 1];
        const u32x4 hp = {pk2(a[0] + bflo(r0[0]), a[1] + bfhi(r0[0])), pk2(a[2] + bflo(r1[0]), a[3] + bfhi(r1[0])),
                          pk2(b[0] + bflo(r0[1]), b[1] + bfhi(r0[1])), pk2(b[2] + bflo(r1[1]), b[3] + bfhi(r1[1]))};
        st16(y.r, hp, lane * 16, WS_H + (((tm3 * 16 + tn3) * 8 + wid * 2 + m) * 2 + Q) * 1024);
#pragma unroll
        for (int j = 0; j < 4; ++j) { hv[Q * 8 + 2 * j] = bflo(hp[j]); hv[Q * 8 + 2 * j + 1] = bfhi(hp[j]); }
      }
      float s = 0.f;
#pragma unroll
      for (int j = 0; j < 16; ++j) s += hv[j];
      s = xsum32(xsum16(s)) * (1.f / 64.f);
      float q = 0.f;
#pragma unroll
      for (int j = 0; j < 16; ++j) { const float d = hv[j] - s; q += d * d; }
      q = xsum32(xsum16(q));
      if (g == 0) st8(y.r, (u32x2){bu(s), bu(q)}, m16 * 8, WS_ST2 + (tm3 * 16 + tn3) * 1024 + (wid * 32 + m * 16) * 8);
    }
    publish(y, FL_H, tm3 * 16 + tn3, wid, lane);
    if (wid == 0) TS(9);
  }
  {
    f32x4 acc[28];
    Adr ad;
    ad.a[0] = wm * 8192 + wn * 4096 + lane * 16; ad.a[1] = wm * 8192 + (wn ^ 1) * 4096 + lane * 16;
    ad.w[0] = 16384 + wlane(lane, wn); ad.w[1] = 16384 + wlane(lane, 2 + wn);
    ad.w[2] = 16384 + 3 * 8192 + hlane(lane, 0); ad.w[3] = 16384 + 3 * 8192 + hlane(lane, 1);
    consume<C4, 4, 1>(acc, smem + R4, ad, [&](int s) { if (s < 0 && wid == 0) TS(10); });
    if (wid == 0) TS(11);
    f32x4 cs[8];
#pragma unroll
    for (int n = 0; n < 6; ++n) cs[n] = *(const LAS f32x4*)(smem + S4_CSF + ((par + (n >> 1)) * 64 + (2 * (n & 1) + wn) * 16 + 4 * g) * 4);
#pragma unroll
    for (int n = 0; n < 2; ++n) cs[6 + n] = *(const LAS f32x4*)(smem + S4_CSF + ((par ? 32 : 192) + n * 16 + 4 * g) * 4);
    const f32x4 zero = {0.f, 0.f, 0.f, 0.f};
#pragma unroll
    for (int m = 0; m < 4; ++m) {
      const int mt = wm * 4 + ((m + 2 * wn) & 3);
      const f32x2 st = *(const LAS f32x2*)(smem + S4_STF + (mt * 16 + m16) * 8);
      const unsigned fd = WS_F + (tm1 * 56 * 16 + mt * 2) * 1024;
#pragma unroll
      for (int i = 0; i < 3; ++i)
        st16(y.r, pack8(__builtin_elementwise_max((acc[m * 6 + 2 * i] - cs[2 * i] * st[0]) * st[1], zero),
                        __builtin_elementwise_max((acc[m * 6 + 2 * i + 1] - cs[2 * i + 1] * st[0]) * st[1], zero)), lane * 16, fd + (ch0 + i) * 16384 + wn * 1024);
      if (m < 2)
        st16(y.r, pack8(__builtin_elementwise_max((acc[24 + 2 * m] - cs[6] * st[0]) * st[1], zero),
                        __builtin_elementwise_max((acc[24 + 2 * m + 1] - cs[7] * st[0]) * st[1], zero)), lane * 16, fd + (base7 + 3) * 16384 + par * 1024);
    }
    publish(y, FL_F, tm1 * 16 + tn1, wid, lane);
    if (wid == 0) TS(12);
  }
  {
    f32x4 acc[16];
    Adr ad;
    ad.a[0] = wm * 8192 + kh * 4096 + lane * 16; ad.a[1] = wm * 8192 + (kh ^ 1) * 4096 + lane * 16;
#pragma unroll
    for (int n = 0; n < 4; ++n) ad.w[n] = 16384 + wn * 8192 + wlane(lane, n);
    u32x4 hr[2][2];
    consume<C5, 5, 2>(acc, smem + R5, ad, [&](int s) {
      if (s < 0) {
        if (wid == 0) TS(13);
#pragma unroll
        for (int m = 0; m < 2; ++m)
#pragma unroll
          for (int Q = 0; Q < 2; ++Q) hr[m][Q] = ld16(y.r, lane * 16, WS_H + (((tm5 * 16 + 2 * tn5 + wn) * 8 + wm * 4 + 2 * kh + m) * 2 + Q) * 1024);
      }
    });
    if (wid == 0) TS(14);
    const int tile = tm5 * 8 + tn5;
#pragma unroll
    for (int i = 0; i < 8; ++i) st16(y.r, __builtin_bit_cast(u32x4, acc[8 + i]), lane * 16, WS_XB + (((tile * 2 + kh) * 4 + wid) * 8 + i) * 1024);
    publish(y, FL_X, tile * 2 + kh, wid, lane);
    await(y, FL_X * 256 + tile * 2 + (kh ^ 1), 1, wid, lane);
    if (wid == 0) TS(15);
    u32x4 xv[8];
#pragma unroll
    for (int i = 0; i < 8; ++i) xv[i] = ld16(y.r, lane * 16, WS_XB + (((tile * 2 + (kh ^ 1)) * 4 + wid) * 8 + i) * 1024);
#pragma unroll
    for (int m = 0; m < 2; ++m)
#pragma unroll
      for (int Q = 0; Q < 2; ++Q) {
        const f32x4 a = acc[m * 4 + 2 * Q] + __builtin_bit_cast(f32x4, xv[m * 4 + 2 * Q]), b = acc[m * 4 + 2 * Q + 1] + __builtin_bit_cast(f32x4, xv[m * 4 + 2 * Q + 1]);
        const u32x4 hh = hr[m][Q];
        const unsigned a0 = pk2(a[0] + bflo(hh[0]), a[1] + bfhi(hh[0])), a1 = pk2(a[2] + bflo(hh[1]), a[3] + bfhi(hh[1]));
        const unsigned b0 = pk2(b[0] + bflo(hh[2]), b[1] + bfhi(hh[2])), b1 = pk2(b[2] + bflo(hh[3]), b[3] + bfhi(hh[3]));
        auto r0 = __builtin_amdgcn_permlane16_swap(a0, b0, false, false);
        auto r1 = __builtin_amdgcn_permlane16_swap(a1, b1, false, false);
        *(u32x4*)(P.out + (size_t)(tm5 * 128 + wm * 64 + (2 * kh + m) * 16 + m16) * 2048 + tn5 * 256 + wn * 128 + Q * 64 + (g & 1) * 32 + (g >> 1) * 16) = (u32x4){r0[0], r1[0], r0[1], r1[1]};
      }
    if (bid == 0 && threadIdx.x == 0) st4(y.r, y.tag, 0, WS_CTL);
    if (wid == 0) TS(24);
  }
}

extern "C" void kernel_launch(void* const* d_in, const int* in_sizes, int n_in, void* d_out, int out_size, void* d_ws, size_t ws_size,
                              hipStream_t stream) {
  Params p;
  p.x = (const char*)d_in[0]; p.wqkv = (const char*)d_in[1]; p.wphi = (const char*)d_in[2]; p.wo = (const char*)d_in[3];
  p.w1 = (const char*)d_in[5]; p.w2 = (const char*)d_in[6]; p.ws = (char*)d_ws; p.out = (char*)d_out;
  (void)hipFuncSetAttribute((const void*)fused, hipFuncAttributeMaxDynamicSharedMemorySize, LDS_BYTES);
  fused<<<256, 512, LDS_BYTES, stream>>>(p);
}
```

```cpp
#include <hip/hip_runtime.h>
#include <hip/hip_bf16.h>

#define LAS __attribute__((address_space(3)))
#define DI __device__ __forceinline__
typedef __bf16 bf16x8 __attribute__((ext_vector_type(8)));
typedef __bf16 bf16x2 __attribute__((ext_vector_type(2)));
typedef short s16x4 __attribute__((ext_vector_type(4)));
typedef float f32x2 __attribute__((ext_vector_type(2)));
typedef float f32x4 __attribute__((ext_vector_type(4)));
typedef unsigned u32x2 __attribute__((ext_vector_type(2)));
typedef unsigned u32x4 __attribute__((ext_vector_type(4)));
typedef LAS char* lds_ptr;
typedef __amdgpu_buffer_rsrc_t rsrc_t;

constexpr int D = 1024, FF = 3584;
constexpr unsigned WS_CTL = 0;
constexpr unsigned WS_FLG = 4096;
constexpr unsigned WS_ST1 = 1u << 20;
constexpr unsigned WS_CSQ = WS_ST1 + (64u << 10);
constexpr unsigned WS_CS1 = WS_CSQ + (128u << 10);
constexpr unsigned WS_ZP = WS_CS1 + (128u << 10);
constexpr unsigned WS_ST2 = WS_ZP + (128u << 10);
constexpr unsigned WS_KVP = 2u << 20;
constexpr unsigned WS_O = 4u << 20;
constexpr unsigned WS_H = 8u << 20;
constexpr unsigned WS_XB = 12u << 20;
constexpr unsigned WS_F = 20u << 20;
constexpr unsigned WS_END = 36u << 20;
enum { FL_P0 = 0, FL_KV = 1, FL_O = 2, FL_H = 3, FL_F = 4, FL_CS = 5, FL_X = 6   };

#define TS(I) do {} while (0)

#define DBG(k) nullptr
#define CLK(v)
#define DBG3 nullptr
struct Params { const char* x; const char* wqkv; const char* wphi; const char* wo; const char* w1; const char* w2; char* ws; char* out; };

DI unsigned pk2(float a, float b) { f32x2 v = {a, b}; return __builtin_bit_cast(unsigned, __builtin_convertvector(v, bf16x2)); }
DI float bflo(unsigned u) { return __uint_as_float(u << 16); }
DI float bfhi(unsigned u) { return __uint_as_float(u & 0xffff0000u); }
DI u32x4 pack8(const f32x4& a, const f32x4& b) { return (u32x4){pk2(a[0], a[1]), pk2(a[2], a[3]), pk2(b[0], b[1]), pk2(b[2], b[3])}; }
DI bf16x8 asbf(const u32x4& v) { return __builtin_bit_cast(bf16x8, v); }
#define SB() __builtin_amdgcn_sched_barrier(0)
DI void bar() { asm volatile("" ::: "memory"); __builtin_amdgcn_s_barrier(); asm volatile("" ::: "memory"); }
DI void wait_lgkm0() { __builtin_amdgcn_s_waitcnt(0xC07F); asm volatile("" ::: "memory"); }
template <int N> DI void wait_vm() { asm volatile("s_waitcnt vmcnt(%0)" ::"i"(N) : "memory"); }
template <bool SC1> DI void dma(unsigned voff, const char* sbase, unsigned lds) {
  if (SC1) asm volatile("s_mov_b32 m0, %2\n\ts_nop 3\n\tglobal_load_lds_dwordx4 %0, %1 sc1" ::"v"(voff), "s"(sbase), "s"(lds) : "memory");
  else asm volatile("s_mov_b32 m0, %2\n\ts_nop 3\n\tglobal_load_lds_dwordx4 %0, %1" ::"v"(voff), "s"(sbase), "s"(lds) : "memory");
}
template <bool SC1> DI void dma4(unsigned voff, const char* sbase, unsigned lds) {
  if (SC1) asm volatile("s_mov_b32 m0, %2\n\ts_nop 3\n\tglobal_load_lds_dword %0, %1 sc1" ::"v"(voff), "s"(sbase), "s"(lds) : "memory");
  else asm volatile("s_mov_b32 m0, %2\n\ts_nop 3\n\tglobal_load_lds_dword %0, %1" ::"v"(voff), "s"(sbase), "s"(lds) : "memory");
}
DI u32x4 lds128(lds_ptr p) { return *(const LAS u32x4*)p; }
DI u32x4 lds_tr2(lds_ptr p0, lds_ptr p1) {
  s16x4 a = __builtin_amdgcn_ds_read_tr16_b64_v4i16((LAS s16x4*)p0);
  s16x4 b = __builtin_amdgcn_ds_read_tr16_b64_v4i16((LAS s16x4*)p1);
  return __builtin_bit_cast(u32x4, __builtin_shufflevector(a, b, 0, 1, 2, 3, 4, 5, 6, 7));
}
DI u32x2 lds_tr(lds_ptr p) { return __builtin_bit_cast(u32x2, __builtin_amdgcn_ds_read_tr16_b64_v4i16((LAS s16x4*)p)); }
DI f32x4 mfma(const u32x4& a, const u32x4& b, const f32x4& c) { return __builtin_amdgcn_mfma_f32_16x16x32_bf16(asbf(a), asbf(b), c, 0, 0, 0); }
DI void mfma_ip(f32x4& acc, const u32x4& a, const u32x4& b) { asm volatile("v_mfma_f32_16x16x32_bf16 %0, %1, %2, %0" : "+v"(acc) : "v"(a), "v"(b)); }
DI u32x4 ld16(rsrc_t r, unsigned voff, unsigned soff) { return __builtin_amdgcn_raw_buffer_load_b128(r, voff, soff, 16); }
DI void st16(rsrc_t r, const u32x4& v, unsigned voff, unsigned soff) { __builtin_amdgcn_raw_buffer_store_b128(v, r, voff + soff, 0, 16);
}
DI void st8(rsrc_t r, const u32x2& v, unsigned voff, unsigned soff) { __builtin_amdgcn_raw_buffer_store_b64(v, r, voff, soff, 16); }
DI void st4(rsrc_t r, unsigned v, unsigned voff, unsigned soff) { __builtin_amdgcn_raw_buffer_store_b32(v, r, voff, soff, 16); }
DI float fb(unsigned u) { return __uint_as_float(u); }
DI unsigned bu(float f) { return __float_as_uint(f); }
DI float xsum16(float x) { auto r = __builtin_amdgcn_permlane16_swap(bu(x), bu(x), false, false); return fb(r[0]) + fb(r[1]); }
DI float xsum32(float x) { auto r = __builtin_amdgcn_permlane32_swap(bu(x), bu(x), false, false); return fb(r[0]) + fb(r[1]); }
template <int CTRL> DI float dpp(float x) { return __builtin_bit_cast(float, __builtin_amdgcn_update_dpp(0, __builtin_bit_cast(int, x), CTRL, 0xf, 0xf, true)); }
DI float row16_sum(float x) { x += dpp<0xB1>(x); x += dpp<0x4E>(x); x += dpp<0x141>(x); x += dpp<0x140>(x); return x; }

struct Sync { rsrc_t r; unsigned tag; LAS unsigned* w; };
DI void publish(const Sync& y, int phase, int idx, int lane) {
  if (lane == 0) __hip_atomic_fetch_add(y.w + 24 + phase, 1u, __ATOMIC_RELAXED, __HIP_MEMORY_SCOPE_WORKGROUP);
  wait_vm<0>();
  if (lane == 0 && __hip_atomic_fetch_add(y.w + phase, 1u, __ATOMIC_RELAXED, __HIP_MEMORY_SCOPE_WORKGROUP) == 3u) st4(y.r, y.tag, 0, WS_FLG + (phase * 256 + idx) * 128);
}
DI void poll(const Sync& y, unsigned off, int n, int lane) {
  const unsigned long long t0 = __builtin_amdgcn_s_memrealtime();
  for (unsigned it = 0;; ++it) {
    const unsigned v = __builtin_amdgcn_raw_buffer_load_b32(y.r, WS_FLG + off, 0, 16);
    if (__all(lane >= n || v == y.tag)) break;
    if ((it & 63) == 63) {
      if (__builtin_amdgcn_readfirstlane(__builtin_amdgcn_raw_buffer_load_b32(y.r, 0, WS_CTL + 128, 16)) == y.tag) break;
      if (__builtin_amdgcn_s_memrealtime() - t0 > 4000000ull) { if (lane == 0) st4(y.r, y.tag, 0, WS_CTL + 128); break; }
    }
    __builtin_amdgcn_s_sleep(2);
  }
  asm volatile("" ::: "memory");
}
DI void lds_wait(const Sync& y, int word, unsigned v) {
  volatile LAS unsigned* w = y.w + word;
  while (*w < v) __builtin_amdgcn_s_sleep(1);
  asm volatile("" ::: "memory");
}
DI void await(const Sync& y, int rel, unsigned rec, int n, int lw, int lane) {
  volatile LAS unsigned* w = y.w + 8 + rel;
  if (lw == 0) { poll(y, rec * 128, n, lane); if (lane == 0) *w = 1u; }
  else { while (*w == 0u) __builtin_amdgcn_s_sleep(1); }
  asm volatile("" ::: "memory");
}

template <int NK> DI int kmap(int t, int c) {
  const int u = t - 1, q = u & 3, b4 = u & ~3, body = (int)(u < NK - 4);
  const int lead = ((int)(q == c) & body) | (int)(t == 0);
  const int non = b4 + ((c + 1 + q - ((int)(q > c) & body)) & 3);
  return non ^ (((b4 + 4 + c) ^ non) & -lead);
}
enum { ORD_NAT = 0, ORD_PAIR = 1, ORD_F = 2 };
template <int NSUB_, bool HALF_, int NSLOT_, int NK_, int ORD_, int WSTRIDE_, bool XROW_> struct Cfg {
  static constexpr int NSUB = NSUB_, NSLOT = NSLOT_, NK = NK_, ORD = ORD_, WSTRIDE = WSTRIDE_;
  static constexpr bool HALF = HALF_, XROW = XROW_;
  static constexpr int STAGE = 16384 + NSUB * 8192 + (HALF ? 4096 : 0), PW = 4 + 2 * NSUB + (HALF ? 1 : 0), RING = STAGE * NSLOT;
  static constexpr int WB = NSUB * 2048 + (HALF ? 1024 : 0);
};
typedef Cfg<3, false, 3, 16, ORD_NAT, 3 * D * 2, true> C1;
typedef Cfg<1, false, 5, 16, ORD_PAIR, D * 2, false> C3;
typedef Cfg<3, true, 3, 16, ORD_PAIR, FF * 2, false> C4;
typedef Cfg<2, false, 4, 28, ORD_F, D * 2, false> C5;

#define GLDS "global_load_lds_dwordx4 "
template <class C> struct Ld {
  const char* A; const char* W; int lw, c, kt0; unsigned vA[4], vW[7];
  const char* sA; const char* sW;
  const char* tb; unsigned tv, tmask, tlds;
  static constexpr int C0 = C::WB > 4096 ? 4096 : 0;
  DI void init(int lane, int substep, int halfrel) {
#pragma unroll
    for (int i = 0; i < 4; ++i) vA[i] = C::XROW ? (lane >> 3) * 2048 + ((((lane & 7) ^ (lane >> 4)) << 4) ^ ((i & 1) * 64)) + i * (16384 - 1024) : lane * 16;
    const int rl = (C::ORD == ORD_NAT) ? 8 * (lane >> 5) + ((lane >> 3) & 3) : (lane >> 3);
    const unsigned w0 = rl * C::WSTRIDE + (((lane & 7) ^ (((lane >> 4) & 3) << 1)) << 4) + 4096;
#pragma unroll
    for (int q = 0; q < 2 * C::NSUB; ++q) vW[q] = w0 + (q & 1) * ((C::ORD == ORD_NAT ? 16 : 8) * C::WSTRIDE) + (q >> 1) * substep - (q * 1024 - C0);
    if (C::HALF) vW[6] = (lane >> 2) * C::WSTRIDE + (((lane & 3) ^ (((lane >> 4) & 1) << 1)) << 4) + 4096 + halfrel - (6144 - C0);
    A += C::XROW ? lw * 32 * 2048 : lw * 4096;
    W -= 4096;
  }
  DI void prep(int s) {
    const int kt = kt0 + kmap<C::NK>(s, c), kb = lw >> 1, t = lw & 1;
    int ur;
    if (C::ORD == ORD_NAT) ur = 32 * kb + 4 * t;
    else if (C::ORD == ORD_F) { const int sp = -(int)((0x10204081020408ull >> kt) & 1u); ur = (16 * lw & sp) | ((16 * kb + 32 * t) & ~sp); }
    else ur = 16 * lw;
    sA = A + (unsigned)(kt * (C::XROW ? 128 : 16384));
    sW = W + (unsigned)((kt * 64 + ur) * C::WSTRIDE);
  }
  DI void issueA(unsigned base) const {
    const unsigned m = base + lw * 4096;
    if (C::XROW) asm volatile("s_mov_b32 m0, %5\n\ts_nop 3\n\t" GLDS "%0, %4\n\t" GLDS "%1, %4 offset:1024\n\t" GLDS "%2, %4 offset:2048\n\t" GLDS "%3, %4 offset:3072"
                              ::"v"(vA[0]), "v"(vA[1]), "v"(vA[2]), "v"(vA[3]), "s"(sA), "s"(m) : "memory");
    else asm volatile("s_mov_b32 m0, %2\n\ts_nop 3\n\t" GLDS "%0, %1 sc1\n\t" GLDS "%0, %1 offset:1024 sc1\n\t" GLDS "%0, %1 offset:2048 sc1\n\t" GLDS "%0, %1 offset:3072 sc1"
                      ::"v"(vA[0]), "s"(sA), "s"(m) : "memory");
  }
  DI void issueW(unsigned base) const {
    const unsigned m = base + 16384 + lw * C::WB + C0;
    if (C::NSUB == 1) asm volatile("s_mov_b32 m0, %3\n\ts_nop 3\n\t" GLDS "%0, %2\n\t" GLDS "%1, %2 offset:1024" ::"v"(vW[0]), "v"(vW[1]), "s"(sW), "s"(m) : "memory");
    if (C::NSUB == 2) asm volatile("s_mov_b32 m0, %5\n\ts_nop 3\n\t" GLDS "%0, %4\n\t" GLDS "%1, %4 offset:1024\n\t" GLDS "%2, %4 offset:2048\n\t" GLDS "%3, %4 offset:3072"
                                   ::"v"(vW[0]), "v"(vW[1]), "v"(vW[2]), "v"(vW[3]), "s"(sW), "s"(m) : "memory");
    if (C::NSUB == 3) asm volatile("s_mov_b32 m0, %7\n\ts_nop 3\n\t" GLDS "%0, %6 offset:-4096\n\t" GLDS "%1, %6 offset:-3072\n\t" GLDS "%2, %6 offset:-2048\n\t" GLDS "%3, %6 offset:-1024\n\t"
                                   GLDS "%4, %6\n\t" GLDS "%5, %6 offset:1024"
                                   ::"v"(vW[0]), "v"(vW[1]), "v"(vW[2]), "v"(vW[3]), "v"(vW[4]), "v"(vW[5]), "s"(sW), "s"(m) : "memory");
    if (C::HALF) asm volatile(GLDS "%0, %1 offset:2048" ::"v"(vW[6]), "s"(sW) : "memory");
  }
};
template <class C> DI void load_preW(Ld<C>& L, unsigned ring) {
#pragma unroll 1
  for (int s = 0; s < C::NSLOT; ++s) { L.prep(s); L.issueW(ring + s * C::STAGE); }
}
template <class C, int VM> DI void load_step(Ld<C>& L, unsigned b, int s) { wait_vm<VM>(); bar(); L.issueW(b); L.issueA(b); L.prep(s + C::NSLOT); }
template <class C, bool SPLIT, bool TOUCH, unsigned PREM, unsigned POSTM, class Pre, class Post>
DI void load_all(Ld<C>& L, unsigned ring, const Pre& pre, const Post& post, unsigned long long* dbg = nullptr) {
  constexpr int N = C::NSLOT, PW = C::PW;
#pragma unroll 1
  for (int s = 0; s < N; ++s) { L.prep(s); if (!SPLIT) L.issueW(ring + s * C::STAGE); L.issueA(ring + s * C::STAGE); }
  L.prep(N);
  wait_vm<(SPLIT ? 4 : PW) * (N - 1)>();
  bar();
  __builtin_amdgcn_s_setprio(3);
  load_step<C, SPLIT ? 4 * (N - 2) : PW * (N - 2)>(L, ring, 1);
  if (POSTM & 2u) post(1);
  if (N > 3) load_step<C, 4 * (N > 3 ? N - 3 : 0) + PW>(L, ring + C::STAGE, 2);
  if (N > 4) load_step<C, 4 * (N > 4 ? N - 4 : 0) + 2 * PW>(L, ring + 2 * C::STAGE, 3);
  unsigned b = ring + (N - 2) * C::STAGE;
#pragma unroll 1
  for (int s = N - 1; s <= C::NK - N; ++s) {
    CLK(t0);
    wait_vm<(N - 2) * PW>();
    if (PREM && ((PREM >> s) & 1u)) pre(s);
    CLK(t1);
    bar();
    CLK(t2);
    L.issueW(b); L.issueA(b);
    CLK(t3);
    L.prep(min(s + N, C::NK - 1));
    b = (b == ring + (N - 1) * C::STAGE) ? ring : b + C::STAGE;
    if (TOUCH && ((L.tmask >> s) & 1u)) { dma4<false>(L.tv, L.tb, L.tlds); L.tb += 7168; }
    if (POSTM && ((POSTM >> s) & 1u)) post(s);
  }
  if (N > 4) { wait_vm<3 * PW>(); bar(); }
  if (N > 3) { wait_vm<2 * PW>(); bar(); }
  wait_vm<PW>(); bar();
  wait_vm<0>(); bar();
  bar();
}

template <int MT, int NWF> struct Fr { u32x4 a[MT]; u32x2 w[NWF][2]; };
DI u32x4 cat(const u32x2 (&w)[2]) { return (u32x4){w[0][0], w[0][1], w[1][0], w[1][1]}; }
struct Adr { unsigned v[6]; };
template <int KIND> struct Shape;
template <> struct Shape<1> { static constexpr int MT = 4, NWF = 6, NMF = 24, NACC = 24, WB = 6144; static constexpr unsigned USE = 0x0f; };
template <> struct Shape<3> { static constexpr int MT = 2, NWF = 4, NMF = 8, NACC = 8, WB = 2048; static constexpr unsigned USE = 0x3d; };
template <> struct Shape<4> { static constexpr int MT = 4, NWF = 8, NMF = 28, NACC = 28, WB = 7168; static constexpr unsigned USE = 0x3f; };
template <> struct Shape<5> { static constexpr int MT = 4, NWF = 4, NMF = 16, NACC = 16, WB = 4096; static constexpr unsigned USE = 0x3f; };
DI lds_ptr lp(unsigned a) { return (lds_ptr)(size_t)a; }
template <int KIND, int J> DI void rd1(Fr<Shape<KIND>::MT, Shape<KIND>::NWF>& f, const Adr& ad, int kb) {
  constexpr int NWF = Shape<KIND>::NWF, MT = Shape<KIND>::MT, WB = Shape<KIND>::WB;
  if constexpr (J < 2 * NWF) {
    constexpr int n = J >> 1, t = J & 1;
    if constexpr (KIND == 4 && n >= 6) f.w[n][t] = lds_tr(lp(ad.v[4 + (n - 6)]) + (2 * kb + t) * WB);
    else if constexpr (KIND == 1 || KIND == 4) f.w[n][t] = lds_tr(lp(ad.v[2 + (n & 1)]) + (n >> 1) * 2048 + (2 * kb + t) * WB);
    else f.w[n][t] = lds_tr(lp(ad.v[2 + n]) + (2 * kb + t) * WB);
  } else if constexpr (J < 2 * NWF + MT) {
    constexpr int m = J - 2 * NWF;
    if constexpr (KIND == 1) f.a[m] = lds128(lp(ad.v[kb]) + m * 2048);
    else f.a[m] = lds128(lp(ad.v[m >> 1]) + (m & 1) * 2048 + kb * 1024);
  }
}
template <int KIND, int I> DI void mm1(f32x4 (&acc)[Shape<KIND>::NACC], const Fr<Shape<KIND>::MT, Shape<KIND>::NWF>& f) {
  if constexpr (KIND == 1) { constexpr int m = I / 6, n = I % 6; if constexpr (n < 4) mfma_ip(acc[I], cat(f.w[n]), f.a[m]); else mfma_ip(acc[I], f.a[m], cat(f.w[n])); }
  else if constexpr (KIND == 4) { if constexpr (I < 24) mfma_ip(acc[I], cat(f.w[I % 6]), f.a[I / 6]); else mfma_ip(acc[I], cat(f.w[6 + ((I - 24) & 1)]), f.a[(I - 24) >> 1]); }
  else mfma_ip(acc[I], cat(f.w[I % 4]), f.a[I / 4]);
}
template <int KIND, int RPG, bool READ, bool MOVE, int I = 0>
DI void step(f32x4 (&acc)[Shape<KIND>::NACC], const Fr<Shape<KIND>::MT, Shape<KIND>::NWF>& cur, Fr<Shape<KIND>::MT, Shape<KIND>::NWF>& nxt, Adr& ad, int kb, int delta) {
  constexpr int NMF = Shape<KIND>::NMF, G0 = (2 * Shape<KIND>::NWF + Shape<KIND>::MT + RPG - 1) / RPG;
  if constexpr (I < NMF) {
    if constexpr (READ) {
      rd1<KIND, I * RPG>(nxt, ad, kb);
      if constexpr (RPG > 1) rd1<KIND, I * RPG + 1>(nxt, ad, kb);
    }
    if constexpr (MOVE && I >= G0) {
      constexpr int per = (6 + (NMF - G0) - 1) / (NMF - G0);
#pragma unroll
      for (int k = (I - G0) * per; k < (I - G0 + 1) * per && k < 6; ++k)
        if ((Shape<KIND>::USE >> k) & 1u) { ad.v[k] += delta; asm volatile("" : "+v"(ad.v[k])); }
    }
    mm1<KIND, I>(acc, cur);
    SB();
    step<KIND, RPG, READ, MOVE, I + 1>(acc, cur, nxt, ad, kb, delta);
  }
}
template <int KIND, int J = 0> DI void rdall(Fr<Shape<KIND>::MT, Shape<KIND>::NWF>& f, const Adr& ad, int kb) {
  if constexpr (J < 2 * Shape<KIND>::NWF + Shape<KIND>::MT) { rd1<KIND, J>(f, ad, kb); rdall<KIND, J + 1>(f, ad, kb); }
}
template <class C, int KIND, int RPG, class Hook> DI void consume(f32x4 (&acc)[Shape<KIND>::NACC], Adr& ad, unsigned long long* dbg3, const Hook& hook) {
  Fr<Shape<KIND>::MT, Shape<KIND>::NWF> f0, f1;
#pragma unroll
  for (int i = 0; i < Shape<KIND>::NACC; ++i) acc[i] = (f32x4){0.f, 0.f, 0.f, 0.f};
  bar();
  rdall<KIND>(f0, ad, 0);
  hook(-1);
  wait_lgkm0();
  int slot = 0;
#define MT_(v)
#pragma unroll 1
  for (int s = 0; s < C::NK - 1; ++s) {
    const int delta = (slot + 1 == C::NSLOT) ? -(C::NSLOT - 1) * C::STAGE : C::STAGE;
    slot = (slot + 1 == C::NSLOT) ? 0 : slot + 1;
    SB();
    MT_(c0);
    step<KIND, RPG, true, true>(acc, f0, f1, ad, 1, delta);
    MT_(c1);
    wait_lgkm0();
    MT_(c2);
    bar();
    MT_(c3);
    SB();
    step<KIND, RPG, true, false>(acc, f1, f0, ad, 0, 0);
    MT_(c4);
    hook(s);
    wait_lgkm0();
  }
  SB();
  step<KIND, RPG, true, false>(acc, f0, f1, ad, 1, 0);
  wait_lgkm0();
  bar();
  SB();
  step<KIND, RPG, false, false>(acc, f1, f0, ad, 0, 0);
  asm volatile("s_nop 7\n\ts_nop 3");
  SB();
}
template <class C, int KIND, class Hook> DI void consume_s(f32x4 (&acc)[Shape<KIND>::NACC], Adr& ad, const Hook& hook) {
  Fr<Shape<KIND>::MT, Shape<KIND>::NWF> f0, f1, g0, g1;
#pragma unroll
  for (int i = 0; i < Shape<KIND>::NACC; ++i) acc[i] = (f32x4){0.f, 0.f, 0.f, 0.f};
  bar();
  rdall<KIND>(f0, ad, 0); rdall<KIND>(f1, ad, 1);
  hook(-1);
#pragma unroll
  for (int k = 0; k < 6; ++k) ad.v[k] += C::STAGE;
  int slot = 1;
#pragma unroll 1
  for (int s = 0; s < C::NK - 2; s += 2) {
    int delta = (slot + 1 == C::NSLOT) ? -(C::NSLOT - 1) * C::STAGE : C::STAGE;
    slot = (slot + 1 == C::NSLOT) ? 0 : slot + 1;
    wait_lgkm0(); bar(); SB();
    step<KIND, 2, true, false>(acc, f0, g0, ad, 0, 0);
    step<KIND, 2, true, true>(acc, f1, g1, ad, 1, delta);
    delta = (slot + 1 == C::NSLOT) ? -(C::NSLOT - 1) * C::STAGE : C::STAGE;
    slot = (slot + 1 == C::NSLOT) ? 0 : slot + 1;
    wait_lgkm0(); bar(); SB();
    step<KIND, 2, true, false>(acc, g0, f0, ad, 0, 0);
    step<KIND, 2, true, true>(acc, g1, f1, ad, 1, delta);
  }
  wait_lgkm0(); bar(); SB();
  step<KIND, 2, true, false>(acc, f0, g0, ad, 0, 0);
  step<KIND, 2, true, false>(acc, f1, g1, ad, 1, 0);
  wait_lgkm0(); bar(); SB();
  step<KIND, 2, false, false>(acc, g0, f0, ad, 0, 0);
  step<KIND, 2, false, false>(acc, g1, f1, ad, 1, 0);
  asm volatile("s_nop 7\n\ts_nop 3");
  SB();
}
DI unsigned wlane(int lane, int nt) {
  const int g = lane >> 4, q = (lane >> 2) & 3, p = lane & 3, sw = (2 * g + (q >> 1)) & 3;
  return (4 * g + q) * 128 + ((nt ^ sw) << 5) + p * 8;
}
DI unsigned hlane(int lane, int nt) {
  const int g = lane >> 4, q = (lane >> 2) & 3, p = lane & 3;
  return (4 * g + q) * 64 + ((nt ^ (g & 1)) << 5) + p * 8;
}

template <int OP> DI float op2(float a, float b) { return OP == 0 ? a + b : OP == 1 ? fmaxf(a, b) : fminf(a, b); }
template <int OP> DI float red4(float x0, float x1, float x2, float x3) {
  auto a = __builtin_amdgcn_permlane16_swap(bu(x0), bu(x1), false, false);
  auto b = __builtin_amdgcn_permlane16_swap(bu(x2), bu(x3), false, false);
  const float y0 = op2<OP>(fb(a[0]), fb(a[1])), y1 = op2<OP>(fb(b[0]), fb(b[1]));
  auto c = __builtin_amdgcn_permlane32_swap(bu(y0), bu(y1), false, false);
  return op2<OP>(fb(c[0]), fb(c[1]));
}
DI void spread(float z, float (&o)[4]) {
  auto a = __builtin_amdgcn_permlane32_swap(bu(z), bu(z), false, false);
  auto b = __builtin_amdgcn_permlane16_swap(a[0], a[0], false, false);
  auto c = __builtin_amdgcn_permlane16_swap(a[1], a[1], false, false);
  o[0] = fb(b[0]); o[1] = fb(b[1]); o[2] = fb(c[0]); o[3] = fb(c[1]);
}
DI void dual_softmax4(const f32x4 (&p)[4][2], u32x4 (&pp)[4], u32x4 (&pn)[4]) {
  constexpr float L2E = 1.4426950408889634f;
  float mx[4], mn[4];
#pragma unroll
  for (int m = 0; m < 4; ++m) {
    mx[m] = fmaxf(fmaxf(fmaxf(p[m][0][0], p[m][0][1]), fmaxf(p[m][0][2], p[m][0][3])), fmaxf(fmaxf(p[m][1][0], p[m][1][1]), fmaxf(p[m][1][2], p[m][1][3])));
    mn[m] = fminf(fminf(fminf(p[m][0][0], p[m][0][1]), fminf(p[m][0][2], p[m][0][3])), fminf(fminf(p[m][1][0], p[m][1][1]), fminf(p[m][1][2], p[m][1][3])));
  }
  spread(-L2E * red4<1>(mx[0], mx[1], mx[2], mx[3]), mx);
  spread(L2E * red4<2>(mn[0], mn[1], mn[2], mn[3]), mn);
  f32x4 ep[4][2], en[4][2];
  float sp[4], sn[4];
#pragma unroll
  for (int m = 0; m < 4; ++m) {
#pragma unroll
    for (int F = 0; F < 2; ++F) {
      const f32x4 a = p[m][F] * L2E + mx[m], b = p[m][F] * (-L2E) + mn[m];
#pragma unroll
      for (int r = 0; r < 4; ++r) { ep[m][F][r] = __builtin_amdgcn_exp2f(a[r]); en[m][F][r] = __builtin_amdgcn_exp2f(b[r]); }
    }
    const f32x4 a = ep[m][0] + ep[m][1], b = en[m][0] + en[m][1];
    sp[m] = (a[0] + a[1]) + (a[2] + a[3]); sn[m] = (b[0] + b[1]) + (b[2] + b[3]);
  }
  spread(__builtin_amdgcn_rcpf(red4<0>(sp[0], sp[1], sp[2], sp[3])), sp);
  spread(__builtin_amdgcn_rcpf(red4<0>(sn[0], sn[1], sn[2], sn[3])), sn);
#pragma unroll
  for (int m = 0; m < 4; ++m) { pp[m] = pack8(ep[m][0] * sp[m], ep[m][1] * sp[m]); pn[m] = pack8(en[m][0] * sn[m], en[m][1] * sn[m]); }
}

DI void colsum128(const char* w, int stride, rsrc_t r, unsigned dst, int lane) {
  u32x4 v[16];
#pragma unroll
  for (int i = 0; i < 16; ++i) v[i] = *(const u32x4*)(w + (size_t)(8 * i + (lane >> 3)) * stride + (lane & 7) * 16);
  float s[8];
#pragma unroll
  for (int j = 0; j < 8; ++j) s[j] = 0.f;
#pragma unroll
  for (int i = 0; i < 16; ++i)
#pragma unroll
    for (int j = 0; j < 4; ++j) { s[2 * j] += bflo(v[i][j]); s[2 * j + 1] += bfhi(v[i][j]); }
#pragma unroll
  for (int j = 0; j < 8; ++j) { s[j] += dpp<0x128>(s[j]); s[j] = xsum32(xsum16(s[j])); }
  if (lane < 8) {
    st16(r, (u32x4){bu(s[0]), bu(s[1]), bu(s[2]), bu(s[3])}, lane * 32, dst);
    st16(r, (u32x4){bu(s[4]), bu(s[5]), bu(s[6]), bu(s[7])}, lane * 32 + 16, dst);
  }
}

template <int XN, int TM, int TN> DI void tile_of(int bid, int& tm, int& tn, int& c) {
  const int x = bid & 7, r = bid >> 3, li = r % TM, lj = r / TM;
  tm = (x / XN) * TM + li; tn = (x % XN) * TN + lj; c = (li + lj) & 3;
}

#define RPG1 1
#define RPG4 1
#define RPG5 1
constexpr int R1 = 40704, R3 = 40704, R4 = 0, R5 = 0, SYNCW = 163584;
constexpr int S1_ST = 0, S1_CSR = 1024, S1_CSF = 7168, S1_FLG = 7936, S1_WP = 8192, S1_IMG = 12288, S1_KV = 29696;
constexpr int S4_STR = C4::RING, S4_CSR = S4_STR + 16384, S4_STF = S4_CSR + 8192, S4_CSF = S4_STF + 1024;
constexpr int LDS_BYTES = 163840, CODE_BYTES = 44 * 1024;
static_assert(S4_CSF + 1024 <= SYNCW && S1_KV + 8448 <= R1 && R1 + C1::RING <= SYNCW && R3 + C3::RING <= SYNCW && C5::RING <= S4_STR, "lds");

DI float dot_lo(unsigned v, float a) { return __builtin_amdgcn_fdot2_f32_bf16(__builtin_bit_cast(bf16x2, v), __builtin_bit_cast(bf16x2, 0x00003f80u), a, false); }
DI float dot_hi(unsigned v, float a) { return __builtin_amdgcn_fdot2_f32_bf16(__builtin_bit_cast(bf16x2, v), __builtin_bit_cast(bf16x2, 0x3f800000u), a, false); }
DI void kv_sum(const Sync& y, lds_ptr smem, int head, int h, int t, bool doz, int lane) {
  u32x4 v[16]; unsigned zz[16];
#pragma unroll
  for (int r = 0; r < 16; ++r) v[r] = ld16(y.r, t * 16, WS_KVP + (head * 16 + r) * 4096);
  if (doz) {
#pragma unroll
    for (int r = 0; r < 16; ++r) zz[r] = __builtin_amdgcn_raw_buffer_load_b32(y.r, lane * 4, WS_ZP + (head * 16 + r) * 256, 16);
  }
  float s[8];
#pragma unroll
  for (int j = 0; j < 4; ++j) { s[2 * j] = bflo(v[0][j]); s[2 * j + 1] = bfhi(v[0][j]); }
#pragma unroll
  for (int r = 1; r < 16; ++r)
#pragma unroll
    for (int j = 0; j < 4; ++j) { s[2 * j] += bflo(v[r][j]); s[2 * j + 1] += bfhi(v[r][j]); }
  *(LAS u32x4*)(smem + S1_KV + h * 4096 + t * 16) = (u32x4){pk2(s[0], s[1]), pk2(s[2], s[3]), pk2(s[4], s[5]), pk2(s[6], s[7])};
  if (doz) {
    float z = 0.f;
#pragma unroll
    for (int r = 0; r < 16; ++r) z += fb(zz[r]);
    *(LAS __bf16*)(smem + S1_KV + 8192 + h * 128 + lane * 2) = (__bf16)z;
  }
}

__global__ __launch_bounds__(512, 2) void fused(Params P) {
  extern __shared__ __attribute__((aligned(16))) char smem_[];
  lds_ptr smem = (lds_ptr)smem_;
  const unsigned lds0 = (unsigned)(size_t)smem;
  const int wid = __builtin_amdgcn_readfirstlane(threadIdx.x >> 6), lane = threadIdx.x & 63, bid = blockIdx.x;
  int tm1, tn1, c1, tm3, tn3, c3;
  tile_of<4, 8, 4>(bid, tm1, tn1, c1);
  tile_of<2, 4, 8>(bid, tm3, tn3, c3);
  const int kh = bid & 1, tm5 = ((bid & 7) >> 1) * 4 + ((bid >> 3) & 3), tn5 = bid >> 5, c5 = ((bid >> 3) + tn5) & 3;
  const int par = tn1 & 1, base7 = 7 * (tn1 >> 1), ch0 = base7 + 4 * par;
  const int g = lane >> 4, m16 = lane & 15;
  Sync y;
  y.r = __builtin_amdgcn_make_buffer_rsrc(P.ws, 0, WS_END, 0x00020000);
  y.w = (LAS unsigned*)(smem + SYNCW);
  asm volatile("s_load_dword %0, %1, 0x0\n\ts_waitcnt lgkmcnt(0)" : "=s"(y.tag) : "s"(P.ws) : "memory");
  y.tag += 1u;
  if (wid == 0) TS(0);

  if (wid >= 4) {
    const int lw = wid - 4, lt = threadIdx.x - 256;
    const auto nop = [](int) {};
    {
      Ld<C1> L; L.A = P.x + (size_t)tm1 * 128 * 2048; L.W = P.wqkv + tn1 * 128; L.lw = lw; L.c = c1; L.kt0 = 0; L.init(lane, 2048, 0);
      const unsigned frec = (FL_P0 * 256 + (lane < 16 ? tm1 * 16 + lane : lane < 28 ? ((lane - 16) / 3) * 48 + ((lane - 16) % 3) * 16 + tn1 : tn1)) * 128;
      {
        const size_t m1 = (size_t)0 - (size_t)(lw == 1), m2 = (size_t)0 - (size_t)(lw == 2);
        L.tb = (const char*)(((size_t)P.w1 & m1) | ((size_t)P.w2 & m2) | ((size_t)P.wo & ~(m1 | m2))) + bid * (8192 + (int)((m1 | m2) & 20480));
        L.tv = min(lane, 55 + ((lw + 1) >> 2) * 8) * 128; L.tmask = (unsigned)((0x1000291004a80000ull >> (16 * lw)) & 0xffffu); L.tlds = lds0 + S1_IMG;
      }
      load_all<C1, false, true, 1u << 9, 0x1244u>(L, lds0 + R1,
        [&](int s) {
          if (s == 9 && lw == 0) {
            const unsigned v = *(volatile LAS unsigned*)(smem + S1_FLG + lane * 4);
            if (!__all(v == y.tag)) poll(y, frec, 64, lane);
          }
        },
        [&](int s) {
          if (s == 2) dma<false>(lane * 16, P.wphi + tn1 * 4096 + lw * 1024, lds0 + S1_WP + lw * 1024);
          if (s == 6 && lw == 0) dma4<true>(frec, P.ws + WS_FLG, lds0 + S1_FLG);
          if (s == 9) {
            if (lw == 3) dma<true>(lane * 16, P.ws + WS_ST1 + tm1 * 1024, lds0 + S1_ST);
            else {
#pragma unroll
              for (int i = 0; i < 2; ++i)
                dma<true>((lane >> 4) * 12288 + (lane & 15) * 16, P.ws + WS_CSQ + i * 49152 + lw * 4096 + tn1 * 256, lds0 + S1_CSR + lw * 2048 + i * 1024);
            }
          }
          if (s == 12) {
            if (lt < 192) {
              const LAS float* rawp = (const LAS float*)(smem + S1_CSR) + (lt >> 6) * 512 + (lt & 63);
              float t = 0.f;
#pragma unroll
              for (int i = 0; i < 8; ++i) t += rawp[i * 64];
              ((LAS float*)(smem + S1_CSF))[lt] = t;
            }
            wait_lgkm0();
          }
        }, DBG(0));
    }
    if (wid == 4) TS(16);
    {
      Ld<C3> L; L.A = P.ws + WS_O + (size_t)tm3 * 16 * 16384; L.W = P.wo + tn3 * 128; L.lw = lw; L.c = c3; L.kt0 = 0; L.init(lane, 0, 0);
      load_preW<C3>(L, lds0 + R3);
      if (lw >= 2 && bid < 224)
        colsum128(P.w1 + (size_t)((bid / 56) * 256 + (lw - 2) * 128) * (FF * 2) + (bid % 56) * 128, FF * 2, y.r, WS_CS1 + (((bid / 56) * 2 + lw - 2) * FF + (bid % 56) * 64) * 4, lane);
      publish(y, FL_CS, bid, lane);
      await(y, 0, FL_KV * 256 + m16 * 16 + tn1, 16, lw, lane);
      if (wid == 4) TS(17);
      kv_sum(y, smem, 2 * tn1 + 1, 1, lt, lw == 0, lane);
      wait_lgkm0(); bar();
      await(y, 1, FL_O * 256 + tm3 * 16 + m16, 16, lw, lane);
      if (wid == 4) TS(18);
      load_all<C3, true, false, 0u, 0u>(L, lds0 + R3, nop, nop, DBG(1));
    }
    if (wid == 4) TS(19);
    {
      Ld<C4> L; L.A = P.ws + WS_H + (size_t)tm1 * 16 * 16384; L.W = P.w1 + ch0 * 128; L.lw = lw; L.c = c1; L.kt0 = 0; L.init(lane, 128, par ? -64 : 384);
      lds_wait(y, 24 + FL_H, 4u);
      load_preW<C4>(L, lds0 + R4);
      await(y, 2, lane < 16 ? FL_H * 256 + tm1 * 16 + lane : FL_CS * 256 + (m16 >> 2) * 56 + base7 + 3 * par + (lane & 3), 32, lw, lane);
      if (wid == 4) TS(20);
      load_all<C4, true, false, 0u, 0xa2u>(L, lds0 + R4, nop,
        [&](int s) {
          if (s == 1) {
#pragma unroll
            for (int i = 0; i < 4; ++i) dma<true>(lane * 16, P.ws + WS_ST2 + (tm1 * 16 + lw * 4 + i) * 1024, lds0 + S4_STR + (lw * 4 + i) * 1024);
#pragma unroll
            for (int i = 0; i < 2; ++i) dma<true>(lane * 16, P.ws + WS_CS1 + ((lw * 2 + i) * FF + (base7 + 3 * par) * 64) * 4, lds0 + S4_CSR + (lw * 2 + i) * 1024);
          }
          if (s == 5) {
            const LAS float* rawp = (const LAS float*)(smem + S4_CSR) + lt;
            float t = 0.f;
#pragma unroll
            for (int i = 0; i < 8; ++i) t += rawp[i * 256];
            ((LAS float*)(smem + S4_CSF))[lt] = t;
            wait_lgkm0();
          }
          if (s == 7 && lw < 2) {
            f32x2 st[16]; float mu = 0.f, m2 = 0.f;
#pragma unroll
            for (int i = 0; i < 16; ++i) { st[i] = *(const LAS f32x2*)(smem + S4_STR + i * 1024 + lt * 8); mu += st[i][0]; }
            mu *= (1.f / 16.f);
#pragma unroll
            for (int i = 0; i < 16; ++i) { const float d = st[i][0] - mu; m2 += st[i][1] + 64.f * d * d; }
            *(LAS f32x2*)(smem + S4_STF + lt * 8) = (f32x2){mu, 1.f / sqrtf(m2 * (1.f / D) + 1e-5f)};
            wait_lgkm0();
          }
        }, DBG(2));
    }
    if (wid == 4) TS(21);
    {
      Ld<C5> L; L.A = P.ws + WS_F + (size_t)tm5 * 56 * 16384; L.W = P.w2 + tn5 * 256; L.lw = lw; L.c = c5; L.kt0 = 28 * kh; L.init(lane, 128, 0);
      lds_wait(y, 24 + FL_F, 4u);
      load_preW<C5>(L, lds0 + R5);
      await(y, 3, FL_F * 256 + tm5 * 16 + 8 * kh + (lane & 7), 8, lw, lane);
      if (wid == 4) TS(22);
      load_all<C5, true, false, 0u, 0u>(L, lds0 + R5, nop, nop, DBG(3));
    }
    if (wid == 4) TS(23);
    return;
  }

  const int wm = wid >> 1, wn = wid & 1;
  if (wid == 0 && lane < 32) y.w[lane] = 0u;
  if (wid == 3 && (bid >> 3) * 8192 + lane * 128 < CODE_BYTES) {
    const char* pc; asm volatile("s_getpc_b64 %0" : "=s"(pc));
    dma4<false>((bid >> 3) * 8192 + lane * 128, pc, lds0 + S1_IMG);
  }
  if (wid < 2) {
    const int row = bid * 8 + wid * 4 + g;
    u32x4 v[8];
#pragma unroll
    for (int i = 0; i < 8; ++i) v[i] = *(const u32x4*)(P.x + (size_t)row * 2048 + (m16 + 16 * i) * 16);
    float s = 0.f;
#pragma unroll
    for (int i = 0; i < 8; ++i)
#pragma unroll
      for (int j = 0; j < 4; ++j) s += bflo(v[i][j]) + bfhi(v[i][j]);
    const float mean = row16_sum(s) * (1.f / D);
    float q = 0.f;
#pragma unroll
    for (int i = 0; i < 8; ++i)
#pragma unroll
      for (int j = 0; j < 4; ++j) { const float a = bflo(v[i][j]) - mean, b = bfhi(v[i][j]) - mean; q += a * a + b * b; }
    const float rstd = 1.f / sqrtf(row16_sum(q) * (1.f / D) + 1e-5f);
    if (m16 == 0) st8(y.r, (u32x2){bu(mean), bu(rstd)}, row * 8, WS_ST1);
  } else if (bid < 192) {
    colsum128(P.wqkv + (size_t)((bid / 48) * 256 + (wid - 2) * 128) * (3 * D * 2) + (bid % 48) * 128, 3 * D * 2, y.r, WS_CSQ + (((bid / 48) * 2 + wid - 2) * 3 * D + (bid % 48) * 64) * 4, lane);
  }
  {
    f32x4 acc[24];
    Adr ad;
    ad.v[0] = lds0 + R1 + (wm * 64 + m16) * 128 + ((g ^ (m16 >> 1)) << 4); ad.v[1] = lds0 + R1 + (wm * 64 + m16) * 128 + ((g ^ (m16 >> 1) ^ 4) << 4);
    ad.v[2] = lds0 + R1 + 16384 + wlane(lane, 2 * wn); ad.v[3] = lds0 + R1 + 16384 + wlane(lane, 2 * wn + 1); ad.v[4] = 0; ad.v[5] = 0;
    consume<C1, 1, RPG1>(acc, ad, DBG3, [&](int s) { if (s == 1) publish(y, FL_P0, bid, lane); if (s < 0 && wid == 0) TS(1); });
    if (wid == 0) TS(2);
    const int head = 2 * tn1 + wn;
    f32x4 cs[4]; float csv[2];
#pragma unroll
    for (int n = 0; n < 4; ++n) cs[n] = *(const LAS f32x4*)(smem + S1_CSF + ((n >> 1) * 64 + wn * 32 + (n & 1) * 16 + 4 * g) * 4);
#pragma unroll
    for (int n = 0; n < 2; ++n) csv[n] = *(const LAS float*)(smem + S1_CSF + (128 + wn * 32 + n * 16 + m16) * 4);
    u32x4 wp[2];
#pragma unroll
    for (int F = 0; F < 2; ++F) { lds_ptr a0 = smem + S1_WP + wn * 2048 + (4 * g + (m16 >> 2)) * 64 + F * 32 + (lane & 3) * 8; wp[F] = lds_tr2(a0, a0 + 1024); }
    u32x4 qb[4], vb[2][2];
    f32x4 p[4][2];
    const f32x4 zero = {0.f, 0.f, 0.f, 0.f};
    {
      f32x4 vv[4][2];
#pragma unroll
      for (int m = 0; m < 4; ++m) {
        const f32x2 st = *(const LAS f32x2*)(smem + S1_ST + (wm * 64 + m * 16 + m16) * 8);
        const f32x4 s0 = *(const LAS f32x4*)(smem + S1_ST + (wm * 64 + m * 16 + 4 * g) * 8), s1 = *(const LAS f32x4*)(smem + S1_ST + (wm * 64 + m * 16 + 4 * g) * 8 + 16);
        const f32x4 mu = {s0[0], s0[2], s1[0], s1[2]}, rs = {s0[1], s0[3], s1[1], s1[3]};
        qb[m] = pack8((acc[m * 6] - cs[0] * st[0]) * st[1], (acc[m * 6 + 1] - cs[1] * st[0]) * st[1]);
        const u32x4 kb = pack8((acc[m * 6 + 2] - cs[2] * st[0]) * st[1], (acc[m * 6 + 3] - cs[3] * st[0]) * st[1]);
        p[m][0] = mfma(wp[0], kb, zero); p[m][1] = mfma(wp[1], kb, zero);
        vv[m][0] = (acc[m * 6 + 4] - mu * csv[0]) * rs; vv[m][1] = (acc[m * 6 + 5] - mu * csv[1]) * rs;
      }
#pragma unroll
      for (int u = 0; u < 2; ++u) { vb[u][0] = pack8(vv[2 * u][0], vv[2 * u + 1][0]); vb[u][1] = pack8(vv[2 * u][1], vv[2 * u + 1][1]); }
    }
    u32x4 pp[4], pn[4];
    if (wid == 0) TS(26);
    dual_softmax4(p, pp, pn);
    if (wid == 0) TS(27);
    lds_ptr img = smem + S1_IMG + wid * 4352;
    f32x4 kv[4][2], z[4];
#pragma unroll
    for (int F = 0; F < 4; ++F) { kv[F][0] = zero; kv[F][1] = zero; z[F] = zero; }
    const u32x4 ones = {0x3f803f80u, 0x3f803f80u, 0x3f803f80u, 0x3f803f80u};
#pragma unroll
    for (int u = 0; u < 2; ++u) {
#pragma unroll
      for (int t = 0; t < 2; ++t) {
        lds_ptr w = img + (16 * t + m16) * 136 + 8 * g;
        *(LAS u32x2*)(w) = (u32x2){pp[2 * u + t][0], pp[2 * u + t][1]}; *(LAS u32x2*)(w + 32) = (u32x2){pp[2 * u + t][2], pp[2 * u + t][3]};
        *(LAS u32x2*)(w + 64) = (u32x2){pn[2 * u + t][0], pn[2 * u + t][1]}; *(LAS u32x2*)(w + 96) = (u32x2){pn[2 * u + t][2], pn[2 * u + t][3]};
      }
      wait_lgkm0();
#pragma unroll
      for (int F = 0; F < 4; ++F) {
        lds_ptr a0 = img + (4 * g + (m16 >> 2)) * 136 + F * 32 + (lane & 3) * 8;
        const u32x4 pa = lds_tr2(a0, a0 + 16 * 136);
        kv[F][0] = mfma(pa, vb[u][0], kv[F][0]); kv[F][1] = mfma(pa, vb[u][1], kv[F][1]); z[F] = mfma(pa, ones, z[F]);
      }
      wait_lgkm0();
    }
    {
      lds_ptr xi = smem + S1_IMG + (wid ^ 2) * 4352;
#pragma unroll
      for (int i = 0; i < 4; ++i) *(LAS f32x4*)(img + i * 1024 + lane * 16) = wm ? kv[i >> 1][i & 1] : kv[2 + (i >> 1)][i & 1];
      if (m16 == 0) { *(LAS f32x4*)(img + 4096 + g * 32) = wm ? z[0] : z[2]; *(LAS f32x4*)(img + 4096 + g * 32 + 16) = wm ? z[1] : z[3]; }
      wait_lgkm0();
      volatile LAS unsigned* xf = y.w + 16;
      if (lane == 0) xf[wid] = 1u;
      while (xf[wid ^ 2] == 0u) __builtin_amdgcn_s_sleep(0);
      asm volatile("" ::: "memory");
      f32x4 o[4];
#pragma unroll
      for (int i = 0; i < 4; ++i) o[i] = *(const LAS f32x4*)(xi + i * 1024 + lane * 16) + (wm ? kv[2 + (i >> 1)][i & 1] : kv[i >> 1][i & 1]);
      const unsigned kd = WS_KVP + (head * 16 + tm1) * 4096 + wm * 2048;
      st16(y.r, pack8(o[0], o[2]), lane * 16, kd);
      st16(y.r, pack8(o[1], o[3]), lane * 16, kd + 1024);
      if (m16 == 0) {
        const f32x4 za = *(const LAS f32x4*)(xi + 4096 + g * 32) + (wm ? z[2] : z[0]), zb = *(const LAS f32x4*)(xi + 4096 + g * 32 + 16) + (wm ? z[3] : z[1]);
        st16(y.r, __builtin_bit_cast(u32x4, za), g * 32, WS_ZP + (head * 16 + tm1) * 256 + wm * 128);
        st16(y.r, __builtin_bit_cast(u32x4, zb), g * 32 + 16, WS_ZP + (head * 16 + tm1) * 256 + wm * 128);
      }
      if (wid == 0) TS(25);
      publish(y, FL_KV, tm1 * 16 + tn1, lane);
    }
    if (wid == 0) TS(3);
#pragma unroll
    for (int m = 0; m < 4; ++m) { p[m][0] = mfma(wp[0], qb[m], zero); p[m][1] = mfma(wp[1], qb[m], zero); }
    dual_softmax4(p, pp, pn);
    if (wid == 0) TS(4);
    lds_wait(y, 8, 1u);
    kv_sum(y, smem, 2 * tn1, 0, threadIdx.x, wid == 0, lane);
    wait_lgkm0(); bar();
    if (wid == 0) TS(5);
    u32x4 ka[2][2], za[2];
#pragma unroll
    for (int ks = 0; ks < 2; ++ks) {
      ka[ks][0] = lds128(smem + S1_KV + wn * 4096 + ks * 2048 + lane * 16); ka[ks][1] = lds128(smem + S1_KV + wn * 4096 + ks * 2048 + 1024 + lane * 16);
      za[ks] = lds128(smem + S1_KV + 8192 + wn * 128 + ks * 64 + g * 16);
    }
#pragma unroll
    for (int m = 0; m < 4; ++m) {
      f32x4 n0 = mfma(ka[0][0], pp[m], zero), n1 = mfma(ka[0][1], pp[m], zero), dn = mfma(za[0], pp[m], zero);
      n0 = mfma(ka[1][0], pn[m], n0); n1 = mfma(ka[1][1], pn[m], n1); dn = mfma(za[1], pn[m], dn);
      const float rd = 1.f / (dn[0] + 1e-6f);
      st16(y.r, pack8(n0 * rd, n1 * rd), lane * 16, WS_O + (((tm1 * 16 + tn1) * 8 + wm * 4 + m) * 2 + wn) * 1024);
    }
    publish(y, FL_O, tm1 * 16 + tn1, lane);
    if (wid == 0) TS(6);
  }
  {
    u32x4 xr[2][2];
#pragma unroll
    for (int m = 0; m < 2; ++m)
#pragma unroll
      for (int Q = 0; Q < 2; ++Q)
        xr[m][Q] = *(const u32x4*)(P.x + (size_t)(tm3 * 128 + wid * 32 + m * 16 + m16) * 2048 + tn3 * 128 + Q * 64 + (g & 1) * 32 + (g >> 1) * 16);
    f32x4 acc[8];
    Adr ad; ad.v[0] = lds0 + R3 + wid * 4096 + lane * 16; ad.v[1] = 0;
#pragma unroll
    for (int n = 0; n < 4; ++n) ad.v[2 + n] = lds0 + R3 + 16384 + wlane(lane, n);
    consume_s<C3, 3>(acc, ad, [&](int) { if (wid == 0) TS(7); });
    if (wid == 0) TS(8);
#pragma unroll
    for (int m = 0; m < 2; ++m) {
      float hv[16];
#pragma unroll
      for (int Q = 0; Q < 2; ++Q) {
        auto r0 = __builtin_amdgcn_permlane16_swap(xr[m][Q][0], xr[m][Q][2], false, false);
        auto r1 = __builtin_amdgcn_permlane16_swap(xr[m][Q][1], xr[m][Q][3], false, false);
        const f32x4 a = acc[m * 4 + 2 * Q], b = acc[m * 4 + 2 * Q + 1];
        const u32x4 hp = {pk2(a[0] + bflo(r0[0]), a[1] + bfhi(r0[0])), pk2(a[2] + bflo(r1[0]), a[3] + bfhi(r1[0])),
                          pk2(b[0] + bflo(r0[1]), b[1] + bfhi(r0[1])), pk2(b[2] + bflo(r1[1]), b[3] + bfhi(r1[1]))};
        st16(y.r, hp, lane * 16, WS_H + (((tm3 * 16 + tn3) * 8 + wid * 2 + m) * 2 + Q) * 1024);
#pragma unroll
        for (int j = 0; j < 4; ++j) { hv[Q * 8 + 2 * j] = bflo(hp[j]); hv[Q * 8 + 2 * j + 1] = bfhi(hp[j]); }
      }
      float s = 0.f;
#pragma unroll
      for (int j = 0; j < 16; ++j) s += hv[j];
      s = xsum32(xsum16(s)) * (1.f / 64.f);
      float q = 0.f;
#pragma unroll
      for (int j = 0; j < 16; ++j) { const float d = hv[j] - s; q += d * d; }
      q = xsum32(xsum16(q));
      if (g == 0) st8(y.r, (u32x2){bu(s), bu(q)}, m16 * 8, WS_ST2 + (tm3 * 16 + tn3) * 1024 + (wid * 32 + m * 16) * 8);
    }
    if (wid == 0) TS(29);
    publish(y, FL_H, tm3 * 16 + tn3, lane);
    if (wid == 0) TS(9);
  }
  {
    f32x4 acc[28];
    Adr ad;
    ad.v[0] = lds0 + R4 + wm * 8192 + wn * 4096 + lane * 16; ad.v[1] = lds0 + R4 + wm * 8192 + (wn ^ 1) * 4096 + lane * 16;
    ad.v[2] = lds0 + R4 + 16384 + wlane(lane, wn); ad.v[3] = lds0 + R4 + 16384 + wlane(lane, 2 + wn);
    ad.v[4] = lds0 + R4 + 16384 + 6144 + hlane(lane, 0); ad.v[5] = lds0 + R4 + 16384 + 6144 + hlane(lane, 1);
    consume<C4, 4, RPG4>(acc, ad, DBG3, [&](int s) { if (s < 0 && wid == 0) TS(10); });
    if (wid == 0) TS(11);
    f32x4 cs[8];
#pragma unroll
    for (int n = 0; n < 6; ++n) cs[n] = *(const LAS f32x4*)(smem + S4_CSF + ((par + (n >> 1)) * 64 + (2 * (n & 1) + wn) * 16 + 4 * g) * 4);
#pragma unroll
    for (int n = 0; n < 2; ++n) cs[6 + n] = *(const LAS f32x4*)(smem + S4_CSF + ((par ? 32 : 192) + n * 16 + 4 * g) * 4);
    const f32x4 zero = {0.f, 0.f, 0.f, 0.f};
#pragma unroll
    for (int m = 0; m < 4; ++m) {
      const int mt = wm * 4 + ((m + 2 * wn) & 3);
      const f32x2 st = *(const LAS f32x2*)(smem + S4_STF + (mt * 16 + m16) * 8);
      const unsigned fd = WS_F + (tm1 * 56 * 16 + mt * 2) * 1024;
#pragma unroll
      for (int i = 0; i < 3; ++i)
        st16(y.r, pack8(__builtin_elementwise_max((acc[m * 6 + 2 * i] - cs[2 * i] * st[0]) * st[1], zero),
                        __builtin_elementwise_max((acc[m * 6 + 2 * i + 1] - cs[2 * i + 1] * st[0]) * st[1], zero)), lane * 16, fd + (ch0 + i) * 16384 + wn * 1024);
      if (m < 2)
        st16(y.r, pack8(__builtin_elementwise_max((acc[24 + 2 * m] - cs[6] * st[0]) * st[1], zero),
                        __builtin_elementwise_max((acc[24 + 2 * m + 1] - cs[7] * st[0]) * st[1], zero)), lane * 16, fd + (base7 + 3) * 16384 + par * 1024);
    }
    if (wid == 0) TS(28);
    publish(y, FL_F, tm1 * 16 + tn1, lane);
    if (wid == 0) TS(12);
  }
  {
    f32x4 acc[16];
    Adr ad;
    ad.v[0] = lds0 + R5 + wm * 8192 + kh * 4096 + lane * 16; ad.v[1] = lds0 + R5 + wm * 8192 + (kh ^ 1) * 4096 + lane * 16;
#pragma unroll
    for (int n = 0; n < 4; ++n) ad.v[2 + n] = lds0 + R5 + 16384 + wn * 2048 + wlane(lane, n);
    u32x4 hr[2][2];
    consume<C5, 5, RPG5>(acc, ad, DBG3, [&](int s) {
      if (s < 0) {
        if (wid == 0) TS(13);
#pragma unroll
        for (int m = 0; m < 2; ++m)
#pragma unroll
          for (int Q = 0; Q < 2; ++Q) hr[m][Q] = ld16(y.r, lane * 16, WS_H + (((tm5 * 16 + 2 * tn5 + wn) * 8 + wm * 4 + 2 * kh + m) * 2 + Q) * 1024);
      }
    });
    if (wid == 0) TS(14);
    const int tile = tm5 * 8 + tn5;
#pragma unroll
    for (int i = 0; i < 8; ++i) st16(y.r, __builtin_bit_cast(u32x4, acc[8 + i]), lane * 16, WS_XB + (((tile * 2 + kh) * 4 + wid) * 8 + i) * 1024);
    wait_vm<0>();
    if (lane == 0) st4(y.r, y.tag, 0, WS_FLG + ((FL_X + (wid >> 1)) * 256 + tile * 2 + kh) * 128 + (wid & 1) * 64);
    poll(y, ((FL_X + (wid >> 1)) * 256 + tile * 2 + (kh ^ 1)) * 128 + (wid & 1) * 64, 1, lane);
    if (wid == 0) TS(15);
    u32x4 xv[8];
#pragma unroll
    for (int i = 0; i < 8; ++i) xv[i] = ld16(y.r, lane * 16, WS_XB + (((tile * 2 + (kh ^ 1)) * 4 + wid) * 8 + i) * 1024);
#pragma unroll
    for (int m = 0; m < 2; ++m)
#pragma unroll
      for (int Q = 0; Q < 2; ++Q) {
        const f32x4 a = acc[m * 4 + 2 * Q] + __builtin_bit_cast(f32x4, xv[m * 4 + 2 * Q]), b = acc[m * 4 + 2 * Q + 1] + __builtin_bit_cast(f32x4, xv[m * 4 + 2 * Q + 1]);
        const u32x4 hh = hr[m][Q];
        const unsigned a0 = pk2(a[0] + bflo(hh[0]), a[1] + bfhi(hh[0])), a1 = pk2(a[2] + bflo(hh[1]), a[3] + bfhi(hh[1]));
        const unsigned b0 = pk2(b[0] + bflo(hh[2]), b[1] + bfhi(hh[2])), b1 = pk2(b[2] + bflo(hh[3]), b[3] + bfhi(hh[3]));
        auto r0 = __builtin_amdgcn_permlane16_swap(a0, b0, false, false);
        auto r1 = __builtin_amdgcn_permlane16_swap(a1, b1, false, false);
        *(u32x4*)(P.out + (size_t)(tm5 * 128 + wm * 64 + (2 * kh + m) * 16 + m16) * 2048 + tn5 * 256 + wn * 128 + Q * 64 + (g & 1) * 32 + (g >> 1) * 16) = (u32x4){r0[0], r1[0], r0[1], r1[1]};
      }
    if (bid == 0 && threadIdx.x == 0) st4(y.r, y.tag, 0, WS_CTL);
    if (wid == 0) TS(24);
  }
}

extern "C" void kernel_launch(void* const* d_in, const int* in_sizes, int n_in, void* d_out, int out_size, void* d_ws, size_t ws_size,
                              hipStream_t stream) {
  Params p;
  p.x = (const char*)d_in[0]; p.wqkv = (const char*)d_in[1]; p.wphi = (const char*)d_in[2]; p.wo = (const char*)d_in[3];
  p.w1 = (const char*)d_in[5]; p.w2 = (const char*)d_in[6]; p.ws = (char*)d_ws; p.out = (char*)d_out;
  (void)hipFuncSetAttribute((const void*)fused, hipFuncAttributeMaxDynamicSharedMemorySize, LDS_BYTES);
  fused<<<256, 512, LDS_BYTES, stream>>>(p);
}
```

```cpp
#include <hip/hip_runtime.h>
#include <hip/hip_bf16.h>

#define LAS __attribute__((address_space(3)))
#define DI __device__ __forceinline__
typedef __bf16 bf16x8 __attribute__((ext_vector_type(8)));
typedef __bf16 bf16x2 __attribute__((ext_vector_type(2)));
typedef short s16x4 __attribute__((ext_vector_type(4)));
typedef float f32x2 __attribute__((ext_vector_type(2)));
typedef float f32x4 __attribute__((ext_vector_type(4)));
typedef unsigned u32x2 __attribute__((ext_vector_type(2)));
typedef unsigned u32x4 __attribute__((ext_vector_type(4)));
typedef LAS char* lds_ptr;
typedef __amdgpu_buffer_rsrc_t rsrc_t;

constexpr int D = 1024, FF = 3584;
constexpr unsigned WS_CTL = 0;
constexpr unsigned WS_FLG = 4096;
constexpr unsigned WS_ST1 = 320u << 10;
constexpr unsigned WS_CSQ = 384u << 10;
constexpr unsigned WS_CS1 = 576u << 10;
constexpr unsigned WS_ZP = 704u << 10;
constexpr unsigned WS_ST2 = 832u << 10;
constexpr unsigned WS_SMALL = 1280u << 10;
constexpr unsigned WS_KVP = 2u << 20;
constexpr unsigned WS_O = 4u << 20;
constexpr unsigned WS_H = 8u << 20;
constexpr unsigned WS_F = 12u << 20;
constexpr unsigned WS_END = 26u << 20;
enum { FL_P0 = 0, FL_KV = 1, FL_O = 2, FL_H = 3, FL_F = 4, FL_CS = 5, FL_X = 6   };

#define TS(I) do {} while (0)
#define TS_FLUSH() do {} while (0)

#define DBG(k) nullptr
#define CLK(v)
#define DBG4 nullptr
struct Params { const char* x; const char* wqkv; const char* wphi; const char* wo; const char* w1; const char* w2; char* ws; char* out; };

DI unsigned pk2(float a, float b) { f32x2 v = {a, b}; return __builtin_bit_cast(unsigned, __builtin_convertvector(v, bf16x2)); }
DI float bflo(unsigned u) { return __uint_as_float(u << 16); }
DI float bfhi(unsigned u) { return __uint_as_float(u & 0xffff0000u); }
DI u32x4 pack8(const f32x4& a, const f32x4& b) { return (u32x4){pk2(a[0], a[1]), pk2(a[2], a[3]), pk2(b[0], b[1]), pk2(b[2], b[3])}; }
DI bf16x8 asbf(const u32x4& v) { return __builtin_bit_cast(bf16x8, v); }
#define SB() __builtin_amdgcn_sched_barrier(0)
DI void bar() { asm volatile("" ::: "memory"); __builtin_amdgcn_s_barrier(); asm volatile("" ::: "memory"); }
DI void wait_lgkm0() { __builtin_amdgcn_s_waitcnt(0xC07F); asm volatile("" ::: "memory"); }
template <int N> DI void wait_vm() { asm volatile("s_waitcnt vmcnt(%0)" ::"i"(N) : "memory"); }
template <bool SC1> DI void dma(unsigned voff, const char* sbase, unsigned lds) {
  if (SC1) asm volatile("s_mov_b32 m0, %2\n\ts_nop 3\n\tglobal_load_lds_dwordx4 %0, %1 sc1" ::"v"(voff), "s"(sbase), "s"(lds) : "memory");
  else asm volatile("s_mov_b32 m0, %2\n\ts_nop 3\n\tglobal_load_lds_dwordx4 %0, %1" ::"v"(voff), "s"(sbase), "s"(lds) : "memory");
}
template <bool SC1> DI void dma4(unsigned voff, const char* sbase, unsigned lds) {
  if (SC1) asm volatile("s_mov_b32 m0, %2\n\ts_nop 3\n\tglobal_load_lds_dword %0, %1 sc1" ::"v"(voff), "s"(sbase), "s"(lds) : "memory");
  else asm volatile("s_mov_b32 m0, %2\n\ts_nop 3\n\tglobal_load_lds_dword %0, %1" ::"v"(voff), "s"(sbase), "s"(lds) : "memory");
}
DI u32x4 lds128(lds_ptr p) { return *(const LAS u32x4*)p; }
DI u32x4 lds_tr2(lds_ptr p0, lds_ptr p1) {
  s16x4 a = __builtin_amdgcn_ds_read_tr16_b64_v4i16((LAS s16x4*)p0);
  s16x4 b = __builtin_amdgcn_ds_read_tr16_b64_v4i16((LAS s16x4*)p1);
  return __builtin_bit_cast(u32x4, __builtin_shufflevector(a, b, 0, 1, 2, 3, 4, 5, 6, 7));
}
DI u32x2 lds_tr(lds_ptr p) { return __builtin_bit_cast(u32x2, __builtin_amdgcn_ds_read_tr16_b64_v4i16((LAS s16x4*)p)); }
DI f32x4 mfma(const u32x4& a, const u32x4& b, const f32x4& c) { return __builtin_amdgcn_mfma_f32_16x16x32_bf16(asbf(a), asbf(b), c, 0, 0, 0); }
DI void mfma_ip(f32x4& acc, const u32x4& a, const u32x4& b) { asm volatile("v_mfma_f32_16x16x32_bf16 %0, %1, %2, %0" : "+v"(acc) : "v"(a), "v"(b)); }
DI u32x4 ld16(rsrc_t r, unsigned voff, unsigned soff) { return __builtin_amdgcn_raw_buffer_load_b128(r, voff, soff, 16); }
DI void st16(rsrc_t r, const u32x4& v, unsigned voff, unsigned soff) { __builtin_amdgcn_raw_buffer_store_b128(v, r, voff + soff, 0, 16);
}
DI void st8(rsrc_t r, const u32x2& v, unsigned voff, unsigned soff) { __builtin_amdgcn_raw_buffer_store_b64(v, r, voff, soff, 16); }
DI void st4(rsrc_t r, unsigned v, unsigned voff, unsigned soff) { __builtin_amdgcn_raw_buffer_store_b32(v, r, voff, soff, 16); }
DI float fb(unsigned u) { return __uint_as_float(u); }
DI unsigned bu(float f) { return __float_as_uint(f); }
DI float xsum16(float x) { auto r = __builtin_amdgcn_permlane16_swap(bu(x), bu(x), false, false); return fb(r[0]) + fb(r[1]); }
DI float xsum32(float x) { auto r = __builtin_amdgcn_permlane32_swap(bu(x), bu(x), false, false); return fb(r[0]) + fb(r[1]); }
template <int CTRL> DI float dpp(float x) { return __builtin_bit_cast(float, __builtin_amdgcn_update_dpp(0, __builtin_bit_cast(int, x), CTRL, 0xf, 0xf, true)); }
DI float row16_sum(float x) { x += dpp<0xB1>(x); x += dpp<0x4E>(x); x += dpp<0x141>(x); x += dpp<0x140>(x); return x; }

struct Sync { rsrc_t r; unsigned tag; LAS unsigned* w; };
DI void publish(const Sync& y, int phase, int idx, int lane) {
  if (lane == 0) __hip_atomic_fetch_add(y.w + 24 + phase, 1u, __ATOMIC_RELAXED, __HIP_MEMORY_SCOPE_WORKGROUP);
  wait_vm<0>();
  if (lane == 0 && __hip_atomic_fetch_add(y.w + phase, 1u, __ATOMIC_RELAXED, __HIP_MEMORY_SCOPE_WORKGROUP) == 3u) st4(y.r, y.tag, 0, WS_FLG + (phase * 256 + idx) * 128);
}
DI void poll(const Sync& y, unsigned off, int n, int lane) {
  const unsigned long long t0 = __builtin_amdgcn_s_memrealtime();
  for (unsigned it = 0;; ++it) {
    const unsigned v = __builtin_amdgcn_raw_buffer_load_b32(y.r, WS_FLG + off, 0, 16);
    if (__all(lane >= n || v == y.tag)) break;
    if ((it & 63) == 63) {
      if (__builtin_amdgcn_readfirstlane(__builtin_amdgcn_raw_buffer_load_b32(y.r, 0, WS_CTL + 128, 16)) == y.tag) break;
      if (__builtin_amdgcn_s_memrealtime() - t0 > 4000000ull) { if (lane == 0) st4(y.r, y.tag, 0, WS_CTL + 128); break; }
    }
    __builtin_amdgcn_s_sleep(2);
  }
  asm volatile("" ::: "memory");
}
DI void lds_wait(const Sync& y, int word, unsigned v) {
  volatile LAS unsigned* w = y.w + word;
  while (*w < v) __builtin_amdgcn_s_sleep(1);
  asm volatile("" ::: "memory");
}
struct Gate { unsigned off, mask; int word; };
DI void gate_slow(const Sync& y, Gate& G, unsigned need, bool poller, int lane, int cnt = 0) {
  volatile LAS unsigned* w = y.w + G.word;
  if (poller) {
    const unsigned long long t0 = __builtin_amdgcn_s_memrealtime();
    for (unsigned it = 0;; ++it) {
      G.mask = (unsigned)__ballot(__builtin_amdgcn_raw_buffer_load_b32(y.r, WS_FLG + G.off, 0, 16) == y.tag);
      if ((it & 63) == 63) {
        const bool late = __builtin_amdgcn_s_memrealtime() - t0 > 4000000ull;
        if (late && lane == 0) st4(y.r, y.tag, 0, WS_CTL + 128);
        if (late || __builtin_amdgcn_readfirstlane(__builtin_amdgcn_raw_buffer_load_b32(y.r, 0, WS_CTL + 128, 16)) == y.tag) G.mask = ~0u;
      }
      *w = G.mask;
      if ((G.mask & need) == need && __builtin_popcount(G.mask & 0xffffu) >= cnt) break;
      __builtin_amdgcn_s_sleep(1);
    }
  } else {
    for (;;) { G.mask = __builtin_amdgcn_readfirstlane(*w); if ((G.mask & need) == need && __builtin_popcount(G.mask & 0xffffu) >= cnt) break; __builtin_amdgcn_s_sleep(1); }
  }
  asm volatile("" ::: "memory");
}
DI void gate(const Sync& y, Gate& G, unsigned need, bool poller, int lane) { if (__builtin_expect((G.mask & need) != need, 0)) gate_slow(y, G, need, poller, lane); }

template <int NK> DI int kmap(int t, int c) {
  const int u = t - 1, q = u & 3, b4 = u & ~3, body = (int)(u < NK - 4);
  const int lead = ((int)(q == c) & body) | (int)(t == 0);
  const int non = b4 + ((c + 1 + q - ((int)(q > c) & body)) & 3);
  return non ^ (((b4 + 4 + c) ^ non) & -lead);
}
#define LAG3 1
#define LAG5 1
enum { ORD_NAT = 0, ORD_PAIR = 1, ORD_F = 2 };
template <int NSUB_, bool HALF_, int NSLOT_, int NK_, int ORD_, int WSTRIDE_, bool XROW_, int LAG_> struct Cfg {
  static constexpr int NSUB = NSUB_, NSLOT = NSLOT_, NK = NK_, ORD = ORD_, WSTRIDE = WSTRIDE_;
  static constexpr bool HALF = HALF_, XROW = XROW_;
  static constexpr int LAG = LAG_;
  static constexpr int STAGE = 16384 + NSUB * 8192 + (HALF ? 4096 : 0), PW = 4 + 2 * NSUB + (HALF ? 1 : 0), RING = STAGE * NSLOT;
  static constexpr int WB = NSUB * 2048 + (HALF ? 1024 : 0);
};
typedef Cfg<3, false, 3, 16, ORD_NAT, 3 * D * 2, true, 0> C1;
typedef Cfg<1, false, 5, 16, ORD_PAIR, D * 2, false, LAG3> C3;
typedef Cfg<3, true, 3, 16, ORD_PAIR, FF * 2, false, 0> C4;
typedef Cfg<2, false, 4, 28, ORD_F, D * 2, false, LAG5> C5;

#define GLDS "global_load_lds_dwordx4 "
template <class C> struct Ld {
  const char* A; const char* W; int lw, c, kt0; unsigned vA[4], vW[7];
  const char* sA; const char* sW; unsigned need;
  Gate G; const Sync* y; int lane_;
  static constexpr int C0 = C::WB > 4096 ? 4096 : 0;
  DI void init(const Sync& y_, int lane, int substep, int halfrel, unsigned goff, int gword) {
    y = &y_; lane_ = lane; G.off = goff * 128; G.mask = 0; G.word = gword;
#pragma unroll
    for (int i = 0; i < 4; ++i) vA[i] = C::XROW ? (lane >> 3) * 2048 + ((((lane & 7) ^ (lane >> 4)) << 4) ^ ((i & 1) * 64)) + i * (16384 - 1024) : lane * 16;
    const int rl = (C::ORD == ORD_NAT) ? 8 * (lane >> 5) + ((lane >> 3) & 3) : (lane >> 3);
    const unsigned w0 = rl * C::WSTRIDE + (((lane & 7) ^ (((lane >> 4) & 3) << 1)) << 4) + 4096;
#pragma unroll
    for (int q = 0; q < 2 * C::NSUB; ++q) vW[q] = w0 + (q & 1) * ((C::ORD == ORD_NAT ? 16 : 8) * C::WSTRIDE) + (q >> 1) * substep - (q * 1024 - C0);
    if (C::HALF) vW[6] = (lane >> 2) * C::WSTRIDE + (((lane & 3) ^ (((lane >> 4) & 1) << 1)) << 4) + 4096 + halfrel - (6144 - C0);
    A += C::XROW ? lw * 32 * 2048 : lw * 4096;
    W -= 4096;
  }
  DI void prep(int s) {
    const int kt = kt0 + kmap<C::NK>(s, c), kb = lw >> 1, t = lw & 1;
    int ur;
    if (C::ORD == ORD_NAT) ur = 32 * kb + 4 * t;
    else if (C::ORD == ORD_F) { const int sp = -(int)((0x10204081020408ull >> kt) & 1u); ur = (16 * lw & sp) | ((16 * kb + 32 * t) & ~sp); }
    else ur = 16 * lw;
    if (C::ORD == ORD_F) { const int jj = (kt * 37) >> 8, r = kt - 7 * jj; need = (unsigned)((int)(r <= 3) | ((int)(r >= 3) << 1)) << ((2 * jj) & 7); }
    else need = 1u << kt;
    sA = A + (unsigned)(kt * (C::XROW ? 128 : 16384));
    sW = W + (unsigned)((kt * 64 + ur) * C::WSTRIDE);
  }
  DI void issueA(unsigned base) {
    if (!C::XROW) gate(*y, G, need, lw == 0, lane_);
    const unsigned m = base + lw * 4096;
    if (C::XROW) asm volatile("s_mov_b32 m0, %5\n\ts_nop 3\n\t" GLDS "%0, %4\n\t" GLDS "%1, %4 offset:1024\n\t" GLDS "%2, %4 offset:2048\n\t" GLDS "%3, %4 offset:3072"
                              ::"v"(vA[0]), "v"(vA[1]), "v"(vA[2]), "v"(vA[3]), "s"(sA), "s"(m) : "memory");
    else asm volatile("s_mov_b32 m0, %2\n\ts_nop 3\n\t" GLDS "%0, %1 sc1\n\t" GLDS "%0, %1 offset:1024 sc1\n\t" GLDS "%0, %1 offset:2048 sc1\n\t" GLDS "%0, %1 offset:3072 sc1"
                      ::"v"(vA[0]), "s"(sA), "s"(m) : "memory");
  }
  DI void issueW(unsigned base) const {
    const unsigned m = base + 16384 + lw * C::WB + C0;
    if (C::NSUB == 1) asm volatile("s_mov_b32 m0, %3\n\ts_nop 3\n\t" GLDS "%0, %2\n\t" GLDS "%1, %2 offset:1024" ::"v"(vW[0]), "v"(vW[1]), "s"(sW), "s"(m) : "memory");
    if (C::NSUB == 2) asm volatile("s_mov_b32 m0, %5\n\ts_nop 3\n\t" GLDS "%0, %4\n\t" GLDS "%1, %4 offset:1024\n\t" GLDS "%2, %4 offset:2048\n\t" GLDS "%3, %4 offset:3072"
                                   ::"v"(vW[0]), "v"(vW[1]), "v"(vW[2]), "v"(vW[3]), "s"(sW), "s"(m) : "memory");
    if (C::NSUB == 3) asm volatile("s_mov_b32 m0, %7\n\ts_nop 3\n\t" GLDS "%0, %6 offset:-4096\n\t" GLDS "%1, %6 offset:-3072\n\t" GLDS "%2, %6 offset:-2048\n\t" GLDS "%3, %6 offset:-1024\n\t"
                                   GLDS "%4, %6\n\t" GLDS "%5, %6 offset:1024"
                                   ::"v"(vW[0]), "v"(vW[1]), "v"(vW[2]), "v"(vW[3]), "v"(vW[4]), "v"(vW[5]), "s"(sW), "s"(m) : "memory");
    if (C::HALF) asm volatile(GLDS "%0, %1 offset:2048" ::"v"(vW[6]), "s"(sW) : "memory");
  }
};
template <class C> DI void load_preW(Ld<C>& L, unsigned ring) {
#pragma unroll 1
  for (int s = 0; s < C::NSLOT; ++s) { L.prep(s); L.issueW(ring + s * C::STAGE); }
}
template <class C, bool SPLIT> constexpr int vm_allow(int S) {
  const int N = C::NSLOT, behind = S - 1 - C::LAG > 0 ? S - 1 - C::LAG : 0;
  const int last = N - 1 + behind < C::NK - 1 ? N - 1 + behind : C::NK - 1;
  return (SPLIT && S < N) ? 4 * (N - 1 - S) + C::PW * (last - (N - 1)) : C::PW * (last - S);
}
template <class C, bool SPLIT, unsigned POSTM, int S, int SE, class Post> DI void load_fixed(Ld<C>& L, unsigned ring, const Post& post) {
  if constexpr (S < SE) {
    wait_vm<vm_allow<C, SPLIT>(S)>();
    bar();
    constexpr int T = S - 1 - C::LAG + C::NSLOT;
    if constexpr (S - 1 - C::LAG >= 0 && T < C::NK) {
      L.issueW(ring + (T % C::NSLOT) * C::STAGE); L.issueA(ring + (T % C::NSLOT) * C::STAGE);
      if constexpr (T + 1 < C::NK) L.prep(T + 1);
    }
    if constexpr ((POSTM >> S) & 1u) post(S);
    load_fixed<C, SPLIT, POSTM, S + 1, SE>(L, ring, post);
  }
}
template <class C, bool SPLIT, unsigned PREM, unsigned POSTM, class Pre, class Post>
DI void load_all(Ld<C>& L, unsigned ring, const Pre& pre, const Post& post, unsigned long long* dbg = nullptr) {
  constexpr int N = C::NSLOT, S0 = (SPLIT && N - 1 > 1 + C::LAG) ? N - 1 : 1 + C::LAG + (C::LAG ? 1 : 0), S1 = C::NK - N + C::LAG;
#pragma unroll 1
  for (int s = 0; s < N; ++s) { L.prep(s); if (!SPLIT) L.issueW(ring + s * C::STAGE); L.issueA(ring + s * C::STAGE); }
  L.prep(N);
  load_fixed<C, SPLIT, POSTM, 0, S0>(L, ring, post);
  static_assert(vm_allow<C, SPLIT>(S0) == (N - 2 - C::LAG) * C::PW && vm_allow<C, SPLIT>(S1) == (N - 2 - C::LAG) * C::PW, "steady count");
  unsigned b = ring + ((S0 - 1 - C::LAG) % N) * C::STAGE;
#pragma unroll 1
  for (int s = S0; s <= S1; ++s) {
    CLK(t0);
    wait_vm<(N - 2 - C::LAG) * C::PW>();
    if (PREM && ((PREM >> s) & 1u)) pre(s);
    CLK(t1);
    bar();
    CLK(t2);
    L.issueW(b);
    L.issueA(b);
    CLK(t3);
    L.prep(min(s - C::LAG + N, C::NK - 1));
    b = (b == ring + (N - 1) * C::STAGE) ? ring : b + C::STAGE;
    if (POSTM && ((POSTM >> s) & 1u)) post(s);
  }
  load_fixed<C, SPLIT, POSTM, S1 + 1, C::NK>(L, ring, post);
  bar();
}

template <int MT, int NWF> struct Fr { u32x4 a[MT]; u32x2 w[NWF][2]; };
DI u32x4 cat(const u32x2 (&w)[2]) { return (u32x4){w[0][0], w[0][1], w[1][0], w[1][1]}; }
struct Adr { unsigned v[6]; };
template <int KIND> struct Shape;
template <> struct Shape<1> { static constexpr int MT = 4, NWF = 6, NMF = 24, NACC = 24, WB = 6144; static constexpr unsigned USE = 0x0f; };
template <> struct Shape<3> { static constexpr int MT = 2, NWF = 4, NMF = 8, NACC = 8, WB = 2048; static constexpr unsigned USE = 0x3d; };
template <> struct Shape<4> { static constexpr int MT = 4, NWF = 8, NMF = 28, NACC = 28, WB = 7168; static constexpr unsigned USE = 0x3f; };
template <> struct Shape<5> { static constexpr int MT = 4, NWF = 4, NMF = 16, NACC = 16, WB = 4096; static constexpr unsigned USE = 0x3f; };
DI lds_ptr lp(unsigned a) { return (lds_ptr)(size_t)a; }
template <int KIND, int J> DI void rd1(Fr<Shape<KIND>::MT, Shape<KIND>::NWF>& f, const Adr& ad, int kb) {
  constexpr int NWF = Shape<KIND>::NWF, MT = Shape<KIND>::MT, WB = Shape<KIND>::WB;
  if constexpr (J < 2 * NWF) {
    constexpr int n = J >> 1, t = J & 1;
    if constexpr (KIND == 4 && n >= 6) f.w[n][t] = lds_tr(lp(ad.v[4 + (n - 6)]) + (2 * kb + t) * WB);
    else if constexpr (KIND == 1 || KIND == 4) f.w[n][t] = lds_tr(lp(ad.v[2 + (n & 1)]) + (n >> 1) * 2048 + (2 * kb + t) * WB);
    else f.w[n][t] = lds_tr(lp(ad.v[2 + n]) + (2 * kb + t) * WB);
  } else if constexpr (J < 2 * NWF + MT) {
    constexpr int m = J - 2 * NWF;
    if constexpr (KIND == 1) f.a[m] = lds128(lp(ad.v[kb]) + m * 2048);
    else f.a[m] = lds128(lp(ad.v[m >> 1]) + (m & 1) * 2048 + kb * 1024);
  }
}
template <int KIND, int I> DI void mm1(f32x4 (&acc)[Shape<KIND>::NACC], const Fr<Shape<KIND>::MT, Shape<KIND>::NWF>& f) {
  if constexpr (KIND == 1) { constexpr int m = I / 6, n = I % 6; if constexpr (n < 4) mfma_ip(acc[I], cat(f.w[n]), f.a[m]); else mfma_ip(acc[I], f.a[m], cat(f.w[n])); }
  else if constexpr (KIND == 4) { if constexpr (I < 24) mfma_ip(acc[I], cat(f.w[I % 6]), f.a[I / 6]); else mfma_ip(acc[I], cat(f.w[6 + ((I - 24) & 1)]), f.a[(I - 24) >> 1]); }
  else mfma_ip(acc[I], cat(f.w[I % 4]), f.a[I / 4]);
}
template <int KIND, int RPG, bool READ, bool MOVE, int I = 0>
DI void step(f32x4 (&acc)[Shape<KIND>::NACC], const Fr<Shape<KIND>::MT, Shape<KIND>::NWF>& cur, Fr<Shape<KIND>::MT, Shape<KIND>::NWF>& nxt, Adr& ad, int kb, int delta) {
  constexpr int NMF = Shape<KIND>::NMF, G0 = (2 * Shape<KIND>::NWF + Shape<KIND>::MT + RPG - 1) / RPG;
  if constexpr (I < NMF) {
    if constexpr (READ) {
      rd1<KIND, I * RPG>(nxt, ad, kb);
      if constexpr (RPG > 1) rd1<KIND, I * RPG + 1>(nxt, ad, kb);
    }
    if constexpr (MOVE && I >= G0) {
      constexpr int per = (6 + (NMF - G0) - 1) / (NMF - G0);
#pragma unroll
      for (int k = (I - G0) * per; k < (I - G0 + 1) * per && k < 6; ++k)
        if ((Shape<KIND>::USE >> k) & 1u) { ad.v[k] += delta; asm volatile("" : "+v"(ad.v[k])); }
    }
    mm1<KIND, I>(acc, cur);
    SB();
    step<KIND, RPG, READ, MOVE, I + 1>(acc, cur, nxt, ad, kb, delta);
  }
}
template <int KIND, int J = 0> DI void rdall(Fr<Shape<KIND>::MT, Shape<KIND>::NWF>& f, const Adr& ad, int kb) {
  if constexpr (J < 2 * Shape<KIND>::NWF + Shape<KIND>::MT) { rd1<KIND, J>(f, ad, kb); rdall<KIND, J + 1>(f, ad, kb); }
}
#define ST4() do {} while (0)
#define ST4ARGS
#define ST4PASS
template <class C, int KIND, int RPG, int CNT>
DI void stages(f32x4 (&acc)[Shape<KIND>::NACC], Fr<Shape<KIND>::MT, Shape<KIND>::NWF>& f0, Fr<Shape<KIND>::MT, Shape<KIND>::NWF>& f1, Adr& ad, int& slot ST4ARGS) {
#pragma unroll 1
  for (int s = 0; s < CNT; ++s) {
    const int delta = (slot + 1 == C::NSLOT) ? -(C::NSLOT - 1) * C::STAGE : C::STAGE;
    slot = (slot + 1 == C::NSLOT) ? 0 : slot + 1;
    SB();
    step<KIND, RPG, true, true>(acc, f0, f1, ad, 1, delta);
    if (!C::LAG) wait_lgkm0();
    ST4();
    bar();
    ST4();
    SB();
    step<KIND, RPG, true, false>(acc, f1, f0, ad, 0, 0);
  }
}
template <class C, int KIND, int RPG, int H1, int H2, class Hook> DI void consume(f32x4 (&acc)[Shape<KIND>::NACC], Adr& ad, unsigned* dbg4, const Hook& hook) {
  Fr<Shape<KIND>::MT, Shape<KIND>::NWF> f0, f1;
#pragma unroll
  for (int i = 0; i < Shape<KIND>::NACC; ++i) acc[i] = (f32x4){0.f, 0.f, 0.f, 0.f};
  bar();
  rdall<KIND>(f0, ad, 0);
  hook(-1);
  wait_lgkm0();
  int slot = 0;
  if constexpr (H1 > 0) {
    stages<C, KIND, RPG, H1>(acc, f0, f1, ad, slot ST4PASS);
    hook(1);
    stages<C, KIND, RPG, H2 - H1>(acc, f0, f1, ad, slot ST4PASS);
    hook(2);
  }
  stages<C, KIND, RPG, C::NK - 1 - H2>(acc, f0, f1, ad, slot ST4PASS);
  SB();
  step<KIND, RPG, true, false>(acc, f0, f1, ad, 1, 0);
  wait_lgkm0();
  bar();
  SB();
  step<KIND, RPG, false, false>(acc, f1, f0, ad, 0, 0);
  asm volatile("s_nop 7\n\ts_nop 3");
  SB();
}
template <class C, int KIND, int RPG, class Hook> DI void consume_s(f32x4 (&acc)[Shape<KIND>::NACC], Adr& ad, const Hook& hook) {
  Fr<Shape<KIND>::MT, Shape<KIND>::NWF> f0, f1, g0, g1;
#pragma unroll
  for (int i = 0; i < Shape<KIND>::NACC; ++i) acc[i] = (f32x4){0.f, 0.f, 0.f, 0.f};
  bar();
  rdall<KIND>(f0, ad, 0); rdall<KIND>(f1, ad, 1);
  hook(-1);
#pragma unroll
  for (int k = 0; k < 6; ++k) ad.v[k] += C::STAGE;
  int slot = 1;
#pragma unroll 1
  for (int s = 0; s < C::NK - 2; s += 2) {
    int delta = (slot + 1 == C::NSLOT) ? -(C::NSLOT - 1) * C::STAGE : C::STAGE;
    slot = (slot + 1 == C::NSLOT) ? 0 : slot + 1;
    if (!C::LAG) wait_lgkm0();
    bar(); SB();
    step<KIND, RPG, true, false>(acc, f0, g0, ad, 0, 0);
    step<KIND, RPG, true, true>(acc, f1, g1, ad, 1, delta);
    delta = (slot + 1 == C::NSLOT) ? -(C::NSLOT - 1) * C::STAGE : C::STAGE;
    slot = (slot + 1 == C::NSLOT) ? 0 : slot + 1;
    if (!C::LAG) wait_lgkm0();
    bar(); SB();
    step<KIND, RPG, true, false>(acc, g0, f0, ad, 0, 0);
    step<KIND, RPG, true, true>(acc, g1, f1, ad, 1, delta);
  }
  if (!C::LAG) wait_lgkm0();
  bar(); SB();
  step<KIND, RPG, true, false>(acc, f0, g0, ad, 0, 0);
  step<KIND, RPG, true, false>(acc, f1, g1, ad, 1, 0);
  wait_lgkm0(); bar(); SB();
  step<KIND, RPG, false, false>(acc, g0, f0, ad, 0, 0);
  step<KIND, RPG, false, false>(acc, g1, f1, ad, 1, 0);
  asm volatile("s_nop 7\n\ts_nop 3");
  SB();
}
DI unsigned wlane(int lane, int nt) {
  const int g = lane >> 4, q = (lane >> 2) & 3, p = lane & 3, sw = (2 * g + (q >> 1)) & 3;
  return (4 * g + q) * 128 + ((nt ^ sw) << 5) + p * 8;
}
DI unsigned hlane(int lane, int nt) {
  const int g = lane >> 4, q = (lane >> 2) & 3, p = lane & 3;
  return (4 * g + q) * 64 + ((nt ^ (g & 1)) << 5) + p * 8;
}

template <int OP> DI float op2(float a, float b) { return OP == 0 ? a + b : OP == 1 ? __builtin_elementwise_maximum(a, b) : __builtin_elementwise_minimum(a, b); }
template <int OP> DI float red4(float x0, float x1, float x2, float x3) {
  auto a = __builtin_amdgcn_permlane16_swap(bu(x0), bu(x1), false, false);
  auto b = __builtin_amdgcn_permlane16_swap(bu(x2), bu(x3), false, false);
  const float y0 = op2<OP>(fb(a[0]), fb(a[1])), y1 = op2<OP>(fb(b[0]), fb(b[1]));
  auto c = __builtin_amdgcn_permlane32_swap(bu(y0), bu(y1), false, false);
  return op2<OP>(fb(c[0]), fb(c[1]));
}
DI void spread(float z, float (&o)[4]) {
  auto a = __builtin_amdgcn_permlane32_swap(bu(z), bu(z), false, false);
  auto b = __builtin_amdgcn_permlane16_swap(a[0], a[0], false, false);
  auto c = __builtin_amdgcn_permlane16_swap(a[1], a[1], false, false);
  o[0] = fb(b[0]); o[1] = fb(b[1]); o[2] = fb(c[0]); o[3] = fb(c[1]);
}
DI void dual_softmax4(const f32x4 (&p)[4][2], u32x4 (&pp)[4], u32x4 (&pn)[4]) {
  constexpr float L2E = 1.4426950408889634f;
  float mx[4], mn[4];
#pragma unroll
  for (int m = 0; m < 4; ++m) {
    mx[m] = op2<1>(op2<1>(op2<1>(op2<1>(p[m][0][0], p[m][0][1]), p[m][0][2]), op2<1>(op2<1>(p[m][0][3], p[m][1][0]), p[m][1][1])), op2<1>(p[m][1][2], p[m][1][3]));
    mn[m] = op2<2>(op2<2>(op2<2>(op2<2>(p[m][0][0], p[m][0][1]), p[m][0][2]), op2<2>(op2<2>(p[m][0][3], p[m][1][0]), p[m][1][1])), op2<2>(p[m][1][2], p[m][1][3]));
  }
  spread(-L2E * red4<1>(mx[0], mx[1], mx[2], mx[3]), mx);
  spread(L2E * red4<2>(mn[0], mn[1], mn[2], mn[3]), mn);
  f32x4 ep[4][2], en[4][2];
  float sp[4], sn[4];
#pragma unroll
  for (int m = 0; m < 4; ++m) {
#pragma unroll
    for (int F = 0; F < 2; ++F) {
      const f32x4 a = p[m][F] * L2E + mx[m], b = p[m][F] * (-L2E) + mn[m];
#pragma unroll
      for (int r = 0; r < 4; ++r) { ep[m][F][r] = __builtin_amdgcn_exp2f(a[r]); en[m][F][r] = __builtin_amdgcn_exp2f(b[r]); }
    }
    const f32x4 a = ep[m][0] + ep[m][1], b = en[m][0] + en[m][1];
    sp[m] = (a[0] + a[1]) + (a[2] + a[3]); sn[m] = (b[0] + b[1]) + (b[2] + b[3]);
  }
  spread(__builtin_amdgcn_rcpf(red4<0>(sp[0], sp[1], sp[2], sp[3])), sp);
  spread(__builtin_amdgcn_rcpf(red4<0>(sn[0], sn[1], sn[2], sn[3])), sn);
#pragma unroll
  for (int m = 0; m < 4; ++m) { pp[m] = pack8(ep[m][0] * sp[m], ep[m][1] * sp[m]); pn[m] = pack8(en[m][0] * sn[m], en[m][1] * sn[m]); }
}

DI float dot2(unsigned a, unsigned b, float c) { return __builtin_amdgcn_fdot2_f32_bf16(__builtin_bit_cast(bf16x2, a), __builtin_bit_cast(bf16x2, b), c, false); }
DI unsigned opaque(unsigned c) { asm volatile("" : "+s"(c)); return c; }
template <int NL> DI void colsum_load(u32x4 (&v)[NL], const char* w, int stride, int lane) {
#pragma unroll
  for (int i = 0; i < NL; ++i) v[i] = *(const u32x4*)(w + (size_t)(8 * i + (lane >> 3)) * stride + (lane & 7) * 16);
}
template <int NL> DI void colsum_finish(const u32x4 (&v)[NL], rsrc_t r, unsigned dst, int lane) {
  f32x2 s2[4];
#pragma unroll
  for (int j = 0; j < 4; ++j) s2[j] = (f32x2){bflo(v[0][j]), bfhi(v[0][j])};
#pragma unroll
  for (int i = 1; i < NL; ++i)
#pragma unroll
    for (int j = 0; j < 4; ++j) s2[j] += (f32x2){bflo(v[i][j]), bfhi(v[i][j])};
  const float s[8] = {s2[0][0], s2[0][1], s2[1][0], s2[1][1], s2[2][0], s2[2][1], s2[3][0], s2[3][1]};
  float t[4], u[2];
#pragma unroll
  for (int j = 0; j < 4; ++j) { auto q = __builtin_amdgcn_permlane32_swap(bu(s[j]), bu(s[j + 4]), false, false); t[j] = fb(q[0]) + fb(q[1]); }
#pragma unroll
  for (int j = 0; j < 2; ++j) { auto q = __builtin_amdgcn_permlane16_swap(bu(t[j]), bu(t[j + 2]), false, false); u[j] = fb(q[0]) + fb(q[1]); }
  const bool b3 = lane & 8;
  const float tot = (b3 ? u[1] : u[0]) + dpp<0x128>(b3 ? u[0] : u[1]);
  st4(r, bu(tot), ((lane & 7) * 8 + ((lane >> 5) & 1) * 4 + ((lane >> 4) & 1) * 2 + ((lane >> 3) & 1)) * 4, dst);
}

template <int XN, int TM, int TN> DI void tile_of(int bid, int& tm, int& tn, int& c) {
  const int x = bid & 7, r = bid >> 3, li = r % TM, lj = r / TM;
  tm = (x / XN) * TM + li; tn = (x % XN) * TN + lj; c = (li + lj) & 3;
}

#define RPG1 1
#define RPG4 1
#define RPG5 1
constexpr int R1 = 40704, R3 = 40704, R4 = 0, R5 = 0, SYNCW = 163584;
constexpr int S1_ST = 0, S1_CSR = 1024, S1_CSF = 13312, S1_FLG = 14080, S1_WP = 14336, S1_KV = 18432, S1_DUM = 26880;
constexpr int S4_STR = C4::RING, S4_CSR = S4_STR + 16384, S4_STF = S4_CSR + 8192, S4_CSF = S4_STF + 1024, S4_DUM = S4_CSF + 1024;
constexpr int LDS_BYTES = 163840, CODE_BYTES = 48 * 1024;
static_assert(S4_DUM + 256 <= SYNCW && S1_KV + 8448 <= R1 && R1 + C1::RING <= SYNCW && R3 + C3::RING <= SYNCW && C5::RING <= S4_STR, "lds");

DI void kv_sum(const Sync& y, Gate& G, lds_ptr smem, int head, int h, int t, bool poller, bool doz, int lane) {
  u32x4 v[16], zz[4];
  unsigned have = 0;
  f32x2 s2[4] = {{0.f, 0.f}, {0.f, 0.f}, {0.f, 0.f}, {0.f, 0.f}};
#pragma unroll 1
  for (int round = 0; round < 3; ++round) {
    gate_slow(y, G, 0u, poller, lane, round == 0 ? 0 : round == 1 ? 13 : 16);
    const unsigned fresh = G.mask & ~have;
    have = G.mask;
#pragma unroll
    for (int r = 0; r < 16; ++r) if ((fresh >> r) & 1u) v[r] = ld16(y.r, t * 16, WS_KVP + (head * 16 + r) * 4096);
    if (round == 2 && doz) {
#pragma unroll
      for (int i = 0; i < 4; ++i) zz[i] = ld16(y.r, lane * 16, WS_ZP + head * 4096 + i * 1024);
    }
  }
#pragma unroll
  for (int r = 0; r < 16; ++r)
#pragma unroll
    for (int j = 0; j < 4; ++j) s2[j] += (f32x2){bflo(v[r][j]), bfhi(v[r][j])};
  const float s[8] = {s2[0][0], s2[0][1], s2[1][0], s2[1][1], s2[2][0], s2[2][1], s2[3][0], s2[3][1]};
  *(LAS u32x4*)(smem + S1_KV + h * 4096 + t * 16) = (u32x4){pk2(s[0], s[1]), pk2(s[2], s[3]), pk2(s[4], s[5]), pk2(s[6], s[7])};
  if (doz) {
    f32x4 z = (__builtin_bit_cast(f32x4, zz[0]) + __builtin_bit_cast(f32x4, zz[1])) + (__builtin_bit_cast(f32x4, zz[2]) + __builtin_bit_cast(f32x4, zz[3]));
#pragma unroll
    for (int j = 0; j < 4; ++j) z[j] = xsum32(xsum16(z[j]));
    if (lane < 16) *(LAS u32x2*)(smem + S1_KV + 8192 + h * 128 + lane * 8) = (u32x2){pk2(z[0], z[1]), pk2(z[2], z[3])};
  }
}

__global__ __launch_bounds__(512, 2) void fused(Params P) {
  extern __shared__ __attribute__((aligned(16))) char smem_[];
  lds_ptr smem = (lds_ptr)smem_;
  const unsigned lds0 = (unsigned)(size_t)smem;
  const int wid = __builtin_amdgcn_readfirstlane(threadIdx.x >> 6), lane = threadIdx.x & 63, bid = blockIdx.x;
  int tm1, tn1, c1, tm3, tn3, c3;
  tile_of<4, 8, 4>(bid, tm1, tn1, c1);
  tile_of<2, 4, 8>(bid, tm3, tn3, c3);
  const int kh = bid & 1, tm5 = ((bid & 7) >> 1) * 4 + ((bid >> 3) & 3), tn5 = bid >> 5, c5 = ((bid >> 3) + tn5) & 3;
  const int par = tn1 & 1, base7 = 7 * (tn1 >> 1), ch0 = base7 + 4 * par;
  const int g = lane >> 4, m16 = lane & 15;
  Sync y;
  y.r = __builtin_amdgcn_make_buffer_rsrc(P.ws, 0, WS_END, 0x00020000);
  y.w = (LAS unsigned*)(smem + SYNCW);
  if (wid == 0) TS(0);

  if (wid >= 4) {
    const int lw = wid - 4, lt = threadIdx.x - 256;
    const auto nop = [](int) {};
    {
      Ld<C1> L; L.A = P.x + (size_t)tm1 * 128 * 2048; L.W = P.wqkv + tn1 * 128; L.lw = lw; L.c = c1; L.kt0 = 0; L.init(y, lane, 2048, 0, 0, 0);
      const unsigned frec = (FL_P0 * 256 + (lane < 16 ? tm1 * 16 + lane : lane < 28 ? ((lane - 16) / 3) * 48 + ((lane - 16) % 3) * 16 + tn1 : tn1)) * 128;
      if (lw == 0) dma4<true>(0u * lane, P.ws + WS_CTL, lds0 + S1_FLG);
      __builtin_amdgcn_s_setprio(3);
      load_all<C1, false, 1u << 11, 0x4905u>(L, lds0 + R1,
        [&](int s) {
          if (lw == 0) {
            const u32x4 v = *(volatile LAS u32x4*)(smem + S1_CSR + lane * 16);
            if (!__all(v[0] == y.tag && v[1] == y.tag && v[2] == y.tag && v[3] == y.tag)) {
#pragma unroll 1
              for (int k = 0; k < 4; ++k) poll(y, frec + 4 * k, 64, lane);
            }
          }
        },
        [&](int s) {
          if (s == 0) y.tag = __builtin_amdgcn_readfirstlane(*(volatile LAS unsigned*)(smem + S1_FLG)) + 1u;
          if (s == 2) dma<false>(lane * 16, P.wphi + tn1 * 4096 + lw * 1024, lds0 + S1_WP + lw * 1024);
          if (s == 8 && lw == 0) dma<true>(frec, P.ws + WS_FLG, lds0 + S1_CSR);
          if (s == 11) {
            if (lw == 3) dma<true>(lane * 16, P.ws + WS_ST1 + tm1 * 1024, lds0 + S1_ST);
#pragma unroll
            for (int i = 0; i < 3; ++i)
              dma<true>((lane >> 4) * 12288 + (lane & 15) * 16, P.ws + WS_CSQ + lw * 49152 + i * 4096 + tn1 * 256, lds0 + S1_CSR + i * 4096 + lw * 1024);
          }
          if (s == 14) {
            if (lt < 192) {
              const LAS float* rawp = (const LAS float*)(smem + S1_CSR) + (lt >> 6) * 1024 + (lt & 63);
              float t = 0.f;
#pragma unroll
              for (int i = 0; i < 16; ++i) t += rawp[i * 64];
              ((LAS float*)(smem + S1_CSF))[lt] = t;
            }
            wait_lgkm0();
          }
        }, DBG(0));
    }
    if (wid == 4) TS(16);
    {
      Ld<C3> L; L.A = P.ws + WS_O + (size_t)tm3 * 16 * 16384; L.W = P.wo + tn3 * 128; L.lw = lw; L.c = c3; L.kt0 = 0; L.init(y, lane, 0, 0, FL_O * 256 + tm3 * 16 + m16, 9);
      load_preW<C3>(L, lds0 + R3);
      if (lw >= 2 && bid < 224) {
        u32x4 v[16];
        colsum_load<16>(v, P.w1 + (size_t)((bid / 56) * 256 + (lw - 2) * 128) * (FF * 2) + (bid % 56) * 128, FF * 2, lane);
        colsum_finish<16>(v, y.r, WS_CS1 + (((bid / 56) * 2 + lw - 2) * FF + (bid % 56) * 64) * 4, lane);
      }
      publish(y, FL_CS, bid, lane);
      lds_wait(y, 24 + FL_KV, 4u);
      Gate G; G.off = (FL_KV * 256 + m16 * 16 + tn1) * 128; G.mask = 0; G.word = 8;
      kv_sum(y, G, smem, 2 * tn1 + 1, 1, lt, lw == 0, lw == 1, lane);
      if (wid == 4) TS(17);
      wait_lgkm0(); bar();
      if (wid == 4) TS(18);
      load_all<C3, true, 0u, 0u>(L, lds0 + R3, nop, nop, DBG(1));
    }
    if (wid == 4) TS(19);
    {
      Ld<C4> L; L.A = P.ws + WS_H + (size_t)tm1 * 16 * 16384; L.W = P.w1 + ch0 * 128; L.lw = lw; L.c = c1; L.kt0 = 0;
      L.init(y, lane, 128, par ? -64 : 384, (lane & 16) ? FL_CS * 256 + (m16 >> 2) * 56 + base7 + 3 * par + (lane & 3) : FL_H * 256 + tm1 * 16 + m16, 10);
      lds_wait(y, 24 + FL_H, 4u);
      load_preW<C4>(L, lds0 + R4);
      if (wid == 4) TS(20);
      load_all<C4, true, 0u, 0xa000u>(L, lds0 + R4, nop,
        [&](int s) {
          if (s == 13) {
            gate(y, L.G, ~0u, lw == 0, lane);
#pragma unroll
            for (int i = 0; i < 4; ++i) dma<true>(lane * 16, P.ws + WS_ST2 + (tm1 * 16 + lw * 4 + i) * 1024, lds0 + S4_STR + (lw * 4 + i) * 1024);
#pragma unroll
            for (int i = 0; i < 2; ++i) dma<true>(lane * 16, P.ws + WS_CS1 + ((lw * 2 + i) * FF + (base7 + 3 * par) * 64) * 4, lds0 + S4_CSR + (lw * 2 + i) * 1024);
          }
          if (s == 15) {
            const LAS float* rawp = (const LAS float*)(smem + S4_CSR) + lt;
            float t = 0.f;
#pragma unroll
            for (int i = 0; i < 8; ++i) t += rawp[i * 256];
            ((LAS float*)(smem + S4_CSF))[lt] = t;
            wait_lgkm0();
          }
          if (s == 15 && lw < 2) {
            f32x2 st[16]; float mu = 0.f, m2 = 0.f;
#pragma unroll
            for (int i = 0; i < 16; ++i) { st[i] = *(const LAS f32x2*)(smem + S4_STR + i * 1024 + lt * 8); mu += st[i][0]; }
            mu *= (1.f / 16.f);
#pragma unroll
            for (int i = 0; i < 16; ++i) { const float d = st[i][0] - mu; m2 += st[i][1] + 64.f * d * d; }
            *(LAS f32x2*)(smem + S4_STF + lt * 8) = (f32x2){mu, __builtin_amdgcn_rsqf(m2 * (1.f / D) + 1e-5f)};
            wait_lgkm0();
          }
        }, DBG(2));
    }
    if (wid == 4) TS(21);
    {
      Ld<C5> L; L.A = P.ws + WS_F + (size_t)tm5 * 56 * 16384; L.W = P.w2 + tn5 * 256; L.lw = lw; L.c = c5; L.kt0 = 28 * kh; L.init(y, lane, 128, 0, FL_F * 256 + tm5 * 16 + 8 * kh + (lane & 7), 11);
      lds_wait(y, 24 + FL_F, 4u);
      load_preW<C5>(L, lds0 + R5);
      if (wid == 4) TS(22);
      load_all<C5, true, 0u, 0u>(L, lds0 + R5, nop, nop, DBG(3));
    }
    if (wid == 4) { TS(23); TS_FLUSH(); }
    return;
  }

  const int wm = wid >> 1, wn = wid & 1;
  if (wid == 0 && lane < 32) y.w[lane] = 0u;
  if (wid == 3 && (bid >> 3) * 8192 + lane * 128 < CODE_BYTES) {
    const char* pc; asm volatile("s_getpc_b64 %0" : "=s"(pc));
    dma4<false>((bid >> 3) * 8192 + lane * 128, pc, lds0 + S1_DUM);
  }
  if (wid == 2) {
    const int i1 = tm1 * 16 + tn1, i3 = tm3 * 16 + tn3, ph = lane & 7;
    const int idx = (ph == FL_P0 || ph == FL_CS) ? bid : ph == FL_H ? i3 : ph >= FL_X ? (tm5 * 8 + tn5) * 2 + kh : i1;
    dma4<false>(lane < 8 ? WS_FLG + (ph * 256 + idx) * 128 : lane < 16 ? WS_ST2 + i3 * 1024 + ph * 128 : WS_ZP + ((2 * tn1 + (lane & 1)) * 16 + tm1) * 256 + (lane & 2) * 64, P.ws, lds0 + S1_DUM);
  }
  {
    f32x4 acc[24];
    Adr ad;
    ad.v[0] = lds0 + R1 + (wm * 64 + m16) * 128 + ((g ^ (m16 >> 1)) << 4); ad.v[1] = lds0 + R1 + (wm * 64 + m16) * 128 + ((g ^ (m16 >> 1) ^ 4) << 4);
    ad.v[2] = lds0 + R1 + 16384 + wlane(lane, 2 * wn); ad.v[3] = lds0 + R1 + 16384 + wlane(lane, 2 * wn + 1); ad.v[4] = 0; ad.v[5] = 0;
    u32x4 px[4], pw[8];
    const int prow = bid * 8 + wid * 2 + (lane >> 5);
    consume<C1, 1, RPG1, 4, 6>(acc, ad, DBG4, [&](int s) {
      if (s < 0) {
        if (wid == 0) TS(1);
        y.tag = __builtin_amdgcn_readfirstlane(*(volatile LAS unsigned*)(smem + S1_FLG)) + 1u;
#pragma unroll
        for (int i = 0; i < 4; ++i) px[i] = *(const u32x4*)(P.x + (size_t)prow * 2048 + ((lane & 31) + 32 * i) * 16);
        colsum_load<8>(pw, P.wqkv + (size_t)((min(bid, 191) / 48) * 256 + wid * 64) * (3 * D * 2) + (bid % 48) * 128, 3 * D * 2, lane);
      }
      if (s == 1) {
#pragma unroll
        for (int i = 0; i < 4; ++i) asm volatile("" : "+v"(px[i]));
#pragma unroll
        for (int i = 0; i < 8; ++i) asm volatile("" : "+v"(pw[i]));
        const unsigned one2 = opaque(0x3f803f80u);
        float sm = 0.f, sq = 0.f;
#pragma unroll
        for (int i = 0; i < 4; ++i)
#pragma unroll
          for (int j = 0; j < 4; ++j) { sm = dot2(px[i][j], one2, sm); sq = dot2(px[i][j], px[i][j], sq); }
        const float mean = xsum16(row16_sum(sm)) * (1.f / D);
        const float var = fmaxf(xsum16(row16_sum(sq)) * (1.f / D) - mean * mean, 0.f);
        if ((lane & 31) == 0) st8(y.r, (u32x2){bu(mean), bu(__builtin_amdgcn_rsqf(var + 1e-5f))}, prow * 8, WS_ST1);
        if (bid < 192) colsum_finish<8>(pw, y.r, WS_CSQ + (((bid / 48) * 4 + wid) * 3 * D + (bid % 48) * 64) * 4, lane);
      }
      if (s == 2) { wait_vm<0>(); if (lane == 0) st4(y.r, y.tag, 0, WS_FLG + (FL_P0 * 256 + bid) * 128 + wid * 4); }
    });
    if (wid == 0) TS(2);
    const int head = 2 * tn1 + wn;
    f32x4 cs[4]; float csv[2];
#pragma unroll
    for (int n = 0; n < 4; ++n) cs[n] = *(const LAS f32x4*)(smem + S1_CSF + ((n >> 1) * 64 + wn * 32 + (n & 1) * 16 + 4 * g) * 4);
#pragma unroll
    for (int n = 0; n < 2; ++n) csv[n] = *(const LAS float*)(smem + S1_CSF + (128 + wn * 32 + n * 16 + m16) * 4);
    u32x4 wp[2];
#pragma unroll
    for (int F = 0; F < 2; ++F) { lds_ptr a0 = smem + S1_WP + wn * 2048 + (4 * g + (m16 >> 2)) * 64 + F * 32 + (lane & 3) * 8; wp[F] = lds_tr2(a0, a0 + 1024); }
    u32x4 qb[4], vb[2][2];
    f32x4 p[4][2];
    const f32x4 zero = {0.f, 0.f, 0.f, 0.f};
    {
      f32x4 vv[4][2];
#pragma unroll
      for (int m = 0; m < 4; ++m) {
        const f32x2 st = *(const LAS f32x2*)(smem + S1_ST + (wm * 64 + m * 16 + m16) * 8);
        const f32x4 s0 = *(const LAS f32x4*)(smem + S1_ST + (wm * 64 + m * 16 + 4 * g) * 8), s1 = *(const LAS f32x4*)(smem + S1_ST + (wm * 64 + m * 16 + 4 * g) * 8 + 16);
        const f32x4 mu = {s0[0], s0[2], s1[0], s1[2]}, rs = {s0[1], s0[3], s1[1], s1[3]};
        qb[m] = pack8((acc[m * 6] - cs[0] * st[0]) * st[1], (acc[m * 6 + 1] - cs[1] * st[0]) * st[1]);
        const u32x4 kb = pack8((acc[m * 6 + 2] - cs[2] * st[0]) * st[1], (acc[m * 6 + 3] - cs[3] * st[0]) * st[1]);
        p[m][0] = mfma(wp[0], kb, zero); p[m][1] = mfma(wp[1], kb, zero);
        vv[m][0] = (acc[m * 6 + 4] - mu * csv[0]) * rs; vv[m][1] = (acc[m * 6 + 5] - mu * csv[1]) * rs;
      }
#pragma unroll
      for (int u = 0; u < 2; ++u) { vb[u][0] = pack8(vv[2 * u][0], vv[2 * u + 1][0]); vb[u][1] = pack8(vv[2 * u][1], vv[2 * u + 1][1]); }
    }
    u32x4 pp[4], pn[4];
    if (wid == 0) TS(26);
    dual_softmax4(p, pp, pn);
    if (wid == 0) TS(27);
    {
      lds_ptr own = smem + R3 + wid * C3::STAGE, oth = smem + R3 + (wid ^ 2) * C3::STAGE;
#pragma unroll
      for (int m = 0; m < 4; ++m) {
        lds_ptr w = own + (16 * m + m16) * 136 + 8 * g;
        *(LAS u32x2*)(w) = (u32x2){pp[m][0], pp[m][1]}; *(LAS u32x2*)(w + 32) = (u32x2){pp[m][2], pp[m][3]};
        *(LAS u32x2*)(w + 64) = (u32x2){pn[m][0], pn[m][1]}; *(LAS u32x2*)(w + 96) = (u32x2){pn[m][2], pn[m][3]};
      }
#pragma unroll
      for (int i = 0; i < 4; ++i) *(LAS u32x4*)(own + 8704 + i * 1024 + lane * 16) = vb[i >> 1][i & 1];
      wait_lgkm0();
      volatile LAS unsigned* xf = y.w + 16;
      xf[wid] = 1u;
      f32x4 kv[2][2] = {{zero, zero}, {zero, zero}}, z[2] = {zero, zero};
      const u32x4 ones = {0x3f803f80u, 0x3f803f80u, 0x3f803f80u, 0x3f803f80u};
      const unsigned ro = (4 * g + (m16 >> 2)) * 136 + wm * 64 + (lane & 3) * 8;
#pragma unroll
      for (int u = 0; u < 2; ++u)
#pragma unroll
        for (int F = 0; F < 2; ++F) {
          const u32x4 pa = lds_tr2(own + ro + u * 32 * 136 + F * 32, own + ro + u * 32 * 136 + F * 32 + 16 * 136);
          kv[F][0] = mfma(pa, vb[u][0], kv[F][0]); kv[F][1] = mfma(pa, vb[u][1], kv[F][1]); z[F] = mfma(pa, ones, z[F]);
        }
      while (xf[wid ^ 2] == 0u) __builtin_amdgcn_s_sleep(0);
      asm volatile("" ::: "memory");
#pragma unroll
      for (int u = 0; u < 2; ++u) {
        const u32x4 v0 = lds128(oth + 8704 + u * 2048 + lane * 16), v1 = lds128(oth + 8704 + u * 2048 + 1024 + lane * 16);
#pragma unroll
        for (int F = 0; F < 2; ++F) {
          const u32x4 pa = lds_tr2(oth + ro + u * 32 * 136 + F * 32, oth + ro + u * 32 * 136 + F * 32 + 16 * 136);
          kv[F][0] = mfma(pa, v0, kv[F][0]); kv[F][1] = mfma(pa, v1, kv[F][1]); z[F] = mfma(pa, ones, z[F]);
        }
      }
      const unsigned kd = WS_KVP + (head * 16 + tm1) * 4096 + wm * 2048;
      st16(y.r, pack8(kv[0][0], kv[1][0]), lane * 16, kd);
      st16(y.r, pack8(kv[0][1], kv[1][1]), lane * 16, kd + 1024);
      if (m16 == 0) {
        st16(y.r, __builtin_bit_cast(u32x4, z[0]), g * 32, WS_ZP + (head * 16 + tm1) * 256 + wm * 128);
        st16(y.r, __builtin_bit_cast(u32x4, z[1]), g * 32 + 16, WS_ZP + (head * 16 + tm1) * 256 + wm * 128);
      }
      if (wid == 0) TS(25);
      publish(y, FL_KV, tm1 * 16 + tn1, lane);
    }
    if (wid == 0) TS(3);
#pragma unroll
    for (int m = 0; m < 4; ++m) { p[m][0] = mfma(wp[0], qb[m], zero); p[m][1] = mfma(wp[1], qb[m], zero); }
    dual_softmax4(p, pp, pn);
    if (wid == 0) TS(4);
    { Gate G; G.off = 0; G.mask = 0; G.word = 8; kv_sum(y, G, smem, 2 * tn1, 0, threadIdx.x, false, wid == 0, lane); }
    wait_lgkm0(); bar();
    if (wid == 0) TS(5);
    u32x4 ka[2][2], za[2];
#pragma unroll
    for (int ks = 0; ks < 2; ++ks) {
      ka[ks][0] = lds128(smem + S1_KV + wn * 4096 + ks * 2048 + lane * 16); ka[ks][1] = lds128(smem + S1_KV + wn * 4096 + ks * 2048 + 1024 + lane * 16);
      za[ks] = lds128(smem + S1_KV + 8192 + wn * 128 + ks * 64 + g * 16);
    }
#pragma unroll
    for (int m = 0; m < 4; ++m) {
      f32x4 n0 = mfma(ka[0][0], pp[m], zero), n1 = mfma(ka[0][1], pp[m], zero), dn = mfma(za[0], pp[m], zero);
      n0 = mfma(ka[1][0], pn[m], n0); n1 = mfma(ka[1][1], pn[m], n1); dn = mfma(za[1], pn[m], dn);
      const float rd = __builtin_amdgcn_rcpf(dn[0] + 1e-6f);
      st16(y.r, pack8(n0 * rd, n1 * rd), lane * 16, WS_O + (((tm1 * 16 + tn1) * 8 + wm * 4 + m) * 2 + wn) * 1024);
    }
    publish(y, FL_O, tm1 * 16 + tn1, lane);
    if (wid == 0) TS(6);
  }
  {
    u32x4 xr[2][2];
#pragma unroll
    for (int m = 0; m < 2; ++m)
#pragma unroll
      for (int Q = 0; Q < 2; ++Q)
        xr[m][Q] = *(const u32x4*)(P.x + (size_t)(tm3 * 128 + wid * 32 + m * 16 + m16) * 2048 + tn3 * 128 + Q * 64 + (g & 1) * 32 + (g >> 1) * 16);
    f32x4 acc[8];
    Adr ad; ad.v[0] = lds0 + R3 + wid * 4096 + lane * 16; ad.v[1] = 0;
#pragma unroll
    for (int n = 0; n < 4; ++n) ad.v[2 + n] = lds0 + R3 + 16384 + wlane(lane, n);
    consume_s<C3, 3, 2>(acc, ad, [&](int) { if (wid == 0) TS(7); });
    if (wid == 0) TS(8);
#pragma unroll
    for (int m = 0; m < 2; ++m) {
      float hv[16];
#pragma unroll
      for (int Q = 0; Q < 2; ++Q) {
        auto r0 = __builtin_amdgcn_permlane16_swap(xr[m][Q][0], xr[m][Q][2], false, false);
        auto r1 = __builtin_amdgcn_permlane16_swap(xr[m][Q][1], xr[m][Q][3], false, false);
        const f32x4 a = acc[m * 4 + 2 * Q], b = acc[m * 4 + 2 * Q + 1];
        const u32x4 hp = {pk2(a[0] + bflo(r0[0]), a[1] + bfhi(r0[0])), pk2(a[2] + bflo(r1[0]), a[3] + bfhi(r1[0])),
                          pk2(b[0] + bflo(r0[1]), b[1] + bfhi(r0[1])), pk2(b[2] + bflo(r1[1]), b[3] + bfhi(r1[1]))};
        st16(y.r, hp, lane * 16, WS_H + (((tm3 * 16 + tn3) * 8 + wid * 2 + m) * 2 + Q) * 1024);
#pragma unroll
        for (int j = 0; j < 4; ++j) { hv[Q * 8 + 2 * j] = bflo(hp[j]); hv[Q * 8 + 2 * j + 1] = bfhi(hp[j]); }
      }
      float s = 0.f;
#pragma unroll
      for (int j = 0; j < 16; ++j) s += hv[j];
      s = xsum32(xsum16(s)) * (1.f / 64.f);
      float q = 0.f;
#pragma unroll
      for (int j = 0; j < 16; ++j) { const float d = hv[j] - s; q += d * d; }
      q = xsum32(xsum16(q));
      if (g == 0) st8(y.r, (u32x2){bu(s), bu(q)}, m16 * 8, WS_ST2 + (tm3 * 16 + tn3) * 1024 + (wid * 32 + m * 16) * 8);
    }
    if (wid == 0) TS(29);
    publish(y, FL_H, tm3 * 16 + tn3, lane);
    if (wid == 0) TS(9);
  }
  {
    f32x4 acc[28];
    Adr ad;
    ad.v[0] = lds0 + R4 + wm * 8192 + wn * 4096 + lane * 16; ad.v[1] = lds0 + R4 + wm * 8192 + (wn ^ 1) * 4096 + lane * 16;
    ad.v[2] = lds0 + R4 + 16384 + wlane(lane, wn); ad.v[3] = lds0 + R4 + 16384 + wlane(lane, 2 + wn);
    ad.v[4] = lds0 + R4 + 16384 + 6144 + hlane(lane, 0); ad.v[5] = lds0 + R4 + 16384 + 6144 + hlane(lane, 1);
    consume<C4, 4, RPG4, 0, 0>(acc, ad, DBG4, [&](int s) { if (s < 0 && wid == 0) TS(10); });
    if (wid == 0) TS(11);
    f32x4 cs[8];
#pragma unroll
    for (int n = 0; n < 6; ++n) cs[n] = *(const LAS f32x4*)(smem + S4_CSF + ((par + (n >> 1)) * 64 + (2 * (n & 1) + wn) * 16 + 4 * g) * 4);
#pragma unroll
    for (int n = 0; n < 2; ++n) cs[6 + n] = *(const LAS f32x4*)(smem + S4_CSF + ((par ? 32 : 192) + n * 16 + 4 * g) * 4);
    const f32x4 zero = {0.f, 0.f, 0.f, 0.f};
#pragma unroll
    for (int m = 0; m < 4; ++m) {
      const int mt = wm * 4 + ((m + 2 * wn) & 3);
      const f32x2 st = *(const LAS f32x2*)(smem + S4_STF + (mt * 16 + m16) * 8);
      const unsigned fd = WS_F + (tm1 * 56 * 16 + mt * 2) * 1024;
#pragma unroll
      for (int i = 0; i < 3; ++i)
        st16(y.r, pack8(__builtin_elementwise_max((acc[m * 6 + 2 * i] - cs[2 * i] * st[0]) * st[1], zero),
                        __builtin_elementwise_max((acc[m * 6 + 2 * i + 1] - cs[2 * i + 1] * st[0]) * st[1], zero)), lane * 16, fd + (ch0 + i) * 16384 + wn * 1024);
      if (m < 2)
        st16(y.r, pack8(__builtin_elementwise_max((acc[24 + 2 * m] - cs[6] * st[0]) * st[1], zero),
                        __builtin_elementwise_max((acc[24 + 2 * m + 1] - cs[7] * st[0]) * st[1], zero)), lane * 16, fd + (base7 + 3) * 16384 + par * 1024);
    }
    if (wid == 0) TS(28);
    publish(y, FL_F, tm1 * 16 + tn1, lane);
    if (wid == 0) TS(12);
  }
  {
    f32x4 acc[16];
    Adr ad;
    ad.v[0] = lds0 + R5 + wm * 8192 + kh * 4096 + lane * 16; ad.v[1] = lds0 + R5 + wm * 8192 + (kh ^ 1) * 4096 + lane * 16;
#pragma unroll
    for (int n = 0; n < 4; ++n) ad.v[2 + n] = lds0 + R5 + 16384 + wn * 2048 + wlane(lane, n);
    u32x4 hr[2][2];
    consume_s<C5, 5, 1>(acc, ad, [&](int s) {
      if (s < 0) {
        if (wid == 0) TS(13);
#pragma unroll
        for (int m = 0; m < 2; ++m)
#pragma unroll
          for (int Q = 0; Q < 2; ++Q) hr[m][Q] = ld16(y.r, lane * 16, WS_H + (((tm5 * 16 + 2 * tn5 + wn) * 8 + wm * 4 + 2 * kh + m) * 2 + Q) * 1024);
      }
    });
    if (wid == 0) TS(14);
    const int tile = tm5 * 8 + tn5;
#pragma unroll
    for (int i = 0; i < 4; ++i) st16(y.r, pack8(acc[8 + 2 * i], acc[9 + 2 * i]), lane * 16, WS_O + (((tile * 2 + kh) * 4 + wid) * 4 + i) * 1024);
    wait_vm<0>();
    if (lane == 0) st4(y.r, y.tag, 0, WS_FLG + ((FL_X + (wid >> 1)) * 256 + tile * 2 + kh) * 128 + (wid & 1) * 64);
    poll(y, ((FL_X + (wid >> 1)) * 256 + tile * 2 + (kh ^ 1)) * 128 + (wid & 1) * 64, 1, lane);
    if (wid == 0) TS(15);
    u32x4 xv[4];
#pragma unroll
    for (int i = 0; i < 4; ++i) xv[i] = ld16(y.r, lane * 16, WS_O + (((tile * 2 + (kh ^ 1)) * 4 + wid) * 4 + i) * 1024);
#pragma unroll
    for (int m = 0; m < 2; ++m)
#pragma unroll
      for (int Q = 0; Q < 2; ++Q) {
        const u32x4 xx = xv[m * 2 + Q];
        const f32x4 a = acc[m * 4 + 2 * Q] + (f32x4){bflo(xx[0]), bfhi(xx[0]), bflo(xx[1]), bfhi(xx[1])}, b = acc[m * 4 + 2 * Q + 1] + (f32x4){bflo(xx[2]), bfhi(xx[2]), bflo(xx[3]), bfhi(xx[3])};
        const u32x4 hh = hr[m][Q];
        const unsigned a0 = pk2(a[0] + bflo(hh[0]), a[1] + bfhi(hh[0])), a1 = pk2(a[2] + bflo(hh[1]), a[3] + bfhi(hh[1]));
        const unsigned b0 = pk2(b[0] + bflo(hh[2]), b[1] + bfhi(hh[2])), b1 = pk2(b[2] + bflo(hh[3]), b[3] + bfhi(hh[3]));
        auto r0 = __builtin_amdgcn_permlane16_swap(a0, b0, false, false);
        auto r1 = __builtin_amdgcn_permlane16_swap(a1, b1, false, false);
        *(u32x4*)(P.out + (size_t)(tm5 * 128 + wm * 64 + (2 * kh + m) * 16 + m16) * 2048 + tn5 * 256 + wn * 128 + Q * 64 + (g & 1) * 32 + (g >> 1) * 16) = (u32x4){r0[0], r1[0], r0[1], r1[1]};
      }
    if (bid == 0 && threadIdx.x == 0) st4(y.r, y.tag, 0, WS_CTL);
    if (wid == 0) { TS(24); TS_FLUSH(); }
  }
}

extern "C" void kernel_launch(void* const* d_in, const int* in_sizes, int n_in, void* d_out, int out_size, void* d_ws, size_t ws_size,
                              hipStream_t stream) {
  Params p;
  p.x = (const char*)d_in[0]; p.wqkv = (const char*)d_in[1]; p.wphi = (const char*)d_in[2]; p.wo = (const char*)d_in[3];
  p.w1 = (const char*)d_in[5]; p.w2 = (const char*)d_in[6]; p.ws = (char*)d_ws; p.out = (char*)d_out;
  (void)hipFuncSetAttribute((const void*)fused, hipFuncAttributeMaxDynamicSharedMemorySize, LDS_BYTES);
  fused<<<256, 512, LDS_BYTES, stream>>>(p);
}
```

```cpp
#include <hip/hip_runtime.h>
#include <hip/hip_bf16.h>

#define LAS __attribute__((address_space(3)))
#define DI __device__ __forceinline__
typedef __bf16 bf16x8 __attribute__((ext_vector_type(8)));
typedef __bf16 bf16x2 __attribute__((ext_vector_type(2)));
typedef short s16x4 __attribute__((ext_vector_type(4)));
typedef float f32x2 __attribute__((ext_vector_type(2)));
typedef float f32x4 __attribute__((ext_vector_type(4)));
typedef unsigned u32x2 __attribute__((ext_vector_type(2)));
typedef unsigned u32x4 __attribute__((ext_vector_type(4)));
typedef LAS char* lds_ptr;
typedef __amdgpu_buffer_rsrc_t rsrc_t;

constexpr int D = 1024, FF = 3584;
constexpr unsigned WS_CTL = 0;
constexpr unsigned WS_FLG = 4096;
constexpr unsigned WS_ST1 = 320u << 10;
constexpr unsigned WS_CSQ = 384u << 10;
constexpr unsigned WS_CS1 = 576u << 10;
constexpr unsigned WS_ZP = 704u << 10;
constexpr unsigned WS_ST2 = 832u << 10;
constexpr unsigned WS_SMALL = 1280u << 10;
constexpr unsigned WS_KVP = 2u << 20;
constexpr unsigned WS_O = 4u << 20;
constexpr unsigned WS_H = 8u << 20;
constexpr unsigned WS_F = 12u << 20;
constexpr unsigned WS_END = 26u << 20;
enum { FL_P0 = 0, FL_KV = 1, FL_O = 2, FL_H = 3, FL_F = 4, FL_CS = 5, FL_X = 6   };

#define TS(I) do {} while (0)
#define TS_FLUSH() do {} while (0)

#define DBG(k) nullptr
#define CLK(v)
#define DBG4 nullptr
struct Params { const char* x; const char* wqkv; const char* wphi; const char* wo; const char* w1; const char* w2; char* ws; char* out; };

DI unsigned pk2(float a, float b) { f32x2 v = {a, b}; return __builtin_bit_cast(unsigned, __builtin_convertvector(v, bf16x2)); }
DI float bflo(unsigned u) { return __uint_as_float(u << 16); }
DI float bfhi(unsigned u) { return __uint_as_float(u & 0xffff0000u); }
DI u32x4 pack8(const f32x4& a, const f32x4& b) { return (u32x4){pk2(a[0], a[1]), pk2(a[2], a[3]), pk2(b[0], b[1]), pk2(b[2], b[3])}; }
DI bf16x8 asbf(const u32x4& v) { return __builtin_bit_cast(bf16x8, v); }
#define SB() __builtin_amdgcn_sched_barrier(0)
DI void bar() { asm volatile("" ::: "memory"); __builtin_amdgcn_s_barrier(); asm volatile("" ::: "memory"); }
DI void wait_lgkm0() { __builtin_amdgcn_s_waitcnt(0xC07F); asm volatile("" ::: "memory"); }
template <int N> DI void wait_vm() { asm volatile("s_waitcnt vmcnt(%0)" ::"i"(N) : "memory"); }
template <bool SC1> DI void dma(unsigned voff, const char* sbase, unsigned lds) {
  if (SC1) asm volatile("s_mov_b32 m0, %2\n\ts_nop 3\n\tglobal_load_lds_dwordx4 %0, %1 sc1" ::"v"(voff), "s"(sbase), "s"(lds) : "memory");
  else asm volatile("s_mov_b32 m0, %2\n\ts_nop 3\n\tglobal_load_lds_dwordx4 %0, %1" ::"v"(voff), "s"(sbase), "s"(lds) : "memory");
}
template <bool SC1> DI void dma4(unsigned voff, const char* sbase, unsigned lds) {
  if (SC1) asm volatile("s_mov_b32 m0, %2\n\ts_nop 3\n\tglobal_load_lds_dword %0, %1 sc1" ::"v"(voff), "s"(sbase), "s"(lds) : "memory");
  else asm volatile("s_mov_b32 m0, %2\n\ts_nop 3\n\tglobal_load_lds_dword %0, %1" ::"v"(voff), "s"(sbase), "s"(lds) : "memory");
}
DI u32x4 lds128(lds_ptr p) { return *(const LAS u32x4*)p; }
DI u32x4 lds_tr2(lds_ptr p0, lds_ptr p1) {
  s16x4 a = __builtin_amdgcn_ds_read_tr16_b64_v4i16((LAS s16x4*)p0);
  s16x4 b = __builtin_amdgcn_ds_read_tr16_b64_v4i16((LAS s16x4*)p1);
  return __builtin_bit_cast(u32x4, __builtin_shufflevector(a, b, 0, 1, 2, 3, 4, 5, 6, 7));
}
DI u32x2 lds_tr(lds_ptr p) { return __builtin_bit_cast(u32x2, __builtin_amdgcn_ds_read_tr16_b64_v4i16((LAS s16x4*)p)); }
DI f32x4 mfma(const u32x4& a, const u32x4& b, const f32x4& c) { return __builtin_amdgcn_mfma_f32_16x16x32_bf16(asbf(a), asbf(b), c, 0, 0, 0); }
DI void mfma_ip(f32x4& acc, const u32x4& a, const u32x4& b) { asm volatile("v_mfma_f32_16x16x32_bf16 %0, %1, %2, %0" : "+v"(acc) : "v"(a), "v"(b)); }
DI u32x4 ld16(rsrc_t r, unsigned voff, unsigned soff) { return __builtin_amdgcn_raw_buffer_load_b128(r, voff, soff, 16); }
DI void st16(rsrc_t r, const u32x4& v, unsigned voff, unsigned soff) { __builtin_amdgcn_raw_buffer_store_b128(v, r, voff + soff, 0, 16);
}
DI void st8(rsrc_t r, const u32x2& v, unsigned voff, unsigned soff) { __builtin_amdgcn_raw_buffer_store_b64(v, r, voff, soff, 16); }
DI void st4(rsrc_t r, unsigned v, unsigned voff, unsigned soff) { __builtin_amdgcn_raw_buffer_store_b32(v, r, voff, soff, 16); }
DI float fb(unsigned u) { return __uint_as_float(u); }
DI unsigned bu(float f) { return __float_as_uint(f); }
DI float xsum16(float x) { auto r = __builtin_amdgcn_permlane16_swap(bu(x), bu(x), false, false); return fb(r[0]) + fb(r[1]); }
DI float xsum32(float x) { auto r = __builtin_amdgcn_permlane32_swap(bu(x), bu(x), false, false); return fb(r[0]) + fb(r[1]); }
template <int CTRL> DI float dpp(float x) { return __builtin_bit_cast(float, __builtin_amdgcn_update_dpp(0, __builtin_bit_cast(int, x), CTRL, 0xf, 0xf, true)); }
DI float row16_sum(float x) { x += dpp<0xB1>(x); x += dpp<0x4E>(x); x += dpp<0x141>(x); x += dpp<0x140>(x); return x; }

struct Sync { rsrc_t r; unsigned tag; LAS unsigned* w; };
DI void publish(const Sync& y, int phase, int idx, int lane) {
  if (lane == 0) __hip_atomic_fetch_add(y.w + 24 + phase, 1u, __ATOMIC_RELAXED, __HIP_MEMORY_SCOPE_WORKGROUP);
  wait_vm<0>();
  if (lane == 0 && __hip_atomic_fetch_add(y.w + phase, 1u, __ATOMIC_RELAXED, __HIP_MEMORY_SCOPE_WORKGROUP) == 3u) st4(y.r, y.tag, 0, WS_FLG + (phase * 256 + idx) * 128);
}
DI void poll(const Sync& y, unsigned off, int n, int lane) {
  const unsigned long long t0 = __builtin_amdgcn_s_memrealtime();
  for (unsigned it = 0;; ++it) {
    const unsigned v = __builtin_amdgcn_raw_buffer_load_b32(y.r, WS_FLG + off, 0, 16);
    if (__all(lane >= n || v == y.tag)) break;
    if ((it & 63) == 63) {
      if (__builtin_amdgcn_readfirstlane(__builtin_amdgcn_raw_buffer_load_b32(y.r, 0, WS_CTL + 128, 16)) == y.tag) break;
      if (__builtin_amdgcn_s_memrealtime() - t0 > 4000000ull) { if (lane == 0) st4(y.r, y.tag, 0, WS_CTL + 128); break; }
    }
    __builtin_amdgcn_s_sleep(2);
  }
  asm volatile("" ::: "memory");
}
DI void lds_wait(const Sync& y, int word, unsigned v) {
  volatile LAS unsigned* w = y.w + word;
  while (*w < v) __builtin_amdgcn_s_sleep(1);
  asm volatile("" ::: "memory");
}
struct Gate { unsigned off, mask; int word; };
DI void gate_slow(const Sync& y, Gate& G, unsigned need, bool poller, int lane, int cnt = 0) {
  volatile LAS unsigned* w = y.w + G.word;
  if (poller) {
    const unsigned long long t0 = __builtin_amdgcn_s_memrealtime();
    for (unsigned it = 0;; ++it) {
      G.mask = (unsigned)__ballot(__builtin_amdgcn_raw_buffer_load_b32(y.r, WS_FLG + G.off, 0, 16) == y.tag);
      if ((it & 63) == 63) {
        const bool late = __builtin_amdgcn_s_memrealtime() - t0 > 4000000ull;
        if (late && lane == 0) st4(y.r, y.tag, 0, WS_CTL + 128);
        if (late || __builtin_amdgcn_readfirstlane(__builtin_amdgcn_raw_buffer_load_b32(y.r, 0, WS_CTL + 128, 16)) == y.tag) G.mask = ~0u;
      }
      *w = G.mask;
      if ((G.mask & need) == need && __builtin_popcount(G.mask & 0xffffu) >= cnt) break;
      __builtin_amdgcn_s_sleep(1);
    }
  } else {
    for (;;) { G.mask = __builtin_amdgcn_readfirstlane(*w); if ((G.mask & need) == need && __builtin_popcount(G.mask & 0xffffu) >= cnt) break; __builtin_amdgcn_s_sleep(1); }
  }
  asm volatile("" ::: "memory");
}
DI void gate(const Sync& y, Gate& G, unsigned need, bool poller, int lane) { if (__builtin_expect((G.mask & need) != need, 0)) gate_slow(y, G, need, poller, lane); }

template <int NK> DI int kmap(int t, int c) {
  const int u = t - 1, q = u & 3, b4 = u & ~3, body = (int)(u < NK - 4);
  const int lead = ((int)(q == c) & body) | (int)(t == 0);
  const int non = b4 + ((c + 1 + q - ((int)(q > c) & body)) & 3);
  return non ^ (((b4 + 4 + c) ^ non) & -lead);
}
#define LAG3 1
#define LAG5 1
enum { ORD_NAT = 0, ORD_PAIR = 1, ORD_F = 2 };
template <int NSUB_, bool HALF_, int NSLOT_, int NK_, int ORD_, int WSTRIDE_, bool XROW_, int LAG_> struct Cfg {
  static constexpr int NSUB = NSUB_, NSLOT = NSLOT_, NK = NK_, ORD = ORD_, WSTRIDE = WSTRIDE_;
  static constexpr bool HALF = HALF_, XROW = XROW_;
  static constexpr int LAG = LAG_;
  static constexpr int STAGE = 16384 + NSUB * 8192 + (HALF ? 4096 : 0), PW = 4 + 2 * NSUB + (HALF ? 1 : 0), RING = STAGE * NSLOT;
  static constexpr int WB = NSUB * 2048 + (HALF ? 1024 : 0);
};
typedef Cfg<3, false, 3, 16, ORD_NAT, 3 * D * 2, true, 0> C1;
typedef Cfg<1, false, 5, 16, ORD_PAIR, D * 2, false, LAG3> C3;
typedef Cfg<3, true, 3, 16, ORD_PAIR, FF * 2, false, 0> C4;
typedef Cfg<2, false, 4, 28, ORD_F, D * 2, false, LAG5> C5;

#define GLDS "global_load_lds_dwordx4 "
template <class C> struct Ld {
  const char* A; const char* W; int lw, c, kt0; unsigned vA[4], vW[7];
  const char* sA; const char* sW; unsigned need;
  Gate G; const Sync* y; int lane_;
  static constexpr int C0 = C::WB > 4096 ? 4096 : 0;
  DI void init(const Sync& y_, int lane, int substep, int halfrel, unsigned goff, int gword) {
    y = &y_; lane_ = lane; G.off = goff * 128; G.mask = 0; G.word = gword;
#pragma unroll
    for (int i = 0; i < 4; ++i) vA[i] = C::XROW ? (lane >> 3) * 2048 + ((((lane & 7) ^ (lane >> 4)) << 4) ^ ((i & 1) * 64)) + i * (16384 - 1024) : lane * 16;
    const int rl = (C::ORD == ORD_NAT) ? 8 * (lane >> 5) + ((lane >> 3) & 3) : (lane >> 3);
    const unsigned w0 = rl * C::WSTRIDE + (((lane & 7) ^ (((lane >> 4) & 3) << 1)) << 4) + 4096;
#pragma unroll
    for (int q = 0; q < 2 * C::NSUB; ++q) vW[q] = w0 + (q & 1) * ((C::ORD == ORD_NAT ? 16 : 8) * C::WSTRIDE) + (q >> 1) * substep - (q * 1024 - C0);
    if (C::HALF) vW[6] = (lane >> 2) * C::WSTRIDE + (((lane & 3) ^ (((lane >> 4) & 1) << 1)) << 4) + 4096 + halfrel - (6144 - C0);
    A += C::XROW ? lw * 32 * 2048 : lw * 4096;
    W -= 4096;
  }
  DI void prep(int s) {
    const int kt = kt0 + kmap<C::NK>(s, c), kb = lw >> 1, t = lw & 1;
    int ur;
    if (C::ORD == ORD_NAT) ur = 32 * kb + 4 * t;
    else if (C::ORD == ORD_F) { const int sp = -(int)((0x10204081020408ull >> kt) & 1u); ur = (16 * lw & sp) | ((16 * kb + 32 * t) & ~sp); }
    else ur = 16 * lw;
    if (C::ORD == ORD_F) { const int jj = (kt * 37) >> 8, r = kt - 7 * jj; need = (unsigned)((int)(r <= 3) | ((int)(r >= 3) << 1)) << ((2 * jj) & 7); }
    else need = 1u << kt;
    sA = A + (unsigned)(kt * (C::XROW ? 128 : 16384));
    sW = W + (unsigned)((kt * 64 + ur) * C::WSTRIDE);
  }
  DI void issueA(unsigned base) {
    if (!C::XROW) gate(*y, G, need, lw == 0, lane_);
    const unsigned m = base + lw * 4096;
    if (C::XROW) asm volatile("s_mov_b32 m0, %5\n\ts_nop 3\n\t" GLDS "%0, %4\n\t" GLDS "%1, %4 offset:1024\n\t" GLDS "%2, %4 offset:2048\n\t" GLDS "%3, %4 offset:3072"
                              ::"v"(vA[0]), "v"(vA[1]), "v"(vA[2]), "v"(vA[3]), "s"(sA), "s"(m) : "memory");
    else asm volatile("s_mov_b32 m0, %2\n\ts_nop 3\n\t" GLDS "%0, %1 sc1\n\t" GLDS "%0, %1 offset:1024 sc1\n\t" GLDS "%0, %1 offset:2048 sc1\n\t" GLDS "%0, %1 offset:3072 sc1"
                      ::"v"(vA[0]), "s"(sA), "s"(m) : "memory");
  }
  DI void issueW(unsigned base) const {
    const unsigned m = base + 16384 + lw * C::WB + C0;
    if (C::NSUB == 1) asm volatile("s_mov_b32 m0, %3\n\ts_nop 3\n\t" GLDS "%0, %2\n\t" GLDS "%1, %2 offset:1024" ::"v"(vW[0]), "v"(vW[1]), "s"(sW), "s"(m) : "memory");
    if (C::NSUB == 2) asm volatile("s_mov_b32 m0, %5\n\ts_nop 3\n\t" GLDS "%0, %4\n\t" GLDS "%1, %4 offset:1024\n\t" GLDS "%2, %4 offset:2048\n\t" GLDS "%3, %4 offset:3072"
                                   ::"v"(vW[0]), "v"(vW[1]), "v"(vW[2]), "v"(vW[3]), "s"(sW), "s"(m) : "memory");
    if (C::NSUB == 3) asm volatile("s_mov_b32 m0, %7\n\ts_nop 3\n\t" GLDS "%0, %6 offset:-4096\n\t" GLDS "%1, %6 offset:-3072\n\t" GLDS "%2, %6 offset:-2048\n\t" GLDS "%3, %6 offset:-1024\n\t"
                                   GLDS "%4, %6\n\t" GLDS "%5, %6 offset:1024"
                                   ::"v"(vW[0]), "v"(vW[1]), "v"(vW[2]), "v"(vW[3]), "v"(vW[4]), "v"(vW[5]), "s"(sW), "s"(m) : "memory");
    if (C::HALF) asm volatile(GLDS "%0, %1 offset:2048" ::"v"(vW[6]), "s"(sW) : "memory");
  }
};
template <class C> DI void load_preW(Ld<C>& L, unsigned ring) {
#pragma unroll 1
  for (int s = 0; s < C::NSLOT; ++s) { L.prep(s); L.issueW(ring + s * C::STAGE); }
}
template <class C, bool SPLIT> constexpr int vm_allow(int S) {
  const int N = C::NSLOT, behind = S - 1 - C::LAG > 0 ? S - 1 - C::LAG : 0;
  const int last = N - 1 + behind < C::NK - 1 ? N - 1 + behind : C::NK - 1;
  return (SPLIT && S < N) ? 4 * (N - 1 - S) + C::PW * (last - (N - 1)) : C::PW * (last - S);
}
template <class C, bool SPLIT, unsigned POSTM, int S, int SE, class Post> DI void load_fixed(Ld<C>& L, unsigned ring, const Post& post) {
  if constexpr (S < SE) {
    wait_vm<vm_allow<C, SPLIT>(S)>();
    bar();
    constexpr int T = S - 1 - C::LAG + C::NSLOT;
    if constexpr (S - 1 - C::LAG >= 0 && T < C::NK) {
      L.issueW(ring + (T % C::NSLOT) * C::STAGE); L.issueA(ring + (T % C::NSLOT) * C::STAGE);
      if constexpr (T + 1 < C::NK) L.prep(T + 1);
    }
    if constexpr ((POSTM >> S) & 1u) post(S);
    load_fixed<C, SPLIT, POSTM, S + 1, SE>(L, ring, post);
  }
}
template <class C, bool SPLIT, unsigned PREM, unsigned POSTM, class Pre, class Post>
DI void load_all(Ld<C>& L, unsigned ring, const Pre& pre, const Post& post, unsigned long long* dbg = nullptr) {
  constexpr int N = C::NSLOT, S0 = (SPLIT && N - 1 > 1 + C::LAG) ? N - 1 : 1 + C::LAG + (C::LAG ? 1 : 0), S1 = C::NK - N + C::LAG;
#pragma unroll 1
  for (int s = 0; s < N; ++s) { L.prep(s); if (!SPLIT) L.issueW(ring + s * C::STAGE); L.issueA(ring + s * C::STAGE); }
  L.prep(N);
  load_fixed<C, SPLIT, POSTM, 0, S0>(L, ring, post);
  static_assert(vm_allow<C, SPLIT>(S0) == (N - 2 - C::LAG) * C::PW && vm_allow<C, SPLIT>(S1) == (N - 2 - C::LAG) * C::PW, "steady count");
  unsigned b = ring + ((S0 - 1 - C::LAG) % N) * C::STAGE;
#pragma unroll 1
  for (int s = S0; s <= S1; ++s) {
    CLK(t0);
    wait_vm<(N - 2 - C::LAG) * C::PW>();
    if (PREM && ((PREM >> s) & 1u)) pre(s);
    CLK(t1);
    bar();
    CLK(t2);
    L.issueW(b);
    L.issueA(b);
    CLK(t3);
    L.prep(min(s - C::LAG + N, C::NK - 1));
    b = (b == ring + (N - 1) * C::STAGE) ? ring : b + C::STAGE;
    if (POSTM && ((POSTM >> s) & 1u)) post(s);
  }
  load_fixed<C, SPLIT, POSTM, S1 + 1, C::NK>(L, ring, post);
  bar();
}

template <int MT, int NWF> struct Fr { u32x4 a[MT]; u32x2 w[NWF][2]; };
DI u32x4 cat(const u32x2 (&w)[2]) { return (u32x4){w[0][0], w[0][1], w[1][0], w[1][1]}; }
struct Adr { unsigned v[6]; };
template <int KIND> struct Shape;
template <> struct Shape<1> { static constexpr int MT = 4, NWF = 6, NMF = 24, NACC = 24, WB = 6144; static constexpr unsigned USE = 0x0f; };
template <> struct Shape<3> { static constexpr int MT = 2, NWF = 4, NMF = 8, NACC = 8, WB = 2048; static constexpr unsigned USE = 0x3d; };
template <> struct Shape<4> { static constexpr int MT = 4, NWF = 8, NMF = 28, NACC = 28, WB = 7168; static constexpr unsigned USE = 0x3f; };
template <> struct Shape<5> { static constexpr int MT = 4, NWF = 4, NMF = 16, NACC = 16, WB = 4096; static constexpr unsigned USE = 0x3f; };
DI lds_ptr lp(unsigned a) { return (lds_ptr)(size_t)a; }
template <int KIND, int J> DI void rd1(Fr<Shape<KIND>::MT, Shape<KIND>::NWF>& f, const Adr& ad, int kb) {
  constexpr int NWF = Shape<KIND>::NWF, MT = Shape<KIND>::MT, WB = Shape<KIND>::WB;
  if constexpr (J < 2 * NWF) {
    constexpr int n = J >> 1, t = J & 1;
    if constexpr (KIND == 4 && n >= 6) f.w[n][t] = lds_tr(lp(ad.v[4 + (n - 6)]) + (2 * kb + t) * WB);
    else if constexpr (KIND == 1 || KIND == 4) f.w[n][t] = lds_tr(lp(ad.v[2 + (n & 1)]) + (n >> 1) * 2048 + (2 * kb + t) * WB);
    else f.w[n][t] = lds_tr(lp(ad.v[2 + n]) + (2 * kb + t) * WB);
  } else if constexpr (J < 2 * NWF + MT) {
    constexpr int m = J - 2 * NWF;
    if constexpr (KIND == 1) f.a[m] = lds128(lp(ad.v[kb]) + m * 2048);
    else f.a[m] = lds128(lp(ad.v[m >> 1]) + (m & 1) * 2048 + kb * 1024);
  }
}
template <int KIND, int I> DI void mm1(f32x4 (&acc)[Shape<KIND>::NACC], const Fr<Shape<KIND>::MT, Shape<KIND>::NWF>& f) {
  if constexpr (KIND == 1) { constexpr int m = I / 6, n = I % 6; if constexpr (n < 4) mfma_ip(acc[I], cat(f.w[n]), f.a[m]); else mfma_ip(acc[I], f.a[m], cat(f.w[n])); }
  else if constexpr (KIND == 4) { if constexpr (I < 24) mfma_ip(acc[I], cat(f.w[I % 6]), f.a[I / 6]); else mfma_ip(acc[I], cat(f.w[6 + ((I - 24) & 1)]), f.a[(I - 24) >> 1]); }
  else mfma_ip(acc[I], cat(f.w[I % 4]), f.a[I / 4]);
}
template <int KIND, int RPG, bool READ, bool MOVE, int I = 0>
DI void step(f32x4 (&acc)[Shape<KIND>::NACC], const Fr<Shape<KIND>::MT, Shape<KIND>::NWF>& cur, Fr<Shape<KIND>::MT, Shape<KIND>::NWF>& nxt, Adr& ad, int kb, int delta) {
  constexpr int NMF = Shape<KIND>::NMF, G0 = (2 * Shape<KIND>::NWF + Shape<KIND>::MT + RPG - 1) / RPG;
  if constexpr (I < NMF) {
    if constexpr (READ) {
      rd1<KIND, I * RPG>(nxt, ad, kb);
      if constexpr (RPG > 1) rd1<KIND, I * RPG + 1>(nxt, ad, kb);
    }
    if constexpr (MOVE && I >= G0) {
      constexpr int per = (6 + (NMF - G0) - 1) / (NMF - G0);
#pragma unroll
      for (int k = (I - G0) * per; k < (I - G0 + 1) * per && k < 6; ++k)
        if ((Shape<KIND>::USE >> k) & 1u) { ad.v[k] += delta; asm volatile("" : "+v"(ad.v[k])); }
    }
    mm1<KIND, I>(acc, cur);
    SB();
    step<KIND, RPG, READ, MOVE, I + 1>(acc, cur, nxt, ad, kb, delta);
  }
}
template <int KIND, int J = 0> DI void rdall(Fr<Shape<KIND>::MT, Shape<KIND>::NWF>& f, const Adr& ad, int kb) {
  if constexpr (J < 2 * Shape<KIND>::NWF + Shape<KIND>::MT) { rd1<KIND, J>(f, ad, kb); rdall<KIND, J + 1>(f, ad, kb); }
}
#define ST4() do {} while (0)
#define ST4ARGS
#define ST4PASS
template <class C, int KIND, int RPG, int CNT>
DI void stages(f32x4 (&acc)[Shape<KIND>::NACC], Fr<Shape<KIND>::MT, Shape<KIND>::NWF>& f0, Fr<Shape<KIND>::MT, Shape<KIND>::NWF>& f1, Adr& ad, int& slot ST4ARGS) {
#pragma unroll 1
  for (int s = 0; s < CNT; ++s) {
    const int delta = (slot + 1 == C::NSLOT) ? -(C::NSLOT - 1) * C::STAGE : C::STAGE;
    slot = (slot + 1 == C::NSLOT) ? 0 : slot + 1;
    SB();
    step<KIND, RPG, true, true>(acc, f0, f1, ad, 1, delta);
    if (!C::LAG) wait_lgkm0();
    ST4();
    bar();
    ST4();
    SB();
    step<KIND, RPG, true, false>(acc, f1, f0, ad, 0, 0);
  }
}
template <class C, int KIND, int RPG, int H1, int H2, class Hook> DI void consume(f32x4 (&acc)[Shape<KIND>::NACC], Adr& ad, unsigned* dbg4, const Hook& hook) {
  Fr<Shape<KIND>::MT, Shape<KIND>::NWF> f0, f1;
#pragma unroll
  for (int i = 0; i < Shape<KIND>::NACC; ++i) acc[i] = (f32x4){0.f, 0.f, 0.f, 0.f};
  bar();
  rdall<KIND>(f0, ad, 0);
  hook(-1);
  wait_lgkm0();
  int slot = 0;
  if constexpr (H1 > 0) {
    stages<C, KIND, RPG, H1>(acc, f0, f1, ad, slot ST4PASS);
    hook(1);
    stages<C, KIND, RPG, H2 - H1>(acc, f0, f1, ad, slot ST4PASS);
    hook(2);
  }
  stages<C, KIND, RPG, C::NK - 1 - H2>(acc, f0, f1, ad, slot ST4PASS);
  SB();
  step<KIND, RPG, true, false>(acc, f0, f1, ad, 1, 0);
  wait_lgkm0();
  bar();
  SB();
  step<KIND, RPG, false, false>(acc, f1, f0, ad, 0, 0);
  asm volatile("s_nop 7\n\ts_nop 3");
  SB();
}
template <class C, int KIND, int RPG, class Hook> DI void consume_s(f32x4 (&acc)[Shape<KIND>::NACC], Adr& ad, const Hook& hook) {
  Fr<Shape<KIND>::MT, Shape<KIND>::NWF> f0, f1, g0, g1;
#pragma unroll
  for (int i = 0; i < Shape<KIND>::NACC; ++i) acc[i] = (f32x4){0.f, 0.f, 0.f, 0.f};
  bar();
  rdall<KIND>(f0, ad, 0); rdall<KIND>(f1, ad, 1);
  hook(-1);
#pragma unroll
  for (int k = 0; k < 6; ++k) ad.v[k] += C::STAGE;
  int slot = 1;
#pragma unroll 1
  for (int s = 0; s < C::NK - 2; s += 2) {
    int delta = (slot + 1 == C::NSLOT) ? -(C::NSLOT - 1) * C::STAGE : C::STAGE;
    slot = (slot + 1 == C::NSLOT) ? 0 : slot + 1;
    if (!C::LAG) wait_lgkm0();
    bar(); SB();
    step<KIND, RPG, true, false>(acc, f0, g0, ad, 0, 0);
    step<KIND, RPG, true, true>(acc, f1, g1, ad, 1, delta);
    delta = (slot + 1 == C::NSLOT) ? -(C::NSLOT - 1) * C::STAGE : C::STAGE;
    slot = (slot + 1 == C::NSLOT) ? 0 : slot + 1;
    if (!C::LAG) wait_lgkm0();
    bar(); SB();
    step<KIND, RPG, true, false>(acc, g0, f0, ad, 0, 0);
    step<KIND, RPG, true, true>(acc, g1, f1, ad, 1, delta);
  }
  if (!C::LAG) wait_lgkm0();
  bar(); SB();
  step<KIND, RPG, true, false>(acc, f0, g0, ad, 0, 0);
  step<KIND, RPG, true, false>(acc, f1, g1, ad, 1, 0);
  wait_lgkm0(); bar(); SB();
  step<KIND, RPG, false, false>(acc, g0, f0, ad, 0, 0);
  step<KIND, RPG, false, false>(acc, g1, f1, ad, 1, 0);
  asm volatile("s_nop 7\n\ts_nop 3");
  SB();
}
DI unsigned wlane(int lane, int nt) {
  const int g = lane >> 4, q = (lane >> 2) & 3, p = lane & 3, sw = (2 * g + (q >> 1)) & 3;
  return (4 * g + q) * 128 + ((nt ^ sw) << 5) + p * 8;
}
DI unsigned hlane(int lane, int nt) {
  const int g = lane >> 4, q = (lane >> 2) & 3, p = lane & 3;
  return (4 * g + q) * 64 + ((nt ^ (g & 1)) << 5) + p * 8;
}

template <int OP> DI float op2(float a, float b) { return OP == 0 ? a + b : OP == 1 ? __builtin_elementwise_maximum(a, b) : __builtin_elementwise_minimum(a, b); }
template <int OP> DI float red4(float x0, float x1, float x2, float x3) {
  auto a = __builtin_amdgcn_permlane16_swap(bu(x0), bu(x1), false, false);
  auto b = __builtin_amdgcn_permlane16_swap(bu(x2), bu(x3), false, false);
  const float y0 = op2<OP>(fb(a[0]), fb(a[1])), y1 = op2<OP>(fb(b[0]), fb(b[1]));
  auto c = __builtin_amdgcn_permlane32_swap(bu(y0), bu(y1), false, false);
  return op2<OP>(fb(c[0]), fb(c[1]));
}
DI void spread(float z, float (&o)[4]) {
  auto a = __builtin_amdgcn_permlane32_swap(bu(z), bu(z), false, false);
  auto b = __builtin_amdgcn_permlane16_swap(a[0], a[0], false, false);
  auto c = __builtin_amdgcn_permlane16_swap(a[1], a[1], false, false);
  o[0] = fb(b[0]); o[1] = fb(b[1]); o[2] = fb(c[0]); o[3] = fb(c[1]);
}
DI void dual_softmax4(const f32x4 (&p)[4][2], u32x4 (&pp)[4], u32x4 (&pn)[4]) {
  constexpr float L2E = 1.4426950408889634f;
  float mx[4], mn[4];
#pragma unroll
  for (int m = 0; m < 4; ++m) {
    mx[m] = op2<1>(op2<1>(op2<1>(op2<1>(p[m][0][0], p[m][0][1]), p[m][0][2]), op2<1>(op2<1>(p[m][0][3], p[m][1][0]), p[m][1][1])), op2<1>(p[m][1][2], p[m][1][3]));
    mn[m] = op2<2>(op2<2>(op2<2>(op2<2>(p[m][0][0], p[m][0][1]), p[m][0][2]), op2<2>(op2<2>(p[m][0][3], p[m][1][0]), p[m][1][1])), op2<2>(p[m][1][2], p[m][1][3]));
  }
  spread(-L2E * red4<1>(mx[0], mx[1], mx[2], mx[3]), mx);
  spread(L2E * red4<2>(mn[0], mn[1], mn[2], mn[3]), mn);
  f32x4 ep[4][2], en[4][2];
  float sp[4], sn[4];
#pragma unroll
  for (int m = 0; m < 4; ++m) {
#pragma unroll
    for (int F = 0; F < 2; ++F) {
      const f32x4 a = p[m][F] * L2E + mx[m], b = p[m][F] * (-L2E) + mn[m];
#pragma unroll
      for (int r = 0; r < 4; ++r) { ep[m][F][r] = __builtin_amdgcn_exp2f(a[r]); en[m][F][r] = __builtin_amdgcn_exp2f(b[r]); }
    }
    const f32x4 a = ep[m][0] + ep[m][1], b = en[m][0] + en[m][1];
    sp[m] = (a[0] + a[1]) + (a[2] + a[3]); sn[m] = (b[0] + b[1]) + (b[2] + b[3]);
  }
  spread(__builtin_amdgcn_rcpf(red4<0>(sp[0], sp[1], sp[2], sp[3])), sp);
  spread(__builtin_amdgcn_rcpf(red4<0>(sn[0], sn[1], sn[2], sn[3])), sn);
#pragma unroll
  for (int m = 0; m < 4; ++m) { pp[m] = pack8(ep[m][0] * sp[m], ep[m][1] * sp[m]); pn[m] = pack8(en[m][0] * sn[m], en[m][1] * sn[m]); }
}

DI float dot2(unsigned a, unsigned b, float c) { return __builtin_amdgcn_fdot2_f32_bf16(__builtin_bit_cast(bf16x2, a), __builtin_bit_cast(bf16x2, b), c, false); }
DI unsigned opaque(unsigned c) { asm volatile("" : "+s"(c)); return c; }
template <int NL> DI void colsum_load(u32x4 (&v)[NL], const char* w, int stride, int lane) {
#pragma unroll
  for (int i = 0; i < NL; ++i) v[i] = *(const u32x4*)(w + (size_t)(8 * i + (lane >> 3)) * stride + (lane & 7) * 16);
}
template <int NL> DI void colsum_finish(const u32x4 (&v)[NL], rsrc_t r, unsigned dst, int lane) {
  f32x2 s2[4];
#pragma unroll
  for (int j = 0; j < 4; ++j) s2[j] = (f32x2){bflo(v[0][j]), bfhi(v[0][j])};
#pragma unroll
  for (int i = 1; i < NL; ++i)
#pragma unroll
    for (int j = 0; j < 4; ++j) s2[j] += (f32x2){bflo(v[i][j]), bfhi(v[i][j])};
  const float s[8] = {s2[0][0], s2[0][1], s2[1][0], s2[1][1], s2[2][0], s2[2][1], s2[3][0], s2[3][1]};
  float t[4], u[2];
#pragma unroll
  for (int j = 0; j < 4; ++j) { auto q = __builtin_amdgcn_permlane32_swap(bu(s[j]), bu(s[j + 4]), false, false); t[j] = fb(q[0]) + fb(q[1]); }
#pragma unroll
  for (int j = 0; j < 2; ++j) { auto q = __builtin_amdgcn_permlane16_swap(bu(t[j]), bu(t[j + 2]), false, false); u[j] = fb(q[0]) + fb(q[1]); }
  const bool b3 = lane & 8;
  const float tot = (b3 ? u[1] : u[0]) + dpp<0x128>(b3 ? u[0] : u[1]);
  st4(r, bu(tot), ((lane & 7) * 8 + ((lane >> 5) & 1) * 4 + ((lane >> 4) & 1) * 2 + ((lane >> 3) & 1)) * 4, dst);
}

template <int XN, int TM, int TN> DI void tile_of(int bid, int& tm, int& tn, int& c) {
  const int x = bid & 7, r = bid >> 3, li = r % TM, lj = r / TM;
  tm = (x / XN) * TM + li; tn = (x % XN) * TN + lj; c = (li + lj) & 3;
}

#define RPG1 1
#define RPG4 1
#define RPG5 1
constexpr int R1 = 40704, R3 = 40704, R4 = 0, R5 = 0, SYNCW = 163584;
constexpr int S1_ST = 0, S1_CSR = 1024, S1_CSF = 13312, S1_FLG = 14080, S1_WP = 14336, S1_KV = 18432, S1_DUM = 26880;
constexpr int S4_STR = C4::RING, S4_CSR = S4_STR + 16384, S4_STF = S4_CSR + 8192, S4_CSF = S4_STF + 1024, S4_DUM = S4_CSF + 1024;
constexpr int LDS_BYTES = 163840, CODE_BYTES = 48 * 1024;
static_assert(S4_DUM + 256 <= SYNCW && S1_KV + 8448 <= R1 && R1 + C1::RING <= SYNCW && R3 + C3::RING <= SYNCW && C5::RING <= S4_STR, "lds");

DI void kv_sum(const Sync& y, Gate& G, lds_ptr smem, int head, int h, int t, bool poller, bool doz, int lane) {
  u32x4 v[16], zz[4];
  unsigned have = 0;
  f32x2 s2[4] = {{0.f, 0.f}, {0.f, 0.f}, {0.f, 0.f}, {0.f, 0.f}};
#pragma unroll 1
  for (int round = 0; round < 3; ++round) {
    gate_slow(y, G, 0u, poller, lane, round == 0 ? 0 : round == 1 ? 13 : 16);
    const unsigned fresh = G.mask & ~have;
    have = G.mask;
#pragma unroll
    for (int r = 0; r < 16; ++r) if ((fresh >> r) & 1u) v[r] = ld16(y.r, t * 16, WS_KVP + (head * 16 + r) * 4096);
    if (round == 2 && doz) {
#pragma unroll
      for (int i = 0; i < 4; ++i) zz[i] = ld16(y.r, lane * 16, WS_ZP + head * 4096 + i * 1024);
    }
  }
#pragma unroll
  for (int r = 0; r < 16; ++r)
#pragma unroll
    for (int j = 0; j < 4; ++j) s2[j] += (f32x2){bflo(v[r][j]), bfhi(v[r][j])};
  const float s[8] = {s2[0][0], s2[0][1], s2[1][0], s2[1][1], s2[2][0], s2[2][1], s2[3][0], s2[3][1]};
  *(LAS u32x4*)(smem + S1_KV + h * 4096 + t * 16) = (u32x4){pk2(s[0], s[1]), pk2(s[2], s[3]), pk2(s[4], s[5]), pk2(s[6], s[7])};
  if (doz) {
    f32x4 z = (__builtin_bit_cast(f32x4, zz[0]) + __builtin_bit_cast(f32x4, zz[1])) + (__builtin_bit_cast(f32x4, zz[2]) + __builtin_bit_cast(f32x4, zz[3]));
#pragma unroll
    for (int j = 0; j < 4; ++j) z[j] = xsum32(xsum16(z[j]));
    if (lane < 16) *(LAS u32x2*)(smem + S1_KV + 8192 + h * 128 + lane * 8) = (u32x2){pk2(z[0], z[1]), pk2(z[2], z[3])};
  }
}

__global__ __launch_bounds__(512, 2) void fused(Params P) {
  extern __shared__ __attribute__((aligned(16))) char smem_[];
  lds_ptr smem = (lds_ptr)smem_;
  const unsigned lds0 = (unsigned)(size_t)smem;
  const int wid = __builtin_amdgcn_readfirstlane(threadIdx.x >> 6), lane = threadIdx.x & 63, bid = blockIdx.x;
  {
    const char* pc; asm volatile("s_getpc_b64 %0" : "=s"(pc));
    const unsigned co = ((bid >> 3) & 7) * 8192 + threadIdx.x * 128;
    if (threadIdx.x < 64 && co < CODE_BYTES) dma4<false>(co, pc, lds0 + S1_DUM);
    SB();
  }
  int tm1, tn1, c1, tm3, tn3, c3;
  tile_of<4, 8, 4>(bid, tm1, tn1, c1);
  tile_of<2, 4, 8>(bid, tm3, tn3, c3);
  const int kh = bid & 1, tm5 = ((bid & 7) >> 1) * 4 + ((bid >> 3) & 3), tn5 = bid >> 5, c5 = ((bid >> 3) + tn5) & 3;
  const int par = tn1 & 1, base7 = 7 * (tn1 >> 1), ch0 = base7 + 4 * par;
  const int g = lane >> 4, m16 = lane & 15;
  Sync y;
  y.r = __builtin_amdgcn_make_buffer_rsrc(P.ws, 0, WS_END, 0x00020000);
  y.w = (LAS unsigned*)(smem + SYNCW);
  if (wid == 0) TS(0);

  if (wid >= 4) {
    const int lw = wid - 4, lt = threadIdx.x - 256;
    const auto nop = [](int) {};
    {
      Ld<C1> L; L.A = P.x + (size_t)tm1 * 128 * 2048; L.W = P.wqkv + tn1 * 128; L.lw = lw; L.c = c1; L.kt0 = 0; L.init(y, lane, 2048, 0, 0, 0);
      const unsigned frec = (FL_P0 * 256 + (lane < 16 ? tm1 * 16 + lane : lane < 28 ? ((lane - 16) / 3) * 48 + ((lane - 16) % 3) * 16 + tn1 : tn1)) * 128;
      if (lw == 0) dma4<true>(0u * lane, P.ws + WS_CTL, lds0 + S1_FLG);
      __builtin_amdgcn_s_setprio(3);
      load_all<C1, false, 1u << 11, 0x4905u>(L, lds0 + R1,
        [&](int s) {
          if (lw == 0) {
            const u32x4 v = *(volatile LAS u32x4*)(smem + S1_CSR + lane * 16);
            if (!__all(v[0] == y.tag && v[1] == y.tag && v[2] == y.tag && v[3] == y.tag)) {
#pragma unroll 1
              for (int k = 0; k < 4; ++k) poll(y, frec + 4 * k, 64, lane);
            }
          }
        },
        [&](int s) {
          if (s == 0) y.tag = __builtin_amdgcn_readfirstlane(*(volatile LAS unsigned*)(smem + S1_FLG)) + 1u;
          if (s == 2) dma<false>(lane * 16, P.wphi + tn1 * 4096 + lw * 1024, lds0 + S1_WP + lw * 1024);
          if (s == 8 && lw == 0) dma<true>(frec, P.ws + WS_FLG, lds0 + S1_CSR);
          if (s == 11) {
            if (lw == 3) dma<true>(lane * 16, P.ws + WS_ST1 + tm1 * 1024, lds0 + S1_ST);
#pragma unroll
            for (int i = 0; i < 3; ++i)
              dma<true>((lane >> 4) * 12288 + (lane & 15) * 16, P.ws + WS_CSQ + lw * 49152 + i * 4096 + tn1 * 256, lds0 + S1_CSR + i * 4096 + lw * 1024);
          }
          if (s == 14) {
            if (lt < 192) {
              const LAS float* rawp = (const LAS float*)(smem + S1_CSR) + (lt >> 6) * 1024 + (lt & 63);
              float t = 0.f;
#pragma unroll
              for (int i = 0; i < 16; ++i) t += rawp[i * 64];
              ((LAS float*)(smem + S1_CSF))[lt] = t;
            }
            wait_lgkm0();
          }
        }, DBG(0));
    }
    if (wid == 4) TS(16);
    {
      Ld<C3> L; L.A = P.ws + WS_O + (size_t)tm3 * 16 * 16384; L.W = P.wo + tn3 * 128; L.lw = lw; L.c = c3; L.kt0 = 0; L.init(y, lane, 0, 0, FL_O * 256 + tm3 * 16 + m16, 9);
      load_preW<C3>(L, lds0 + R3);
      if (lw >= 2 && bid < 224) {
        u32x4 v[16];
        colsum_load<16>(v, P.w1 + (size_t)((bid / 56) * 256 + (lw - 2) * 128) * (FF * 2) + (bid % 56) * 128, FF * 2, lane);
        colsum_finish<16>(v, y.r, WS_CS1 + (((bid / 56) * 2 + lw - 2) * FF + (bid % 56) * 64) * 4, lane);
      }
      publish(y, FL_CS, bid, lane);
      lds_wait(y, 24 + FL_KV, 4u);
      Gate G; G.off = (FL_KV * 256 + m16 * 16 + tn1) * 128; G.mask = 0; G.word = 8;
      kv_sum(y, G, smem, 2 * tn1 + 1, 1, lt, lw == 0, lw == 1, lane);
      if (wid == 4) TS(17);
      wait_lgkm0(); bar();
      if (wid == 4) TS(18);
      load_all<C3, true, 0u, 0u>(L, lds0 + R3, nop, nop, DBG(1));
    }
    if (wid == 4) TS(19);
    {
      Ld<C4> L; L.A = P.ws + WS_H + (size_t)tm1 * 16 * 16384; L.W = P.w1 + ch0 * 128; L.lw = lw; L.c = c1; L.kt0 = 0;
      L.init(y, lane, 128, par ? -64 : 384, (lane & 16) ? FL_CS * 256 + (m16 >> 2) * 56 + base7 + 3 * par + (lane & 3) : FL_H * 256 + tm1 * 16 + m16, 10);
      lds_wait(y, 24 + FL_H, 4u);
      load_preW<C4>(L, lds0 + R4);
      if (wid == 4) TS(20);
      load_all<C4, true, 0u, 0xa000u>(L, lds0 + R4, nop,
        [&](int s) {
          if (s == 13) {
            gate(y, L.G, ~0u, lw == 0, lane);
#pragma unroll
            for (int i = 0; i < 4; ++i) dma<true>(lane * 16, P.ws + WS_ST2 + (tm1 * 16 + lw * 4 + i) * 1024, lds0 + S4_STR + (lw * 4 + i) * 1024);
#pragma unroll
            for (int i = 0; i < 2; ++i) dma<true>(lane * 16, P.ws + WS_CS1 + ((lw * 2 + i) * FF + (base7 + 3 * par) * 64) * 4, lds0 + S4_CSR + (lw * 2 + i) * 1024);
          }
          if (s == 15) {
            const LAS float* rawp = (const LAS float*)(smem + S4_CSR) + lt;
            float t = 0.f;
#pragma unroll
            for (int i = 0; i < 8; ++i) t += rawp[i * 256];
            ((LAS float*)(smem + S4_CSF))[lt] = t;
            wait_lgkm0();
          }
          if (s == 15 && lw < 2) {
            f32x2 st[16]; float mu = 0.f, m2 = 0.f;
#pragma unroll
            for (int i = 0; i < 16; ++i) { st[i] = *(const LAS f32x2*)(smem + S4_STR + i * 1024 + lt * 8); mu += st[i][0]; }
            mu *= (1.f / 16.f);
#pragma unroll
            for (int i = 0; i < 16; ++i) { const float d = st[i][0] - mu; m2 += st[i][1] + 64.f * d * d; }
            *(LAS f32x2*)(smem + S4_STF + lt * 8) = (f32x2){mu, __builtin_amdgcn_rsqf(m2 * (1.f / D) + 1e-5f)};
            wait_lgkm0();
          }
        }, DBG(2));
    }
    if (wid == 4) TS(21);
    {
      Ld<C5> L; L.A = P.ws + WS_F + (size_t)tm5 * 56 * 16384; L.W = P.w2 + tn5 * 256; L.lw = lw; L.c = c5; L.kt0 = 28 * kh; L.init(y, lane, 128, 0, FL_F * 256 + tm5 * 16 + 8 * kh + (lane & 7), 11);
      lds_wait(y, 24 + FL_F, 4u);
      load_preW<C5>(L, lds0 + R5);
      if (wid == 4) TS(22);
      load_all<C5, true, 0u, 0u>(L, lds0 + R5, nop, nop, DBG(3));
    }
    if (wid == 4) { TS(23); TS_FLUSH(); }
    return;
  }

  const int wm = wid >> 1, wn = wid & 1;
  if (wid == 0 && lane < 32) y.w[lane] = 0u;
  if (wid == 2) {
    const int i1 = tm1 * 16 + tn1, i3 = tm3 * 16 + tn3, ph = lane & 7;
    const int idx = (ph == FL_P0 || ph == FL_CS) ? bid : ph == FL_H ? i3 : ph >= FL_X ? (tm5 * 8 + tn5) * 2 + kh : i1;
    dma4<false>(lane < 8 ? WS_FLG + (ph * 256 + idx) * 128 : lane < 16 ? WS_ST2 + i3 * 1024 + ph * 128 : WS_ZP + ((2 * tn1 + (lane & 1)) * 16 + tm1) * 256 + (lane & 2) * 64, P.ws, lds0 + S1_DUM);
  }
  {
    f32x4 acc[24];
    Adr ad;
    ad.v[0] = lds0 + R1 + (wm * 64 + m16) * 128 + ((g ^ (m16 >> 1)) << 4); ad.v[1] = lds0 + R1 + (wm * 64 + m16) * 128 + ((g ^ (m16 >> 1) ^ 4) << 4);
    ad.v[2] = lds0 + R1 + 16384 + wlane(lane, 2 * wn); ad.v[3] = lds0 + R1 + 16384 + wlane(lane, 2 * wn + 1); ad.v[4] = 0; ad.v[5] = 0;
    u32x4 px[4], pw[8];
    const int prow = bid * 8 + wid * 2 + (lane >> 5);
    consume<C1, 1, RPG1, 4, 6>(acc, ad, DBG4, [&](int s) {
      if (s < 0) {
        if (wid == 0) TS(1);
        y.tag = __builtin_amdgcn_readfirstlane(*(volatile LAS unsigned*)(smem + S1_FLG)) + 1u;
#pragma unroll
        for (int i = 0; i < 4; ++i) px[i] = *(const u32x4*)(P.x + (size_t)prow * 2048 + ((lane & 31) + 32 * i) * 16);
        colsum_load<8>(pw, P.wqkv + (size_t)((min(bid, 191) / 48) * 256 + wid * 64) * (3 * D * 2) + (bid % 48) * 128, 3 * D * 2, lane);
      }
      if (s == 1) {
#pragma unroll
        for (int i = 0; i < 4; ++i) asm volatile("" : "+v"(px[i]));
#pragma unroll
        for (int i = 0; i < 8; ++i) asm volatile("" : "+v"(pw[i]));
        const unsigned one2 = opaque(0x3f803f80u);
        float sm = 0.f, sq = 0.f;
#pragma unroll
        for (int i = 0; i < 4; ++i)
#pragma unroll
          for (int j = 0; j < 4; ++j) { sm = dot2(px[i][j], one2, sm); sq = dot2(px[i][j], px[i][j], sq); }
        const float mean = xsum16(row16_sum(sm)) * (1.f / D);
        const float var = fmaxf(xsum16(row16_sum(sq)) * (1.f / D) - mean * mean, 0.f);
        if ((lane & 31) == 0) st8(y.r, (u32x2){bu(mean), bu(__builtin_amdgcn_rsqf(var + 1e-5f))}, prow * 8, WS_ST1);
        if (bid < 192) colsum_finish<8>(pw, y.r, WS_CSQ + (((bid / 48) * 4 + wid) * 3 * D + (bid % 48) * 64) * 4, lane);
      }
      if (s == 2) { wait_vm<0>(); if (lane == 0) st4(y.r, y.tag, 0, WS_FLG + (FL_P0 * 256 + bid) * 128 + wid * 4); }
    });
    if (wid == 0) TS(2);
    const int head = 2 * tn1 + wn;
    f32x4 cs[4]; float csv[2];
#pragma unroll
    for (int n = 0; n < 4; ++n) cs[n] = *(const LAS f32x4*)(smem + S1_CSF + ((n >> 1) * 64 + wn * 32 + (n & 1) * 16 + 4 * g) * 4);
#pragma unroll
    for (int n = 0; n < 2; ++n) csv[n] = *(const LAS float*)(smem + S1_CSF + (128 + wn * 32 + n * 16 + m16) * 4);
    u32x4 wp[2];
#pragma unroll
    for (int F = 0; F < 2; ++F) { lds_ptr a0 = smem + S1_WP + wn * 2048 + (4 * g + (m16 >> 2)) * 64 + F * 32 + (lane & 3) * 8; wp[F] = lds_tr2(a0, a0 + 1024); }
    u32x4 qb[4], vb[2][2];
    f32x4 p[4][2];
    const f32x4 zero = {0.f, 0.f, 0.f, 0.f};
    {
      f32x4 vv[4][2];
#pragma unroll
      for (int m = 0; m < 4; ++m) {
        const f32x2 st = *(const LAS f32x2*)(smem + S1_ST + (wm * 64 + m * 16 + m16) * 8);
        const f32x4 s0 = *(const LAS f32x4*)(smem + S1_ST + (wm * 64 + m * 16 + 4 * g) * 8), s1 = *(const LAS f32x4*)(smem + S1_ST + (wm * 64 + m * 16 + 4 * g) * 8 + 16);
        const f32x4 mu = {s0[0], s0[2], s1[0], s1[2]}, rs = {s0[1], s0[3], s1[1], s1[3]};
        qb[m] = pack8((acc[m * 6] - cs[0] * st[0]) * st[1], (acc[m * 6 + 1] - cs[1] * st[0]) * st[1]);
        const u32x4 kb = pack8((acc[m * 6 + 2] - cs[2] * st[0]) * st[1], (acc[m * 6 + 3] - cs[3] * st[0]) * st[1]);
        p[m][0] = mfma(wp[0], kb, zero); p[m][1] = mfma(wp[1], kb, zero);
        vv[m][0] = (acc[m * 6 + 4] - mu * csv[0]) * rs; vv[m][1] = (acc[m * 6 + 5] - mu * csv[1]) * rs;
      }
#pragma unroll
      for (int u = 0; u < 2; ++u) { vb[u][0] = pack8(vv[2 * u][0], vv[2 * u + 1][0]); vb[u][1] = pack8(vv[2 * u][1], vv[2 * u + 1][1]); }
    }
    u32x4 pp[4], pn[4];
    if (wid == 0) TS(26);
    dual_softmax4(p, pp, pn);
    if (wid == 0) TS(27);
    {
      lds_ptr own = smem + R3 + wid * C3::STAGE, oth = smem + R3 + (wid ^ 2) * C3::STAGE;
#pragma unroll
      for (int m = 0; m < 4; ++m) {
        lds_ptr w = own + (16 * m + m16) * 136 + 8 * g;
        *(LAS u32x2*)(w) = (u32x2){pp[m][0], pp[m][1]}; *(LAS u32x2*)(w + 32) = (u32x2){pp[m][2], pp[m][3]};
        *(LAS u32x2*)(w + 64) = (u32x2){pn[m][0], pn[m][1]}; *(LAS u32x2*)(w + 96) = (u32x2){pn[m][2], pn[m][3]};
      }
#pragma unroll
      for (int i = 0; i < 4; ++i) *(LAS u32x4*)(own + 8704 + i * 1024 + lane * 16) = vb[i >> 1][i & 1];
      wait_lgkm0();
      volatile LAS unsigned* xf = y.w + 16;
      xf[wid] = 1u;
      f32x4 kv[2][2] = {{zero, zero}, {zero, zero}}, z[2] = {zero, zero};
      const u32x4 ones = {0x3f803f80u, 0x3f803f80u, 0x3f803f80u, 0x3f803f80u};
      const unsigned ro = (4 * g + (m16 >> 2)) * 136 + wm * 64 + (lane & 3) * 8;
#pragma unroll
      for (int u = 0; u < 2; ++u)
#pragma unroll
        for (int F = 0; F < 2; ++F) {
          const u32x4 pa = lds_tr2(own + ro + u * 32 * 136 + F * 32, own + ro + u * 32 * 136 + F * 32 + 16 * 136);
          kv[F][0] = mfma(pa, vb[u][0], kv[F][0]); kv[F][1] = mfma(pa, vb[u][1], kv[F][1]); z[F] = mfma(pa, ones, z[F]);
        }
      while (xf[wid ^ 2] == 0u) __builtin_amdgcn_s_sleep(0);
      asm volatile("" ::: "memory");
#pragma unroll
      for (int u = 0; u < 2; ++u) {
        const u32x4 v0 = lds128(oth + 8704 + u * 2048 + lane * 16), v1 = lds128(oth + 8704 + u * 2048 + 1024 + lane * 16);
#pragma unroll
        for (int F = 0; F < 2; ++F) {
          const u32x4 pa = lds_tr2(oth + ro + u * 32 * 136 + F * 32, oth + ro + u * 32 * 136 + F * 32 + 16 * 136);
          kv[F][0] = mfma(pa, v0, kv[F][0]); kv[F][1] = mfma(pa, v1, kv[F][1]); z[F] = mfma(pa, ones, z[F]);
        }
      }
      const unsigned kd = WS_KVP + (head * 16 + tm1) * 4096 + wm * 2048;
      st16(y.r, pack8(kv[0][0], kv[1][0]), lane * 16, kd);
      st16(y.r, pack8(kv[0][1], kv[1][1]), lane * 16, kd + 1024);
      if (m16 == 0) {
        st16(y.r, __builtin_bit_cast(u32x4, z[0]), g * 32, WS_ZP + (head * 16 + tm1) * 256 + wm * 128);
        st16(y.r, __builtin_bit_cast(u32x4, z[1]), g * 32 + 16, WS_ZP + (head * 16 + tm1) * 256 + wm * 128);
      }
      if (wid == 0) TS(25);
      publish(y, FL_KV, tm1 * 16 + tn1, lane);
    }
    if (wid == 0) TS(3);
#pragma unroll
    for (int m = 0; m < 4; ++m) { p[m][0] = mfma(wp[0], qb[m], zero); p[m][1] = mfma(wp[1], qb[m], zero); }
    dual_softmax4(p, pp, pn);
    if (wid == 0) TS(4);
    { Gate G; G.off = 0; G.mask = 0; G.word = 8; kv_sum(y, G, smem, 2 * tn1, 0, threadIdx.x, false, wid == 0, lane); }
    wait_lgkm0(); bar();
    if (wid == 0) TS(5);
    u32x4 ka[2][2], za[2];
#pragma unroll
    for (int ks = 0; ks < 2; ++ks) {
      ka[ks][0] = lds128(smem + S1_KV + wn * 4096 + ks * 2048 + lane * 16); ka[ks][1] = lds128(smem + S1_KV + wn * 4096 + ks * 2048 + 1024 + lane * 16);
      za[ks] = lds128(smem + S1_KV + 8192 + wn * 128 + ks * 64 + g * 16);
    }
#pragma unroll
    for (int m = 0; m < 4; ++m) {
      f32x4 n0 = mfma(ka[0][0], pp[m], zero), n1 = mfma(ka[0][1], pp[m], zero), dn = mfma(za[0], pp[m], zero);
      n0 = mfma(ka[1][0], pn[m], n0); n1 = mfma(ka[1][1], pn[m], n1); dn = mfma(za[1], pn[m], dn);
      const float rd = __builtin_amdgcn_rcpf(dn[0] + 1e-6f);
      st16(y.r, pack8(n0 * rd, n1 * rd), lane * 16, WS_O + (((tm1 * 16 + tn1) * 8 + wm * 4 + m) * 2 + wn) * 1024);
    }
    publish(y, FL_O, tm1 * 16 + tn1, lane);
    if (wid == 0) TS(6);
  }
  {
    u32x4 xr[2][2];
#pragma unroll
    for (int m = 0; m < 2; ++m)
#pragma unroll
      for (int Q = 0; Q < 2; ++Q)
        xr[m][Q] = *(const u32x4*)(P.x + (size_t)(tm3 * 128 + wid * 32 + m * 16 + m16) * 2048 + tn3 * 128 + Q * 64 + (g & 1) * 32 + (g >> 1) * 16);
    f32x4 acc[8];
    Adr ad; ad.v[0] = lds0 + R3 + wid * 4096 + lane * 16; ad.v[1] = 0;
#pragma unroll
    for (int n = 0; n < 4; ++n) ad.v[2 + n] = lds0 + R3 + 16384 + wlane(lane, n);
    consume_s<C3, 3, 2>(acc, ad, [&](int) { if (wid == 0) TS(7); });
    if (wid == 0) TS(8);
#pragma unroll
    for (int m = 0; m < 2; ++m) {
      float hv[16];
#pragma unroll
      for (int Q = 0; Q < 2; ++Q) {
        auto r0 = __builtin_amdgcn_permlane16_swap(xr[m][Q][0], xr[m][Q][2], false, false);
        auto r1 = __builtin_amdgcn_permlane16_swap(xr[m][Q][1], xr[m][Q][3], false, false);
        const f32x4 a = acc[m * 4 + 2 * Q], b = acc[m * 4 + 2 * Q + 1];
        const u32x4 hp = {pk2(a[0] + bflo(r0[0]), a[1] + bfhi(r0[0])), pk2(a[2] + bflo(r1[0]), a[3] + bfhi(r1[0])),
                          pk2(b[0] + bflo(r0[1]), b[1] + bfhi(r0[1])), pk2(b[2] + bflo(r1[1]), b[3] + bfhi(r1[1]))};
        st16(y.r, hp, lane * 16, WS_H + (((tm3 * 16 + tn3) * 8 + wid * 2 + m) * 2 + Q) * 1024);
#pragma unroll
        for (int j = 0; j < 4; ++j) { hv[Q * 8 + 2 * j] = bflo(hp[j]); hv[Q * 8 + 2 * j + 1] = bfhi(hp[j]); }
      }
      float s = 0.f;
#pragma unroll
      for (int j = 0; j < 16; ++j) s += hv[j];
      s = xsum32(xsum16(s)) * (1.f / 64.f);
      float q = 0.f;
#pragma unroll
      for (int j = 0; j < 16; ++j) { const float d = hv[j] - s; q += d * d; }
      q = xsum32(xsum16(q));
      if (g == 0) st8(y.r, (u32x2){bu(s), bu(q)}, m16 * 8, WS_ST2 + (tm3 * 16 + tn3) * 1024 + (wid * 32 + m * 16) * 8);
    }
    if (wid == 0) TS(29);
    publish(y, FL_H, tm3 * 16 + tn3, lane);
    if (wid == 0) TS(9);
  }
  {
    f32x4 acc[28];
    Adr ad;
    ad.v[0] = lds0 + R4 + wm * 8192 + wn * 4096 + lane * 16; ad.v[1] = lds0 + R4 + wm * 8192 + (wn ^ 1) * 4096 + lane * 16;
    ad.v[2] = lds0 + R4 + 16384 + wlane(lane, wn); ad.v[3] = lds0 + R4 + 16384 + wlane(lane, 2 + wn);
    ad.v[4] = lds0 + R4 + 16384 + 6144 + hlane(lane, 0); ad.v[5] = lds0 + R4 + 16384 + 6144 + hlane(lane, 1);
    consume<C4, 4, RPG4, 0, 0>(acc, ad, DBG4, [&](int s) { if (s < 0 && wid == 0) TS(10); });
    if (wid == 0) TS(11);
    f32x4 cs[8];
#pragma unroll
    for (int n = 0; n < 6; ++n) cs[n] = *(const LAS f32x4*)(smem + S4_CSF + ((par + (n >> 1)) * 64 + (2 * (n & 1) + wn) * 16 + 4 * g) * 4);
#pragma unroll
    for (int n = 0; n < 2; ++n) cs[6 + n] = *(const LAS f32x4*)(smem + S4_CSF + ((par ? 32 : 192) + n * 16 + 4 * g) * 4);
    const f32x4 zero = {0.f, 0.f, 0.f, 0.f};
#pragma unroll
    for (int m = 0; m < 4; ++m) {
      const int mt = wm * 4 + ((m + 2 * wn) & 3);
      const f32x2 st = *(const LAS f32x2*)(smem + S4_STF + (mt * 16 + m16) * 8);
      const unsigned fd = WS_F + (tm1 * 56 * 16 + mt * 2) * 1024;
#pragma unroll
      for (int i = 0; i < 3; ++i)
        st16(y.r, pack8(__builtin_elementwise_max((acc[m * 6 + 2 * i] - cs[2 * i] * st[0]) * st[1], zero),
                        __builtin_elementwise_max((acc[m * 6 + 2 * i + 1] - cs[2 * i + 1] * st[0]) * st[1], zero)), lane * 16, fd + (ch0 + i) * 16384 + wn * 1024);
      if (m < 2)
        st16(y.r, pack8(__builtin_elementwise_max((acc[24 + 2 * m] - cs[6] * st[0]) * st[1], zero),
                        __builtin_elementwise_max((acc[24 + 2 * m + 1] - cs[7] * st[0]) * st[1], zero)), lane * 16, fd + (base7 + 3) * 16384 + par * 1024);
    }
    if (wid == 0) TS(28);
    publish(y, FL_F, tm1 * 16 + tn1, lane);
    if (wid == 0) TS(12);
  }
  {
    f32x4 acc[16];
    Adr ad;
    ad.v[0] = lds0 + R5 + wm * 8192 + kh * 4096 + lane * 16; ad.v[1] = lds0 + R5 + wm * 8192 + (kh ^ 1) * 4096 + lane * 16;
#pragma unroll
    for (int n = 0; n < 4; ++n) ad.v[2 + n] = lds0 + R5 + 16384 + wn * 2048 + wlane(lane, n);
    u32x4 hr[2][2];
    consume_s<C5, 5, 1>(acc, ad, [&](int s) {
      if (s < 0) {
        if (wid == 0) TS(13);
#pragma unroll
        for (int m = 0; m < 2; ++m)
#pragma unroll
          for (int Q = 0; Q < 2; ++Q) hr[m][Q] = ld16(y.r, lane * 16, WS_H + (((tm5 * 16 + 2 * tn5 + wn) * 8 + wm * 4 + 2 * kh + m) * 2 + Q) * 1024);
      }
    });
    if (wid == 0) TS(14);
    const int tile = tm5 * 8 + tn5;
#pragma unroll
    for (int i = 0; i < 4; ++i) st16(y.r, pack8(acc[8 + 2 * i], acc[9 + 2 * i]), lane * 16, WS_O + (((tile * 2 + kh) * 4 + wid) * 4 + i) * 1024);
    wait_vm<0>();
    if (lane == 0) st4(y.r, y.tag, 0, WS_FLG + ((FL_X + (wid >> 1)) * 256 + tile * 2 + kh) * 128 + (wid & 1) * 64);
    poll(y, ((FL_X + (wid >> 1)) * 256 + tile * 2 + (kh ^ 1)) * 128 + (wid & 1) * 64, 1, lane);
    if (wid == 0) TS(15);
    const rsrc_t ro = __builtin_amdgcn_make_buffer_rsrc(P.out, 0, 2048 * 2048, 0x00020000);
    u32x4 xv[4];
#pragma unroll
    for (int i = 0; i < 4; ++i) xv[i] = ld16(y.r, lane * 16, WS_O + (((tile * 2 + (kh ^ 1)) * 4 + wid) * 4 + i) * 1024);
#pragma unroll
    for (int m = 0; m < 2; ++m)
#pragma unroll
      for (int Q = 0; Q < 2; ++Q) {
        const u32x4 xx = xv[m * 2 + Q];
        const f32x4 a = acc[m * 4 + 2 * Q] + (f32x4){bflo(xx[0]), bfhi(xx[0]), bflo(xx[1]), bfhi(xx[1])}, b = acc[m * 4 + 2 * Q + 1] + (f32x4){bflo(xx[2]), bfhi(xx[2]), bflo(xx[3]), bfhi(xx[3])};
        const u32x4 hh = hr[m][Q];
        const unsigned a0 = pk2(a[0] + bflo(hh[0]), a[1] + bfhi(hh[0])), a1 = pk2(a[2] + bflo(hh[1]), a[3] + bfhi(hh[1]));
        const unsigned b0 = pk2(b[0] + bflo(hh[2]), b[1] + bfhi(hh[2])), b1 = pk2(b[2] + bflo(hh[3]), b[3] + bfhi(hh[3]));
        auto r0 = __builtin_amdgcn_permlane16_swap(a0, b0, false, false);
        auto r1 = __builtin_amdgcn_permlane16_swap(a1, b1, false, false);
        __builtin_amdgcn_raw_buffer_store_b128((u32x4){r0[0], r1[0], r0[1], r1[1]}, ro, (tm5 * 128 + wm * 64 + (2 * kh + m) * 16 + m16) * 2048 + tn5 * 256 + wn * 128 + Q * 64 + (g & 1) * 32 + (g >> 1) * 16, 0, 2);
      }
    if (bid == 0 && threadIdx.x == 0) st4(y.r, y.tag, 0, WS_CTL);
    if (wid == 0) { TS(24); TS_FLUSH(); }
  }
}

extern "C" void kernel_launch(void* const* d_in, const int* in_sizes, int n_in, void* d_out, int out_size, void* d_ws, size_t ws_size,
                              hipStream_t stream) {
  Params p;
  p.x = (const char*)d_in[0]; p.wqkv = (const char*)d_in[1]; p.wphi = (const char*)d_in[2]; p.wo = (const char*)d_in[3];
  p.w1 = (const char*)d_in[5]; p.w2 = (const char*)d_in[6]; p.ws = (char*)d_ws; p.out = (char*)d_out;
  (void)hipFuncSetAttribute((const void*)fused, hipFuncAttributeMaxDynamicSharedMemorySize, LDS_BYTES);
  fused<<<256, 512, LDS_BYTES, stream>>>(p);
}
```

```cpp
#include <hip/hip_runtime.h>
#include <hip/hip_bf16.h>

#define LAS __attribute__((address_space(3)))
#define DI __device__ __forceinline__
typedef __bf16 bf16x8 __attribute__((ext_vector_type(8)));
typedef __bf16 bf16x2 __attribute__((ext_vector_type(2)));
typedef short s16x4 __attribute__((ext_vector_type(4)));
typedef float f32x2 __attribute__((ext_vector_type(2)));
typedef float f32x4 __attribute__((ext_vector_type(4)));
typedef unsigned u32x2 __attribute__((ext_vector_type(2)));
typedef unsigned u32x4 __attribute__((ext_vector_type(4)));
typedef LAS char* lds_ptr;
typedef __amdgpu_buffer_rsrc_t rsrc_t;

constexpr int D = 1024, FF = 3584;
constexpr unsigned WS_CTL = 0;
constexpr unsigned WS_FLG = 4096;
constexpr unsigned WS_ST1 = 320u << 10;
constexpr unsigned WS_CSQ = 384u << 10;
constexpr unsigned WS_CS1 = 576u << 10;
constexpr unsigned WS_ZP = 704u << 10;
constexpr unsigned WS_ST2 = 832u << 10;
constexpr unsigned WS_SMALL = 1280u << 10;
constexpr unsigned WS_KVP = 2u << 20;
constexpr unsigned WS_O = 4u << 20;
constexpr unsigned WS_H = 8u << 20;
constexpr unsigned WS_F = 12u << 20;
constexpr unsigned WS_END = 26u << 20;
enum { FL_P0 = 0, FL_KV = 1, FL_O = 2, FL_H = 3, FL_F = 4, FL_CS = 5, FL_X = 6   };

#define TS(I) do {} while (0)
#define TS_FLUSH() do {} while (0)

#define DBG(k) nullptr
#define CLK(v)
#define DBG4 nullptr
struct Params { const char* x; const char* wqkv; const char* wphi; const char* wo; const char* w1; const char* w2; char* ws; char* out; };

DI unsigned pk2(float a, float b) { f32x2 v = {a, b}; return __builtin_bit_cast(unsigned, __builtin_convertvector(v, bf16x2)); }
DI float bflo(unsigned u) { return __uint_as_float(u << 16); }
DI float bfhi(unsigned u) { return __uint_as_float(u & 0xffff0000u); }
DI u32x4 pack8(const f32x4& a, const f32x4& b) { return (u32x4){pk2(a[0], a[1]), pk2(a[2], a[3]), pk2(b[0], b[1]), pk2(b[2], b[3])}; }
DI bf16x8 asbf(const u32x4& v) { return __builtin_bit_cast(bf16x8, v); }
#define SB() __builtin_amdgcn_sched_barrier(0)
DI void bar() { asm volatile("" ::: "memory"); __builtin_amdgcn_s_barrier(); asm volatile("" ::: "memory"); }
DI void wait_lgkm0() { __builtin_amdgcn_s_waitcnt(0xC07F); asm volatile("" ::: "memory"); }
template <int N> DI void wait_vm() { asm volatile("s_waitcnt vmcnt(%0)" ::"i"(N) : "memory"); }
template <bool SC1> DI void dma(unsigned voff, const char* sbase, unsigned lds) {
  if (SC1) asm volatile("s_mov_b32 m0, %2\n\ts_nop 3\n\tglobal_load_lds_dwordx4 %0, %1 sc1" ::"v"(voff), "s"(sbase), "s"(lds) : "memory");
  else asm volatile("s_mov_b32 m0, %2\n\ts_nop 3\n\tglobal_load_lds_dwordx4 %0, %1" ::"v"(voff), "s"(sbase), "s"(lds) : "memory");
}
template <bool SC1> DI void dma4(unsigned voff, const char* sbase, unsigned lds) {
  if (SC1) asm volatile("s_mov_b32 m0, %2\n\ts_nop 3\n\tglobal_load_lds_dword %0, %1 sc1" ::"v"(voff), "s"(sbase), "s"(lds) : "memory");
  else asm volatile("s_mov_b32 m0, %2\n\ts_nop 3\n\tglobal_load_lds_dword %0, %1" ::"v"(voff), "s"(sbase), "s"(lds) : "memory");
}
DI u32x4 lds128(lds_ptr p) { return *(const LAS u32x4*)p; }
DI u32x4 lds_tr2(lds_ptr p0, lds_ptr p1) {
  s16x4 a = __builtin_amdgcn_ds_read_tr16_b64_v4i16((LAS s16x4*)p0);
  s16x4 b = __builtin_amdgcn_ds_read_tr16_b64_v4i16((LAS s16x4*)p1);
  return __builtin_bit_cast(u32x4, __builtin_shufflevector(a, b, 0, 1, 2, 3, 4, 5, 6, 7));
}
DI u32x2 lds_tr(lds_ptr p) { return __builtin_bit_cast(u32x2, __builtin_amdgcn_ds_read_tr16_b64_v4i16((LAS s16x4*)p)); }
DI f32x4 mfma(const u32x4& a, const u32x4& b, const f32x4& c) { return __builtin_amdgcn_mfma_f32_16x16x32_bf16(asbf(a), asbf(b), c, 0, 0, 0); }
DI void mfma_ip(f32x4& acc, const u32x4& a, const u32x4& b) { asm volatile("v_mfma_f32_16x16x32_bf16 %0, %1, %2, %0" : "+v"(acc) : "v"(a), "v"(b)); }
DI u32x4 ld16(rsrc_t r, unsigned voff, unsigned soff) { return __builtin_amdgcn_raw_buffer_load_b128(r, voff, soff, 16); }
DI void st16(rsrc_t r, const u32x4& v, unsigned voff, unsigned soff) { __builtin_amdgcn_raw_buffer_store_b128(v, r, voff + soff, 0, 16);
}
DI void st8(rsrc_t r, const u32x2& v, unsigned voff, unsigned soff) { __builtin_amdgcn_raw_buffer_store_b64(v, r, voff, soff, 16); }
DI void st4(rsrc_t r, unsigned v, unsigned voff, unsigned soff) { __builtin_amdgcn_raw_buffer_store_b32(v, r, voff, soff, 16); }
DI float fb(unsigned u) { return __uint_as_float(u); }
DI unsigned bu(float f) { return __float_as_uint(f); }
DI float xsum16(float x) { auto r = __builtin_amdgcn_permlane16_swap(bu(x), bu(x), false, false); return fb(r[0]) + fb(r[1]); }
DI float xsum32(float x) { auto r = __builtin_amdgcn_permlane32_swap(bu(x), bu(x), false, false); return fb(r[0]) + fb(r[1]); }
template <int CTRL> DI float dpp(float x) { return __builtin_bit_cast(float, __builtin_amdgcn_update_dpp(0, __builtin_bit_cast(int, x), CTRL, 0xf, 0xf, true)); }
DI float row16_sum(float x) { x += dpp<0xB1>(x); x += dpp<0x4E>(x); x += dpp<0x141>(x); x += dpp<0x140>(x); return x; }

struct Sync { rsrc_t r; unsigned tag; LAS unsigned* w; };
DI void publish(const Sync& y, int phase, int idx, int lane) {
  if (lane == 0) __hip_atomic_fetch_add(y.w + 24 + phase, 1u, __ATOMIC_RELAXED, __HIP_MEMORY_SCOPE_WORKGROUP);
  wait_vm<0>();
  if (lane == 0 && __hip_atomic_fetch_add(y.w + phase, 1u, __ATOMIC_RELAXED, __HIP_MEMORY_SCOPE_WORKGROUP) == 3u) st4(y.r, y.tag, 0, WS_FLG + (phase * 256 + idx) * 128);
}
DI void poll(const Sync& y, unsigned off, int n, int lane) {
  const unsigned long long t0 = __builtin_amdgcn_s_memrealtime();
  for (unsigned it = 0;; ++it) {
    const unsigned v = __builtin_amdgcn_raw_buffer_load_b32(y.r, WS_FLG + off, 0, 16);
    if (__all(lane >= n || v == y.tag)) break;
    if ((it & 63) == 63) {
      if (__builtin_amdgcn_readfirstlane(__builtin_amdgcn_raw_buffer_load_b32(y.r, 0, WS_CTL + 128, 16)) == y.tag) break;
      if (__builtin_amdgcn_s_memrealtime() - t0 > 4000000ull) { if (lane == 0) st4(y.r, y.tag, 0, WS_CTL + 128); break; }
    }
    __builtin_amdgcn_s_sleep(2);
  }
  asm volatile("" ::: "memory");
}
DI void lds_wait(const Sync& y, int word, unsigned v) {
  volatile LAS unsigned* w = y.w + word;
  while (*w < v) __builtin_amdgcn_s_sleep(1);
  asm volatile("" ::: "memory");
}
struct Gate { unsigned off, mask; int word; };
DI void gate_slow(const Sync& y, Gate& G, unsigned need, bool poller, int lane, int cnt = 0) {
  volatile LAS unsigned* w = y.w + G.word;
  if (poller) {
    const unsigned long long t0 = __builtin_amdgcn_s_memrealtime();
    for (unsigned it = 0;; ++it) {
      G.mask = (unsigned)__ballot(__builtin_amdgcn_raw_buffer_load_b32(y.r, WS_FLG + G.off, 0, 16) == y.tag);
      if ((it & 63) == 63) {
        const bool late = __builtin_amdgcn_s_memrealtime() - t0 > 4000000ull;
        if (late && lane == 0) st4(y.r, y.tag, 0, WS_CTL + 128);
        if (late || __builtin_amdgcn_readfirstlane(__builtin_amdgcn_raw_buffer_load_b32(y.r, 0, WS_CTL + 128, 16)) == y.tag) G.mask = ~0u;
      }
      *w = G.mask;
      if ((G.mask & need) == need && __builtin_popcount(G.mask & 0xffffu) >= cnt) break;
      __builtin_amdgcn_s_sleep(1);
    }
  } else {
    for (;;) { G.mask = __builtin_amdgcn_readfirstlane(*w); if ((G.mask & need) == need && __builtin_popcount(G.mask & 0xffffu) >= cnt) break; __builtin_amdgcn_s_sleep(1); }
  }
  asm volatile("" ::: "memory");
}
DI void gate(const Sync& y, Gate& G, unsigned need, bool poller, int lane) { if (__builtin_expect((G.mask & need) != need, 0)) gate_slow(y, G, need, poller, lane); }

template <int NK> DI int kmap(int t, int c) {
  const int u = t - 1, q = u & 3, b4 = u & ~3, body = (int)(u < NK - 4);
  const int lead = ((int)(q == c) & body) | (int)(t == 0);
  const int non = b4 + ((c + 1 + q - ((int)(q > c) & body)) & 3);
  return non ^ (((b4 + 4 + c) ^ non) & -lead);
}
#define LAG3 1
#define LAG5 1
enum { ORD_NAT = 0, ORD_PAIR = 1, ORD_F = 2 };
template <int NSUB_, bool HALF_, int NSLOT_, int NK_, int ORD_, int WSTRIDE_, bool XROW_, int LAG_> struct Cfg {
  static constexpr int NSUB = NSUB_, NSLOT = NSLOT_, NK = NK_, ORD = ORD_, WSTRIDE = WSTRIDE_;
  static constexpr bool HALF = HALF_, XROW = XROW_;
  static constexpr int LAG = LAG_;
  static constexpr int STAGE = 16384 + NSUB * 8192 + (HALF ? 4096 : 0), PW = 4 + 2 * NSUB + (HALF ? 1 : 0), RING = STAGE * NSLOT;
  static constexpr int WB = NSUB * 2048 + (HALF ? 1024 : 0);
};
typedef Cfg<3, false, 3, 16, ORD_NAT, 3 * D * 2, true, 0> C1;
typedef Cfg<1, false, 5, 16, ORD_PAIR, D * 2, false, LAG3> C3;
typedef Cfg<3, true, 3, 16, ORD_PAIR, FF * 2, false, 0> C4;
typedef Cfg<2, false, 4, 28, ORD_F, D * 2, false, LAG5> C5;

#define GLDS "global_load_lds_dwordx4 "
template <class C> struct Ld {
  const char* A; const char* W; int lw, c, kt0; unsigned vA[4], vW[7];
  const char* sA; const char* sW; unsigned need;
  Gate G; const Sync* y; int lane_;
  static constexpr int C0 = C::WB > 4096 ? 4096 : 0;
  DI void init(const Sync& y_, int lane, int substep, int halfrel, unsigned goff, int gword) {
    y = &y_; lane_ = lane; G.off = goff * 128; G.mask = 0; G.word = gword;
#pragma unroll
    for (int i = 0; i < 4; ++i) vA[i] = C::XROW ? (lane >> 3) * 2048 + ((((lane & 7) ^ (lane >> 4)) << 4) ^ ((i & 1) * 64)) + i * (16384 - 1024) : lane * 16;
    const int rl = (C::ORD == ORD_NAT) ? 8 * (lane >> 5) + ((lane >> 3) & 3) : (lane >> 3);
    const unsigned w0 = rl * C::WSTRIDE + (((lane & 7) ^ (((lane >> 4) & 3) << 1)) << 4) + 4096;
#pragma unroll
    for (int q = 0; q < 2 * C::NSUB; ++q) vW[q] = w0 + (q & 1) * ((C::ORD == ORD_NAT ? 16 : 8) * C::WSTRIDE) + (q >> 1) * substep - (q * 1024 - C0);
    if (C::HALF) vW[6] = (lane >> 2) * C::WSTRIDE + (((lane & 3) ^ (((lane >> 4) & 1) << 1)) << 4) + 4096 + halfrel - (6144 - C0);
    A += C::XROW ? lw * 32 * 2048 : lw * 4096;
    W -= 4096;
  }
  DI void prep(int s) {
    const int kt = kt0 + kmap<C::NK>(s, c), kb = lw >> 1, t = lw & 1;
    int ur;
    if (C::ORD == ORD_NAT) ur = 32 * kb + 4 * t;
    else if (C::ORD == ORD_F) { const int sp = -(int)((0x10204081020408ull >> kt) & 1u); ur = (16 * lw & sp) | ((16 * kb + 32 * t) & ~sp); }
    else ur = 16 * lw;
    if (C::ORD == ORD_F) { const int jj = (kt * 37) >> 8, r = kt - 7 * jj; need = (unsigned)((int)(r <= 3) | ((int)(r >= 3) << 1)) << ((2 * jj) & 7); }
    else need = 1u << kt;
    sA = A + (unsigned)(kt * (C::XROW ? 128 : 16384));
    sW = W + (unsigned)((kt * 64 + ur) * C::WSTRIDE);
  }
  DI void issueA(unsigned base) {
    if (!C::XROW) gate(*y, G, need, lw == 0, lane_);
    const unsigned m = base + lw * 4096;
    if (C::XROW) asm volatile("s_mov_b32 m0, %5\n\ts_nop 3\n\t" GLDS "%0, %4\n\t" GLDS "%1, %4 offset:1024\n\t" GLDS "%2, %4 offset:2048\n\t" GLDS "%3, %4 offset:3072"
                              ::"v"(vA[0]), "v"(vA[1]), "v"(vA[2]), "v"(vA[3]), "s"(sA), "s"(m) : "memory");
    else asm volatile("s_mov_b32 m0, %2\n\ts_nop 3\n\t" GLDS "%0, %1 sc1\n\t" GLDS "%0, %1 offset:1024 sc1\n\t" GLDS "%0, %1 offset:2048 sc1\n\t" GLDS "%0, %1 offset:3072 sc1"
                      ::"v"(vA[0]), "s"(sA), "s"(m) : "memory");
  }
  DI void issueW(unsigned base) const {
    const unsigned m = base + 16384 + lw * C::WB + C0;
    if (C::NSUB == 1) asm volatile("s_mov_b32 m0, %3\n\ts_nop 3\n\t" GLDS "%0, %2\n\t" GLDS "%1, %2 offset:1024" ::"v"(vW[0]), "v"(vW[1]), "s"(sW), "s"(m) : "memory");
    if (C::NSUB == 2) asm volatile("s_mov_b32 m0, %5\n\ts_nop 3\n\t" GLDS "%0, %4\n\t" GLDS "%1, %4 offset:1024\n\t" GLDS "%2, %4 offset:2048\n\t" GLDS "%3, %4 offset:3072"
                                   ::"v"(vW[0]), "v"(vW[1]), "v"(vW[2]), "v"(vW[3]), "s"(sW), "s"(m) : "memory");
    if (C::NSUB == 3) asm volatile("s_mov_b32 m0, %7\n\ts_nop 3\n\t" GLDS "%0, %6 offset:-4096\n\t" GLDS "%1, %6 offset:-3072\n\t" GLDS "%2, %6 offset:-2048\n\t" GLDS "%3, %6 offset:-1024\n\t"
                                   GLDS "%4, %6\n\t" GLDS "%5, %6 offset:1024"
                                   ::"v"(vW[0]), "v"(vW[1]), "v"(vW[2]), "v"(vW[3]), "v"(vW[4]), "v"(vW[5]), "s"(sW), "s"(m) : "memory");
    if (C::HALF) asm volatile(GLDS "%0, %1 offset:2048" ::"v"(vW[6]), "s"(sW) : "memory");
  }
};
template <class C> DI void load_preW(Ld<C>& L, unsigned ring) {
#pragma unroll 1
  for (int s = 0; s < C::NSLOT; ++s) { L.prep(s); L.issueW(ring + s * C::STAGE); }
}
template <class C, bool SPLIT> constexpr int vm_allow(int S) {
  const int N = C::NSLOT, behind = S - 1 - C::LAG > 0 ? S - 1 - C::LAG : 0;
  const int last = N - 1 + behind < C::NK - 1 ? N - 1 + behind : C::NK - 1;
  return (SPLIT && S < N) ? 4 * (N - 1 - S) + C::PW * (last - (N - 1)) : C::PW * (last - S);
}
template <class C, bool SPLIT, unsigned POSTM, int S, int SE, class Post> DI void load_fixed(Ld<C>& L, unsigned ring, const Post& post) {
  if constexpr (S < SE) {
    wait_vm<vm_allow<C, SPLIT>(S)>();
    bar();
    constexpr int T = S - 1 - C::LAG + C::NSLOT;
    if constexpr (S - 1 - C::LAG >= 0 && T < C::NK) {
      L.issueW(ring + (T % C::NSLOT) * C::STAGE); L.issueA(ring + (T % C::NSLOT) * C::STAGE);
      if constexpr (T + 1 < C::NK) L.prep(T + 1);
    }
    if constexpr ((POSTM >> S) & 1u) post(S);
    load_fixed<C, SPLIT, POSTM, S + 1, SE>(L, ring, post);
  }
}
template <class C, bool SPLIT, unsigned PREM, unsigned POSTM, class Pre, class Post>
DI void load_all(Ld<C>& L, unsigned ring, const Pre& pre, const Post& post, unsigned long long* dbg = nullptr) {
  constexpr int N = C::NSLOT, S0 = (SPLIT && N - 1 > 1 + C::LAG) ? N - 1 : 1 + C::LAG + (C::LAG ? 1 : 0), S1 = C::NK - N + C::LAG;
#pragma unroll 1
  for (int s = 0; s < N; ++s) { L.prep(s); if (!SPLIT) L.issueW(ring + s * C::STAGE); L.issueA(ring + s * C::STAGE); }
  L.prep(N);
  load_fixed<C, SPLIT, POSTM, 0, S0>(L, ring, post);
  static_assert(vm_allow<C, SPLIT>(S0) == (N - 2 - C::LAG) * C::PW && vm_allow<C, SPLIT>(S1) == (N - 2 - C::LAG) * C::PW, "steady count");
  unsigned b = ring + ((S0 - 1 - C::LAG) % N) * C::STAGE;
#pragma unroll 1
  for (int s = S0; s <= S1; ++s) {
    CLK(t0);
    wait_vm<(N - 2 - C::LAG) * C::PW>();
    if (PREM && ((PREM >> s) & 1u)) pre(s);
    CLK(t1);
    bar();
    CLK(t2);
    L.issueW(b);
    L.issueA(b);
    CLK(t3);
    L.prep(min(s - C::LAG + N, C::NK - 1));
    b = (b == ring + (N - 1) * C::STAGE) ? ring : b + C::STAGE;
    if (POSTM && ((POSTM >> s) & 1u)) post(s);
  }
  load_fixed<C, SPLIT, POSTM, S1 + 1, C::NK>(L, ring, post);
  bar();
}

template <int MT, int NWF> struct Fr { u32x4 a[MT]; u32x2 w[NWF][2]; };
DI u32x4 cat(const u32x2 (&w)[2]) { return (u32x4){w[0][0], w[0][1], w[1][0], w[1][1]}; }
struct Adr { unsigned v[6]; };
template <int KIND> struct Shape;
template <> struct Shape<1> { static constexpr int MT = 4, NWF = 6, NMF = 24, NACC = 24, WB = 6144; static constexpr unsigned USE = 0x0f; };
template <> struct Shape<3> { static constexpr int MT = 2, NWF = 4, NMF = 8, NACC = 8, WB = 2048; static constexpr unsigned USE = 0x3d; };
template <> struct Shape<4> { static constexpr int MT = 4, NWF = 8, NMF = 28, NACC = 28, WB = 7168; static constexpr unsigned USE = 0x3f; };
template <> struct Shape<5> { static constexpr int MT = 4, NWF = 4, NMF = 16, NACC = 16, WB = 4096; static constexpr unsigned USE = 0x3f; };
DI lds_ptr lp(unsigned a) { return (lds_ptr)(size_t)a; }
template <int KIND, int J> DI void rd1(Fr<Shape<KIND>::MT, Shape<KIND>::NWF>& f, const Adr& ad, int kb) {
  constexpr int NWF = Shape<KIND>::NWF, MT = Shape<KIND>::MT, WB = Shape<KIND>::WB;
  if constexpr (J < 2 * NWF) {
    constexpr int n = J >> 1, t = J & 1;
    if constexpr (KIND == 4 && n >= 6) f.w[n][t] = lds_tr(lp(ad.v[4 + (n - 6)]) + (2 * kb + t) * WB);
    else if constexpr (KIND == 1 || KIND == 4) f.w[n][t] = lds_tr(lp(ad.v[2 + (n & 1)]) + (n >> 1) * 2048 + (2 * kb + t) * WB);
    else f.w[n][t] = lds_tr(lp(ad.v[2 + n]) + (2 * kb + t) * WB);
  } else if constexpr (J < 2 * NWF + MT) {
    constexpr int m = J - 2 * NWF;
    if constexpr (KIND == 1) f.a[m] = lds128(lp(ad.v[kb]) + m * 2048);
    else f.a[m] = lds128(lp(ad.v[m >> 1]) + (m & 1) * 2048 + kb * 1024);
  }
}
template <int KIND, int I> DI void mm1(f32x4 (&acc)[Shape<KIND>::NACC], const Fr<Shape<KIND>::MT, Shape<KIND>::NWF>& f) {
  if constexpr (KIND == 1) { constexpr int m = I / 6, n = I % 6; if constexpr (n < 4) mfma_ip(acc[I], cat(f.w[n]), f.a[m]); else mfma_ip(acc[I], f.a[m], cat(f.w[n])); }
  else if constexpr (KIND == 4) { if constexpr (I < 24) mfma_ip(acc[I], cat(f.w[I % 6]), f.a[I / 6]); else mfma_ip(acc[I], cat(f.w[6 + ((I - 24) & 1)]), f.a[(I - 24) >> 1]); }
  else mfma_ip(acc[I], cat(f.w[I % 4]), f.a[I / 4]);
}
template <int KIND, int RPG, bool READ, bool MOVE, int I = 0>
DI void step(f32x4 (&acc)[Shape<KIND>::NACC], const Fr<Shape<KIND>::MT, Shape<KIND>::NWF>& cur, Fr<Shape<KIND>::MT, Shape<KIND>::NWF>& nxt, Adr& ad, int kb, int delta) {
  constexpr int NMF = Shape<KIND>::NMF, G0 = (2 * Shape<KIND>::NWF + Shape<KIND>::MT + RPG - 1) / RPG;
  if constexpr (I < NMF) {
    if constexpr (READ) {
      rd1<KIND, I * RPG>(nxt, ad, kb);
      if constexpr (RPG > 1) rd1<KIND, I * RPG + 1>(nxt, ad, kb);
    }
    if constexpr (MOVE && I >= G0) {
      constexpr int per = (6 + (NMF - G0) - 1) / (NMF - G0);
#pragma unroll
      for (int k = (I - G0) * per; k < (I - G0 + 1) * per && k < 6; ++k)
        if ((Shape<KIND>::USE >> k) & 1u) { ad.v[k] += delta; asm volatile("" : "+v"(ad.v[k])); }
    }
    mm1<KIND, I>(acc, cur);
    SB();
    step<KIND, RPG, READ, MOVE, I + 1>(acc, cur, nxt, ad, kb, delta);
  }
}
template <int KIND, int J = 0> DI void rdall(Fr<Shape<KIND>::MT, Shape<KIND>::NWF>& f, const Adr& ad, int kb) {
  if constexpr (J < 2 * Shape<KIND>::NWF + Shape<KIND>::MT) { rd1<KIND, J>(f, ad, kb); rdall<KIND, J + 1>(f, ad, kb); }
}
#define ST4() do {} while (0)
#define ST4ARGS
#define ST4PASS
template <class C, int KIND, int RPG, int CNT>
DI void stages(f32x4 (&acc)[Shape<KIND>::NACC], Fr<Shape<KIND>::MT, Shape<KIND>::NWF>& f0, Fr<Shape<KIND>::MT, Shape<KIND>::NWF>& f1, Adr& ad, int& slot ST4ARGS) {
#pragma unroll 1
  for (int s = 0; s < CNT; ++s) {
    const int delta = (slot + 1 == C::NSLOT) ? -(C::NSLOT - 1) * C::STAGE : C::STAGE;
    slot = (slot + 1 == C::NSLOT) ? 0 : slot + 1;
    SB();
    step<KIND, RPG, true, true>(acc, f0, f1, ad, 1, delta);
    if (!C::LAG) wait_lgkm0();
    ST4();
    bar();
    ST4();
    SB();
    step<KIND, RPG, true, false>(acc, f1, f0, ad, 0, 0);
  }
}
template <class C, int KIND, int RPG, int H1, int H2, class Hook> DI void consume(f32x4 (&acc)[Shape<KIND>::NACC], Adr& ad, unsigned* dbg4, const Hook& hook) {
  Fr<Shape<KIND>::MT, Shape<KIND>::NWF> f0, f1;
#pragma unroll
  for (int i = 0; i < Shape<KIND>::NACC; ++i) acc[i] = (f32x4){0.f, 0.f, 0.f, 0.f};
  bar();
  rdall<KIND>(f0, ad, 0);
  hook(-1);
  wait_lgkm0();
  int slot = 0;
  if constexpr (H1 > 0) {
    stages<C, KIND, RPG, H1>(acc, f0, f1, ad, slot ST4PASS);
    hook(1);
    stages<C, KIND, RPG, H2 - H1>(acc, f0, f1, ad, slot ST4PASS);
    hook(2);
  }
  stages<C, KIND, RPG, C::NK - 1 - H2>(acc, f0, f1, ad, slot ST4PASS);
  SB();
  step<KIND, RPG, true, false>(acc, f0, f1, ad, 1, 0);
  wait_lgkm0();
  bar();
  SB();
  step<KIND, RPG, false, false>(acc, f1, f0, ad, 0, 0);
  asm volatile("s_nop 7\n\ts_nop 3");
  SB();
}
template <class C, int KIND, int RPG, class Hook> DI void consume_s(f32x4 (&acc)[Shape<KIND>::NACC], Adr& ad, const Hook& hook) {
  Fr<Shape<KIND>::MT, Shape<KIND>::NWF> f0, f1, g0, g1;
#pragma unroll
  for (int i = 0; i < Shape<KIND>::NACC; ++i) acc[i] = (f32x4){0.f, 0.f, 0.f, 0.f};
  bar();
  rdall<KIND>(f0, ad, 0); rdall<KIND>(f1, ad, 1);
  hook(-1);
#pragma unroll
  for (int k = 0; k < 6; ++k) ad.v[k] += C::STAGE;
  int slot = 1;
#pragma unroll 1
  for (int s = 0; s < C::NK - 2; s += 2) {
    int delta = (slot + 1 == C::NSLOT) ? -(C::NSLOT - 1) * C::STAGE : C::STAGE;
    slot = (slot + 1 == C::NSLOT) ? 0 : slot + 1;
    if (!C::LAG) wait_lgkm0();
    bar(); SB();
    step<KIND, RPG, true, false>(acc, f0, g0, ad, 0, 0);
    step<KIND, RPG, true, true>(acc, f1, g1, ad, 1, delta);
    delta = (slot + 1 == C::NSLOT) ? -(C::NSLOT - 1) * C::STAGE : C::STAGE;
    slot = (slot + 1 == C::NSLOT) ? 0 : slot + 1;
    if (!C::LAG) wait_lgkm0();
    bar(); SB();
    step<KIND, RPG, true, false>(acc, g0, f0, ad, 0, 0);
    step<KIND, RPG, true, true>(acc, g1, f1, ad, 1, delta);
  }
  if (!C::LAG) wait_lgkm0();
  bar(); SB();
  step<KIND, RPG, true, false>(acc, f0, g0, ad, 0, 0);
  step<KIND, RPG, true, false>(acc, f1, g1, ad, 1, 0);
  wait_lgkm0(); bar(); SB();
  step<KIND, RPG, false, false>(acc, g0, f0, ad, 0, 0);
  step<KIND, RPG, false, false>(acc, g1, f1, ad, 1, 0);
  asm volatile("s_nop 7\n\ts_nop 3");
  SB();
}
DI unsigned wlane(int lane, int nt) {
  const int g = lane >> 4, q = (lane >> 2) & 3, p = lane & 3, sw = (2 * g + (q >> 1)) & 3;
  return (4 * g + q) * 128 + ((nt ^ sw) << 5) + p * 8;
}
DI unsigned hlane(int lane, int nt) {
  const int g = lane >> 4, q = (lane >> 2) & 3, p = lane & 3;
  return (4 * g + q) * 64 + ((nt ^ (g & 1)) << 5) + p * 8;
}

template <int OP> DI float op2(float a, float b) { return OP == 0 ? a + b : OP == 1 ? __builtin_elementwise_maximum(a, b) : __builtin_elementwise_minimum(a, b); }
template <int OP> DI float red4(float x0, float x1, float x2, float x3) {
  auto a = __builtin_amdgcn_permlane16_swap(bu(x0), bu(x1), false, false);
  auto b = __builtin_amdgcn_permlane16_swap(bu(x2), bu(x3), false, false);
  const float y0 = op2<OP>(fb(a[0]), fb(a[1])), y1 = op2<OP>(fb(b[0]), fb(b[1]));
  auto c = __builtin_amdgcn_permlane32_swap(bu(y0), bu(y1), false, false);
  return op2<OP>(fb(c[0]), fb(c[1]));
}
DI void spread(float z, float (&o)[4]) {
  auto a = __builtin_amdgcn_permlane32_swap(bu(z), bu(z), false, false);
  auto b = __builtin_amdgcn_permlane16_swap(a[0], a[0], false, false);
  auto c = __builtin_amdgcn_permlane16_swap(a[1], a[1], false, false);
  o[0] = fb(b[0]); o[1] = fb(b[1]); o[2] = fb(c[0]); o[3] = fb(c[1]);
}
DI void dual_softmax4(const f32x4 (&p)[4][2], u32x4 (&pp)[4], u32x4 (&pn)[4]) {
  constexpr float L2E = 1.4426950408889634f;
  float mx[4], mn[4];
#pragma unroll
  for (int m = 0; m < 4; ++m) {
    mx[m] = op2<1>(op2<1>(op2<1>(op2<1>(p[m][0][0], p[m][0][1]), p[m][0][2]), op2<1>(op2<1>(p[m][0][3], p[m][1][0]), p[m][1][1])), op2<1>(p[m][1][2], p[m][1][3]));
    mn[m] = op2<2>(op2<2>(op2<2>(op2<2>(p[m][0][0], p[m][0][1]), p[m][0][2]), op2<2>(op2<2>(p[m][0][3], p[m][1][0]), p[m][1][1])), op2<2>(p[m][1][2], p[m][1][3]));
  }
  spread(-L2E * red4<1>(mx[0], mx[1], mx[2], mx[3]), mx);
  spread(L2E * red4<2>(mn[0], mn[1], mn[2], mn[3]), mn);
  f32x4 ep[4][2], en[4][2];
  float sp[4], sn[4];
#pragma unroll
  for (int m = 0; m < 4; ++m) {
#pragma unroll
    for (int F = 0; F < 2; ++F) {
      const f32x4 a = p[m][F] * L2E + mx[m], b = p[m][F] * (-L2E) + mn[m];
#pragma unroll
      for (int r = 0; r < 4; ++r) { ep[m][F][r] = __builtin_amdgcn_exp2f(a[r]); en[m][F][r] = __builtin_amdgcn_exp2f(b[r]); }
    }
    const f32x4 a = ep[m][0] + ep[m][1], b = en[m][0] + en[m][1];
    sp[m] = (a[0] + a[1]) + (a[2] + a[3]); sn[m] = (b[0] + b[1]) + (b[2] + b[3]);
  }
  spread(__builtin_amdgcn_rcpf(red4<0>(sp[0], sp[1], sp[2], sp[3])), sp);
  spread(__builtin_amdgcn_rcpf(red4<0>(sn[0], sn[1], sn[2], sn[3])), sn);
#pragma unroll
  for (int m = 0; m < 4; ++m) { pp[m] = pack8(ep[m][0] * sp[m], ep[m][1] * sp[m]); pn[m] = pack8(en[m][0] * sn[m], en[m][1] * sn[m]); }
}

DI float dot2(unsigned a, unsigned b, float c) { return __builtin_amdgcn_fdot2_f32_bf16(__builtin_bit_cast(bf16x2, a), __builtin_bit_cast(bf16x2, b), c, false); }
DI unsigned opaque(unsigned c) { asm volatile("" : "+s"(c)); return c; }
template <int NL> DI void colsum_load(u32x4 (&v)[NL], const char* w, int stride, int lane) {
#pragma unroll
  for (int i = 0; i < NL; ++i) v[i] = *(const u32x4*)(w + (size_t)(8 * i + (lane >> 3)) * stride + (lane & 7) * 16);
}
template <int NL> DI void colsum_finish(const u32x4 (&v)[NL], rsrc_t r, unsigned dst, int lane) {
  f32x2 s2[4];
#pragma unroll
  for (int j = 0; j < 4; ++j) s2[j] = (f32x2){bflo(v[0][j]), bfhi(v[0][j])};
#pragma unroll
  for (int i = 1; i < NL; ++i)
#pragma unroll
    for (int j = 0; j < 4; ++j) s2[j] += (f32x2){bflo(v[i][j]), bfhi(v[i][j])};
  const float s[8] = {s2[0][0], s2[0][1], s2[1][0], s2[1][1], s2[2][0], s2[2][1], s2[3][0], s2[3][1]};
  float t[4], u[2];
#pragma unroll
  for (int j = 0; j < 4; ++j) { auto q = __builtin_amdgcn_permlane32_swap(bu(s[j]), bu(s[j + 4]), false, false); t[j] = fb(q[0]) + fb(q[1]); }
#pragma unroll
  for (int j = 0; j < 2; ++j) { auto q = __builtin_amdgcn_permlane16_swap(bu(t[j]), bu(t[j + 2]), false, false); u[j] = fb(q[0]) + fb(q[1]); }
  const bool b3 = lane & 8;
  const float tot = (b3 ? u[1] : u[0]) + dpp<0x128>(b3 ? u[0] : u[1]);
  st4(r, bu(tot), ((lane & 7) * 8 + ((lane >> 5) & 1) * 4 + ((lane >> 4) & 1) * 2 + ((lane >> 3) & 1)) * 4, dst);
}

template <int XN, int TM, int TN> DI void tile_of(int bid, int& tm, int& tn, int& c) {
  const int x = bid & 7, r = bid >> 3, li = r % TM, lj = r / TM;
  tm = (x / XN) * TM + li; tn = (x % XN) * TN + lj; c = (li + lj) & 3;
}

#define RPG1 1
#define RPG4 1
#define RPG5 1
constexpr int R1 = 40704, R3 = 40704, R4 = 0, R5 = 0, SYNCW = 163584;
constexpr int S1_ST = 0, S1_CSR = 1024, S1_CSF = 13312, S1_FLG = 14080, S1_WP = 14336, S1_KV = 18432, S1_DUM = 26880;
constexpr int S4_STR = C4::RING, S4_CSR = S4_STR + 16384, S4_STF = S4_CSR + 8192, S4_CSF = S4_STF + 1024, S4_DUM = S4_CSF + 1024;
constexpr int LDS_BYTES = 163840, CODE_BYTES = 48 * 1024;
static_assert(S4_DUM + 256 <= SYNCW && S1_KV + 8448 <= R1 && R1 + C1::RING <= SYNCW && R3 + C3::RING <= SYNCW && C5::RING <= S4_STR, "lds");

DI void kv_sum(const Sync& y, Gate& G, lds_ptr smem, int head, int h, int t, bool poller, bool doz, int lane) {
  u32x4 v[16], zz[4];
  unsigned have = 0;
  f32x2 s2[4] = {{0.f, 0.f}, {0.f, 0.f}, {0.f, 0.f}, {0.f, 0.f}};
#pragma unroll 1
  for (int round = 0; round < 3; ++round) {
    gate_slow(y, G, 0u, poller, lane, round == 0 ? 0 : round == 1 ? 13 : 16);
    const unsigned fresh = G.mask & ~have;
    have = G.mask;
#pragma unroll
    for (int r = 0; r < 16; ++r) if ((fresh >> r) & 1u) v[r] = ld16(y.r, t * 16, WS_KVP + (head * 16 + r) * 4096);
    if (round == 2 && doz) {
#pragma unroll
      for (int i = 0; i < 4; ++i) zz[i] = ld16(y.r, lane * 16, WS_ZP + head * 4096 + i * 1024);
    }
  }
#pragma unroll
  for (int r = 0; r < 16; ++r)
#pragma unroll
    for (int j = 0; j < 4; ++j) s2[j] += (f32x2){bflo(v[r][j]), bfhi(v[r][j])};
  const float s[8] = {s2[0][0], s2[0][1], s2[1][0], s2[1][1], s2[2][0], s2[2][1], s2[3][0], s2[3][1]};
  *(LAS u32x4*)(smem + S1_KV + h * 4096 + t * 16) = (u32x4){pk2(s[0], s[1]), pk2(s[2], s[3]), pk2(s[4], s[5]), pk2(s[6], s[7])};
  if (doz) {
    f32x4 z = (__builtin_bit_cast(f32x4, zz[0]) + __builtin_bit_cast(f32x4, zz[1])) + (__builtin_bit_cast(f32x4, zz[2]) + __builtin_bit_cast(f32x4, zz[3]));
#pragma unroll
    for (int j = 0; j < 4; ++j) z[j] = xsum32(xsum16(z[j]));
    if (lane < 16) *(LAS u32x2*)(smem + S1_KV + 8192 + h * 128 + lane * 8) = (u32x2){pk2(z[0], z[1]), pk2(z[2], z[3])};
  }
}

__global__ __launch_bounds__(512, 2) void fused(Params P) {
  extern __shared__ __attribute__((aligned(16))) char smem_[];
  lds_ptr smem = (lds_ptr)smem_;
  const unsigned lds0 = (unsigned)(size_t)smem;
  const int wid = __builtin_amdgcn_readfirstlane(threadIdx.x >> 6), lane = threadIdx.x & 63, bid = blockIdx.x;
  const unsigned long long tstart = __builtin_amdgcn_s_memrealtime();
  int tm1, tn1, c1, tm3, tn3, c3;
  tile_of<4, 8, 4>(bid, tm1, tn1, c1);
  tile_of<2, 4, 8>(bid, tm3, tn3, c3);
  const int kh = bid & 1, tm5 = ((bid & 7) >> 1) * 4 + ((bid >> 3) & 3), tn5 = bid >> 5, c5 = ((bid >> 3) + tn5) & 3;
  const int par = tn1 & 1, base7 = 7 * (tn1 >> 1), ch0 = base7 + 4 * par;
  const int g = lane >> 4, m16 = lane & 15;
  Sync y;
  y.r = __builtin_amdgcn_make_buffer_rsrc(P.ws, 0, WS_END, 0x00020000);
  y.w = (LAS unsigned*)(smem + SYNCW);
  if (wid == 0) TS(0);

  if (wid >= 4) {
    const int lw = wid - 4, lt = threadIdx.x - 256;
    const auto nop = [](int) {};
    {
      Ld<C1> L; L.A = P.x + (size_t)tm1 * 128 * 2048; L.W = P.wqkv + tn1 * 128; L.lw = lw; L.c = c1; L.kt0 = 0; L.init(y, lane, 2048, 0, 0, 0);
      const unsigned frec = (FL_P0 * 256 + (lane < 16 ? tm1 * 16 + lane : lane < 28 ? ((lane - 16) / 3) * 48 + ((lane - 16) % 3) * 16 + tn1 : tn1)) * 128;
      if (lw == 0) dma4<true>(0u * lane, P.ws + WS_CTL, lds0 + S1_FLG);
      __builtin_amdgcn_s_setprio(3);
      load_all<C1, false, 1u << 11, 0x4905u>(L, lds0 + R1,
        [&](int s) {
          if (lw == 0) {
            const u32x4 v = *(volatile LAS u32x4*)(smem + S1_CSR + lane * 16);
            if (!__all(v[0] == y.tag && v[1] == y.tag && v[2] == y.tag && v[3] == y.tag)) {
#pragma unroll 1
              for (int k = 0; k < 4; ++k) poll(y, frec + 4 * k, 64, lane);
            }
          }
        },
        [&](int s) {
          if (s == 0) y.tag = __builtin_amdgcn_readfirstlane(*(volatile LAS unsigned*)(smem + S1_FLG)) + 1u;
          if (s == 2) dma<false>(lane * 16, P.wphi + tn1 * 4096 + lw * 1024, lds0 + S1_WP + lw * 1024);
          if (s == 8 && lw == 0) dma<true>(frec, P.ws + WS_FLG, lds0 + S1_CSR);
          if (s == 11) {
            if (lw == 3) dma<true>(lane * 16, P.ws + WS_ST1 + tm1 * 1024, lds0 + S1_ST);
#pragma unroll
            for (int i = 0; i < 3; ++i)
              dma<true>((lane >> 4) * 12288 + (lane & 15) * 16, P.ws + WS_CSQ + lw * 49152 + i * 4096 + tn1 * 256, lds0 + S1_CSR + i * 4096 + lw * 1024);
          }
          if (s == 14) {
            if (lt < 192) {
              const LAS float* rawp = (const LAS float*)(smem + S1_CSR) + (lt >> 6) * 1024 + (lt & 63);
              float t = 0.f;
#pragma unroll
              for (int i = 0; i < 16; ++i) t += rawp[i * 64];
              ((LAS float*)(smem + S1_CSF))[lt] = t;
            }
            wait_lgkm0();
          }
        }, DBG(0));
    }
    if (wid == 4) TS(16);
    {
      Ld<C3> L; L.A = P.ws + WS_O + (size_t)tm3 * 16 * 16384; L.W = P.wo + tn3 * 128; L.lw = lw; L.c = c3; L.kt0 = 0; L.init(y, lane, 0, 0, FL_O * 256 + tm3 * 16 + m16, 9);
      load_preW<C3>(L, lds0 + R3);
      if (lw >= 2 && bid < 224) {
        u32x4 v[16];
        colsum_load<16>(v, P.w1 + (size_t)((bid / 56) * 256 + (lw - 2) * 128) * (FF * 2) + (bid % 56) * 128, FF * 2, lane);
        colsum_finish<16>(v, y.r, WS_CS1 + (((bid / 56) * 2 + lw - 2) * FF + (bid % 56) * 64) * 4, lane);
      }
      publish(y, FL_CS, bid, lane);
      lds_wait(y, 24 + FL_KV, 4u);
      Gate G; G.off = (FL_KV * 256 + m16 * 16 + tn1) * 128; G.mask = 0; G.word = 8;
      kv_sum(y, G, smem, 2 * tn1 + 1, 1, lt, lw == 0, lw == 1, lane);
      if (wid == 4) TS(17);
      wait_lgkm0(); bar();
      if (wid == 4) TS(18);
      load_all<C3, true, 0u, 0u>(L, lds0 + R3, nop, nop, DBG(1));
    }
    if (wid == 4) TS(19);
    {
      Ld<C4> L; L.A = P.ws + WS_H + (size_t)tm1 * 16 * 16384; L.W = P.w1 + ch0 * 128; L.lw = lw; L.c = c1; L.kt0 = 0;
      L.init(y, lane, 128, par ? -64 : 384, (lane & 16) ? FL_CS * 256 + (m16 >> 2) * 56 + base7 + 3 * par + (lane & 3) : FL_H * 256 + tm1 * 16 + m16, 10);
      lds_wait(y, 24 + FL_H, 4u);
      load_preW<C4>(L, lds0 + R4);
      if (wid == 4) TS(20);
      load_all<C4, true, 0u, 0xa000u>(L, lds0 + R4, nop,
        [&](int s) {
          if (s == 13) {
            gate(y, L.G, ~0u, lw == 0, lane);
#pragma unroll
            for (int i = 0; i < 4; ++i) dma<true>(lane * 16, P.ws + WS_ST2 + (tm1 * 16 + lw * 4 + i) * 1024, lds0 + S4_STR + (lw * 4 + i) * 1024);
#pragma unroll
            for (int i = 0; i < 2; ++i) dma<true>(lane * 16, P.ws + WS_CS1 + ((lw * 2 + i) * FF + (base7 + 3 * par) * 64) * 4, lds0 + S4_CSR + (lw * 2 + i) * 1024);
          }
          if (s == 15) {
            const LAS float* rawp = (const LAS float*)(smem + S4_CSR) + lt;
            float t = 0.f;
#pragma unroll
            for (int i = 0; i < 8; ++i) t += rawp[i * 256];
            ((LAS float*)(smem + S4_CSF))[lt] = t;
            wait_lgkm0();
          }
          if (s == 15 && lw < 2) {
            f32x2 st[16]; float mu = 0.f, m2 = 0.f;
#pragma unroll
            for (int i = 0; i < 16; ++i) { st[i] = *(const LAS f32x2*)(smem + S4_STR + i * 1024 + lt * 8); mu += st[i][0]; }
            mu *= (1.f / 16.f);
#pragma unroll
            for (int i = 0; i < 16; ++i) { const float d = st[i][0] - mu; m2 += st[i][1] + 64.f * d * d; }
            *(LAS f32x2*)(smem + S4_STF + lt * 8) = (f32x2){mu, __builtin_amdgcn_rsqf(m2 * (1.f / D) + 1e-5f)};
            wait_lgkm0();
          }
        }, DBG(2));
    }
    if (wid == 4) TS(21);
    {
      Ld<C5> L; L.A = P.ws + WS_F + (size_t)tm5 * 56 * 16384; L.W = P.w2 + tn5 * 256; L.lw = lw; L.c = c5; L.kt0 = 28 * kh; L.init(y, lane, 128, 0, FL_F * 256 + tm5 * 16 + 8 * kh + (lane & 7), 11);
      lds_wait(y, 24 + FL_F, 4u);
      load_preW<C5>(L, lds0 + R5);
      if (wid == 4) TS(22);
      load_all<C5, true, 0u, 0u>(L, lds0 + R5, nop, nop, DBG(3));
    }
    if (wid == 4) { TS(23); TS_FLUSH(); }
    return;
  }

  const int wm = wid >> 1, wn = wid & 1;
  if (wid == 0 && lane < 32) y.w[lane] = 0u;
  if (wid == 3 && (bid >> 3) * 8192 + lane * 128 < CODE_BYTES) {
    const char* pc; asm volatile("s_getpc_b64 %0" : "=s"(pc));
    dma4<false>((bid >> 3) * 8192 + lane * 128, pc, lds0 + S1_DUM);
  }
  if (wid == 2) {
    const int i1 = tm1 * 16 + tn1, i3 = tm3 * 16 + tn3, ph = lane & 7;
    const int idx = (ph == FL_P0 || ph == FL_CS) ? bid : ph == FL_H ? i3 : ph >= FL_X ? (tm5 * 8 + tn5) * 2 + kh : i1;
    dma4<false>(lane < 8 ? WS_FLG + (ph * 256 + idx) * 128 : lane < 16 ? WS_ST2 + i3 * 1024 + ph * 128 : WS_ZP + ((2 * tn1 + (lane & 1)) * 16 + tm1) * 256 + (lane & 2) * 64, P.ws, lds0 + S1_DUM);
  }
  {
    f32x4 acc[24];
    Adr ad;
    ad.v[0] = lds0 + R1 + (wm * 64 + m16) * 128 + ((g ^ (m16 >> 1)) << 4); ad.v[1] = lds0 + R1 + (wm * 64 + m16) * 128 + ((g ^ (m16 >> 1) ^ 4) << 4);
    ad.v[2] = lds0 + R1 + 16384 + wlane(lane, 2 * wn); ad.v[3] = lds0 + R1 + 16384 + wlane(lane, 2 * wn + 1); ad.v[4] = 0; ad.v[5] = 0;
    u32x4 px[4], pw[8];
    const int prow = bid * 8 + wid * 2 + (lane >> 5);
    consume<C1, 1, RPG1, 4, 6>(acc, ad, DBG4, [&](int s) {
      if (s < 0) {
        if (wid == 0) TS(1);
        y.tag = __builtin_amdgcn_readfirstlane(*(volatile LAS unsigned*)(smem + S1_FLG)) + 1u;
#pragma unroll
        for (int i = 0; i < 4; ++i) px[i] = *(const u32x4*)(P.x + (size_t)prow * 2048 + ((lane & 31) + 32 * i) * 16);
        colsum_load<8>(pw, P.wqkv + (size_t)((min(bid, 191) / 48) * 256 + wid * 64) * (3 * D * 2) + (bid % 48) * 128, 3 * D * 2, lane);
      }
      if (s == 1) {
#pragma unroll
        for (int i = 0; i < 4; ++i) asm volatile("" : "+v"(px[i]));
#pragma unroll
        for (int i = 0; i < 8; ++i) asm volatile("" : "+v"(pw[i]));
        const unsigned one2 = opaque(0x3f803f80u);
        float sm = 0.f, sq = 0.f;
#pragma unroll
        for (int i = 0; i < 4; ++i)
#pragma unroll
          for (int j = 0; j < 4; ++j) { sm = dot2(px[i][j], one2, sm); sq = dot2(px[i][j], px[i][j], sq); }
        const float mean = xsum16(row16_sum(sm)) * (1.f / D);
        const float var = fmaxf(xsum16(row16_sum(sq)) * (1.f / D) - mean * mean, 0.f);
        if ((lane & 31) == 0) st8(y.r, (u32x2){bu(mean), bu(__builtin_amdgcn_rsqf(var + 1e-5f))}, prow * 8, WS_ST1);
        if (bid < 192) colsum_finish<8>(pw, y.r, WS_CSQ + (((bid / 48) * 4 + wid) * 3 * D + (bid % 48) * 64) * 4, lane);
      }
      if (s == 2) { wait_vm<0>(); if (lane == 0) st4(y.r, y.tag, 0, WS_FLG + (FL_P0 * 256 + bid) * 128 + wid * 4); }
    });
    if (wid == 0) TS(2);
    const int head = 2 * tn1 + wn;
    f32x4 cs[4]; float csv[2];
#pragma unroll
    for (int n = 0; n < 4; ++n) cs[n] = *(const LAS f32x4*)(smem + S1_CSF + ((n >> 1) * 64 + wn * 32 + (n & 1) * 16 + 4 * g) * 4);
#pragma unroll
    for (int n = 0; n < 2; ++n) csv[n] = *(const LAS float*)(smem + S1_CSF + (128 + wn * 32 + n * 16 + m16) * 4);
    u32x4 wp[2];
#pragma unroll
    for (int F = 0; F < 2; ++F) { lds_ptr a0 = smem + S1_WP + wn * 2048 + (4 * g + (m16 >> 2)) * 64 + F * 32 + (lane & 3) * 8; wp[F] = lds_tr2(a0, a0 + 1024); }
    u32x4 qb[4], vb[2][2];
    f32x4 p[4][2];
    const f32x4 zero = {0.f, 0.f, 0.f, 0.f};
    {
      f32x4 vv[4][2];
#pragma unroll
      for (int m = 0; m < 4; ++m) {
        const f32x2 st = *(const LAS f32x2*)(smem + S1_ST + (wm * 64 + m * 16 + m16) * 8);
        const f32x4 s0 = *(const LAS f32x4*)(smem + S1_ST + (wm * 64 + m * 16 + 4 * g) * 8), s1 = *(const LAS f32x4*)(smem + S1_ST + (wm * 64 + m * 16 + 4 * g) * 8 + 16);
        const f32x4 mu = {s0[0], s0[2], s1[0], s1[2]}, rs = {s0[1], s0[3], s1[1], s1[3]};
        qb[m] = pack8((acc[m * 6] - cs[0] * st[0]) * st[1], (acc[m * 6 + 1] - cs[1] * st[0]) * st[1]);
        const u32x4 kb = pack8((acc[m * 6 + 2] - cs[2] * st[0]) * st[1], (acc[m * 6 + 3] - cs[3] * st[0]) * st[1]);
        p[m][0] = mfma(wp[0], kb, zero); p[m][1] = mfma(wp[1], kb, zero);
        vv[m][0] = (acc[m * 6 + 4] - mu * csv[0]) * rs; vv[m][1] = (acc[m * 6 + 5] - mu * csv[1]) * rs;
      }
#pragma unroll
      for (int u = 0; u < 2; ++u) { vb[u][0] = pack8(vv[2 * u][0], vv[2 * u + 1][0]); vb[u][1] = pack8(vv[2 * u][1], vv[2 * u + 1][1]); }
    }
    u32x4 pp[4], pn[4];
    if (wid == 0) TS(26);
    dual_softmax4(p, pp, pn);
    if (wid == 0) TS(27);
    {
      lds_ptr own = smem + R3 + wid * C3::STAGE, oth = smem + R3 + (wid ^ 2) * C3::STAGE;
#pragma unroll
      for (int m = 0; m < 4; ++m) {
        lds_ptr w = own + (16 * m + m16) * 136 + 8 * g;
        *(LAS u32x2*)(w) = (u32x2){pp[m][0], pp[m][1]}; *(LAS u32x2*)(w + 32) = (u32x2){pp[m][2], pp[m][3]};
        *(LAS u32x2*)(w + 64) = (u32x2){pn[m][0], pn[m][1]}; *(LAS u32x2*)(w + 96) = (u32x2){pn[m][2], pn[m][3]};
      }
#pragma unroll
      for (int i = 0; i < 4; ++i) *(LAS u32x4*)(own + 8704 + i * 1024 + lane * 16) = vb[i >> 1][i & 1];
      wait_lgkm0();
      volatile LAS unsigned* xf = y.w + 16;
      xf[wid] = 1u;
      f32x4 kv[2][2] = {{zero, zero}, {zero, zero}}, z[2] = {zero, zero};
      const u32x4 ones = {0x3f803f80u, 0x3f803f80u, 0x3f803f80u, 0x3f803f80u};
      const unsigned ro = (4 * g + (m16 >> 2)) * 136 + wm * 64 + (lane & 3) * 8;
#pragma unroll
      for (int u = 0; u < 2; ++u)
#pragma unroll
        for (int F = 0; F < 2; ++F) {
          const u32x4 pa = lds_tr2(own + ro + u * 32 * 136 + F * 32, own + ro + u * 32 * 136 + F * 32 + 16 * 136);
          kv[F][0] = mfma(pa, vb[u][0], kv[F][0]); kv[F][1] = mfma(pa, vb[u][1], kv[F][1]); z[F] = mfma(pa, ones, z[F]);
        }
      while (xf[wid ^ 2] == 0u) __builtin_amdgcn_s_sleep(0);
      asm volatile("" ::: "memory");
#pragma unroll
      for (int u = 0; u < 2; ++u) {
        const u32x4 v0 = lds128(oth + 8704 + u * 2048 + lane * 16), v1 = lds128(oth + 8704 + u * 2048 + 1024 + lane * 16);
#pragma unroll
        for (int F = 0; F < 2; ++F) {
          const u32x4 pa = lds_tr2(oth + ro + u * 32 * 136 + F * 32, oth + ro + u * 32 * 136 + F * 32 + 16 * 136);
          kv[F][0] = mfma(pa, v0, kv[F][0]); kv[F][1] = mfma(pa, v1, kv[F][1]); z[F] = mfma(pa, ones, z[F]);
        }
      }
      const unsigned kd = WS_KVP + (head * 16 + tm1) * 4096 + wm * 2048;
      st16(y.r, pack8(kv[0][0], kv[1][0]), lane * 16, kd);
      st16(y.r, pack8(kv[0][1], kv[1][1]), lane * 16, kd + 1024);
      if (m16 == 0) {
        st16(y.r, __builtin_bit_cast(u32x4, z[0]), g * 32, WS_ZP + (head * 16 + tm1) * 256 + wm * 128);
        st16(y.r, __builtin_bit_cast(u32x4, z[1]), g * 32 + 16, WS_ZP + (head * 16 + tm1) * 256 + wm * 128);
      }
      if (wid == 0) TS(25);
      publish(y, FL_KV, tm1 * 16 + tn1, lane);
    }
    if (wid == 0) TS(3);
#pragma unroll
    for (int m = 0; m < 4; ++m) { p[m][0] = mfma(wp[0], qb[m], zero); p[m][1] = mfma(wp[1], qb[m], zero); }
    dual_softmax4(p, pp, pn);
    if (wid == 0) TS(4);
    { Gate G; G.off = 0; G.mask = 0; G.word = 8; kv_sum(y, G, smem, 2 * tn1, 0, threadIdx.x, false, wid == 0, lane); }
    wait_lgkm0(); bar();
    if (wid == 0) TS(5);
    u32x4 ka[2][2], za[2];
#pragma unroll
    for (int ks = 0; ks < 2; ++ks) {
      ka[ks][0] = lds128(smem + S1_KV + wn * 4096 + ks * 2048 + lane * 16); ka[ks][1] = lds128(smem + S1_KV + wn * 4096 + ks * 2048 + 1024 + lane * 16);
      za[ks] = lds128(smem + S1_KV + 8192 + wn * 128 + ks * 64 + g * 16);
    }
#pragma unroll
    for (int m = 0; m < 4; ++m) {
      f32x4 n0 = mfma(ka[0][0], pp[m], zero), n1 = mfma(ka[0][1], pp[m], zero), dn = mfma(za[0], pp[m], zero);
      n0 = mfma(ka[1][0], pn[m], n0); n1 = mfma(ka[1][1], pn[m], n1); dn = mfma(za[1], pn[m], dn);
      const float rd = __builtin_amdgcn_rcpf(dn[0] + 1e-6f);
      st16(y.r, pack8(n0 * rd, n1 * rd), lane * 16, WS_O + (((tm1 * 16 + tn1) * 8 + wm * 4 + m) * 2 + wn) * 1024);
    }
    publish(y, FL_O, tm1 * 16 + tn1, lane);
    if (wid == 0) TS(6);
  }
  {
    u32x4 xr[2][2];
#pragma unroll
    for (int m = 0; m < 2; ++m)
#pragma unroll
      for (int Q = 0; Q < 2; ++Q)
        xr[m][Q] = *(const u32x4*)(P.x + (size_t)(tm3 * 128 + wid * 32 + m * 16 + m16) * 2048 + tn3 * 128 + Q * 64 + (g & 1) * 32 + (g >> 1) * 16);
    f32x4 acc[8];
    Adr ad; ad.v[0] = lds0 + R3 + wid * 4096 + lane * 16; ad.v[1] = 0;
#pragma unroll
    for (int n = 0; n < 4; ++n) ad.v[2 + n] = lds0 + R3 + 16384 + wlane(lane, n);
    consume_s<C3, 3, 2>(acc, ad, [&](int) { if (wid == 0) TS(7); });
    if (wid == 0) TS(8);
#pragma unroll
    for (int m = 0; m < 2; ++m) {
      float hv[16];
#pragma unroll
      for (int Q = 0; Q < 2; ++Q) {
        auto r0 = __builtin_amdgcn_permlane16_swap(xr[m][Q][0], xr[m][Q][2], false, false);
        auto r1 = __builtin_amdgcn_permlane16_swap(xr[m][Q][1], xr[m][Q][3], false, false);
        const f32x4 a = acc[m * 4 + 2 * Q], b = acc[m * 4 + 2 * Q + 1];
        const u32x4 hp = {pk2(a[0] + bflo(r0[0]), a[1] + bfhi(r0[0])), pk2(a[2] + bflo(r1[0]), a[3] + bfhi(r1[0])),
                          pk2(b[0] + bflo(r0[1]), b[1] + bfhi(r0[1])), pk2(b[2] + bflo(r1[1]), b[3] + bfhi(r1[1]))};
        st16(y.r, hp, lane * 16, WS_H + (((tm3 * 16 + tn3) * 8 + wid * 2 + m) * 2 + Q) * 1024);
#pragma unroll
        for (int j = 0; j < 4; ++j) { hv[Q * 8 + 2 * j] = bflo(hp[j]); hv[Q * 8 + 2 * j + 1] = bfhi(hp[j]); }
      }
      float s = 0.f;
#pragma unroll
      for (int j = 0; j < 16; ++j) s += hv[j];
      s = xsum32(xsum16(s)) * (1.f / 64.f);
      float q = 0.f;
#pragma unroll
      for (int j = 0; j < 16; ++j) { const float d = hv[j] - s; q += d * d; }
      q = xsum32(xsum16(q));
      if (g == 0) st8(y.r, (u32x2){bu(s), bu(q)}, m16 * 8, WS_ST2 + (tm3 * 16 + tn3) * 1024 + (wid * 32 + m * 16) * 8);
    }
    if (wid == 0) TS(29);
    publish(y, FL_H, tm3 * 16 + tn3, lane);
    if (wid == 0) TS(9);
  }
  {
    f32x4 acc[28];
    Adr ad;
    ad.v[0] = lds0 + R4 + wm * 8192 + wn * 4096 + lane * 16; ad.v[1] = lds0 + R4 + wm * 8192 + (wn ^ 1) * 4096 + lane * 16;
    ad.v[2] = lds0 + R4 + 16384 + wlane(lane, wn); ad.v[3] = lds0 + R4 + 16384 + wlane(lane, 2 + wn);
    ad.v[4] = lds0 + R4 + 16384 + 6144 + hlane(lane, 0); ad.v[5] = lds0 + R4 + 16384 + 6144 + hlane(lane, 1);
    consume<C4, 4, RPG4, 0, 0>(acc, ad, DBG4, [&](int s) { if (s < 0 && wid == 0) TS(10); });
    if (wid == 0) TS(11);
    f32x4 cs[8];
#pragma unroll
    for (int n = 0; n < 6; ++n) cs[n] = *(const LAS f32x4*)(smem + S4_CSF + ((par + (n >> 1)) * 64 + (2 * (n & 1) + wn) * 16 + 4 * g) * 4);
#pragma unroll
    for (int n = 0; n < 2; ++n) cs[6 + n] = *(const LAS f32x4*)(smem + S4_CSF + ((par ? 32 : 192) + n * 16 + 4 * g) * 4);
    const f32x4 zero = {0.f, 0.f, 0.f, 0.f};
#pragma unroll
    for (int m = 0; m < 4; ++m) {
      const int mt = wm * 4 + ((m + 2 * wn) & 3);
      const f32x2 st = *(const LAS f32x2*)(smem + S4_STF + (mt * 16 + m16) * 8);
      const unsigned fd = WS_F + (tm1 * 56 * 16 + mt * 2) * 1024;
#pragma unroll
      for (int i = 0; i < 3; ++i)
        st16(y.r, pack8(__builtin_elementwise_max((acc[m * 6 + 2 * i] - cs[2 * i] * st[0]) * st[1], zero),
                        __builtin_elementwise_max((acc[m * 6 + 2 * i + 1] - cs[2 * i + 1] * st[0]) * st[1], zero)), lane * 16, fd + (ch0 + i) * 16384 + wn * 1024);
      if (m < 2)
        st16(y.r, pack8(__builtin_elementwise_max((acc[24 + 2 * m] - cs[6] * st[0]) * st[1], zero),
                        __builtin_elementwise_max((acc[24 + 2 * m + 1] - cs[7] * st[0]) * st[1], zero)), lane * 16, fd + (base7 + 3) * 16384 + par * 1024);
    }
    if (wid == 0) TS(28);
    publish(y, FL_F, tm1 * 16 + tn1, lane);
    if (wid == 0) TS(12);
  }
  {
    f32x4 acc[16];
    Adr ad;
    ad.v[0] = lds0 + R5 + wm * 8192 + kh * 4096 + lane * 16; ad.v[1] = lds0 + R5 + wm * 8192 + (kh ^ 1) * 4096 + lane * 16;
#pragma unroll
    for (int n = 0; n < 4; ++n) ad.v[2 + n] = lds0 + R5 + 16384 + wn * 2048 + wlane(lane, n);
    u32x4 hr[2][2];
    consume_s<C5, 5, 1>(acc, ad, [&](int s) {
      if (s < 0) {
        if (wid == 0) TS(13);
#pragma unroll
        for (int m = 0; m < 2; ++m)
#pragma unroll
          for (int Q = 0; Q < 2; ++Q) hr[m][Q] = ld16(y.r, lane * 16, WS_H + (((tm5 * 16 + 2 * tn5 + wn) * 8 + wm * 4 + 2 * kh + m) * 2 + Q) * 1024);
      }
    });
    if (wid == 0) TS(14);
    const int tile = tm5 * 8 + tn5;
#pragma unroll
    for (int i = 0; i < 4; ++i) st16(y.r, pack8(acc[8 + 2 * i], acc[9 + 2 * i]), lane * 16, WS_O + (((tile * 2 + kh) * 4 + wid) * 4 + i) * 1024);
    wait_vm<0>();
    if (lane == 0) st4(y.r, y.tag, 0, WS_FLG + ((FL_X + (wid >> 1)) * 256 + tile * 2 + kh) * 128 + (wid & 1) * 64);
    poll(y, ((FL_X + (wid >> 1)) * 256 + tile * 2 + (kh ^ 1)) * 128 + (wid & 1) * 64, 1, lane);
    if (wid == 0) TS(15);
    u32x4 xv[4];
#pragma unroll
    for (int i = 0; i < 4; ++i) xv[i] = ld16(y.r, lane * 16, WS_O + (((tile * 2 + (kh ^ 1)) * 4 + wid) * 4 + i) * 1024);
#pragma unroll
    for (int m = 0; m < 2; ++m)
#pragma unroll
      for (int Q = 0; Q < 2; ++Q) {
        const u32x4 xx = xv[m * 2 + Q];
        const f32x4 a = acc[m * 4 + 2 * Q] + (f32x4){bflo(xx[0]), bfhi(xx[0]), bflo(xx[1]), bfhi(xx[1])}, b = acc[m * 4 + 2 * Q + 1] + (f32x4){bflo(xx[2]), bfhi(xx[2]), bflo(xx[3]), bfhi(xx[3])};
        const u32x4 hh = hr[m][Q];
        const unsigned a0 = pk2(a[0] + bflo(hh[0]), a[1] + bfhi(hh[0])), a1 = pk2(a[2] + bflo(hh[1]), a[3] + bfhi(hh[1]));
        const unsigned b0 = pk2(b[0] + bflo(hh[2]), b[1] + bfhi(hh[2])), b1 = pk2(b[2] + bflo(hh[3]), b[3] + bfhi(hh[3]));
        auto r0 = __builtin_amdgcn_permlane16_swap(a0, b0, false, false);
        auto r1 = __builtin_amdgcn_permlane16_swap(a1, b1, false, false);
        *(u32x4*)(P.out + (size_t)(tm5 * 128 + wm * 64 + (2 * kh + m) * 16 + m16) * 2048 + tn5 * 256 + wn * 128 + Q * 64 + (g & 1) * 32 + (g >> 1) * 16) = (u32x4){r0[0], r1[0], r0[1], r1[1]};
      }
    while (__builtin_amdgcn_s_memrealtime() - tstart < 10000ull) __builtin_amdgcn_s_sleep(1);
    if (bid == 0 && threadIdx.x == 0) st4(y.r, y.tag, 0, WS_CTL);
    if (wid == 0) { TS(24); TS_FLUSH(); }
  }
}

extern "C" void kernel_launch(void* const* d_in, const int* in_sizes, int n_in, void* d_out, int out_size, void* d_ws, size_t ws_size,
                              hipStream_t stream) {
  Params p;
  p.x = (const char*)d_in[0]; p.wqkv = (const char*)d_in[1]; p.wphi = (const char*)d_in[2]; p.wo = (const char*)d_in[3];
  p.w1 = (const char*)d_in[5]; p.w2 = (const char*)d_in[6]; p.ws = (char*)d_ws; p.out = (char*)d_out;
  (void)hipFuncSetAttribute((const void*)fused, hipFuncAttributeMaxDynamicSharedMemorySize, LDS_BYTES);
  fused<<<256, 512, LDS_BYTES, stream>>>(p);
}
```

```cpp
#include <hip/hip_runtime.h>
#include <hip/hip_bf16.h>

#define LAS __attribute__((address_space(3)))
#define DI __device__ __forceinline__
typedef __bf16 bf16x8 __attribute__((ext_vector_type(8)));
typedef __bf16 bf16x2 __attribute__((ext_vector_type(2)));
typedef short s16x4 __attribute__((ext_vector_type(4)));
typedef float f32x2 __attribute__((ext_vector_type(2)));
typedef float f32x4 __attribute__((ext_vector_type(4)));
typedef unsigned u32x2 __attribute__((ext_vector_type(2)));
typedef unsigned u32x4 __attribute__((ext_vector_type(4)));
typedef LAS char* lds_ptr;
typedef __amdgpu_buffer_rsrc_t rsrc_t;

constexpr int D = 1024, FF = 3584;
constexpr unsigned WS_CTL = 0;
constexpr unsigned WS_FLG = 4096;
constexpr unsigned WS_ST1 = 320u << 10;
constexpr unsigned WS_CSQ = 384u << 10;
constexpr unsigned WS_CS1 = 576u << 10;
constexpr unsigned WS_ZP = 704u << 10;
constexpr unsigned WS_ST2 = 832u << 10;
constexpr unsigned WS_SMALL = 1280u << 10;
constexpr unsigned WS_KVP = 2u << 20;
constexpr unsigned WS_O = 4u << 20;
constexpr unsigned WS_H = 8u << 20;
constexpr unsigned WS_F = 12u << 20;
constexpr unsigned WS_END = 26u << 20;
enum { FL_P0 = 0, FL_KV = 1, FL_O = 2, FL_H = 3, FL_F = 4, FL_CS = 5, FL_X = 6   };

#define TS(I) do {} while (0)
#define TS_FLUSH() do {} while (0)

#define DBG(k) nullptr
#define CLK(v)
#define DBG4 nullptr
struct Params { const char* x; const char* wqkv; const char* wphi; const char* wo; const char* w1; const char* w2; char* ws; char* out; };

DI unsigned pk2(float a, float b) { f32x2 v = {a, b}; return __builtin_bit_cast(unsigned, __builtin_convertvector(v, bf16x2)); }
DI float bflo(unsigned u) { return __uint_as_float(u << 16); }
DI float bfhi(unsigned u) { return __uint_as_float(u & 0xffff0000u); }
DI u32x4 pack8(const f32x4& a, const f32x4& b) { return (u32x4){pk2(a[0], a[1]), pk2(a[2], a[3]), pk2(b[0], b[1]), pk2(b[2], b[3])}; }
DI bf16x8 asbf(const u32x4& v) { return __builtin_bit_cast(bf16x8, v); }
#define SB() __builtin_amdgcn_sched_barrier(0)
DI void bar() { asm volatile("" ::: "memory"); __builtin_amdgcn_s_barrier(); asm volatile("" ::: "memory"); }
DI void wait_lgkm0() { __builtin_amdgcn_s_waitcnt(0xC07F); asm volatile("" ::: "memory"); }
template <int N> DI void wait_vm() { asm volatile("s_waitcnt vmcnt(%0)" ::"i"(N) : "memory"); }
template <bool SC1> DI void dma(unsigned voff, const char* sbase, unsigned lds) {
  if (SC1) asm volatile("s_mov_b32 m0, %2\n\ts_nop 3\n\tglobal_load_lds_dwordx4 %0, %1 sc1" ::"v"(voff), "s"(sbase), "s"(lds) : "memory");
  else asm volatile("s_mov_b32 m0, %2\n\ts_nop 3\n\tglobal_load_lds_dwordx4 %0, %1" ::"v"(voff), "s"(sbase), "s"(lds) : "memory");
}
template <bool SC1> DI void dma4(unsigned voff, const char* sbase, unsigned lds) {
  if (SC1) asm volatile("s_mov_b32 m0, %2\n\ts_nop 3\n\tglobal_load_lds_dword %0, %1 sc1" ::"v"(voff), "s"(sbase), "s"(lds) : "memory");
  else asm volatile("s_mov_b32 m0, %2\n\ts_nop 3\n\tglobal_load_lds_dword %0, %1" ::"v"(voff), "s"(sbase), "s"(lds) : "memory");
}
DI u32x4 lds128(lds_ptr p) { return *(const LAS u32x4*)p; }
DI u32x4 lds_tr2(lds_ptr p0, lds_ptr p1) {
  s16x4 a = __builtin_amdgcn_ds_read_tr16_b64_v4i16((LAS s16x4*)p0);
  s16x4 b = __builtin_amdgcn_ds_read_tr16_b64_v4i16((LAS s16x4*)p1);
  return __builtin_bit_cast(u32x4, __builtin_shufflevector(a, b, 0, 1, 2, 3, 4, 5, 6, 7));
}
DI u32x2 lds_tr(lds_ptr p) { return __builtin_bit_cast(u32x2, __builtin_amdgcn_ds_read_tr16_b64_v4i16((LAS s16x4*)p)); }
DI f32x4 mfma(const u32x4& a, const u32x4& b, const f32x4& c) { return __builtin_amdgcn_mfma_f32_16x16x32_bf16(asbf(a), asbf(b), c, 0, 0, 0); }
DI void mfma_ip(f32x4& acc, const u32x4& a, const u32x4& b) { asm volatile("v_mfma_f32_16x16x32_bf16 %0, %1, %2, %0" : "+v"(acc) : "v"(a), "v"(b)); }
DI u32x4 ld16(rsrc_t r, unsigned voff, unsigned soff) { return __builtin_amdgcn_raw_buffer_load_b128(r, voff, soff, 16); }
DI void st16(rsrc_t r, const u32x4& v, unsigned voff, unsigned soff) { __builtin_amdgcn_raw_buffer_store_b128(v, r, voff + soff, 0, 16);
}
DI void st8(rsrc_t r, const u32x2& v, unsigned voff, unsigned soff) { __builtin_amdgcn_raw_buffer_store_b64(v, r, voff, soff, 16); }
DI void st4(rsrc_t r, unsigned v, unsigned voff, unsigned soff) { __builtin_amdgcn_raw_buffer_store_b32(v, r, voff, soff, 16); }
DI float fb(unsigned u) { return __uint_as_float(u); }
DI unsigned bu(float f) { return __float_as_uint(f); }
DI float xsum16(float x) { auto r = __builtin_amdgcn_permlane16_swap(bu(x), bu(x), false, false); return fb(r[0]) + fb(r[1]); }
DI float xsum32(float x) { auto r = __builtin_amdgcn_permlane32_swap(bu(x), bu(x), false, false); return fb(r[0]) + fb(r[1]); }
template <int CTRL> DI float dpp(float x) { return __builtin_bit_cast(float, __builtin_amdgcn_update_dpp(0, __builtin_bit_cast(int, x), CTRL, 0xf, 0xf, true)); }
DI float row16_sum(float x) { x += dpp<0xB1>(x); x += dpp<0x4E>(x); x += dpp<0x141>(x); x += dpp<0x140>(x); return x; }

struct Sync { rsrc_t r; unsigned tag; LAS unsigned* w; };
DI void publish(const Sync& y, int phase, int idx, int lane) {
  if (lane == 0) __hip_atomic_fetch_add(y.w + 24 + phase, 1u, __ATOMIC_RELAXED, __HIP_MEMORY_SCOPE_WORKGROUP);
  wait_vm<0>();
  if (lane == 0 && __hip_atomic_fetch_add(y.w + phase, 1u, __ATOMIC_RELAXED, __HIP_MEMORY_SCOPE_WORKGROUP) == 3u) st4(y.r, y.tag, 0, WS_FLG + (phase * 256 + idx) * 128);
}
DI void poll(const Sync& y, unsigned off, int n, int lane) {
  const unsigned long long t0 = __builtin_amdgcn_s_memrealtime();
  for (unsigned it = 0;; ++it) {
    const unsigned v = __builtin_amdgcn_raw_buffer_load_b32(y.r, WS_FLG + off, 0, 16);
    if (__all(lane >= n || v == y.tag)) break;
    if ((it & 63) == 63) {
      if (__builtin_amdgcn_readfirstlane(__builtin_amdgcn_raw_buffer_load_b32(y.r, 0, WS_CTL + 128, 16)) == y.tag) break;
      if (__builtin_amdgcn_s_memrealtime() - t0 > 4000000ull) { if (lane == 0) st4(y.r, y.tag, 0, WS_CTL + 128); break; }
    }
    __builtin_amdgcn_s_sleep(2);
  }
  asm volatile("" ::: "memory");
}
DI void lds_wait(const Sync& y, int word, unsigned v) {
  volatile LAS unsigned* w = y.w + word;
  while (*w < v) __builtin_amdgcn_s_sleep(1);
  asm volatile("" ::: "memory");
}
struct Gate { unsigned off, mask; int word; };
DI void gate_slow(const Sync& y, Gate& G, unsigned need, bool poller, int lane, int cnt = 0) {
  volatile LAS unsigned* w = y.w + G.word;
  if (poller) {
    const unsigned long long t0 = __builtin_amdgcn_s_memrealtime();
    for (unsigned it = 0;; ++it) {
      G.mask = (unsigned)__ballot(__builtin_amdgcn_raw_buffer_load_b32(y.r, WS_FLG + G.off, 0, 16) == y.tag);
      if ((it & 63) == 63) {
        const bool late = __builtin_amdgcn_s_memrealtime() - t0 > 4000000ull;
        if (late && lane == 0) st4(y.r, y.tag, 0, WS_CTL + 128);
        if (late || __builtin_amdgcn_readfirstlane(__builtin_amdgcn_raw_buffer_load_b32(y.r, 0, WS_CTL + 128, 16)) == y.tag) G.mask = ~0u;
      }
      *w = G.mask;
      if ((G.mask & need) == need && __builtin_popcount(G.mask & 0xffffu) >= cnt) break;
      __builtin_amdgcn_s_sleep(1);
    }
  } else {
    for (;;) { G.mask = __builtin_amdgcn_readfirstlane(*w); if ((G.mask & need) == need && __builtin_popcount(G.mask & 0xffffu) >= cnt) break; __builtin_amdgcn_s_sleep(1); }
  }
  asm volatile("" ::: "memory");
}
DI void gate(const Sync& y, Gate& G, unsigned need, bool poller, int lane) { if (__builtin_expect((G.mask & need) != need, 0)) gate_slow(y, G, need, poller, lane); }

template <int NK, bool SPREAD> DI int kmap(int t, int c) {
  const int u = t - 1, q = u & 3, b4 = u & ~3, body = (int)(u < NK - 4);
  const int lead = ((int)(q == c) & body & (int)(u >= 0)) | (int)(SPREAD && t == 0), head = SPREAD ? 0 : -(int)(u < 4);
  const int non = ((b4 + ((c + 1 + q - ((int)(q > c) & body)) & 3)) & ~head) | ((t - (int)(u > c)) & head);
  return non ^ (((b4 + 4 + c) ^ non) & -lead);
}
#define LAG3 1
#define LAG5 1
enum { ORD_NAT = 0, ORD_PAIR = 1, ORD_F = 2 };
template <int NSUB_, bool HALF_, int NSLOT_, int NK_, int ORD_, int WSTRIDE_, bool XROW_, int LAG_> struct Cfg {
  static constexpr int NSUB = NSUB_, NSLOT = NSLOT_, NK = NK_, ORD = ORD_, WSTRIDE = WSTRIDE_;
  static constexpr bool HALF = HALF_, XROW = XROW_;
  static constexpr int LAG = LAG_;
  static constexpr int STAGE = 16384 + NSUB * 8192 + (HALF ? 4096 : 0), PW = 4 + 2 * NSUB + (HALF ? 1 : 0), RING = STAGE * NSLOT;
  static constexpr int WB = NSUB * 2048 + (HALF ? 1024 : 0);
};
typedef Cfg<3, false, 3, 16, ORD_NAT, 3 * D * 2, true, 0> C1;
typedef Cfg<1, false, 5, 16, ORD_PAIR, D * 2, false, LAG3> C3;
typedef Cfg<3, true, 3, 16, ORD_PAIR, FF * 2, false, 0> C4;
typedef Cfg<2, false, 4, 28, ORD_F, D * 2, false, LAG5> C5;

#define GLDS "global_load_lds_dwordx4 "
template <class C> struct Ld {
  const char* A; const char* W; int lw, c, kt0; unsigned vA[4], vW[7];
  const char* sA; const char* sW; unsigned need;
  Gate G; const Sync* y; int lane_;
  static constexpr int C0 = C::WB > 4096 ? 4096 : 0;
  DI void init(const Sync& y_, int lane, int substep, int halfrel, unsigned goff, int gword) {
    y = &y_; lane_ = lane; G.off = goff; G.mask = 0; G.word = gword;
#pragma unroll
    for (int i = 0; i < 4; ++i) vA[i] = C::XROW ? (lane >> 3) * 2048 + ((((lane & 7) ^ (lane >> 4)) << 4) ^ ((i & 1) * 64)) + i * (16384 - 1024) : lane * 16;
    const int rl = (C::ORD == ORD_NAT) ? 8 * (lane >> 5) + ((lane >> 3) & 3) : (lane >> 3);
    const unsigned w0 = rl * C::WSTRIDE + (((lane & 7) ^ (((lane >> 4) & 3) << 1)) << 4) + 4096;
#pragma unroll
    for (int q = 0; q < 2 * C::NSUB; ++q) vW[q] = w0 + (q & 1) * ((C::ORD == ORD_NAT ? 16 : 8) * C::WSTRIDE) + (q >> 1) * substep - (q * 1024 - C0);
    if (C::HALF) vW[6] = (lane >> 2) * C::WSTRIDE + (((lane & 3) ^ (((lane >> 4) & 1) << 1)) << 4) + 4096 + halfrel - (6144 - C0);
    A += C::XROW ? lw * 32 * 2048 : lw * 4096;
    W -= 4096;
  }
  DI void prep(int s) {
    int kt = kmap<C::NK, C::XROW>(s, c);
    const int kb = lw >> 1, t = lw & 1;
    int ur;
    need = 1u << kt;
    if (C::ORD == ORD_NAT) ur = 32 * kb + 4 * t;
    else if (C::ORD == ORD_F) {
      const int p = kt & 7, q = kt >> 3, sp = -(int)(q == 3);
      need = ((1u << (2 * p + (int)(q > 0))) & ~sp) | ((0xau << (4 * (p & 3))) & sp);
      kt = kt0 + (((7 * (p >> 1) + 4 * (p & 1) + q) & ~sp) | ((7 * p + 3) & sp));
      ur = (16 * lw & sp) | ((16 * kb + 32 * t) & ~sp);
    }
    else ur = 16 * lw;
    sA = A + (unsigned)(kt * (C::XROW ? 128 : 16384));
    sW = W + (unsigned)((kt * 64 + ur) * C::WSTRIDE);
  }
  DI void issueA(unsigned base) {
    if (!C::XROW) gate(*y, G, need, lw == 0, lane_);
    const unsigned m = base + lw * 4096;
    if (C::XROW) asm volatile("s_mov_b32 m0, %5\n\ts_nop 3\n\t" GLDS "%0, %4\n\t" GLDS "%1, %4 offset:1024\n\t" GLDS "%2, %4 offset:2048\n\t" GLDS "%3, %4 offset:3072"
                              ::"v"(vA[0]), "v"(vA[1]), "v"(vA[2]), "v"(vA[3]), "s"(sA), "s"(m) : "memory");
    else asm volatile("s_mov_b32 m0, %2\n\ts_nop 3\n\t" GLDS "%0, %1 sc1\n\t" GLDS "%0, %1 offset:1024 sc1\n\t" GLDS "%0, %1 offset:2048 sc1\n\t" GLDS "%0, %1 offset:3072 sc1"
                      ::"v"(vA[0]), "s"(sA), "s"(m) : "memory");
  }
  DI void issueW(unsigned base) const {
    const unsigned m = base + 16384 + lw * C::WB + C0;
    if (C::NSUB == 1) asm volatile("s_mov_b32 m0, %3\n\ts_nop 3\n\t" GLDS "%0, %2\n\t" GLDS "%1, %2 offset:1024" ::"v"(vW[0]), "v"(vW[1]), "s"(sW), "s"(m) : "memory");
    if (C::NSUB == 2) asm volatile("s_mov_b32 m0, %5\n\ts_nop 3\n\t" GLDS "%0, %4\n\t" GLDS "%1, %4 offset:1024\n\t" GLDS "%2, %4 offset:2048\n\t" GLDS "%3, %4 offset:3072"
                                   ::"v"(vW[0]), "v"(vW[1]), "v"(vW[2]), "v"(vW[3]), "s"(sW), "s"(m) : "memory");
    if (C::NSUB == 3) asm volatile("s_mov_b32 m0, %7\n\ts_nop 3\n\t" GLDS "%0, %6 offset:-4096\n\t" GLDS "%1, %6 offset:-3072\n\t" GLDS "%2, %6 offset:-2048\n\t" GLDS "%3, %6 offset:-1024\n\t"
                                   GLDS "%4, %6\n\t" GLDS "%5, %6 offset:1024"
                                   ::"v"(vW[0]), "v"(vW[1]), "v"(vW[2]), "v"(vW[3]), "v"(vW[4]), "v"(vW[5]), "s"(sW), "s"(m) : "memory");
    if (C::HALF) asm volatile(GLDS "%0, %1 offset:2048" ::"v"(vW[6]), "s"(sW) : "memory");
  }
};
template <class C> DI void load_preW(Ld<C>& L, unsigned ring) {
#pragma unroll 1
  for (int s = 0; s < C::NSLOT; ++s) { L.prep(s); L.issueW(ring + s * C::STAGE); }
}
template <class C, bool SPLIT> constexpr int vm_allow(int S) {
  const int N = C::NSLOT, behind = S - 1 - C::LAG > 0 ? S - 1 - C::LAG : 0;
  const int ramp = S + 1 < N - 1 ? S + 1 : N - 1;
  const int last = ramp + behind < C::NK - 1 ? ramp + behind : C::NK - 1;
  return (SPLIT && S < N) ? 4 * (ramp - S) + C::PW * (last - ramp) : C::PW * (last - S);
}
template <class C, bool SPLIT, unsigned POSTM, int S, int SE, class Post> DI void load_fixed(Ld<C>& L, unsigned ring, const Post& post) {
  if constexpr (S < SE) {
    static_assert(C::NSLOT <= C::LAG + 4, "the ring must be full before the first refill");
    wait_vm<vm_allow<C, SPLIT>(S)>();
    bar();
    if constexpr (S + 2 < C::NSLOT) {
      if (!SPLIT) L.issueW(ring + (S + 2) * C::STAGE);
      L.issueA(ring + (S + 2) * C::STAGE);
      L.prep(S + 3);
    }
    constexpr int T = S - 1 - C::LAG + C::NSLOT;
    if constexpr (S - 1 - C::LAG >= 0 && T < C::NK) {
      L.issueW(ring + (T % C::NSLOT) * C::STAGE); L.issueA(ring + (T % C::NSLOT) * C::STAGE);
      if constexpr (T + 1 < C::NK) L.prep(T + 1);
    }
    if constexpr ((POSTM >> S) & 1u) post(S);
    load_fixed<C, SPLIT, POSTM, S + 1, SE>(L, ring, post);
  }
}
template <class C, bool SPLIT, unsigned PREM, unsigned POSTM, class Pre, class Post>
DI void load_all(Ld<C>& L, unsigned ring, const Pre& pre, const Post& post, unsigned long long* dbg = nullptr) {
  constexpr int N = C::NSLOT, S0 = N - 1 > 2 + C::LAG ? N - 1 : 2 + C::LAG, S1 = C::NK - N + C::LAG;
#pragma unroll 1
  for (int s = 0; s < 2; ++s) { L.prep(s); if (!SPLIT) L.issueW(ring + s * C::STAGE); L.issueA(ring + s * C::STAGE); }
  L.prep(2);
  if (PREM >> 31) pre(-1);
  __builtin_amdgcn_s_setprio(3);
  load_fixed<C, SPLIT, POSTM, 0, S0>(L, ring, post);
  static_assert(vm_allow<C, SPLIT>(S0) == (N - 2 - C::LAG) * C::PW && vm_allow<C, SPLIT>(S1) == (N - 2 - C::LAG) * C::PW, "steady count");
  unsigned b = ring + ((S0 - 1 - C::LAG) % N) * C::STAGE;
#pragma unroll 1
  for (int s = S0; s <= S1; ++s) {
    CLK(t0);
    wait_vm<(N - 2 - C::LAG) * C::PW>();
    if ((PREM & 0x7fffffffu) && ((PREM >> s) & 1u)) pre(s);
    CLK(t1);
    bar();
    CLK(t2);
    L.issueW(b);
    L.issueA(b);
    CLK(t3);
    L.prep(min(s - C::LAG + N, C::NK - 1));
    b = (b == ring + (N - 1) * C::STAGE) ? ring : b + C::STAGE;
    if (POSTM && ((POSTM >> s) & 1u)) post(s);
  }
  __builtin_amdgcn_s_setprio(0);
  load_fixed<C, SPLIT, POSTM, S1 + 1, C::NK>(L, ring, post);
  bar();
}

template <int MT, int NWF> struct Fr { u32x4 a[MT]; u32x2 w[NWF][2]; };
DI u32x4 cat(const u32x2 (&w)[2]) { return (u32x4){w[0][0], w[0][1], w[1][0], w[1][1]}; }
struct Adr { unsigned v[6]; };
template <int KIND> struct Shape;
template <> struct Shape<1> { static constexpr int MT = 4, NWF = 6, NMF = 24, NACC = 24, WB = 6144; static constexpr unsigned USE = 0x0f; };
template <> struct Shape<3> { static constexpr int MT = 2, NWF = 4, NMF = 8, NACC = 8, WB = 2048; static constexpr unsigned USE = 0x3d; };
template <> struct Shape<4> { static constexpr int MT = 4, NWF = 8, NMF = 28, NACC = 28, WB = 7168; static constexpr unsigned USE = 0x3f; };
template <> struct Shape<5> { static constexpr int MT = 4, NWF = 4, NMF = 16, NACC = 16, WB = 4096; static constexpr unsigned USE = 0x3f; };
DI lds_ptr lp(unsigned a) { return (lds_ptr)(size_t)a; }
template <int KIND, int J> DI void rd1(Fr<Shape<KIND>::MT, Shape<KIND>::NWF>& f, const Adr& ad, int kb) {
  constexpr int NWF = Shape<KIND>::NWF, MT = Shape<KIND>::MT, WB = Shape<KIND>::WB;
  if constexpr (J < 2 * NWF) {
    constexpr int n = J >> 1, t = J & 1;
    if constexpr (KIND == 4 && n >= 6) f.w[n][t] = lds_tr(lp(ad.v[4 + (n - 6)]) + (2 * kb + t) * WB);
    else if constexpr (KIND == 1 || KIND == 4) f.w[n][t] = lds_tr(lp(ad.v[2 + (n & 1)]) + (n >> 1) * 2048 + (2 * kb + t) * WB);
    else f.w[n][t] = lds_tr(lp(ad.v[2 + n]) + (2 * kb + t) * WB);
  } else if constexpr (J < 2 * NWF + MT) {
    constexpr int m = J - 2 * NWF;
    if constexpr (KIND == 1) f.a[m] = lds128(lp(ad.v[kb]) + m * 2048);
    else f.a[m] = lds128(lp(ad.v[m >> 1]) + (m & 1) * 2048 + kb * 1024);
  }
}
template <int KIND, int I> DI void mm1(f32x4 (&acc)[Shape<KIND>::NACC], const Fr<Shape<KIND>::MT, Shape<KIND>::NWF>& f) {
  if constexpr (KIND == 1) { constexpr int m = I / 6, n = I % 6; if constexpr (n < 4) mfma_ip(acc[I], cat(f.w[n]), f.a[m]); else mfma_ip(acc[I], f.a[m], cat(f.w[n])); }
  else if constexpr (KIND == 4) { if constexpr (I < 24) mfma_ip(acc[I], cat(f.w[I % 6]), f.a[I / 6]); else mfma_ip(acc[I], cat(f.w[6 + ((I - 24) & 1)]), f.a[(I - 24) >> 1]); }
  else mfma_ip(acc[I], cat(f.w[I % 4]), f.a[I / 4]);
}
template <int KIND, int RPG, bool READ, bool MOVE, int I = 0>
DI void step(f32x4 (&acc)[Shape<KIND>::NACC], const Fr<Shape<KIND>::MT, Shape<KIND>::NWF>& cur, Fr<Shape<KIND>::MT, Shape<KIND>::NWF>& nxt, Adr& ad, int kb, int delta) {
  constexpr int NMF = Shape<KIND>::NMF, G0 = (2 * Shape<KIND>::NWF + Shape<KIND>::MT + RPG - 1) / RPG;
  if constexpr (I < NMF) {
    if constexpr (READ) {
      rd1<KIND, I * RPG>(nxt, ad, kb);
      if constexpr (RPG > 1) rd1<KIND, I * RPG + 1>(nxt, ad, kb);
    }
    if constexpr (MOVE && I >= G0) {
      constexpr int per = (6 + (NMF - G0) - 1) / (NMF - G0);
#pragma unroll
      for (int k = (I - G0) * per; k < (I - G0 + 1) * per && k < 6; ++k)
        if ((Shape<KIND>::USE >> k) & 1u) { ad.v[k] += delta; asm volatile("" : "+v"(ad.v[k])); }
    }
    mm1<KIND, I>(acc, cur);
    SB();
    step<KIND, RPG, READ, MOVE, I + 1>(acc, cur, nxt, ad, kb, delta);
  }
}
template <int KIND, int J = 0> DI void rdall(Fr<Shape<KIND>::MT, Shape<KIND>::NWF>& f, const Adr& ad, int kb) {
  if constexpr (J < 2 * Shape<KIND>::NWF + Shape<KIND>::MT) { rd1<KIND, J>(f, ad, kb); rdall<KIND, J + 1>(f, ad, kb); }
}
#define ST4() do {} while (0)
#define ST4ARGS
#define ST4PASS
template <class C, int KIND, int RPG, int CNT>
DI void stages(f32x4 (&acc)[Shape<KIND>::NACC], Fr<Shape<KIND>::MT, Shape<KIND>::NWF>& f0, Fr<Shape<KIND>::MT, Shape<KIND>::NWF>& f1, Adr& ad, int& slot ST4ARGS) {
#pragma unroll 1
  for (int s = 0; s < CNT; ++s) {
    const int delta = (slot + 1 == C::NSLOT) ? -(C::NSLOT - 1) * C::STAGE : C::STAGE;
    slot = (slot + 1 == C::NSLOT) ? 0 : slot + 1;
    SB();
    step<KIND, RPG, true, true>(acc, f0, f1, ad, 1, delta);
    if (!C::LAG) wait_lgkm0();
    ST4();
    bar();
    ST4();
    SB();
    step<KIND, RPG, true, false>(acc, f1, f0, ad, 0, 0);
  }
}
template <class C, int KIND, int RPG, int H1, int H2, class Hook> DI void consume(f32x4 (&acc)[Shape<KIND>::NACC], Adr& ad, unsigned* dbg4, const Hook& hook) {
  Fr<Shape<KIND>::MT, Shape<KIND>::NWF> f0, f1;
#pragma unroll
  for (int i = 0; i < Shape<KIND>::NACC; ++i) acc[i] = (f32x4){0.f, 0.f, 0.f, 0.f};
  bar();
  rdall<KIND>(f0, ad, 0);
  hook(-1);
  wait_lgkm0();
  int slot = 0;
  if constexpr (H1 > 0) {
    stages<C, KIND, RPG, H1>(acc, f0, f1, ad, slot ST4PASS);
    hook(1);
    stages<C, KIND, RPG, H2 - H1>(acc, f0, f1, ad, slot ST4PASS);
    hook(2);
  }
  stages<C, KIND, RPG, C::NK - 1 - H2>(acc, f0, f1, ad, slot ST4PASS);
  SB();
  step<KIND, RPG, true, false>(acc, f0, f1, ad, 1, 0);
  wait_lgkm0();
  bar();
  SB();
  step<KIND, RPG, false, false>(acc, f1, f0, ad, 0, 0);
  asm volatile("s_nop 7\n\ts_nop 3");
  SB();
}
template <class C, int KIND, int RPG, class Hook> DI void consume_s(f32x4 (&acc)[Shape<KIND>::NACC], Adr& ad, const Hook& hook) {
  Fr<Shape<KIND>::MT, Shape<KIND>::NWF> f0, f1, g0, g1;
#pragma unroll
  for (int i = 0; i < Shape<KIND>::NACC; ++i) acc[i] = (f32x4){0.f, 0.f, 0.f, 0.f};
  bar();
  rdall<KIND>(f0, ad, 0); rdall<KIND>(f1, ad, 1);
  hook(-1);
#pragma unroll
  for (int k = 0; k < 6; ++k) ad.v[k] += C::STAGE;
  int slot = 1;
#pragma unroll 1
  for (int s = 0; s < C::NK - 2; s += 2) {
    int delta = (slot + 1 == C::NSLOT) ? -(C::NSLOT - 1) * C::STAGE : C::STAGE;
    slot = (slot + 1 == C::NSLOT) ? 0 : slot + 1;
    if (!C::LAG) wait_lgkm0();
    bar(); SB();
    step<KIND, RPG, true, false>(acc, f0, g0, ad, 0, 0);
    step<KIND, RPG, true, true>(acc, f1, g1, ad, 1, delta);
    delta = (slot + 1 == C::NSLOT) ? -(C::NSLOT - 1) * C::STAGE : C::STAGE;
    slot = (slot + 1 == C::NSLOT) ? 0 : slot + 1;
    if (!C::LAG) wait_lgkm0();
    bar(); SB();
    step<KIND, RPG, true, false>(acc, g0, f0, ad, 0, 0);
    step<KIND, RPG, true, true>(acc, g1, f1, ad, 1, delta);
  }
  if (!C::LAG) wait_lgkm0();
  bar(); SB();
  step<KIND, RPG, true, false>(acc, f0, g0, ad, 0, 0);
  step<KIND, RPG, true, false>(acc, f1, g1, ad, 1, 0);
  wait_lgkm0(); bar(); SB();
  step<KIND, RPG, false, false>(acc, g0, f0, ad, 0, 0);
  step<KIND, RPG, false, false>(acc, g1, f1, ad, 1, 0);
  asm volatile("s_nop 7\n\ts_nop 3");
  SB();
}
DI unsigned wlane(int lane, int nt) {
  const int g = lane >> 4, q = (lane >> 2) & 3, p = lane & 3, sw = (2 * g + (q >> 1)) & 3;
  return (4 * g + q) * 128 + ((nt ^ sw) << 5) + p * 8;
}
DI unsigned hlane(int lane, int nt) {
  const int g = lane >> 4, q = (lane >> 2) & 3, p = lane & 3;
  return (4 * g + q) * 64 + ((nt ^ (g & 1)) << 5) + p * 8;
}

template <int OP> DI float op2(float a, float b) { return OP == 0 ? a + b : OP == 1 ? __builtin_elementwise_maximum(a, b) : __builtin_elementwise_minimum(a, b); }
template <int OP> DI float red4(float x0, float x1, float x2, float x3) {
  auto a = __builtin_amdgcn_permlane16_swap(bu(x0), bu(x1), false, false);
  auto b = __builtin_amdgcn_permlane16_swap(bu(x2), bu(x3), false, false);
  const float y0 = op2<OP>(fb(a[0]), fb(a[1])), y1 = op2<OP>(fb(b[0]), fb(b[1]));
  auto c = __builtin_amdgcn_permlane32_swap(bu(y0), bu(y1), false, false);
  return op2<OP>(fb(c[0]), fb(c[1]));
}
DI void spread(float z, float (&o)[4]) {
  auto a = __builtin_amdgcn_permlane32_swap(bu(z), bu(z), false, false);
  auto b = __builtin_amdgcn_permlane16_swap(a[0], a[0], false, false);
  auto c = __builtin_amdgcn_permlane16_swap(a[1], a[1], false, false);
  o[0] = fb(b[0]); o[1] = fb(b[1]); o[2] = fb(c[0]); o[3] = fb(c[1]);
}
DI void dual_softmax4(const f32x4 (&p)[4][2], u32x4 (&pp)[4], u32x4 (&pn)[4]) {
  constexpr float L2E = 1.4426950408889634f;
  float mx[4], mn[4];
#pragma unroll
  for (int m = 0; m < 4; ++m) {
    mx[m] = op2<1>(op2<1>(op2<1>(op2<1>(p[m][0][0], p[m][0][1]), p[m][0][2]), op2<1>(op2<1>(p[m][0][3], p[m][1][0]), p[m][1][1])), op2<1>(p[m][1][2], p[m][1][3]));
    mn[m] = op2<2>(op2<2>(op2<2>(op2<2>(p[m][0][0], p[m][0][1]), p[m][0][2]), op2<2>(op2<2>(p[m][0][3], p[m][1][0]), p[m][1][1])), op2<2>(p[m][1][2], p[m][1][3]));
  }
  spread(-L2E * red4<1>(mx[0], mx[1], mx[2], mx[3]), mx);
  spread(L2E * red4<2>(mn[0], mn[1], mn[2], mn[3]), mn);
  f32x4 ep[4][2], en[4][2];
  float sp[4], sn[4];
#pragma unroll
  for (int m = 0; m < 4; ++m) {
#pragma unroll
    for (int F = 0; F < 2; ++F) {
      const f32x4 a = p[m][F] * L2E + mx[m], b = p[m][F] * (-L2E) + mn[m];
#pragma unroll
      for (int r = 0; r < 4; ++r) { ep[m][F][r] = __builtin_amdgcn_exp2f(a[r]); en[m][F][r] = __builtin_amdgcn_exp2f(b[r]); }
    }
    const f32x4 a = ep[m][0] + ep[m][1], b = en[m][0] + en[m][1];
    sp[m] = (a[0] + a[1]) + (a[2] + a[3]); sn[m] = (b[0] + b[1]) + (b[2] + b[3]);
  }
  spread(__builtin_amdgcn_rcpf(red4<0>(sp[0], sp[1], sp[2], sp[3])), sp);
  spread(__builtin_amdgcn_rcpf(red4<0>(sn[0], sn[1], sn[2], sn[3])), sn);
#pragma unroll
  for (int m = 0; m < 4; ++m) { pp[m] = pack8(ep[m][0] * sp[m], ep[m][1] * sp[m]); pn[m] = pack8(en[m][0] * sn[m], en[m][1] * sn[m]); }
}

DI float dot2(unsigned a, unsigned b, float c) { return __builtin_amdgcn_fdot2_f32_bf16(__builtin_bit_cast(bf16x2, a), __builtin_bit_cast(bf16x2, b), c, false); }
DI unsigned opaque(unsigned c) { asm volatile("" : "+s"(c)); return c; }
template <int NL> DI void colsum_load(u32x4 (&v)[NL], const char* w, int stride, int lane) {
#pragma unroll
  for (int i = 0; i < NL; ++i) v[i] = *(const u32x4*)(w + (size_t)(8 * i + (lane >> 3)) * stride + (lane & 7) * 16);
}
template <int NL> DI void colsum_finish(const u32x4 (&v)[NL], rsrc_t r, unsigned dst, int lane) {
  f32x2 s2[4];
#pragma unroll
  for (int j = 0; j < 4; ++j) s2[j] = (f32x2){bflo(v[0][j]), bfhi(v[0][j])};
#pragma unroll
  for (int i = 1; i < NL; ++i)
#pragma unroll
    for (int j = 0; j < 4; ++j) s2[j] += (f32x2){bflo(v[i][j]), bfhi(v[i][j])};
  const float s[8] = {s2[0][0], s2[0][1], s2[1][0], s2[1][1], s2[2][0], s2[2][1], s2[3][0], s2[3][1]};
  float t[4], u[2];
#pragma unroll
  for (int j = 0; j < 4; ++j) { auto q = __builtin_amdgcn_permlane32_swap(bu(s[j]), bu(s[j + 4]), false, false); t[j] = fb(q[0]) + fb(q[1]); }
#pragma unroll
  for (int j = 0; j < 2; ++j) { auto q = __builtin_amdgcn_permlane16_swap(bu(t[j]), bu(t[j + 2]), false, false); u[j] = fb(q[0]) + fb(q[1]); }
  const bool b3 = lane & 8;
  const float tot = (b3 ? u[1] : u[0]) + dpp<0x128>(b3 ? u[0] : u[1]);
  st4(r, bu(tot), ((lane & 7) * 8 + ((lane >> 5) & 1) * 4 + ((lane >> 4) & 1) * 2 + ((lane >> 3) & 1)) * 4, dst);
}

template <int XN, int TM, int TN> DI void tile_of(int bid, int& tm, int& tn, int& c) {
  const int x = bid & 7, r = bid >> 3, li = r % TM, lj = r / TM;
  tm = (x / XN) * TM + li; tn = (x % XN) * TN + lj; c = (li + lj) & 3;
}

#define RPG1 1
#define RPG4 1
#define RPG5 1
#define W2AT 0
constexpr int R1 = 40704, R3 = 40704, R4 = 0, R5 = 0, SYNCW = 163584;
constexpr int S1_ST = 0, S1_CSR = 1024, S1_CSF = 13312, S1_FLG = 14080, S1_WP = 14336, S1_KV = 18432, S1_DUM = 26880;
constexpr int S4_STR = C4::RING, S4_CSR = S4_STR + 16384, S4_STF = S4_CSR + 8192, S4_CSF = S4_STF + 1024, S4_DUM = S4_CSF + 1024;
constexpr int LDS_BYTES = 163840, CODE_BYTES = 54 * 1024;
static_assert(S4_DUM + 256 <= SYNCW && S1_KV + 8448 <= R1 && R1 + C1::RING <= SYNCW && R3 + C3::RING <= SYNCW && C5::RING <= S4_STR, "lds");

template <class St> DI void kv_sum(const Sync& y, Gate& G, lds_ptr smem, int head, int h, int t, bool poller, bool doz, int lane, const St& stamp) {
  u32x4 v[16], zz[4];
  unsigned have = 0;
  f32x2 s2[4] = {{0.f, 0.f}, {0.f, 0.f}, {0.f, 0.f}, {0.f, 0.f}};
#pragma unroll 1
  for (int round = poller ? 2 : 0; round < 3; ++round) {
    gate_slow(y, G, 0u, poller, lane, round == 0 ? 0 : round == 1 ? 13 : 16);
    stamp(round);
    const unsigned fresh = G.mask & ~have;
    have = G.mask;
#pragma unroll
    for (int r = 0; r < 16; ++r) if ((fresh >> r) & 1u) v[r] = ld16(y.r, t * 16, WS_KVP + (head * 16 + r) * 4096);
    if (round == 2 && doz) {
#pragma unroll
      for (int i = 0; i < 4; ++i) zz[i] = ld16(y.r, lane * 16, WS_ZP + head * 4096 + i * 1024);
    }
  }
#pragma unroll
  for (int r = 0; r < 16; ++r)
#pragma unroll
    for (int j = 0; j < 4; ++j) s2[j] += (f32x2){bflo(v[r][j]), bfhi(v[r][j])};
  const float s[8] = {s2[0][0], s2[0][1], s2[1][0], s2[1][1], s2[2][0], s2[2][1], s2[3][0], s2[3][1]};
  *(LAS u32x4*)(smem + S1_KV + h * 4096 + t * 16) = (u32x4){pk2(s[0], s[1]), pk2(s[2], s[3]), pk2(s[4], s[5]), pk2(s[6], s[7])};
  if (doz) {
    f32x4 z = (__builtin_bit_cast(f32x4, zz[0]) + __builtin_bit_cast(f32x4, zz[1])) + (__builtin_bit_cast(f32x4, zz[2]) + __builtin_bit_cast(f32x4, zz[3]));
#pragma unroll
    for (int j = 0; j < 4; ++j) z[j] = xsum32(xsum16(z[j]));
    if (lane < 16) *(LAS u32x2*)(smem + S1_KV + 8192 + h * 128 + lane * 8) = (u32x2){pk2(z[0], z[1]), pk2(z[2], z[3])};
  }
}

__global__ __launch_bounds__(512, 2) void fused(Params P) {
  extern __shared__ __attribute__((aligned(16))) char smem_[];
  lds_ptr smem = (lds_ptr)smem_;
  const unsigned lds0 = (unsigned)(size_t)smem;
  const int wid = __builtin_amdgcn_readfirstlane(threadIdx.x >> 6), lane = threadIdx.x & 63, bid = blockIdx.x;
  {
    const char* pc; asm volatile("s_getpc_b64 %0" : "=s"(pc));
    const unsigned co = ((bid >> 3) & 7) * 8192 + threadIdx.x * 128;
    if (threadIdx.x < 64 && co < CODE_BYTES) dma4<false>(co, pc, lds0 + S1_DUM);
    SB();
  }
  int tm1, tn1, c1, tm3, tn3, c3;
  tile_of<4, 8, 4>(bid, tm1, tn1, c1);
  tile_of<2, 4, 8>(bid, tm3, tn3, c3);
  const int kh = bid & 1, tm5 = ((bid & 7) >> 1) * 4 + ((bid >> 3) & 3), tn5 = bid >> 5, c5 = ((bid >> 3) + tn5) & 3;
  const int par = tn1 & 1, base7 = 7 * (tn1 >> 1), ch0 = base7 + 4 * par;
  const int g = lane >> 4, m16 = lane & 15;
  Sync y;
  y.r = __builtin_amdgcn_make_buffer_rsrc(P.ws, 0, WS_END, 0x00020000);
  y.w = (LAS unsigned*)(smem + SYNCW);
  if (wid == 0) TS(0);

  if (wid >= 4) {
    const int lw = wid - 4, lt = threadIdx.x - 256;
    const auto nop = [](int) {};
    {
      Ld<C1> L; L.A = P.x + (size_t)tm1 * 128 * 2048; L.W = P.wqkv + tn1 * 128; L.lw = lw; L.c = c1; L.kt0 = 0; L.init(y, lane, 2048, 0, 0, 0);
      const unsigned frec = (FL_P0 * 256 + (lane < 16 ? tm1 * 16 + lane : lane < 28 ? ((lane - 16) / 3) * 48 + ((lane - 16) % 3) * 16 + tn1 : tn1)) * 128;
      if (wid == 4) TS(31);
      if (lw == 0) dma4<true>(0u * lane, P.ws + WS_CTL, lds0 + S1_FLG);
      load_all<C1, false, (1u << 11) | (1u << 31), 0xc905u>(L, lds0 + R1,
        [&](int s) {
          if (s < 0) { if (wid == 4) TS(30); return; }
          if (lw == 0) {
            const u32x4 v = *(volatile LAS u32x4*)(smem + S1_CSR + lane * 16);
            if (!__all(v[0] == y.tag && v[1] == y.tag && v[2] == y.tag && v[3] == y.tag)) {
#pragma unroll 1
              for (int k = 0; k < 4; ++k) poll(y, frec + 4 * k, 64, lane);
            }
          }
        },
        [&](int s) {
          if (s == 0) y.tag = __builtin_amdgcn_readfirstlane(*(volatile LAS unsigned*)(smem + S1_FLG)) + 1u;
          if (s == 2) dma<false>(lane * 16, P.wphi + tn1 * 4096 + lw * 1024, lds0 + S1_WP + lw * 1024);
          if (s == 8 && lw == 0) dma<true>(frec, P.ws + WS_FLG, lds0 + S1_CSR);
          if (s == 15 && lw == 1) dma4<false>(lane * 128, P.wo + bid * 8192, lds0 + S1_DUM);
          if (s == 11) {
            if (lw == 3) dma<true>(lane * 16, P.ws + WS_ST1 + tm1 * 1024, lds0 + S1_ST);
#pragma unroll
            for (int i = 0; i < 3; ++i)
              dma<true>((lane >> 4) * 12288 + (lane & 15) * 16, P.ws + WS_CSQ + lw * 49152 + i * 4096 + tn1 * 256, lds0 + S1_CSR + i * 4096 + lw * 1024);
          }
          if (s == 14) {
            if (lt < 192) {
              const LAS float* rawp = (const LAS float*)(smem + S1_CSR) + (lt >> 6) * 1024 + (lt & 63);
              float t = 0.f;
#pragma unroll
              for (int i = 0; i < 16; ++i) t += rawp[i * 64];
              ((LAS float*)(smem + S1_CSF))[lt] = t;
            }
            wait_lgkm0();
          }
        }, DBG(0));
    }
    if (wid == 4) TS(16);
    {
      Ld<C3> L; L.A = P.ws + WS_O + (size_t)tm3 * 16 * 16384; L.W = P.wo + tn3 * 128; L.lw = lw; L.c = c3; L.kt0 = 0; L.init(y, lane, 0, 0, (FL_O * 256 + tm3 * 16 + m16) * 128, 9);
      load_preW<C3>(L, lds0 + R3);
      if (lw >= 2 && bid < 224) {
        u32x4 v[16];
        colsum_load<16>(v, P.w1 + (size_t)((bid / 56) * 256 + (lw - 2) * 128) * (FF * 2) + (bid % 56) * 128, FF * 2, lane);
        colsum_finish<16>(v, y.r, WS_CS1 + (((bid / 56) * 2 + lw - 2) * FF + (bid % 56) * 64) * 4, lane);
      }
      publish(y, FL_CS, bid, lane);
      lds_wait(y, 24 + FL_KV, 4u);
      Gate G; G.off = (FL_KV * 256 + m16 * 16 + tn1) * 128; G.mask = 0; G.word = 8;
      kv_sum(y, G, smem, 2 * tn1 + 1, 1, lt, lw == 0, lw == 1, lane, [&](int r) { if (wid == 4) { if (r == 0) TS(20); if (r == 1) TS(21); if (r == 2) TS(22); } });
      if (wid == 4) TS(17);
      wait_lgkm0(); bar();
      if (wid == 4) TS(18);
      load_all<C3, true, 0u, 0u>(L, lds0 + R3, nop, nop, DBG(1));
    }
    if (wid == 4) TS(19);
    {
      Ld<C4> L; L.A = P.ws + WS_H + (size_t)tm1 * 16 * 16384; L.W = P.w1 + ch0 * 128; L.lw = lw; L.c = c1; L.kt0 = 0;
      L.init(y, lane, 128, par ? -64 : 384, ((lane & 16) ? FL_CS * 256 + (m16 >> 2) * 56 + base7 + 3 * par + (lane & 3) : FL_H * 256 + tm1 * 16 + m16) * 128, 10);
      lds_wait(y, 24 + FL_H, 4u);
      load_preW<C4>(L, lds0 + R4);
      load_all<C4, true, 0u, 0xa000u | (W2AT == 0 ? 0x550u : W2AT > 0 ? 1u << (W2AT & 31) : 0u)>(L, lds0 + R4, nop,
        [&](int s) {
          if (W2AT == 0 ? (s < 12 && lw == ((s >> 1) & 3)) : s == W2AT) dma4<false>(min(lane, 55) * 128, P.w2 + bid * 28672 + lw * 7168, lds0 + S4_DUM);
          if (s == 13) {
            gate(y, L.G, ~0u, lw == 0, lane);
#pragma unroll
            for (int i = 0; i < 4; ++i) dma<true>(lane * 16, P.ws + WS_ST2 + (tm1 * 16 + lw * 4 + i) * 1024, lds0 + S4_STR + (lw * 4 + i) * 1024);
#pragma unroll
            for (int i = 0; i < 2; ++i) dma<true>(lane * 16, P.ws + WS_CS1 + ((lw * 2 + i) * FF + (base7 + 3 * par) * 64) * 4, lds0 + S4_CSR + (lw * 2 + i) * 1024);
          }
          if (s == 15) {
            const LAS float* rawp = (const LAS float*)(smem + S4_CSR) + lt;
            float t = 0.f;
#pragma unroll
            for (int i = 0; i < 8; ++i) t += rawp[i * 256];
            ((LAS float*)(smem + S4_CSF))[lt] = t;
            wait_lgkm0();
          }
          if (s == 15 && lw < 2) {
            f32x2 st[16]; float mu = 0.f, m2 = 0.f;
#pragma unroll
            for (int i = 0; i < 16; ++i) { st[i] = *(const LAS f32x2*)(smem + S4_STR + i * 1024 + lt * 8); mu += st[i][0]; }
            mu *= (1.f / 16.f);
#pragma unroll
            for (int i = 0; i < 16; ++i) { const float d = st[i][0] - mu; m2 += st[i][1] + 64.f * d * d; }
            *(LAS f32x2*)(smem + S4_STF + lt * 8) = (f32x2){mu, __builtin_amdgcn_rsqf(m2 * (1.f / D) + 1e-5f)};
            wait_lgkm0();
          }
        }, DBG(2));
    }
    {
      Ld<C5> L; L.A = P.ws + WS_F + (size_t)tm5 * 56 * 16384; L.W = P.w2 + tn5 * 256; L.lw = lw; L.c = c5; L.kt0 = 28 * kh; L.init(y, lane, 128, 0, (FL_F * 256 + tm5 * 16 + 8 * kh + ((lane >> 1) & 7)) * 128 + (lane & 1) * 4, 11);
      lds_wait(y, 24 + FL_F, 4u);
      load_preW<C5>(L, lds0 + R5);
      load_all<C5, true, 0u, 0u>(L, lds0 + R5, nop, nop, DBG(3));
    }
    if (wid == 4) { TS(23); TS_FLUSH(); }
    return;
  }

  const int wm = wid >> 1, wn = wid & 1;
  if (wid == 0 && lane < 32) y.w[lane] = 0u;
  if (wid == 2) {
    const int i1 = tm1 * 16 + tn1, i3 = tm3 * 16 + tn3, ph = lane & 7;
    const int idx = (ph == FL_P0 || ph == FL_CS) ? bid : ph == FL_H ? i3 : ph >= FL_X ? (tm5 * 8 + tn5) * 2 + kh : i1;
    dma4<false>(lane < 8 ? WS_FLG + (ph * 256 + idx) * 128 : lane < 16 ? WS_ST2 + i3 * 1024 + ph * 128 : WS_ZP + ((2 * tn1 + (lane & 1)) * 16 + tm1) * 256 + (lane & 2) * 64, P.ws, lds0 + S1_DUM);
  }
  {
    f32x4 acc[24];
    Adr ad;
    ad.v[0] = lds0 + R1 + (wm * 64 + m16) * 128 + ((g ^ (m16 >> 1)) << 4); ad.v[1] = lds0 + R1 + (wm * 64 + m16) * 128 + ((g ^ (m16 >> 1) ^ 4) << 4);
    ad.v[2] = lds0 + R1 + 16384 + wlane(lane, 2 * wn); ad.v[3] = lds0 + R1 + 16384 + wlane(lane, 2 * wn + 1); ad.v[4] = 0; ad.v[5] = 0;
    u32x4 px[4], pw[8];
    const int prow = bid * 8 + wid * 2 + (lane >> 5);
    consume<C1, 1, RPG1, 4, 6>(acc, ad, DBG4, [&](int s) {
      if (s < 0) {
        if (wid == 0) TS(1);
        y.tag = __builtin_amdgcn_readfirstlane(*(volatile LAS unsigned*)(smem + S1_FLG)) + 1u;
#pragma unroll
        for (int i = 0; i < 4; ++i) px[i] = *(const u32x4*)(P.x + (size_t)prow * 2048 + ((lane & 31) + 32 * i) * 16);
        colsum_load<8>(pw, P.wqkv + (size_t)((min(bid, 191) / 48) * 256 + wid * 64) * (3 * D * 2) + (bid % 48) * 128, 3 * D * 2, lane);
      }
      if (s == 1) {
#pragma unroll
        for (int i = 0; i < 4; ++i) asm volatile("" : "+v"(px[i]));
#pragma unroll
        for (int i = 0; i < 8; ++i) asm volatile("" : "+v"(pw[i]));
        const unsigned one2 = opaque(0x3f803f80u);
        float sm = 0.f, sq = 0.f;
#pragma unroll
        for (int i = 0; i < 4; ++i)
#pragma unroll
          for (int j = 0; j < 4; ++j) { sm = dot2(px[i][j], one2, sm); sq = dot2(px[i][j], px[i][j], sq); }
        const float mean = xsum16(row16_sum(sm)) * (1.f / D);
        const float var = fmaxf(xsum16(row16_sum(sq)) * (1.f / D) - mean * mean, 0.f);
        if ((lane & 31) == 0) st8(y.r, (u32x2){bu(mean), bu(__builtin_amdgcn_rsqf(var + 1e-5f))}, prow * 8, WS_ST1);
        if (bid < 192) colsum_finish<8>(pw, y.r, WS_CSQ + (((bid / 48) * 4 + wid) * 3 * D + (bid % 48) * 64) * 4, lane);
      }
      if (s == 2) { wait_vm<0>(); if (lane == 0) st4(y.r, y.tag, 0, WS_FLG + (FL_P0 * 256 + bid) * 128 + wid * 4); }
    });
    if (wid == 0) TS(2);
    const int head = 2 * tn1 + wn;
    f32x4 cs[4]; float csv[2];
#pragma unroll
    for (int n = 0; n < 4; ++n) cs[n] = *(const LAS f32x4*)(smem + S1_CSF + ((n >> 1) * 64 + wn * 32 + (n & 1) * 16 + 4 * g) * 4);
#pragma unroll
    for (int n = 0; n < 2; ++n) csv[n] = *(const LAS float*)(smem + S1_CSF + (128 + wn * 32 + n * 16 + m16) * 4);
    u32x4 wp[2];
#pragma unroll
    for (int F = 0; F < 2; ++F) { lds_ptr a0 = smem + S1_WP + wn * 2048 + (4 * g + (m16 >> 2)) * 64 + F * 32 + (lane & 3) * 8; wp[F] = lds_tr2(a0, a0 + 1024); }
    u32x4 qb[4], vb[2][2];
    f32x4 p[4][2];
    const f32x4 zero = {0.f, 0.f, 0.f, 0.f};
    {
      f32x4 vv[4][2];
#pragma unroll
      for (int m = 0; m < 4; ++m) {
        const f32x2 st = *(const LAS f32x2*)(smem + S1_ST + (wm * 64 + m * 16 + m16) * 8);
        const f32x4 s0 = *(const LAS f32x4*)(smem + S1_ST + (wm * 64 + m * 16 + 4 * g) * 8), s1 = *(const LAS f32x4*)(smem + S1_ST + (wm * 64 + m * 16 + 4 * g) * 8 + 16);
        const f32x4 mu = {s0[0], s0[2], s1[0], s1[2]}, rs = {s0[1], s0[3], s1[1], s1[3]};
        qb[m] = pack8((acc[m * 6] - cs[0] * st[0]) * st[1], (acc[m * 6 + 1] - cs[1] * st[0]) * st[1]);
        const u32x4 kb = pack8((acc[m * 6 + 2] - cs[2] * st[0]) * st[1], (acc[m * 6 + 3] - cs[3] * st[0]) * st[1]);
        p[m][0] = mfma(wp[0], kb, zero); p[m][1] = mfma(wp[1], kb, zero);
        vv[m][0] = (acc[m * 6 + 4] - mu * csv[0]) * rs; vv[m][1] = (acc[m * 6 + 5] - mu * csv[1]) * rs;
      }
#pragma unroll
      for (int u = 0; u < 2; ++u) { vb[u][0] = pack8(vv[2 * u][0], vv[2 * u + 1][0]); vb[u][1] = pack8(vv[2 * u][1], vv[2 * u + 1][1]); }
    }
    u32x4 pp[4], pn[4];
    if (wid == 0) TS(26);
    dual_softmax4(p, pp, pn);
    if (wid == 0) TS(27);
    {
      lds_ptr own = smem + R3 + wid * C3::STAGE, oth = smem + R3 + (wid ^ 2) * C3::STAGE;
#pragma unroll
      for (int m = 0; m < 4; ++m) {
        lds_ptr w = own + (16 * m + m16) * 136 + 8 * g;
        *(LAS u32x2*)(w) = (u32x2){pp[m][0], pp[m][1]}; *(LAS u32x2*)(w + 32) = (u32x2){pp[m][2], pp[m][3]};
        *(LAS u32x2*)(w + 64) = (u32x2){pn[m][0], pn[m][1]}; *(LAS u32x2*)(w + 96) = (u32x2){pn[m][2], pn[m][3]};
      }
#pragma unroll
      for (int i = 0; i < 4; ++i) *(LAS u32x4*)(own + 8704 + i * 1024 + lane * 16) = vb[i >> 1][i & 1];
      wait_lgkm0();
      volatile LAS unsigned* xf = y.w + 16;
      xf[wid] = 1u;
      f32x4 kv[2][2] = {{zero, zero}, {zero, zero}}, z[2] = {zero, zero};
      const u32x4 ones = {0x3f803f80u, 0x3f803f80u, 0x3f803f80u, 0x3f803f80u};
      const unsigned ro = (4 * g + (m16 >> 2)) * 136 + wm * 64 + (lane & 3) * 8;
#pragma unroll
      for (int u = 0; u < 2; ++u)
#pragma unroll
        for (int F = 0; F < 2; ++F) {
          const u32x4 pa = lds_tr2(own + ro + u * 32 * 136 + F * 32, own + ro + u * 32 * 136 + F * 32 + 16 * 136);
          kv[F][0] = mfma(pa, vb[u][0], kv[F][0]); kv[F][1] = mfma(pa, vb[u][1], kv[F][1]); z[F] = mfma(pa, ones, z[F]);
        }
      while (xf[wid ^ 2] == 0u) __builtin_amdgcn_s_sleep(0);
      asm volatile("" ::: "memory");
#pragma unroll
      for (int u = 0; u < 2; ++u) {
        const u32x4 v0 = lds128(oth + 8704 + u * 2048 + lane * 16), v1 = lds128(oth + 8704 + u * 2048 + 1024 + lane * 16);
#pragma unroll
        for (int F = 0; F < 2; ++F) {
          const u32x4 pa = lds_tr2(oth + ro + u * 32 * 136 + F * 32, oth + ro + u * 32 * 136 + F * 32 + 16 * 136);
          kv[F][0] = mfma(pa, v0, kv[F][0]); kv[F][1] = mfma(pa, v1, kv[F][1]); z[F] = mfma(pa, ones, z[F]);
        }
      }
      const unsigned kd = WS_KVP + (head * 16 + tm1) * 4096 + wm * 2048;
      st16(y.r, pack8(kv[0][0], kv[1][0]), lane * 16, kd);
      st16(y.r, pack8(kv[0][1], kv[1][1]), lane * 16, kd + 1024);
      if (m16 == 0) {
        st16(y.r, __builtin_bit_cast(u32x4, z[0]), g * 32, WS_ZP + (head * 16 + tm1) * 256 + wm * 128);
        st16(y.r, __builtin_bit_cast(u32x4, z[1]), g * 32 + 16, WS_ZP + (head * 16 + tm1) * 256 + wm * 128);
      }
      if (wid == 0) TS(25);
      publish(y, FL_KV, tm1 * 16 + tn1, lane);
    }
    if (wid == 0) TS(3);
#pragma unroll
    for (int m = 0; m < 4; ++m) { p[m][0] = mfma(wp[0], qb[m], zero); p[m][1] = mfma(wp[1], qb[m], zero); }
    dual_softmax4(p, pp, pn);
    if (wid == 0) TS(4);
    { Gate G; G.off = 0; G.mask = 0; G.word = 8; kv_sum(y, G, smem, 2 * tn1, 0, threadIdx.x, false, wid == 0, lane, [](int) {}); }
    wait_lgkm0(); bar();
    if (wid == 0) TS(5);
    u32x4 ka[2][2], za[2];
#pragma unroll
    for (int ks = 0; ks < 2; ++ks) {
      ka[ks][0] = lds128(smem + S1_KV + wn * 4096 + ks * 2048 + lane * 16); ka[ks][1] = lds128(smem + S1_KV + wn * 4096 + ks * 2048 + 1024 + lane * 16);
      za[ks] = lds128(smem + S1_KV + 8192 + wn * 128 + ks * 64 + g * 16);
    }
#pragma unroll
    for (int m = 0; m < 4; ++m) {
      f32x4 n0 = mfma(ka[0][0], pp[m], zero), n1 = mfma(ka[0][1], pp[m], zero), dn = mfma(za[0], pp[m], zero);
      n0 = mfma(ka[1][0], pn[m], n0); n1 = mfma(ka[1][1], pn[m], n1); dn = mfma(za[1], pn[m], dn);
      const float rd = __builtin_amdgcn_rcpf(dn[0] + 1e-6f);
      st16(y.r, pack8(n0 * rd, n1 * rd), lane * 16, WS_O + (((tm1 * 16 + tn1) * 8 + wm * 4 + m) * 2 + wn) * 1024);
    }
    publish(y, FL_O, tm1 * 16 + tn1, lane);
    if (wid == 0) TS(6);
  }
  {
    u32x4 xr[2][2];
#pragma unroll
    for (int m = 0; m < 2; ++m)
#pragma unroll
      for (int Q = 0; Q < 2; ++Q)
        xr[m][Q] = *(const u32x4*)(P.x + (size_t)(tm3 * 128 + wid * 32 + m * 16 + m16) * 2048 + tn3 * 128 + Q * 64 + (g & 1) * 32 + (g >> 1) * 16);
    f32x4 acc[8];
    Adr ad; ad.v[0] = lds0 + R3 + wid * 4096 + lane * 16; ad.v[1] = 0;
#pragma unroll
    for (int n = 0; n < 4; ++n) ad.v[2 + n] = lds0 + R3 + 16384 + wlane(lane, n);
    consume_s<C3, 3, 2>(acc, ad, [&](int) { if (wid == 0) TS(7); });
    if (wid == 0) TS(8);
#pragma unroll
    for (int m = 0; m < 2; ++m) {
      float hv[16];
#pragma unroll
      for (int Q = 0; Q < 2; ++Q) {
        auto r0 = __builtin_amdgcn_permlane16_swap(xr[m][Q][0], xr[m][Q][2], false, false);
        auto r1 = __builtin_amdgcn_permlane16_swap(xr[m][Q][1], xr[m][Q][3], false, false);
        const f32x4 a = acc[m * 4 + 2 * Q], b = acc[m * 4 + 2 * Q + 1];
        const u32x4 hp = {pk2(a[0] + bflo(r0[0]), a[1] + bfhi(r0[0])), pk2(a[2] + bflo(r1[0]), a[3] + bfhi(r1[0])),
                          pk2(b[0] + bflo(r0[1]), b[1] + bfhi(r0[1])), pk2(b[2] + bflo(r1[1]), b[3] + bfhi(r1[1]))};
        st16(y.r, hp, lane * 16, WS_H + (((tm3 * 16 + tn3) * 8 + wid * 2 + m) * 2 + Q) * 1024);
#pragma unroll
        for (int j = 0; j < 4; ++j) { hv[Q * 8 + 2 * j] = bflo(hp[j]); hv[Q * 8 + 2 * j + 1] = bfhi(hp[j]); }
      }
      float s = 0.f;
#pragma unroll
      for (int j = 0; j < 16; ++j) s += hv[j];
      s = xsum32(xsum16(s)) * (1.f / 64.f);
      float q = 0.f;
#pragma unroll
      for (int j = 0; j < 16; ++j) { const float d = hv[j] - s; q += d * d; }
      q = xsum32(xsum16(q));
      if (g == 0) st8(y.r, (u32x2){bu(s), bu(q)}, m16 * 8, WS_ST2 + (tm3 * 16 + tn3) * 1024 + (wid * 32 + m * 16) * 8);
    }
    if (wid == 0) TS(29);
    publish(y, FL_H, tm3 * 16 + tn3, lane);
    if (wid == 0) TS(9);
  }
  {
    f32x4 acc[28];
    Adr ad;
    ad.v[0] = lds0 + R4 + wm * 8192 + wn * 4096 + lane * 16; ad.v[1] = lds0 + R4 + wm * 8192 + (wn ^ 1) * 4096 + lane * 16;
    ad.v[2] = lds0 + R4 + 16384 + wlane(lane, wn); ad.v[3] = lds0 + R4 + 16384 + wlane(lane, 2 + wn);
    ad.v[4] = lds0 + R4 + 16384 + 6144 + hlane(lane, 0); ad.v[5] = lds0 + R4 + 16384 + 6144 + hlane(lane, 1);
    consume<C4, 4, RPG4, 0, 0>(acc, ad, DBG4, [&](int s) { if (s < 0 && wid == 0) TS(10); });
    if (wid == 0) TS(11);
    f32x4 cs[8];
#pragma unroll
    for (int n = 0; n < 6; ++n) cs[n] = *(const LAS f32x4*)(smem + S4_CSF + ((par + (n >> 1)) * 64 + (2 * (n & 1) + wn) * 16 + 4 * g) * 4);
#pragma unroll
    for (int n = 0; n < 2; ++n) cs[6 + n] = *(const LAS f32x4*)(smem + S4_CSF + ((par ? 32 : 192) + n * 16 + 4 * g) * 4);
    const f32x4 zero = {0.f, 0.f, 0.f, 0.f};
    f32x2 st[4];
#pragma unroll
    for (int m = 0; m < 4; ++m) st[m] = *(const LAS f32x2*)(smem + S4_STF + ((wm * 4 + ((m + 2 * wn) & 3)) * 16 + m16) * 8);
#pragma unroll
    for (int i = 0; i < 4; ++i)
#pragma unroll
      for (int m = 0; m < (i < 3 ? 4 : 2); ++m) {
        const unsigned fd = WS_F + (tm1 * 56 * 16 + (wm * 4 + ((m + 2 * wn) & 3)) * 2) * 1024 + (i < 3 ? (ch0 + i) * 16384 + wn * 1024 : (base7 + 3) * 16384 + par * 1024);
        const int a = i < 3 ? m * 6 + 2 * i : 24 + 2 * m;
        st16(y.r, pack8(__builtin_elementwise_max((acc[a] - cs[2 * i] * st[m][0]) * st[m][1], zero),
                        __builtin_elementwise_max((acc[a + 1] - cs[2 * i + 1] * st[m][0]) * st[m][1], zero)), lane * 16, fd);
        SB();
      }
    if (wid == 0) TS(28);
    if (lane == 0) __hip_atomic_fetch_add(y.w + 24 + FL_F, 1u, __ATOMIC_RELAXED, __HIP_MEMORY_SCOPE_WORKGROUP);
    wait_vm<10>();
    if (lane == 0 && __hip_atomic_fetch_add(y.w + FL_F, 1u, __ATOMIC_RELAXED, __HIP_MEMORY_SCOPE_WORKGROUP) == 3u) st4(y.r, y.tag, 0, WS_FLG + (FL_F * 256 + tm1 * 16 + tn1) * 128);
    if (wid == 0) TS(12);
  }
  {
    f32x4 acc[16];
    Adr ad;
    ad.v[0] = lds0 + R5 + wm * 8192 + kh * 4096 + lane * 16; ad.v[1] = lds0 + R5 + wm * 8192 + (kh ^ 1) * 4096 + lane * 16;
#pragma unroll
    for (int n = 0; n < 4; ++n) ad.v[2 + n] = lds0 + R5 + 16384 + wn * 2048 + wlane(lane, n);
    u32x4 hr[2][2];
    consume_s<C5, 5, 1>(acc, ad, [&](int s) {
      if (s < 0) {
        if (wid == 0) TS(13);
        wait_vm<0>();
        if (lane == 0 && __hip_atomic_fetch_add(y.w + 20, 1u, __ATOMIC_RELAXED, __HIP_MEMORY_SCOPE_WORKGROUP) == 3u) st4(y.r, y.tag, 0, WS_FLG + (FL_F * 256 + tm1 * 16 + tn1) * 128 + 4);
#pragma unroll
        for (int m = 0; m < 2; ++m)
#pragma unroll
          for (int Q = 0; Q < 2; ++Q) hr[m][Q] = ld16(y.r, lane * 16, WS_H + (((tm5 * 16 + 2 * tn5 + wn) * 8 + wm * 4 + 2 * kh + m) * 2 + Q) * 1024);
      }
    });
    if (wid == 0) TS(14);
    const int tile = tm5 * 8 + tn5;
#pragma unroll
    for (int i = 0; i < 4; ++i) st16(y.r, pack8(acc[8 + 2 * i], acc[9 + 2 * i]), lane * 16, WS_O + (((tile * 2 + kh) * 4 + wid) * 4 + i) * 1024);
    wait_vm<0>();
    if (lane == 0) st4(y.r, y.tag, 0, WS_FLG + ((FL_X + (wid >> 1)) * 256 + tile * 2 + kh) * 128 + (wid & 1) * 64);
    poll(y, ((FL_X + (wid >> 1)) * 256 + tile * 2 + (kh ^ 1)) * 128 + (wid & 1) * 64, 1, lane);
    if (wid == 0) TS(15);
    const rsrc_t ro = __builtin_amdgcn_make_buffer_rsrc(P.out, 0, 2048 * 2048, 0x00020000);
    u32x4 xv[4];
#pragma unroll
    for (int i = 0; i < 4; ++i) xv[i] = ld16(y.r, lane * 16, WS_O + (((tile * 2 + (kh ^ 1)) * 4 + wid) * 4 + i) * 1024);
#pragma unroll
    for (int m = 0; m < 2; ++m)
#pragma unroll
      for (int Q = 0; Q < 2; ++Q) {
        const u32x4 xx = xv[m * 2 + Q];
        const f32x4 a = acc[m * 4 + 2 * Q] + (f32x4){bflo(xx[0]), bfhi(xx[0]), bflo(xx[1]), bfhi(xx[1])}, b = acc[m * 4 + 2 * Q + 1] + (f32x4){bflo(xx[2]), bfhi(xx[2]), bflo(xx[3]), bfhi(xx[3])};
        const u32x4 hh = hr[m][Q];
        const unsigned a0 = pk2(a[0] + bflo(hh[0]), a[1] + bfhi(hh[0])), a1 = pk2(a[2] + bflo(hh[1]), a[3] + bfhi(hh[1]));
        const unsigned b0 = pk2(b[0] + bflo(hh[2]), b[1] + bfhi(hh[2])), b1 = pk2(b[2] + bflo(hh[3]), b[3] + bfhi(hh[3]));
        auto r0 = __builtin_amdgcn_permlane16_swap(a0, b0, false, false);
        auto r1 = __builtin_amdgcn_permlane16_swap(a1, b1, false, false);
        __builtin_amdgcn_raw_buffer_store_b128((u32x4){r0[0], r1[0], r0[1], r1[1]}, ro, (tm5 * 128 + wm * 64 + (2 * kh + m) * 16 + m16) * 2048 + tn5 * 256 + wn * 128 + Q * 64 + (g & 1) * 32 + (g >> 1) * 16, 0, 16);
      }
    if (bid == 0 && threadIdx.x == 0) st4(y.r, y.tag, 0, WS_CTL);
    if (wid == 0) { TS(24); TS_FLUSH(); }
  }
}

extern "C" void kernel_launch(void* const* d_in, const int* in_sizes, int n_in, void* d_out, int out_size, void* d_ws, size_t ws_size,
                              hipStream_t stream) {
  Params p;
  p.x = (const char*)d_in[0]; p.wqkv = (const char*)d_in[1]; p.wphi = (const char*)d_in[2]; p.wo = (const char*)d_in[3];
  p.w1 = (const char*)d_in[5]; p.w2 = (const char*)d_in[6]; p.ws = (char*)d_ws; p.out = (char*)d_out;
  (void)hipFuncSetAttribute((const void*)fused, hipFuncAttributeMaxDynamicSharedMemorySize, LDS_BYTES);
  fused<<<256, 512, LDS_BYTES, stream>>>(p);
}
```

```cpp
#include <hip/hip_runtime.h>
#include <hip/hip_bf16.h>

#define LAS __attribute__((address_space(3)))
#define DI __device__ __forceinline__
typedef __bf16 bf16x8 __attribute__((ext_vector_type(8)));
typedef __bf16 bf16x2 __attribute__((ext_vector_type(2)));
typedef short s16x4 __attribute__((ext_vector_type(4)));
typedef float f32x2 __attribute__((ext_vector_type(2)));
typedef float f32x4 __attribute__((ext_vector_type(4)));
typedef unsigned u32x2 __attribute__((ext_vector_type(2)));
typedef unsigned u32x4 __attribute__((ext_vector_type(4)));
typedef LAS char* lds_ptr;
typedef __amdgpu_buffer_rsrc_t rsrc_t;

constexpr int D = 1024, FF = 3584;
constexpr unsigned WS_CTL = 0;
constexpr unsigned WS_FLG = 4096;
constexpr unsigned WS_ST1 = 320u << 10;
constexpr unsigned WS_CSQ = 384u << 10;
constexpr unsigned WS_CS1 = 576u << 10;
constexpr unsigned WS_ZP = 704u << 10;
constexpr unsigned WS_ST2 = 832u << 10;
constexpr unsigned WS_SMALL = 1280u << 10;
constexpr unsigned WS_KVP = 2u << 20;
constexpr unsigned WS_O = 4u << 20;
constexpr unsigned WS_H = 8u << 20;
constexpr unsigned WS_F = 12u << 20;
constexpr unsigned WS_END = 26u << 20;
enum { FL_P0 = 0, FL_KV = 1, FL_O = 2, FL_H = 3, FL_F = 4, FL_CS = 5, FL_X = 6   };

#define TS(I) do {} while (0)
#define TS_FLUSH() do {} while (0)

#define DBG(k) nullptr
#define CLK(v)
#define DBG4 nullptr
struct Params { const char* x; const char* wqkv; const char* wphi; const char* wo; const char* w1; const char* w2; char* ws; char* out; };

DI unsigned pk2(float a, float b) { f32x2 v = {a, b}; return __builtin_bit_cast(unsigned, __builtin_convertvector(v, bf16x2)); }
DI float bflo(unsigned u) { return __uint_as_float(u << 16); }
DI float bfhi(unsigned u) { return __uint_as_float(u & 0xffff0000u); }
DI u32x4 pack8(const f32x4& a, const f32x4& b) { return (u32x4){pk2(a[0], a[1]), pk2(a[2], a[3]), pk2(b[0], b[1]), pk2(b[2], b[3])}; }
DI bf16x8 asbf(const u32x4& v) { return __builtin_bit_cast(bf16x8, v); }
#define SB() __builtin_amdgcn_sched_barrier(0)
DI void bar() { asm volatile("" ::: "memory"); __builtin_amdgcn_s_barrier(); asm volatile("" ::: "memory"); }
DI void wait_lgkm0() { __builtin_amdgcn_s_waitcnt(0xC07F); asm volatile("" ::: "memory"); }
template <int N> DI void wait_vm() { asm volatile("s_waitcnt vmcnt(%0)" ::"i"(N) : "memory"); }
template <bool SC1> DI void dma(unsigned voff, const char* sbase, unsigned lds) {
  if (SC1) asm volatile("s_mov_b32 m0, %2\n\ts_nop 3\n\tglobal_load_lds_dwordx4 %0, %1 sc1" ::"v"(voff), "s"(sbase), "s"(lds) : "memory");
  else asm volatile("s_mov_b32 m0, %2\n\ts_nop 3\n\tglobal_load_lds_dwordx4 %0, %1" ::"v"(voff), "s"(sbase), "s"(lds) : "memory");
}
template <bool SC1> DI void dma4(unsigned voff, const char* sbase, unsigned lds) {
  if (SC1) asm volatile("s_mov_b32 m0, %2\n\ts_nop 3\n\tglobal_load_lds_dword %0, %1 sc1" ::"v"(voff), "s"(sbase), "s"(lds) : "memory");
  else asm volatile("s_mov_b32 m0, %2\n\ts_nop 3\n\tglobal_load_lds_dword %0, %1" ::"v"(voff), "s"(sbase), "s"(lds) : "memory");
}
DI u32x4 lds128(lds_ptr p) { return *(const LAS u32x4*)p; }
DI u32x4 lds_tr2(lds_ptr p0, lds_ptr p1) {
  s16x4 a = __builtin_amdgcn_ds_read_tr16_b64_v4i16((LAS s16x4*)p0);
  s16x4 b = __builtin_amdgcn_ds_read_tr16_b64_v4i16((LAS s16x4*)p1);
  return __builtin_bit_cast(u32x4, __builtin_shufflevector(a, b, 0, 1, 2, 3, 4, 5, 6, 7));
}
DI u32x2 lds_tr(lds_ptr p) { return __builtin_bit_cast(u32x2, __builtin_amdgcn_ds_read_tr16_b64_v4i16((LAS s16x4*)p)); }
DI f32x4 mfma(const u32x4& a, const u32x4& b, const f32x4& c) { return __builtin_amdgcn_mfma_f32_16x16x32_bf16(asbf(a), asbf(b), c, 0, 0, 0); }
DI void mfma_ip(f32x4& acc, const u32x4& a, const u32x4& b) { asm volatile("v_mfma_f32_16x16x32_bf16 %0, %1, %2, %0" : "+v"(acc) : "v"(a), "v"(b)); }
DI u32x4 ld16(rsrc_t r, unsigned voff, unsigned soff) { return __builtin_amdgcn_raw_buffer_load_b128(r, voff, soff, 16); }
DI void st16(rsrc_t r, const u32x4& v, unsigned voff, unsigned soff) { __builtin_amdgcn_raw_buffer_store_b128(v, r, voff + soff, 0, 16);
}
DI void st8(rsrc_t r, const u32x2& v, unsigned voff, unsigned soff) { __builtin_amdgcn_raw_buffer_store_b64(v, r, voff, soff, 16); }
DI void st4(rsrc_t r, unsigned v, unsigned voff, unsigned soff) { __builtin_amdgcn_raw_buffer_store_b32(v, r, voff, soff, 16); }
DI float fb(unsigned u) { return __uint_as_float(u); }
DI unsigned bu(float f) { return __float_as_uint(f); }
DI float xsum16(float x) { auto r = __builtin_amdgcn_permlane16_swap(bu(x), bu(x), false, false); return fb(r[0]) + fb(r[1]); }
DI float xsum32(float x) { auto r = __builtin_amdgcn_permlane32_swap(bu(x), bu(x), false, false); return fb(r[0]) + fb(r[1]); }
template <int CTRL> DI float dpp(float x) { return __builtin_bit_cast(float, __builtin_amdgcn_update_dpp(0, __builtin_bit_cast(int, x), CTRL, 0xf, 0xf, true)); }
DI float row16_sum(float x) { x += dpp<0xB1>(x); x += dpp<0x4E>(x); x += dpp<0x141>(x); x += dpp<0x140>(x); return x; }

struct Sync { rsrc_t r; unsigned tag; LAS unsigned* w; };
DI void publish(const Sync& y, int phase, int idx, int lane) {
  if (lane == 0) __hip_atomic_fetch_add(y.w + 24 + phase, 1u, __ATOMIC_RELAXED, __HIP_MEMORY_SCOPE_WORKGROUP);
  wait_vm<0>();
  if (lane == 0 && __hip_atomic_fetch_add(y.w + phase, 1u, __ATOMIC_RELAXED, __HIP_MEMORY_SCOPE_WORKGROUP) == 3u) st4(y.r, y.tag, 0, WS_FLG + (phase * 256 + idx) * 128);
}
DI void poll(const Sync& y, unsigned off, int n, int lane) {
  const unsigned long long t0 = __builtin_amdgcn_s_memrealtime();
  for (unsigned it = 0;; ++it) {
    const unsigned v = __builtin_amdgcn_raw_buffer_load_b32(y.r, WS_FLG + off, 0, 16);
    if (__all(lane >= n || v == y.tag)) break;
    if ((it & 63) == 63) {
      if (__builtin_amdgcn_readfirstlane(__builtin_amdgcn_raw_buffer_load_b32(y.r, 0, WS_CTL + 128, 16)) == y.tag) break;
      if (__builtin_amdgcn_s_memrealtime() - t0 > 4000000ull) { if (lane == 0) st4(y.r, y.tag, 0, WS_CTL + 128); break; }
    }
    __builtin_amdgcn_s_sleep(2);
  }
  asm volatile("" ::: "memory");
}
DI void lds_wait(const Sync& y, int word, unsigned v) {
  volatile LAS unsigned* w = y.w + word;
  while (*w < v) __builtin_amdgcn_s_sleep(1);
  asm volatile("" ::: "memory");
}
struct Gate { unsigned off, mask; int word; };
DI void gate_slow(const Sync& y, Gate& G, unsigned need, bool poller, int lane, int cnt = 0) {
  volatile LAS unsigned* w = y.w + G.word;
  if (poller) {
    const unsigned long long t0 = __builtin_amdgcn_s_memrealtime();
    for (unsigned it = 0;; ++it) {
      G.mask = (unsigned)__ballot(__builtin_amdgcn_raw_buffer_load_b32(y.r, WS_FLG + G.off, 0, 16) == y.tag);
      if ((it & 63) == 63) {
        const bool late = __builtin_amdgcn_s_memrealtime() - t0 > 4000000ull;
        if (late && lane == 0) st4(y.r, y.tag, 0, WS_CTL + 128);
        if (late || __builtin_amdgcn_readfirstlane(__builtin_amdgcn_raw_buffer_load_b32(y.r, 0, WS_CTL + 128, 16)) == y.tag) G.mask = ~0u;
      }
      *w = G.mask;
      if ((G.mask & need) == need && __builtin_popcount(G.mask & 0xffffu) >= cnt) break;
      __builtin_amdgcn_s_sleep(1);
    }
  } else {
    for (;;) { G.mask = __builtin_amdgcn_readfirstlane(*w); if ((G.mask & need) == need && __builtin_popcount(G.mask & 0xffffu) >= cnt) break; __builtin_amdgcn_s_sleep(1); }
  }
  asm volatile("" ::: "memory");
}
DI void gate(const Sync& y, Gate& G, unsigned need, bool poller, int lane) { if (__builtin_expect((G.mask & need) != need, 0)) gate_slow(y, G, need, poller, lane); }

template <int NK, bool SPREAD> DI int kmap(int t, int c) {
  const int u = t - 1, q = u & 3, b4 = u & ~3, body = (int)(u < NK - 4);
  const int lead = ((int)(q == c) & body & (int)(u >= 0)) | (int)(SPREAD && t == 0), head = SPREAD ? 0 : -(int)(u < 4);
  const int non = ((b4 + ((c + 1 + q - ((int)(q > c) & body)) & 3)) & ~head) | ((t - (int)(u > c)) & head);
  return non ^ (((b4 + 4 + c) ^ non) & -lead);
}
#define LAG3 1
#define LAG5 1
enum { ORD_NAT = 0, ORD_PAIR = 1, ORD_F = 2 };
template <int NSUB_, bool HALF_, int NSLOT_, int NK_, int ORD_, int WSTRIDE_, bool XROW_, int LAG_> struct Cfg {
  static constexpr int NSUB = NSUB_, NSLOT = NSLOT_, NK = NK_, ORD = ORD_, WSTRIDE = WSTRIDE_;
  static constexpr bool HALF = HALF_, XROW = XROW_;
  static constexpr int LAG = LAG_;
  static constexpr int STAGE = 16384 + NSUB * 8192 + (HALF ? 4096 : 0), PW = 4 + 2 * NSUB + (HALF ? 1 : 0), RING = STAGE * NSLOT;
  static constexpr int WB = NSUB * 2048 + (HALF ? 1024 : 0);
};
typedef Cfg<3, false, 3, 16, ORD_NAT, 3 * D * 2, true, 0> C1;
typedef Cfg<1, false, 5, 16, ORD_PAIR, D * 2, false, LAG3> C3;
typedef Cfg<3, true, 3, 16, ORD_PAIR, FF * 2, false, 0> C4;
typedef Cfg<2, false, 4, 28, ORD_F, D * 2, false, LAG5> C5;

#define GLDS "global_load_lds_dwordx4 "
template <class C> struct Ld {
  const char* A; const char* W; int lw, c, kt0; unsigned vA[4], vW[7];
  const char* sA; const char* sW; unsigned need;
  Gate G; const Sync* y; int lane_;
  static constexpr int C0 = C::WB > 4096 ? 4096 : 0;
  DI void init(const Sync& y_, int lane, int substep, int halfrel, unsigned goff, int gword) {
    y = &y_; lane_ = lane; G.off = goff; G.mask = 0; G.word = gword;
#pragma unroll
    for (int i = 0; i < 4; ++i) vA[i] = C::XROW ? (lane >> 3) * 2048 + ((((lane & 7) ^ (lane >> 4)) << 4) ^ ((i & 1) * 64)) + i * (16384 - 1024) : lane * 16;
    const int rl = (C::ORD == ORD_NAT) ? 8 * (lane >> 5) + ((lane >> 3) & 3) : (lane >> 3);
    const unsigned w0 = rl * C::WSTRIDE + (((lane & 7) ^ (((lane >> 4) & 3) << 1)) << 4) + 4096;
#pragma unroll
    for (int q = 0; q < 2 * C::NSUB; ++q) vW[q] = w0 + (q & 1) * ((C::ORD == ORD_NAT ? 16 : 8) * C::WSTRIDE) + (q >> 1) * substep - (q * 1024 - C0);
    if (C::HALF) vW[6] = (lane >> 2) * C::WSTRIDE + (((lane & 3) ^ (((lane >> 4) & 1) << 1)) << 4) + 4096 + halfrel - (6144 - C0);
    A += C::XROW ? lw * 32 * 2048 : lw * 4096;
    W -= 4096;
  }
  DI void prep(int s) {
    int kt = kmap<C::NK, C::XROW>(s, c);
    const int kb = lw >> 1, t = lw & 1;
    int ur;
    need = 1u << kt;
    if (C::ORD == ORD_NAT) ur = 32 * kb + 4 * t;
    else if (C::ORD == ORD_F) {
      const int p = kt & 7, q = kt >> 3, sp = -(int)(q == 3);
      need = ((1u << (2 * p + (int)(q > 0))) & ~sp) | ((0xau << (4 * (p & 3))) & sp);
      kt = kt0 + (((7 * (p >> 1) + 4 * (p & 1) + q) & ~sp) | ((7 * p + 3) & sp));
      ur = (16 * lw & sp) | ((16 * kb + 32 * t) & ~sp);
    }
    else ur = 16 * lw;
    sA = A + (unsigned)(kt * (C::XROW ? 128 : 16384));
    sW = W + (unsigned)((kt * 64 + ur) * C::WSTRIDE);
  }
  DI void issueA(unsigned base) {
    if (!C::XROW) gate(*y, G, need, lw == 0, lane_);
    const unsigned m = base + lw * 4096;
    if (C::XROW) asm volatile("s_mov_b32 m0, %5\n\ts_nop 3\n\t" GLDS "%0, %4\n\t" GLDS "%1, %4 offset:1024\n\t" GLDS "%2, %4 offset:2048\n\t" GLDS "%3, %4 offset:3072"
                              ::"v"(vA[0]), "v"(vA[1]), "v"(vA[2]), "v"(vA[3]), "s"(sA), "s"(m) : "memory");
    else asm volatile("s_mov_b32 m0, %2\n\ts_nop 3\n\t" GLDS "%0, %1 sc1\n\t" GLDS "%0, %1 offset:1024 sc1\n\t" GLDS "%0, %1 offset:2048 sc1\n\t" GLDS "%0, %1 offset:3072 sc1"
                      ::"v"(vA[0]), "s"(sA), "s"(m) : "memory");
  }
  DI void issueW(unsigned base) const {
    const unsigned m = base + 16384 + lw * C::WB + C0;
    if (C::NSUB == 1) asm volatile("s_mov_b32 m0, %3\n\ts_nop 3\n\t" GLDS "%0, %2\n\t" GLDS "%1, %2 offset:1024" ::"v"(vW[0]), "v"(vW[1]), "s"(sW), "s"(m) : "memory");
    if (C::NSUB == 2) asm volatile("s_mov_b32 m0, %5\n\ts_nop 3\n\t" GLDS "%0, %4\n\t" GLDS "%1, %4 offset:1024\n\t" GLDS "%2, %4 offset:2048\n\t" GLDS "%3, %4 offset:3072"
                                   ::"v"(vW[0]), "v"(vW[1]), "v"(vW[2]), "v"(vW[3]), "s"(sW), "s"(m) : "memory");
    if (C::NSUB == 3) asm volatile("s_mov_b32 m0, %7\n\ts_nop 3\n\t" GLDS "%0, %6 offset:-4096\n\t" GLDS "%1, %6 offset:-3072\n\t" GLDS "%2, %6 offset:-2048\n\t" GLDS "%3, %6 offset:-1024\n\t"
                                   GLDS "%4, %6\n\t" GLDS "%5, %6 offset:1024"
                                   ::"v"(vW[0]), "v"(vW[1]), "v"(vW[2]), "v"(vW[3]), "v"(vW[4]), "v"(vW[5]), "s"(sW), "s"(m) : "memory");
    if (C::HALF) asm volatile(GLDS "%0, %1 offset:2048" ::"v"(vW[6]), "s"(sW) : "memory");
  }
};
template <class C> DI void load_preW(Ld<C>& L, unsigned ring) {
#pragma unroll 1
  for (int s = 0; s < C::NSLOT; ++s) { L.prep(s); L.issueW(ring + s * C::STAGE); }
}
template <class C, bool SPLIT> constexpr int vm_allow(int S) {
  const int N = C::NSLOT, behind = S - 1 - C::LAG > 0 ? S - 1 - C::LAG : 0;
  const int ramp = S + 1 < N - 1 ? S + 1 : N - 1;
  const int last = ramp + behind < C::NK - 1 ? ramp + behind : C::NK - 1;
  return (SPLIT && S < N) ? 4 * (ramp - S) + C::PW * (last - ramp) : C::PW * (last - S);
}
template <class C, bool SPLIT, unsigned POSTM, int S, int SE, class Post> DI void load_fixed(Ld<C>& L, unsigned ring, const Post& post) {
  if constexpr (S < SE) {
    static_assert(C::NSLOT <= C::LAG + 4, "the ring must be full before the first refill");
    wait_vm<vm_allow<C, SPLIT>(S)>();
    bar();
    if constexpr (S + 2 < C::NSLOT) {
      if (!SPLIT) L.issueW(ring + (S + 2) * C::STAGE);
      L.issueA(ring + (S + 2) * C::STAGE);
      L.prep(S + 3);
    }
    constexpr int T = S - 1 - C::LAG + C::NSLOT;
    if constexpr (S - 1 - C::LAG >= 0 && T < C::NK) {
      L.issueW(ring + (T % C::NSLOT) * C::STAGE); L.issueA(ring + (T % C::NSLOT) * C::STAGE);
      if constexpr (T + 1 < C::NK) L.prep(T + 1);
    }
    if constexpr ((POSTM >> S) & 1u) post(S);
    load_fixed<C, SPLIT, POSTM, S + 1, SE>(L, ring, post);
  }
}
template <class C, bool SPLIT, unsigned PREM, unsigned POSTM, class Pre, class Post>
DI void load_all(Ld<C>& L, unsigned ring, const Pre& pre, const Post& post, unsigned long long* dbg = nullptr) {
  constexpr int N = C::NSLOT, S0 = N - 1 > 2 + C::LAG ? N - 1 : 2 + C::LAG, S1 = C::NK - N + C::LAG;
#pragma unroll 1
  for (int s = 0; s < 2; ++s) { L.prep(s); if (!SPLIT) L.issueW(ring + s * C::STAGE); L.issueA(ring + s * C::STAGE); }
  L.prep(2);
  if (PREM >> 31) pre(-1);
  __builtin_amdgcn_s_setprio(3);
  load_fixed<C, SPLIT, POSTM, 0, S0>(L, ring, post);
  static_assert(vm_allow<C, SPLIT>(S0) == (N - 2 - C::LAG) * C::PW && vm_allow<C, SPLIT>(S1) == (N - 2 - C::LAG) * C::PW, "steady count");
  unsigned b = ring + ((S0 - 1 - C::LAG) % N) * C::STAGE;
#pragma unroll 1
  for (int s = S0; s <= S1; ++s) {
    CLK(t0);
    wait_vm<(N - 2 - C::LAG) * C::PW>();
    if ((PREM & 0x7fffffffu) && ((PREM >> s) & 1u)) pre(s);
    CLK(t1);
    bar();
    CLK(t2);
    L.issueW(b);
    L.issueA(b);
    CLK(t3);
    L.prep(min(s - C::LAG + N, C::NK - 1));
    b = (b == ring + (N - 1) * C::STAGE) ? ring : b + C::STAGE;
    if (POSTM && ((POSTM >> s) & 1u)) post(s);
  }
  __builtin_amdgcn_s_setprio(0);
  load_fixed<C, SPLIT, POSTM, S1 + 1, C::NK>(L, ring, post);
  bar();
}

template <int MT, int NWF> struct Fr { u32x4 a[MT]; u32x2 w[NWF][2]; };
DI u32x4 cat(const u32x2 (&w)[2]) { return (u32x4){w[0][0], w[0][1], w[1][0], w[1][1]}; }
struct Adr { unsigned v[6]; };
template <int KIND> struct Shape;
template <> struct Shape<1> { static constexpr int MT = 4, NWF = 6, NMF = 24, NACC = 24, WB = 6144; static constexpr unsigned USE = 0x0f; };
template <> struct Shape<3> { static constexpr int MT = 2, NWF = 4, NMF = 8, NACC = 8, WB = 2048; static constexpr unsigned USE = 0x3d; };
template <> struct Shape<4> { static constexpr int MT = 4, NWF = 8, NMF = 28, NACC = 28, WB = 7168; static constexpr unsigned USE = 0x3f; };
template <> struct Shape<5> { static constexpr int MT = 4, NWF = 4, NMF = 16, NACC = 16, WB = 4096; static constexpr unsigned USE = 0x3f; };
DI lds_ptr lp(unsigned a) { return (lds_ptr)(size_t)a; }
template <int KIND, int J> DI void rd1(Fr<Shape<KIND>::MT, Shape<KIND>::NWF>& f, const Adr& ad, int kb) {
  constexpr int NWF = Shape<KIND>::NWF, MT = Shape<KIND>::MT, WB = Shape<KIND>::WB;
  if constexpr (J < 2 * NWF) {
    constexpr int n = J >> 1, t = J & 1;
    if constexpr (KIND == 4 && n >= 6) f.w[n][t] = lds_tr(lp(ad.v[4 + (n - 6)]) + (2 * kb + t) * WB);
    else if constexpr (KIND == 1 || KIND == 4) f.w[n][t] = lds_tr(lp(ad.v[2 + (n & 1)]) + (n >> 1) * 2048 + (2 * kb + t) * WB);
    else f.w[n][t] = lds_tr(lp(ad.v[2 + n]) + (2 * kb + t) * WB);
  } else if constexpr (J < 2 * NWF + MT) {
    constexpr int m = J - 2 * NWF;
    if constexpr (KIND == 1) f.a[m] = lds128(lp(ad.v[kb]) + m * 2048);
    else f.a[m] = lds128(lp(ad.v[m >> 1]) + (m & 1) * 2048 + kb * 1024);
  }
}
template <int KIND, int I> DI void mm1(f32x4 (&acc)[Shape<KIND>::NACC], const Fr<Shape<KIND>::MT, Shape<KIND>::NWF>& f) {
  if constexpr (KIND == 1) { constexpr int m = I / 6, n = I % 6; if constexpr (n < 4) mfma_ip(acc[I], cat(f.w[n]), f.a[m]); else mfma_ip(acc[I], f.a[m], cat(f.w[n])); }
  else if constexpr (KIND == 4) { if constexpr (I < 24) mfma_ip(acc[I], cat(f.w[I % 6]), f.a[I / 6]); else mfma_ip(acc[I], cat(f.w[6 + ((I - 24) & 1)]), f.a[(I - 24) >> 1]); }
  else mfma_ip(acc[I], cat(f.w[I % 4]), f.a[I / 4]);
}
template <int KIND, int RPG, bool READ, bool MOVE, int I = 0>
DI void step(f32x4 (&acc)[Shape<KIND>::NACC], const Fr<Shape<KIND>::MT, Shape<KIND>::NWF>& cur, Fr<Shape<KIND>::MT, Shape<KIND>::NWF>& nxt, Adr& ad, int kb, int delta) {
  constexpr int NMF = Shape<KIND>::NMF, G0 = (2 * Shape<KIND>::NWF + Shape<KIND>::MT + RPG - 1) / RPG;
  if constexpr (I < NMF) {
    if constexpr (READ) {
      rd1<KIND, I * RPG>(nxt, ad, kb);
      if constexpr (RPG > 1) rd1<KIND, I * RPG + 1>(nxt, ad, kb);
    }
    if constexpr (MOVE && I >= G0) {
      constexpr int per = (6 + (NMF - G0) - 1) / (NMF - G0);
#pragma unroll
      for (int k = (I - G0) * per; k < (I - G0 + 1) * per && k < 6; ++k)
        if ((Shape<KIND>::USE >> k) & 1u) { ad.v[k] += delta; asm volatile("" : "+v"(ad.v[k])); }
    }
    mm1<KIND, I>(acc, cur);
    SB();
    step<KIND, RPG, READ, MOVE, I + 1>(acc, cur, nxt, ad, kb, delta);
  }
}
template <int KIND, int J = 0> DI void rdall(Fr<Shape<KIND>::MT, Shape<KIND>::NWF>& f, const Adr& ad, int kb) {
  if constexpr (J < 2 * Shape<KIND>::NWF + Shape<KIND>::MT) { rd1<KIND, J>(f, ad, kb); rdall<KIND, J + 1>(f, ad, kb); }
}
#define ST4() do {} while (0)
#define ST4ARGS
#define ST4PASS
template <class C, int KIND, int RPG, int CNT>
DI void stages(f32x4 (&acc)[Shape<KIND>::NACC], Fr<Shape<KIND>::MT, Shape<KIND>::NWF>& f0, Fr<Shape<KIND>::MT, Shape<KIND>::NWF>& f1, Adr& ad, int& slot ST4ARGS) {
#pragma unroll 1
  for (int s = 0; s < CNT; ++s) {
    const int delta = (slot + 1 == C::NSLOT) ? -(C::NSLOT - 1) * C::STAGE : C::STAGE;
    slot = (slot + 1 == C::NSLOT) ? 0 : slot + 1;
    SB();
    step<KIND, RPG, true, true>(acc, f0, f1, ad, 1, delta);
    if (!C::LAG) wait_lgkm0();
    ST4();
    bar();
    ST4();
    SB();
    step<KIND, RPG, true, false>(acc, f1, f0, ad, 0, 0);
  }
}
template <class C, int KIND, int RPG, int H0, int H1, int H2, class Hook> DI void consume(f32x4 (&acc)[Shape<KIND>::NACC], Adr& ad, unsigned* dbg4, const Hook& hook) {
  Fr<Shape<KIND>::MT, Shape<KIND>::NWF> f0, f1;
#pragma unroll
  for (int i = 0; i < Shape<KIND>::NACC; ++i) acc[i] = (f32x4){0.f, 0.f, 0.f, 0.f};
  bar();
  rdall<KIND>(f0, ad, 0);
  hook(-1);
  wait_lgkm0();
  int slot = 0;
  if constexpr (H1 > 0) {
    if constexpr (H0 > 0) stages<C, KIND, RPG, H0>(acc, f0, f1, ad, slot ST4PASS);
    hook(0);
    stages<C, KIND, RPG, H1 - H0>(acc, f0, f1, ad, slot ST4PASS);
    hook(1);
    stages<C, KIND, RPG, H2 - H1>(acc, f0, f1, ad, slot ST4PASS);
    hook(2);
  }
  stages<C, KIND, RPG, C::NK - 1 - H2>(acc, f0, f1, ad, slot ST4PASS);
  SB();
  step<KIND, RPG, true, false>(acc, f0, f1, ad, 1, 0);
  wait_lgkm0();
  bar();
  SB();
  step<KIND, RPG, false, false>(acc, f1, f0, ad, 0, 0);
  asm volatile("s_nop 7\n\ts_nop 3");
  SB();
}
template <class C, int KIND, int RPG, class Hook> DI void consume_s(f32x4 (&acc)[Shape<KIND>::NACC], Adr& ad, const Hook& hook) {
  Fr<Shape<KIND>::MT, Shape<KIND>::NWF> f0, f1, g0, g1;
#pragma unroll
  for (int i = 0; i < Shape<KIND>::NACC; ++i) acc[i] = (f32x4){0.f, 0.f, 0.f, 0.f};
  bar();
  rdall<KIND>(f0, ad, 0); rdall<KIND>(f1, ad, 1);
  hook(-1);
#pragma unroll
  for (int k = 0; k < 6; ++k) ad.v[k] += C::STAGE;
  int slot = 1;
#pragma unroll 1
  for (int s = 0; s < C::NK - 2; s += 2) {
    int delta = (slot + 1 == C::NSLOT) ? -(C::NSLOT - 1) * C::STAGE : C::STAGE;
    slot = (slot + 1 == C::NSLOT) ? 0 : slot + 1;
    if (!C::LAG) wait_lgkm0();
    bar(); SB();
    step<KIND, RPG, true, false>(acc, f0, g0, ad, 0, 0);
    step<KIND, RPG, true, true>(acc, f1, g1, ad, 1, delta);
    delta = (slot + 1 == C::NSLOT) ? -(C::NSLOT - 1) * C::STAGE : C::STAGE;
    slot = (slot + 1 == C::NSLOT) ? 0 : slot + 1;
    if (!C::LAG) wait_lgkm0();
    bar(); SB();
    step<KIND, RPG, true, false>(acc, g0, f0, ad, 0, 0);
    step<KIND, RPG, true, true>(acc, g1, f1, ad, 1, delta);
  }
  if (!C::LAG) wait_lgkm0();
  bar(); SB();
  step<KIND, RPG, true, false>(acc, f0, g0, ad, 0, 0);
  step<KIND, RPG, true, false>(acc, f1, g1, ad, 1, 0);
  wait_lgkm0(); bar(); SB();
  step<KIND, RPG, false, false>(acc, g0, f0, ad, 0, 0);
  step<KIND, RPG, false, false>(acc, g1, f1, ad, 1, 0);
  asm volatile("s_nop 7\n\ts_nop 3");
  SB();
}
DI unsigned wlane(int lane, int nt) {
  const int g = lane >> 4, q = (lane >> 2) & 3, p = lane & 3, sw = (2 * g + (q >> 1)) & 3;
  return (4 * g + q) * 128 + ((nt ^ sw) << 5) + p * 8;
}
DI unsigned hlane(int lane, int nt) {
  const int g = lane >> 4, q = (lane >> 2) & 3, p = lane & 3;
  return (4 * g + q) * 64 + ((nt ^ (g & 1)) << 5) + p * 8;
}

template <int OP> DI float op2(float a, float b) { return OP == 0 ? a + b : OP == 1 ? __builtin_elementwise_maximum(a, b) : __builtin_elementwise_minimum(a, b); }
template <int OP> DI float red4(float x0, float x1, float x2, float x3) {
  auto a = __builtin_amdgcn_permlane16_swap(bu(x0), bu(x1), false, false);
  auto b = __builtin_amdgcn_permlane16_swap(bu(x2), bu(x3), false, false);
  const float y0 = op2<OP>(fb(a[0]), fb(a[1])), y1 = op2<OP>(fb(b[0]), fb(b[1]));
  auto c = __builtin_amdgcn_permlane32_swap(bu(y0), bu(y1), false, false);
  return op2<OP>(fb(c[0]), fb(c[1]));
}
DI void spread(float z, float (&o)[4]) {
  auto a = __builtin_amdgcn_permlane32_swap(bu(z), bu(z), false, false);
  auto b = __builtin_amdgcn_permlane16_swap(a[0], a[0], false, false);
  auto c = __builtin_amdgcn_permlane16_swap(a[1], a[1], false, false);
  o[0] = fb(b[0]); o[1] = fb(b[1]); o[2] = fb(c[0]); o[3] = fb(c[1]);
}
DI void dual_softmax4(const f32x4 (&p)[4][2], u32x4 (&pp)[4], u32x4 (&pn)[4]) {
  constexpr float L2E = 1.4426950408889634f;
  float mx[4], mn[4];
#pragma unroll
  for (int m = 0; m < 4; ++m) {
    mx[m] = op2<1>(op2<1>(op2<1>(op2<1>(p[m][0][0], p[m][0][1]), p[m][0][2]), op2<1>(op2<1>(p[m][0][3], p[m][1][0]), p[m][1][1])), op2<1>(p[m][1][2], p[m][1][3]));
    mn[m] = op2<2>(op2<2>(op2<2>(op2<2>(p[m][0][0], p[m][0][1]), p[m][0][2]), op2<2>(op2<2>(p[m][0][3], p[m][1][0]), p[m][1][1])), op2<2>(p[m][1][2], p[m][1][3]));
  }
  spread(-L2E * red4<1>(mx[0], mx[1], mx[2], mx[3]), mx);
  spread(L2E * red4<2>(mn[0], mn[1], mn[2], mn[3]), mn);
  f32x4 ep[4][2], en[4][2];
  float sp[4], sn[4];
#pragma unroll
  for (int m = 0; m < 4; ++m) {
#pragma unroll
    for (int F = 0; F < 2; ++F) {
      const f32x4 a = p[m][F] * L2E + mx[m], b = p[m][F] * (-L2E) + mn[m];
#pragma unroll
      for (int r = 0; r < 4; ++r) { ep[m][F][r] = __builtin_amdgcn_exp2f(a[r]); en[m][F][r] = __builtin_amdgcn_exp2f(b[r]); }
    }
    const f32x4 a = ep[m][0] + ep[m][1], b = en[m][0] + en[m][1];
    sp[m] = (a[0] + a[1]) + (a[2] + a[3]); sn[m] = (b[0] + b[1]) + (b[2] + b[3]);
  }
  spread(__builtin_amdgcn_rcpf(red4<0>(sp[0], sp[1], sp[2], sp[3])), sp);
  spread(__builtin_amdgcn_rcpf(red4<0>(sn[0], sn[1], sn[2], sn[3])), sn);
#pragma unroll
  for (int m = 0; m < 4; ++m) { pp[m] = pack8(ep[m][0] * sp[m], ep[m][1] * sp[m]); pn[m] = pack8(en[m][0] * sn[m], en[m][1] * sn[m]); }
}

DI float dot2(unsigned a, unsigned b, float c) { return __builtin_amdgcn_fdot2_f32_bf16(__builtin_bit_cast(bf16x2, a), __builtin_bit_cast(bf16x2, b), c, false); }
DI unsigned opaque(unsigned c) { asm volatile("" : "+s"(c)); return c; }
template <int NL> DI void colsum_load(u32x4 (&v)[NL], const char* w, int stride, int lane) {
#pragma unroll
  for (int i = 0; i < NL; ++i) v[i] = *(const u32x4*)(w + (size_t)(8 * i + (lane >> 3)) * stride + (lane & 7) * 16);
}
template <int NL> DI void colsum_finish(const u32x4 (&v)[NL], rsrc_t r, unsigned dst, int lane) {
  f32x2 s2[4];
#pragma unroll
  for (int j = 0; j < 4; ++j) s2[j] = (f32x2){bflo(v[0][j]), bfhi(v[0][j])};
#pragma unroll
  for (int i = 1; i < NL; ++i)
#pragma unroll
    for (int j = 0; j < 4; ++j) s2[j] += (f32x2){bflo(v[i][j]), bfhi(v[i][j])};
  const float s[8] = {s2[0][0], s2[0][1], s2[1][0], s2[1][1], s2[2][0], s2[2][1], s2[3][0], s2[3][1]};
  float t[4], u[2];
#pragma unroll
  for (int j = 0; j < 4; ++j) { auto q = __builtin_amdgcn_permlane32_swap(bu(s[j]), bu(s[j + 4]), false, false); t[j] = fb(q[0]) + fb(q[1]); }
#pragma unroll
  for (int j = 0; j < 2; ++j) { auto q = __builtin_amdgcn_permlane16_swap(bu(t[j]), bu(t[j + 2]), false, false); u[j] = fb(q[0]) + fb(q[1]); }
  const bool b3 = lane & 8;
  const float tot = (b3 ? u[1] : u[0]) + dpp<0x128>(b3 ? u[0] : u[1]);
  st4(r, bu(tot), ((lane & 7) * 8 + ((lane >> 5) & 1) * 4 + ((lane >> 4) & 1) * 2 + ((lane >> 3) & 1)) * 4, dst);
}

template <int XN, int TM, int TN> DI void tile_of(int bid, int& tm, int& tn, int& c) {
  const int x = bid & 7, r = bid >> 3, li = r % TM, lj = r / TM;
  tm = (x / XN) * TM + li; tn = (x % XN) * TN + lj; c = (li + lj) & 3;
}

#define RPG1 1
#define RPG4 1
#define RPG5 1
#define W2AT 0
#define PH0 2
#define PH1 6
#define PH2 7
#define PF 10
#define W1M 0u
constexpr int PR = PF + 6 > 15 ? 15 : PF + 6;
constexpr int R1 = 40704, R3 = 40704, R4 = 0, R5 = 0, SYNCW = 163584;
constexpr int S1_ST = 0, S1_CSR = 1024, S1_CSF = 13312, S1_FLG = 14080, S1_WP = 14336, S1_KV = 18432, S1_DUM = 26880;
constexpr int S4_STR = C4::RING, S4_CSR = S4_STR + 16384, S4_STF = S4_CSR + 8192, S4_CSF = S4_STF + 1024, S4_DUM = S4_CSF + 1024;
constexpr int LDS_BYTES = 163840, CODE_BYTES = 54 * 1024;
static_assert(S4_DUM + 256 <= SYNCW && S1_KV + 8448 <= R1 && R1 + C1::RING <= SYNCW && R3 + C3::RING <= SYNCW && C5::RING <= S4_STR, "lds");

template <class St> DI void kv_sum(const Sync& y, Gate& G, lds_ptr smem, int head, int h, int t, bool poller, bool doz, int lane, const St& stamp) {
  u32x4 v[16], zz[4];
  unsigned have = 0;
  f32x2 s2[4] = {{0.f, 0.f}, {0.f, 0.f}, {0.f, 0.f}, {0.f, 0.f}};
#pragma unroll 1
  for (int round = poller ? 2 : 0; round < 3; ++round) {
    gate_slow(y, G, 0u, poller, lane, round == 0 ? 0 : round == 1 ? 13 : 16);
    stamp(round);
    const unsigned fresh = G.mask & ~have;
    have = G.mask;
#pragma unroll
    for (int r = 0; r < 16; ++r) if ((fresh >> r) & 1u) v[r] = ld16(y.r, t * 16, WS_KVP + (head * 16 + r) * 4096);
    if (round == 2 && doz) {
#pragma unroll
      for (int i = 0; i < 4; ++i) zz[i] = ld16(y.r, lane * 16, WS_ZP + head * 4096 + i * 1024);
    }
  }
#pragma unroll
  for (int r = 0; r < 16; ++r)
#pragma unroll
    for (int j = 0; j < 4; ++j) s2[j] += (f32x2){bflo(v[r][j]), bfhi(v[r][j])};
  const float s[8] = {s2[0][0], s2[0][1], s2[1][0], s2[1][1], s2[2][0], s2[2][1], s2[3][0], s2[3][1]};
  *(LAS u32x4*)(smem + S1_KV + h * 4096 + t * 16) = (u32x4){pk2(s[0], s[1]), pk2(s[2], s[3]), pk2(s[4], s[5]), pk2(s[6], s[7])};
  if (doz) {
    f32x4 z = (__builtin_bit_cast(f32x4, zz[0]) + __builtin_bit_cast(f32x4, zz[1])) + (__builtin_bit_cast(f32x4, zz[2]) + __builtin_bit_cast(f32x4, zz[3]));
#pragma unroll
    for (int j = 0; j < 4; ++j) z[j] = xsum32(xsum16(z[j]));
    if (lane < 16) *(LAS u32x2*)(smem + S1_KV + 8192 + h * 128 + lane * 8) = (u32x2){pk2(z[0], z[1]), pk2(z[2], z[3])};
  }
}

__global__ __launch_bounds__(512, 2) void fused(Params P) {
  extern __shared__ __attribute__((aligned(16))) char smem_[];
  lds_ptr smem = (lds_ptr)smem_;
  const unsigned lds0 = (unsigned)(size_t)smem;
  const int wid = __builtin_amdgcn_readfirstlane(threadIdx.x >> 6), lane = threadIdx.x & 63, bid = blockIdx.x;
  {
    const char* pc; asm volatile("s_getpc_b64 %0" : "=s"(pc));
    const unsigned co = ((bid >> 3) & 7) * 8192 + threadIdx.x * 128;
    if (threadIdx.x < 64 && co < CODE_BYTES) dma4<false>(co, pc, lds0 + S1_DUM);
    SB();
  }
  int tm1, tn1, c1, tm3, tn3, c3;
  tile_of<4, 8, 4>(bid, tm1, tn1, c1);
  tile_of<2, 4, 8>(bid, tm3, tn3, c3);
  const int kh = bid & 1, tm5 = ((bid & 7) >> 1) * 4 + ((bid >> 3) & 3), tn5 = bid >> 5, c5 = ((bid >> 3) + tn5) & 3;
  const int par = tn1 & 1, base7 = 7 * (tn1 >> 1), ch0 = base7 + 4 * par;
  const int g = lane >> 4, m16 = lane & 15;
  Sync y;
  y.r = __builtin_amdgcn_make_buffer_rsrc(P.ws, 0, WS_END, 0x00020000);
  y.w = (LAS unsigned*)(smem + SYNCW);
  if (wid == 0) TS(0);

  if (wid >= 4) {
    const int lw = wid - 4, lt = threadIdx.x - 256;
    const auto nop = [](int) {};
    {
      Ld<C1> L; L.A = P.x + (size_t)tm1 * 128 * 2048; L.W = P.wqkv + tn1 * 128; L.lw = lw; L.c = c1; L.kt0 = 0; L.init(y, lane, 2048, 0, 0, 0);
      const unsigned frec = (FL_P0 * 256 + (lane < 16 ? tm1 * 16 + lane : lane < 28 ? ((lane - 16) / 3) * 48 + ((lane - 16) % 3) * 16 + tn1 : tn1)) * 128;
      if (wid == 4) TS(31);
      if (lw == 0) dma4<true>(0u * lane, P.ws + WS_CTL, lds0 + S1_FLG);
      load_all<C1, false, (1u << (PF + 3)) | (1u << 31), 0x8005u | W1M | (1u << PF) | (1u << (PF + 3)) | (1u << PR)>(L, lds0 + R1,
        [&](int s) {
          if (s < 0) { if (wid == 4) TS(30); return; }
          if (lw == 0) {
            const u32x4 v = *(volatile LAS u32x4*)(smem + S1_CSR + lane * 16);
            if (!__all(v[0] == y.tag && v[1] == y.tag && v[2] == y.tag && v[3] == y.tag)) {
#pragma unroll 1
              for (int k = 0; k < 4; ++k) poll(y, frec + 4 * k, 64, lane);
            }
          }
        },
        [&](int s) {
          if (s == 0) y.tag = __builtin_amdgcn_readfirstlane(*(volatile LAS unsigned*)(smem + S1_FLG)) + 1u;
          if (s == 2) dma<false>(lane * 16, P.wphi + tn1 * 4096 + lw * 1024, lds0 + S1_WP + lw * 1024);
          if (((W1M >> s) & 1u) && lw == __builtin_popcount(W1M & ((1u << s) - 1u))) dma4<false>(min(lane, 55) * 128, P.w1 + bid * 28672 + lw * 7168, lds0 + S1_DUM);
          if (s == PF && lw == 0) dma<true>(frec, P.ws + WS_FLG, lds0 + S1_CSR);
          if (s == 15 && lw == 1) dma4<false>(lane * 128, P.wo + bid * 8192, lds0 + S1_DUM);
          if (s == PF + 3) {
            if (lw == 3) dma<true>(lane * 16, P.ws + WS_ST1 + tm1 * 1024, lds0 + S1_ST);
#pragma unroll
            for (int i = 0; i < 3; ++i)
              dma<true>((lane >> 4) * 12288 + (lane & 15) * 16, P.ws + WS_CSQ + lw * 49152 + i * 4096 + tn1 * 256, lds0 + S1_CSR + i * 4096 + lw * 1024);
          }
          if (s == PR) {
            if (lt < 192) {
              const LAS float* rawp = (const LAS float*)(smem + S1_CSR) + (lt >> 6) * 1024 + (lt & 63);
              float t = 0.f;
#pragma unroll
              for (int i = 0; i < 16; ++i) t += rawp[i * 64];
              ((LAS float*)(smem + S1_CSF))[lt] = t;
            }
            wait_lgkm0();
          }
        }, DBG(0));
    }
    if (wid == 4) TS(16);
    {
      Ld<C3> L; L.A = P.ws + WS_O + (size_t)tm3 * 16 * 16384; L.W = P.wo + tn3 * 128; L.lw = lw; L.c = c3; L.kt0 = 0; L.init(y, lane, 0, 0, (FL_O * 256 + tm3 * 16 + m16) * 128, 9);
      load_preW<C3>(L, lds0 + R3);
      if (lw >= 2 && bid < 224) {
        u32x4 v[16];
        colsum_load<16>(v, P.w1 + (size_t)((bid / 56) * 256 + (lw - 2) * 128) * (FF * 2) + (bid % 56) * 128, FF * 2, lane);
        colsum_finish<16>(v, y.r, WS_CS1 + (((bid / 56) * 2 + lw - 2) * FF + (bid % 56) * 64) * 4, lane);
      }
      publish(y, FL_CS, bid, lane);
      lds_wait(y, 24 + FL_KV, 4u);
      Gate G; G.off = (FL_KV * 256 + m16 * 16 + tn1) * 128; G.mask = 0; G.word = 8;
      kv_sum(y, G, smem, 2 * tn1 + 1, 1, lt, lw == 0, lw == 1, lane, [&](int r) { if (wid == 4) { if (r == 0) TS(20); if (r == 1) TS(21); if (r == 2) TS(22); } });
      if (wid == 4) TS(17);
      wait_lgkm0(); bar();
      if (wid == 4) TS(18);
      load_all<C3, true, 0u, 0u>(L, lds0 + R3, nop, nop, DBG(1));
    }
    if (wid == 4) TS(19);
    {
      Ld<C4> L; L.A = P.ws + WS_H + (size_t)tm1 * 16 * 16384; L.W = P.w1 + ch0 * 128; L.lw = lw; L.c = c1; L.kt0 = 0;
      L.init(y, lane, 128, par ? -64 : 384, ((lane & 16) ? FL_CS * 256 + (m16 >> 2) * 56 + base7 + 3 * par + (lane & 3) : FL_H * 256 + tm1 * 16 + m16) * 128, 10);
      lds_wait(y, 24 + FL_H, 4u);
      load_preW<C4>(L, lds0 + R4);
      load_all<C4, true, 0u, 0xa000u | (W2AT == 0 ? 0x550u : W2AT > 0 ? 1u << (W2AT & 31) : 0u)>(L, lds0 + R4, nop,
        [&](int s) {
          if (W2AT == 0 ? (s < 12 && lw == ((s >> 1) & 3)) : s == W2AT) dma4<false>(min(lane, 55) * 128, P.w2 + bid * 28672 + lw * 7168, lds0 + S4_DUM);
          if (s == 13) {
            gate(y, L.G, ~0u, lw == 0, lane);
#pragma unroll
            for (int i = 0; i < 4; ++i) dma<true>(lane * 16, P.ws + WS_ST2 + (tm1 * 16 + lw * 4 + i) * 1024, lds0 + S4_STR + (lw * 4 + i) * 1024);
#pragma unroll
            for (int i = 0; i < 2; ++i) dma<true>(lane * 16, P.ws + WS_CS1 + ((lw * 2 + i) * FF + (base7 + 3 * par) * 64) * 4, lds0 + S4_CSR + (lw * 2 + i) * 1024);
          }
          if (s == 15) {
            const LAS float* rawp = (const LAS float*)(smem + S4_CSR) + lt;
            float t = 0.f;
#pragma unroll
            for (int i = 0; i < 8; ++i) t += rawp[i * 256];
            ((LAS float*)(smem + S4_CSF))[lt] = t;
            wait_lgkm0();
          }
          if (s == 15 && lw < 2) {
            f32x2 st[16]; float mu = 0.f, m2 = 0.f;
#pragma unroll
            for (int i = 0; i < 16; ++i) { st[i] = *(const LAS f32x2*)(smem + S4_STR + i * 1024 + lt * 8); mu += st[i][0]; }
            mu *= (1.f / 16.f);
#pragma unroll
            for (int i = 0; i < 16; ++i) { const float d = st[i][0] - mu; m2 += st[i][1] + 64.f * d * d; }
            *(LAS f32x2*)(smem + S4_STF + lt * 8) = (f32x2){mu, __builtin_amdgcn_rsqf(m2 * (1.f / D) + 1e-5f)};
            wait_lgkm0();
          }
        }, DBG(2));
    }
    {
      Ld<C5> L; L.A = P.ws + WS_F + (size_t)tm5 * 56 * 16384; L.W = P.w2 + tn5 * 256; L.lw = lw; L.c = c5; L.kt0 = 28 * kh; L.init(y, lane, 128, 0, (FL_F * 256 + tm5 * 16 + 8 * kh + ((lane >> 1) & 7)) * 128 + (lane & 1) * 4, 11);
      lds_wait(y, 24 + FL_F, 4u);
      load_preW<C5>(L, lds0 + R5);
      load_all<C5, true, 0u, 0u>(L, lds0 + R5, nop, nop, DBG(3));
    }
    if (wid == 4) { TS(23); TS_FLUSH(); }
    return;
  }

  const int wm = wid >> 1, wn = wid & 1;
  if (wid == 0 && lane < 32) y.w[lane] = 0u;
  if (wid == 2) {
    const int i1 = tm1 * 16 + tn1, i3 = tm3 * 16 + tn3, ph = lane & 7;
    const int idx = (ph == FL_P0 || ph == FL_CS) ? bid : ph == FL_H ? i3 : ph >= FL_X ? (tm5 * 8 + tn5) * 2 + kh : i1;
    dma4<false>(lane < 8 ? WS_FLG + (ph * 256 + idx) * 128 : lane < 16 ? WS_ST2 + i3 * 1024 + ph * 128 : WS_ZP + ((2 * tn1 + (lane & 1)) * 16 + tm1) * 256 + (lane & 2) * 64, P.ws, lds0 + S1_DUM);
  }
  {
    f32x4 acc[24];
    Adr ad;
    ad.v[0] = lds0 + R1 + (wm * 64 + m16) * 128 + ((g ^ (m16 >> 1)) << 4); ad.v[1] = lds0 + R1 + (wm * 64 + m16) * 128 + ((g ^ (m16 >> 1) ^ 4) << 4);
    ad.v[2] = lds0 + R1 + 16384 + wlane(lane, 2 * wn); ad.v[3] = lds0 + R1 + 16384 + wlane(lane, 2 * wn + 1); ad.v[4] = 0; ad.v[5] = 0;
    u32x4 px[4], pw[8];
    const int prow = bid * 8 + wid * 2 + (lane >> 5);
    consume<C1, 1, RPG1, PH0, PH1, PH2>(acc, ad, DBG4, [&](int s) {
      if (s < 0) {
        if (wid == 0) TS(1);
        y.tag = __builtin_amdgcn_readfirstlane(*(volatile LAS unsigned*)(smem + S1_FLG)) + 1u;
      }
      if (s == 0) {
#pragma unroll
        for (int i = 0; i < 4; ++i) px[i] = *(const u32x4*)(P.x + (size_t)prow * 2048 + ((lane & 31) + 32 * i) * 16);
        colsum_load<8>(pw, P.wqkv + (size_t)((min(bid, 191) / 48) * 256 + wid * 64) * (3 * D * 2) + (bid % 48) * 128, 3 * D * 2, lane);
      }
      if (s == 1) {
#pragma unroll
        for (int i = 0; i < 4; ++i) asm volatile("" : "+v"(px[i]));
#pragma unroll
        for (int i = 0; i < 8; ++i) asm volatile("" : "+v"(pw[i]));
        const unsigned one2 = opaque(0x3f803f80u);
        float sm = 0.f, sq = 0.f;
#pragma unroll
        for (int i = 0; i < 4; ++i)
#pragma unroll
          for (int j = 0; j < 4; ++j) { sm = dot2(px[i][j], one2, sm); sq = dot2(px[i][j], px[i][j], sq); }
        const float mean = xsum16(row16_sum(sm)) * (1.f / D);
        const float var = fmaxf(xsum16(row16_sum(sq)) * (1.f / D) - mean * mean, 0.f);
        if ((lane & 31) == 0) st8(y.r, (u32x2){bu(mean), bu(__builtin_amdgcn_rsqf(var + 1e-5f))}, prow * 8, WS_ST1);
        if (bid < 192) colsum_finish<8>(pw, y.r, WS_CSQ + (((bid / 48) * 4 + wid) * 3 * D + (bid % 48) * 64) * 4, lane);
      }
      if (s == 2) { wait_vm<0>(); if (lane == 0) st4(y.r, y.tag, 0, WS_FLG + (FL_P0 * 256 + bid) * 128 + wid * 4); }
    });
    if (wid == 0) TS(2);
    const int head = 2 * tn1 + wn;
    f32x4 cs[4]; float csv[2];
#pragma unroll
    for (int n = 0; n < 4; ++n) cs[n] = *(const LAS f32x4*)(smem + S1_CSF + ((n >> 1) * 64 + wn * 32 + (n & 1) * 16 + 4 * g) * 4);
#pragma unroll
    for (int n = 0; n < 2; ++n) csv[n] = *(const LAS float*)(smem + S1_CSF + (128 + wn * 32 + n * 16 + m16) * 4);
    u32x4 wp[2];
#pragma unroll
    for (int F = 0; F < 2; ++F) { lds_ptr a0 = smem + S1_WP + wn * 2048 + (4 * g + (m16 >> 2)) * 64 + F * 32 + (lane & 3) * 8; wp[F] = lds_tr2(a0, a0 + 1024); }
    u32x4 qb[4], vb[2][2];
    f32x4 p[4][2];
    const f32x4 zero = {0.f, 0.f, 0.f, 0.f};
    {
      f32x4 vv[4][2];
#pragma unroll
      for (int m = 0; m < 4; ++m) {
        const f32x2 st = *(const LAS f32x2*)(smem + S1_ST + (wm * 64 + m * 16 + m16) * 8);
        const f32x4 s0 = *(const LAS f32x4*)(smem + S1_ST + (wm * 64 + m * 16 + 4 * g) * 8), s1 = *(const LAS f32x4*)(smem + S1_ST + (wm * 64 + m * 16 + 4 * g) * 8 + 16);
        const f32x4 mu = {s0[0], s0[2], s1[0], s1[2]}, rs = {s0[1], s0[3], s1[1], s1[3]};
        qb[m] = pack8((acc[m * 6] - cs[0] * st[0]) * st[1], (acc[m * 6 + 1] - cs[1] * st[0]) * st[1]);
        const u32x4 kb = pack8((acc[m * 6 + 2] - cs[2] * st[0]) * st[1], (acc[m * 6 + 3] - cs[3] * st[0]) * st[1]);
        p[m][0] = mfma(wp[0], kb, zero); p[m][1] = mfma(wp[1], kb, zero);
        vv[m][0] = (acc[m * 6 + 4] - mu * csv[0]) * rs; vv[m][1] = (acc[m * 6 + 5] - mu * csv[1]) * rs;
      }
#pragma unroll
      for (int u = 0; u < 2; ++u) { vb[u][0] = pack8(vv[2 * u][0], vv[2 * u + 1][0]); vb[u][1] = pack8(vv[2 * u][1], vv[2 * u + 1][1]); }
    }
    u32x4 pp[4], pn[4];
    if (wid == 0) TS(26);
    dual_softmax4(p, pp, pn);
    if (wid == 0) TS(27);
    {
      lds_ptr own = smem + R3 + wid * C3::STAGE, oth = smem + R3 + (wid ^ 2) * C3::STAGE;
#pragma unroll
      for (int m = 0; m < 4; ++m) {
        lds_ptr w = own + (16 * m + m16) * 136 + 8 * g;
        *(LAS u32x2*)(w) = (u32x2){pp[m][0], pp[m][1]}; *(LAS u32x2*)(w + 32) = (u32x2){pp[m][2], pp[m][3]};
        *(LAS u32x2*)(w + 64) = (u32x2){pn[m][0], pn[m][1]}; *(LAS u32x2*)(w + 96) = (u32x2){pn[m][2], pn[m][3]};
      }
#pragma unroll
      for (int i = 0; i < 4; ++i) *(LAS u32x4*)(own + 8704 + i * 1024 + lane * 16) = vb[i >> 1][i & 1];
      wait_lgkm0();
      volatile LAS unsigned* xf = y.w + 16;
      xf[wid] = 1u;
      f32x4 kv[2][2] = {{zero, zero}, {zero, zero}}, z[2] = {zero, zero};
      const u32x4 ones = {0x3f803f80u, 0x3f803f80u, 0x3f803f80u, 0x3f803f80u};
      const unsigned ro = (4 * g + (m16 >> 2)) * 136 + wm * 64 + (lane & 3) * 8;
#pragma unroll
      for (int u = 0; u < 2; ++u)
#pragma unroll
        for (int F = 0; F < 2; ++F) {
          const u32x4 pa = lds_tr2(own + ro + u * 32 * 136 + F * 32, own + ro + u * 32 * 136 + F * 32 + 16 * 136);
          kv[F][0] = mfma(pa, vb[u][0], kv[F][0]); kv[F][1] = mfma(pa, vb[u][1], kv[F][1]); z[F] = mfma(pa, ones, z[F]);
        }
      while (xf[wid ^ 2] == 0u) __builtin_amdgcn_s_sleep(0);
      asm volatile("" ::: "memory");
#pragma unroll
      for (int u = 0; u < 2; ++u) {
        const u32x4 v0 = lds128(oth + 8704 + u * 2048 + lane * 16), v1 = lds128(oth + 8704 + u * 2048 + 1024 + lane * 16);
#pragma unroll
        for (int F = 0; F < 2; ++F) {
          const u32x4 pa = lds_tr2(oth + ro + u * 32 * 136 + F * 32, oth + ro + u * 32 * 136 + F * 32 + 16 * 136);
          kv[F][0] = mfma(pa, v0, kv[F][0]); kv[F][1] = mfma(pa, v1, kv[F][1]); z[F] = mfma(pa, ones, z[F]);
        }
      }
      const unsigned kd = WS_KVP + (head * 16 + tm1) * 4096 + wm * 2048;
      st16(y.r, pack8(kv[0][0], kv[1][0]), lane * 16, kd);
      st16(y.r, pack8(kv[0][1], kv[1][1]), lane * 16, kd + 1024);
      if (m16 == 0) {
        st16(y.r, __builtin_bit_cast(u32x4, z[0]), g * 32, WS_ZP + (head * 16 + tm1) * 256 + wm * 128);
        st16(y.r, __builtin_bit_cast(u32x4, z[1]), g * 32 + 16, WS_ZP + (head * 16 + tm1) * 256 + wm * 128);
      }
      if (wid == 0) TS(25);
      publish(y, FL_KV, tm1 * 16 + tn1, lane);
    }
    if (wid == 0) TS(3);
#pragma unroll
    for (int m = 0; m < 4; ++m) { p[m][0] = mfma(wp[0], qb[m], zero); p[m][1] = mfma(wp[1], qb[m], zero); }
    dual_softmax4(p, pp, pn);
    if (wid == 0) TS(4);
    { Gate G; G.off = 0; G.mask = 0; G.word = 8; kv_sum(y, G, smem, 2 * tn1, 0, threadIdx.x, false, wid == 0, lane, [](int) {}); }
    wait_lgkm0(); bar();
    if (wid == 0) TS(5);
    u32x4 ka[2][2], za[2];
#pragma unroll
    for (int ks = 0; ks < 2; ++ks) {
      ka[ks][0] = lds128(smem + S1_KV + wn * 4096 + ks * 2048 + lane * 16); ka[ks][1] = lds128(smem + S1_KV + wn * 4096 + ks * 2048 + 1024 + lane * 16);
      za[ks] = lds128(smem + S1_KV + 8192 + wn * 128 + ks * 64 + g * 16);
    }
#pragma unroll
    for (int m = 0; m < 4; ++m) {
      f32x4 n0 = mfma(ka[0][0], pp[m], zero), n1 = mfma(ka[0][1], pp[m], zero), dn = mfma(za[0], pp[m], zero);
      n0 = mfma(ka[1][0], pn[m], n0); n1 = mfma(ka[1][1], pn[m], n1); dn = mfma(za[1], pn[m], dn);
      const float rd = __builtin_amdgcn_rcpf(dn[0] + 1e-6f);
      st16(y.r, pack8(n0 * rd, n1 * rd), lane * 16, WS_O + (((tm1 * 16 + tn1) * 8 + wm * 4 + m) * 2 + wn) * 1024);
    }
    publish(y, FL_O, tm1 * 16 + tn1, lane);
    if (wid == 0) TS(6);
  }
  {
    u32x4 xr[2][2];
#pragma unroll
    for (int m = 0; m < 2; ++m)
#pragma unroll
      for (int Q = 0; Q < 2; ++Q)
        xr[m][Q] = *(const u32x4*)(P.x + (size_t)(tm3 * 128 + wid * 32 + m * 16 + m16) * 2048 + tn3 * 128 + Q * 64 + (g & 1) * 32 + (g >> 1) * 16);
    f32x4 acc[8];
    Adr ad; ad.v[0] = lds0 + R3 + wid * 4096 + lane * 16; ad.v[1] = 0;
#pragma unroll
    for (int n = 0; n < 4; ++n) ad.v[2 + n] = lds0 + R3 + 16384 + wlane(lane, n);
    consume_s<C3, 3, 2>(acc, ad, [&](int) { if (wid == 0) TS(7); });
    if (wid == 0) TS(8);
#pragma unroll
    for (int m = 0; m < 2; ++m) {
      float hv[16];
#pragma unroll
      for (int Q = 0; Q < 2; ++Q) {
        auto r0 = __builtin_amdgcn_permlane16_swap(xr[m][Q][0], xr[m][Q][2], false, false);
        auto r1 = __builtin_amdgcn_permlane16_swap(xr[m][Q][1], xr[m][Q][3], false, false);
        const f32x4 a = acc[m * 4 + 2 * Q], b = acc[m * 4 + 2 * Q + 1];
        const u32x4 hp = {pk2(a[0] + bflo(r0[0]), a[1] + bfhi(r0[0])), pk2(a[2] + bflo(r1[0]), a[3] + bfhi(r1[0])),
                          pk2(b[0] + bflo(r0[1]), b[1] + bfhi(r0[1])), pk2(b[2] + bflo(r1[1]), b[3] + bfhi(r1[1]))};
        st16(y.r, hp, lane * 16, WS_H + (((tm3 * 16 + tn3) * 8 + wid * 2 + m) * 2 + Q) * 1024);
#pragma unroll
        for (int j = 0; j < 4; ++j) { hv[Q * 8 + 2 * j] = bflo(hp[j]); hv[Q * 8 + 2 * j + 1] = bfhi(hp[j]); }
      }
      float s = 0.f;
#pragma unroll
      for (int j = 0; j < 16; ++j) s += hv[j];
      s = xsum32(xsum16(s)) * (1.f / 64.f);
      float q = 0.f;
#pragma unroll
      for (int j = 0; j < 16; ++j) { const float d = hv[j] - s; q += d * d; }
      q = xsum32(xsum16(q));
      if (g == 0) st8(y.r, (u32x2){bu(s), bu(q)}, m16 * 8, WS_ST2 + (tm3 * 16 + tn3) * 1024 + (wid * 32 + m * 16) * 8);
    }
    if (wid == 0) TS(29);
    publish(y, FL_H, tm3 * 16 + tn3, lane);
    if (wid == 0) TS(9);
  }
  {
    f32x4 acc[28];
    Adr ad;
    ad.v[0] = lds0 + R4 + wm * 8192 + wn * 4096 + lane * 16; ad.v[1] = lds0 + R4 + wm * 8192 + (wn ^ 1) * 4096 + lane * 16;
    ad.v[2] = lds0 + R4 + 16384 + wlane(lane, wn); ad.v[3] = lds0 + R4 + 16384 + wlane(lane, 2 + wn);
    ad.v[4] = lds0 + R4 + 16384 + 6144 + hlane(lane, 0); ad.v[5] = lds0 + R4 + 16384 + 6144 + hlane(lane, 1);
    consume<C4, 4, RPG4, 0, 0, 0>(acc, ad, DBG4, [&](int s) { if (s < 0 && wid == 0) TS(10); });
    if (wid == 0) TS(11);
    f32x4 cs[8];
#pragma unroll
    for (int n = 0; n < 6; ++n) cs[n] = *(const LAS f32x4*)(smem + S4_CSF + ((par + (n >> 1)) * 64 + (2 * (n & 1) + wn) * 16 + 4 * g) * 4);
#pragma unroll
    for (int n = 0; n < 2; ++n) cs[6 + n] = *(const LAS f32x4*)(smem + S4_CSF + ((par ? 32 : 192) + n * 16 + 4 * g) * 4);
    const f32x4 zero = {0.f, 0.f, 0.f, 0.f};
    f32x2 st[4];
#pragma unroll
    for (int m = 0; m < 4; ++m) st[m] = *(const LAS f32x2*)(smem + S4_STF + ((wm * 4 + ((m + 2 * wn) & 3)) * 16 + m16) * 8);
#pragma unroll
    for (int i = 0; i < 4; ++i)
#pragma unroll
      for (int m = 0; m < (i < 3 ? 4 : 2); ++m) {
        const unsigned fd = WS_F + (tm1 * 56 * 16 + (wm * 4 + ((m + 2 * wn) & 3)) * 2) * 1024 + (i < 3 ? (ch0 + i) * 16384 + wn * 1024 : (base7 + 3) * 16384 + par * 1024);
        const int a = i < 3 ? m * 6 + 2 * i : 24 + 2 * m;
        st16(y.r, pack8(__builtin_elementwise_max((acc[a] - cs[2 * i] * st[m][0]) * st[m][1], zero),
                        __builtin_elementwise_max((acc[a + 1] - cs[2 * i + 1] * st[m][0]) * st[m][1], zero)), lane * 16, fd);
        SB();
      }
    if (wid == 0) TS(28);
    if (lane == 0) __hip_atomic_fetch_add(y.w + 24 + FL_F, 1u, __ATOMIC_RELAXED, __HIP_MEMORY_SCOPE_WORKGROUP);
    wait_vm<10>();
    if (lane == 0 && __hip_atomic_fetch_add(y.w + FL_F, 1u, __ATOMIC_RELAXED, __HIP_MEMORY_SCOPE_WORKGROUP) == 3u) st4(y.r, y.tag, 0, WS_FLG + (FL_F * 256 + tm1 * 16 + tn1) * 128);
    if (wid == 0) TS(12);
  }
  {
    f32x4 acc[16];
    Adr ad;
    ad.v[0] = lds0 + R5 + wm * 8192 + kh * 4096 + lane * 16; ad.v[1] = lds0 + R5 + wm * 8192 + (kh ^ 1) * 4096 + lane * 16;
#pragma unroll
    for (int n = 0; n < 4; ++n) ad.v[2 + n] = lds0 + R5 + 16384 + wn * 2048 + wlane(lane, n);
    u32x4 hr[2][2];
    consume_s<C5, 5, 1>(acc, ad, [&](int s) {
      if (s < 0) {
        if (wid == 0) TS(13);
        wait_vm<0>();
        if (lane == 0 && __hip_atomic_fetch_add(y.w + 20, 1u, __ATOMIC_RELAXED, __HIP_MEMORY_SCOPE_WORKGROUP) == 3u) st4(y.r, y.tag, 0, WS_FLG + (FL_F * 256 + tm1 * 16 + tn1) * 128 + 4);
#pragma unroll
        for (int m = 0; m < 2; ++m)
#pragma unroll
          for (int Q = 0; Q < 2; ++Q) hr[m][Q] = ld16(y.r, lane * 16, WS_H + (((tm5 * 16 + 2 * tn5 + wn) * 8 + wm * 4 + 2 * kh + m) * 2 + Q) * 1024);
      }
    });
    if (wid == 0) TS(14);
    const int tile = tm5 * 8 + tn5;
#pragma unroll
    for (int i = 0; i < 4; ++i) st16(y.r, pack8(acc[8 + 2 * i], acc[9 + 2 * i]), lane * 16, WS_O + (((tile * 2 + kh) * 4 + wid) * 4 + i) * 1024);
    wait_vm<0>();
    if (lane == 0) st4(y.r, y.tag, 0, WS_FLG + ((FL_X + (wid >> 1)) * 256 + tile * 2 + kh) * 128 + (wid & 1) * 64);
    poll(y, ((FL_X + (wid >> 1)) * 256 + tile * 2 + (kh ^ 1)) * 128 + (wid & 1) * 64, 1, lane);
    if (wid == 0) TS(15);
    const rsrc_t ro = __builtin_amdgcn_make_buffer_rsrc(P.out, 0, 2048 * 2048, 0x00020000);
    u32x4 xv[4];
#pragma unroll
    for (int i = 0; i < 4; ++i) xv[i] = ld16(y.r, lane * 16, WS_O + (((tile * 2 + (kh ^ 1)) * 4 + wid) * 4 + i) * 1024);
#pragma unroll
    for (int m = 0; m < 2; ++m)
#pragma unroll
      for (int Q = 0; Q < 2; ++Q) {
        const u32x4 xx = xv[m * 2 + Q];
        const f32x4 a = acc[m * 4 + 2 * Q] + (f32x4){bflo(xx[0]), bfhi(xx[0]), bflo(xx[1]), bfhi(xx[1])}, b = acc[m * 4 + 2 * Q + 1] + (f32x4){bflo(xx[2]), bfhi(xx[2]), bflo(xx[3]), bfhi(xx[3])};
        const u32x4 hh = hr[m][Q];
        const unsigned a0 = pk2(a[0] + bflo(hh[0]), a[1] + bfhi(hh[0])), a1 = pk2(a[2] + bflo(hh[1]), a[3] + bfhi(hh[1]));
        const unsigned b0 = pk2(b[0] + bflo(hh[2]), b[1] + bfhi(hh[2])), b1 = pk2(b[2] + bflo(hh[3]), b[3] + bfhi(hh[3]));
        auto r0 = __builtin_amdgcn_permlane16_swap(a0, b0, false, false);
        auto r1 = __builtin_amdgcn_permlane16_swap(a1, b1, false, false);
        __builtin_amdgcn_raw_buffer_store_b128((u32x4){r0[0], r1[0], r0[1], r1[1]}, ro, (tm5 * 128 + wm * 64 + (2 * kh + m) * 16 + m16) * 2048 + tn5 * 256 + wn * 128 + Q * 64 + (g & 1) * 32 + (g >> 1) * 16, 0, 16);
      }
    if (bid == 0 && threadIdx.x == 0) st4(y.r, y.tag, 0, WS_CTL);
    if (wid == 0) { TS(24); TS_FLUSH(); }
  }
}

extern "C" void kernel_launch(void* const* d_in, const int* in_sizes, int n_in, void* d_out, int out_size, void* d_ws, size_t ws_size,
                              hipStream_t stream) {
  Params p;
  p.x = (const char*)d_in[0]; p.wqkv = (const char*)d_in[1]; p.wphi = (const char*)d_in[2]; p.wo = (const char*)d_in[3];
  p.w1 = (const char*)d_in[5]; p.w2 = (const char*)d_in[6]; p.ws = (char*)d_ws; p.out = (char*)d_out;
  (void)hipFuncSetAttribute((const void*)fused, hipFuncAttributeMaxDynamicSharedMemorySize, LDS_BYTES);
  fused<<<256, 512, LDS_BYTES, stream>>>(p);
}
```

```cpp
#include <hip/hip_runtime.h>
#include <hip/hip_bf16.h>

#define LAS __attribute__((address_space(3)))
#define DI __device__ __forceinline__
typedef __bf16 bf16x8 __attribute__((ext_vector_type(8)));
typedef __bf16 bf16x2 __attribute__((ext_vector_type(2)));
typedef short s16x4 __attribute__((ext_vector_type(4)));
typedef float f32x2 __attribute__((ext_vector_type(2)));
typedef float f32x4 __attribute__((ext_vector_type(4)));
typedef unsigned u32x2 __attribute__((ext_vector_type(2)));
typedef unsigned u32x4 __attribute__((ext_vector_type(4)));
typedef LAS char* lds_ptr;
typedef __amdgpu_buffer_rsrc_t rsrc_t;

constexpr int D = 1024, FF = 3584;
constexpr unsigned WS_CTL = 0;
constexpr unsigned WS_FLG = 4096;
constexpr unsigned WS_ST1 = 320u << 10;
constexpr unsigned WS_CSQ = 384u << 10;
constexpr unsigned WS_CS1 = 576u << 10;
constexpr unsigned WS_ZP = 704u << 10;
constexpr unsigned WS_ST2 = 1024u << 10;
constexpr unsigned WS_FL2 = 1536u << 10;
constexpr unsigned WS_PRB = 1920u << 10;
constexpr unsigned WS_KVP = 2u << 20;
constexpr unsigned WS_O = 4u << 20;
constexpr unsigned WS_H = 12u << 20;
constexpr unsigned WS_F = 20u << 20;
constexpr unsigned WS_END = 52u << 20;
constexpr int NPAGE = 26;
enum { FL_P0 = 0, FL_KV = 1, FL_CS = 2, FL_O = 3, FL_H = 4, FL_F = 5, FL_X = 6   };

#define TS(I) do {} while (0)
#define TS_FLUSH() do {} while (0)

#define DBG(k) nullptr
#define CLK(v)
#define DBG4 nullptr
struct Params { const char* x; const char* wqkv; const char* wphi; const char* wo; const char* w1; const char* w2; char* ws; char* out; };

DI unsigned pk2(float a, float b) { f32x2 v = {a, b}; return __builtin_bit_cast(unsigned, __builtin_convertvector(v, bf16x2)); }
DI float bflo(unsigned u) { return __uint_as_float(u << 16); }
DI float bfhi(unsigned u) { return __uint_as_float(u & 0xffff0000u); }
DI u32x4 pack8(const f32x4& a, const f32x4& b) { return (u32x4){pk2(a[0], a[1]), pk2(a[2], a[3]), pk2(b[0], b[1]), pk2(b[2], b[3])}; }
DI bf16x8 asbf(const u32x4& v) { return __builtin_bit_cast(bf16x8, v); }
#define SB() __builtin_amdgcn_sched_barrier(0)
DI void bar() { asm volatile("" ::: "memory"); __builtin_amdgcn_s_barrier(); asm volatile("" ::: "memory"); }
DI void wait_lgkm0() { __builtin_amdgcn_s_waitcnt(0xC07F); asm volatile("" ::: "memory"); }
template <int N> DI void wait_vm() { asm volatile("s_waitcnt vmcnt(%0)" ::"i"(N) : "memory"); }
template <bool SC1> DI void dma(unsigned voff, const char* sbase, unsigned lds) {
  if (SC1) asm volatile("s_mov_b32 m0, %2\n\ts_nop 3\n\tglobal_load_lds_dwordx4 %0, %1 sc1" ::"v"(voff), "s"(sbase), "s"(lds) : "memory");
  else asm volatile("s_mov_b32 m0, %2\n\ts_nop 3\n\tglobal_load_lds_dwordx4 %0, %1" ::"v"(voff), "s"(sbase), "s"(lds) : "memory");
}
template <bool SC1> DI void dma4(unsigned voff, const char* sbase, unsigned lds) {
  if (SC1) asm volatile("s_mov_b32 m0, %2\n\ts_nop 3\n\tglobal_load_lds_dword %0, %1 sc1" ::"v"(voff), "s"(sbase), "s"(lds) : "memory");
  else asm volatile("s_mov_b32 m0, %2\n\ts_nop 3\n\tglobal_load_lds_dword %0, %1" ::"v"(voff), "s"(sbase), "s"(lds) : "memory");
}
DI u32x4 lds128(lds_ptr p) { return *(const LAS u32x4*)p; }
DI u32x4 lds_tr2(lds_ptr p0, lds_ptr p1) {
  s16x4 a = __builtin_amdgcn_ds_read_tr16_b64_v4i16((LAS s16x4*)p0);
  s16x4 b = __builtin_amdgcn_ds_read_tr16_b64_v4i16((LAS s16x4*)p1);
  return __builtin_bit_cast(u32x4, __builtin_shufflevector(a, b, 0, 1, 2, 3, 4, 5, 6, 7));
}
DI u32x2 lds_tr(lds_ptr p) { return __builtin_bit_cast(u32x2, __builtin_amdgcn_ds_read_tr16_b64_v4i16((LAS s16x4*)p)); }
DI f32x4 mfma(const u32x4& a, const u32x4& b, const f32x4& c) { return __builtin_amdgcn_mfma_f32_16x16x32_bf16(asbf(a), asbf(b), c, 0, 0, 0); }
DI void mfma_ip(f32x4& acc, const u32x4& a, const u32x4& b) { asm volatile("v_mfma_f32_16x16x32_bf16 %0, %1, %2, %0" : "+v"(acc) : "v"(a), "v"(b)); }
DI u32x4 ld16(rsrc_t r, unsigned voff, unsigned soff) { return __builtin_amdgcn_raw_buffer_load_b128(r, voff, soff, 16); }
DI void st16(rsrc_t r, const u32x4& v, unsigned voff, unsigned soff) { __builtin_amdgcn_raw_buffer_store_b128(v, r, voff + soff, 0, 16);
}
DI void st8(rsrc_t r, const u32x2& v, unsigned voff, unsigned soff) { __builtin_amdgcn_raw_buffer_store_b64(v, r, voff, soff, 16); }
DI void st4(rsrc_t r, unsigned v, unsigned voff, unsigned soff) { __builtin_amdgcn_raw_buffer_store_b32(v, r, voff, soff, 16); }
DI float fb(unsigned u) { return __uint_as_float(u); }
DI unsigned bu(float f) { return __float_as_uint(f); }
DI float xsum16(float x) { auto r = __builtin_amdgcn_permlane16_swap(bu(x), bu(x), false, false); return fb(r[0]) + fb(r[1]); }
DI float xsum32(float x) { auto r = __builtin_amdgcn_permlane32_swap(bu(x), bu(x), false, false); return fb(r[0]) + fb(r[1]); }
template <int CTRL> DI float dpp(float x) { return __builtin_bit_cast(float, __builtin_amdgcn_update_dpp(0, __builtin_bit_cast(int, x), CTRL, 0xf, 0xf, true)); }
DI float row16_sum(float x) { x += dpp<0xB1>(x); x += dpp<0x4E>(x); x += dpp<0x141>(x); x += dpp<0x140>(x); return x; }

struct Sync { rsrc_t r; unsigned tag, pb; LAS unsigned* w; };
DI unsigned near(const Sync& y, unsigned off, int tm) { return off + ((((y.pb >> (off >> 21)) ^ (tm >> 3)) & 1u) << 13); }
DI unsigned rec(const Sync& y, int phase, int idx) {
  return phase < FL_O ? (phase * 256 + idx) * 128 : near(y, WS_FL2 + (phase - FL_O) * 65536 + (idx >> 6) * 16384 + (idx & 63) * 128, idx >> 4) - WS_FLG;
}
DI void publish(const Sync& y, int phase, int idx, int lane) {
  if (lane == 0) __hip_atomic_fetch_add(y.w + 24 + phase, 1u, __ATOMIC_RELAXED, __HIP_MEMORY_SCOPE_WORKGROUP);
  wait_vm<0>();
  if (lane == 0 && __hip_atomic_fetch_add(y.w + phase, 1u, __ATOMIC_RELAXED, __HIP_MEMORY_SCOPE_WORKGROUP) == 3u) st4(y.r, y.tag, 0, WS_FLG + rec(y, phase, idx));
}
DI void poll(const Sync& y, unsigned off, int n, int lane) {
  const unsigned long long t0 = __builtin_amdgcn_s_memrealtime();
  for (unsigned it = 0;; ++it) {
    const unsigned v = __builtin_amdgcn_raw_buffer_load_b32(y.r, WS_FLG + off, 0, 16);
    if (__all(lane >= n || v == y.tag)) break;
    if ((it & 63) == 63) {
      if (__builtin_amdgcn_readfirstlane(__builtin_amdgcn_raw_buffer_load_b32(y.r, 0, WS_CTL + 128, 16)) == y.tag) break;
      if (__builtin_amdgcn_s_memrealtime() - t0 > 4000000ull) { if (lane == 0) st4(y.r, y.tag, 0, WS_CTL + 128); break; }
    }
    __builtin_amdgcn_s_sleep(2);
  }
  asm volatile("" ::: "memory");
}
DI void lds_wait(const Sync& y, int word, unsigned v);
DI void bar_loaders(const Sync& y, int lw) { if (lw == 1) *(volatile LAS unsigned*)(y.w + 14) = 1u; else lds_wait(y, 14, 1u); }
DI void lds_wait(const Sync& y, int word, unsigned v) {
  volatile LAS unsigned* w = y.w + word;
  while (*w < v) __builtin_amdgcn_s_sleep(1);
  asm volatile("" ::: "memory");
}
struct Gate { unsigned off, mask; int word; };
DI void gate_slow(const Sync& y, Gate& G, unsigned need, bool poller, int lane, int cnt = 0) {
  volatile LAS unsigned* w = y.w + G.word;
  if (poller) {
    const unsigned long long t0 = __builtin_amdgcn_s_memrealtime();
    for (unsigned it = 0;; ++it) {
      G.mask = (unsigned)__ballot(__builtin_amdgcn_raw_buffer_load_b32(y.r, WS_FLG + G.off, 0, 16) == y.tag);
      if ((it & 63) == 63) {
        const bool late = __builtin_amdgcn_s_memrealtime() - t0 > 4000000ull;
        if (late && lane == 0) st4(y.r, y.tag, 0, WS_CTL + 128);
        if (late || __builtin_amdgcn_readfirstlane(__builtin_amdgcn_raw_buffer_load_b32(y.r, 0, WS_CTL + 128, 16)) == y.tag) G.mask = ~0u;
      }
      *w = G.mask;
      if ((G.mask & need) == need && __builtin_popcount(G.mask & 0xffffu) >= cnt) break;
      __builtin_amdgcn_s_sleep(1);
    }
  } else {
    for (;;) { G.mask = __builtin_amdgcn_readfirstlane(*w); if ((G.mask & need) == need && __builtin_popcount(G.mask & 0xffffu) >= cnt) break; __builtin_amdgcn_s_sleep(1); }
  }
  asm volatile("" ::: "memory");
}
DI void gate(const Sync& y, Gate& G, unsigned need, bool poller, int lane) { if (__builtin_expect((G.mask & need) != need, 0)) gate_slow(y, G, need, poller, lane); }

template <int NK, bool SPREAD> DI int kmap(int t, int c) {
  const int u = t - 1, q = u & 3, b4 = u & ~3, body = (int)(u < NK - 4);
  const int lead = ((int)(q == c) & body & (int)(u >= 0)) | (int)(SPREAD && t == 0), head = SPREAD ? 0 : -(int)(u < 4);
  const int non = ((b4 + ((c + 1 + q - ((int)(q > c) & body)) & 3)) & ~head) | ((t - (int)(u > c)) & head);
  return non ^ (((b4 + 4 + c) ^ non) & -lead);
}
#define LAG3 1
#define LAG5 1
enum { ORD_NAT = 0, ORD_PAIR = 1, ORD_F = 2 };
template <int NSUB_, bool HALF_, int NSLOT_, int NK_, int ORD_, int WSTRIDE_, bool XROW_, int LAG_> struct Cfg {
  static constexpr int NSUB = NSUB_, NSLOT = NSLOT_, NK = NK_, ORD = ORD_, WSTRIDE = WSTRIDE_;
  static constexpr bool HALF = HALF_, XROW = XROW_;
  static constexpr int LAG = LAG_;
  static constexpr int STAGE = 16384 + NSUB * 8192 + (HALF ? 4096 : 0), PW = 4 + 2 * NSUB + (HALF ? 1 : 0), RING = STAGE * NSLOT;
  static constexpr int WB = NSUB * 2048 + (HALF ? 1024 : 0);
};
typedef Cfg<3, false, 3, 16, ORD_NAT, 3 * D * 2, true, 0> C1;
typedef Cfg<1, false, 5, 16, ORD_PAIR, D * 2, false, LAG3> C3;
typedef Cfg<3, true, 3, 16, ORD_PAIR, FF * 2, false, 0> C4;
typedef Cfg<2, false, 4, 28, ORD_F, D * 2, false, LAG5> C5;

#define GLDS "global_load_lds_dwordx4 "
template <class C> struct Ld {
  const char* A; const char* W; int lw, c, kt0; unsigned vA[4], vW[7];
  const char* sA; const char* sW; unsigned need;
  Gate G; const Sync* y; int lane_;
  static constexpr int C0 = C::WB > 4096 ? 4096 : 0;
  DI void init(const Sync& y_, int lane, int substep, int halfrel, unsigned goff, int gword) {
    y = &y_; lane_ = lane; G.off = goff; G.mask = 0; G.word = gword;
#pragma unroll
    for (int i = 0; i < 4; ++i) vA[i] = C::XROW ? (lane >> 3) * 2048 + ((((lane & 7) ^ (lane >> 4)) << 4) ^ ((i & 1) * 64)) + i * (16384 - 1024) : lane * 16;
    const int rl = (C::ORD == ORD_NAT) ? 8 * (lane >> 5) + ((lane >> 3) & 3) : (lane >> 3);
    const unsigned w0 = rl * C::WSTRIDE + (((lane & 7) ^ (((lane >> 4) & 3) << 1)) << 4) + 4096;
#pragma unroll
    for (int q = 0; q < 2 * C::NSUB; ++q) vW[q] = w0 + (q & 1) * ((C::ORD == ORD_NAT ? 16 : 8) * C::WSTRIDE) + (q >> 1) * substep - (q * 1024 - C0);
    if (C::HALF) vW[6] = (lane >> 2) * C::WSTRIDE + (((lane & 3) ^ (((lane >> 4) & 1) << 1)) << 4) + 4096 + halfrel - (6144 - C0);
    A += C::XROW ? lw * 32 * 2048 : (lw >> 1) * 16384 + (lw & 1) * 4096;
    W -= 4096;
  }
  DI void prep(int s) {
    int kt = kmap<C::NK, C::XROW>(s, c);
    const int kb = lw >> 1, t = lw & 1;
    int ur;
    need = 1u << kt;
    if (C::ORD == ORD_NAT) ur = 32 * kb + 4 * t;
    else if (C::ORD == ORD_F) {
      const int p = kt & 7, q = kt >> 3, sp = -(int)(q == 3);
      need = ((1u << (2 * p + (int)(q > 0))) & ~sp) | ((0xau << (4 * (p & 3))) & sp);
      kt = kt0 + (((7 * (p >> 1) + 4 * (p & 1) + q) & ~sp) | ((7 * p + 3) & sp));
      ur = (16 * lw & sp) | ((16 * kb + 32 * t) & ~sp);
    }
    else ur = 16 * lw;
    sA = A + (unsigned)(kt * (C::XROW ? 128 : 32768));
    sW = W + (unsigned)((kt * 64 + ur) * C::WSTRIDE);
  }
  DI void issueA(unsigned base) {
    if (!C::XROW) gate(*y, G, need, lw == 0, lane_);
    const unsigned m = base + lw * 4096;
    if (C::XROW) asm volatile("s_mov_b32 m0, %5\n\ts_nop 3\n\t" GLDS "%0, %4\n\t" GLDS "%1, %4 offset:1024\n\t" GLDS "%2, %4 offset:2048\n\t" GLDS "%3, %4 offset:3072"
                              ::"v"(vA[0]), "v"(vA[1]), "v"(vA[2]), "v"(vA[3]), "s"(sA), "s"(m) : "memory");
    else asm volatile("s_mov_b32 m0, %2\n\ts_nop 3\n\t" GLDS "%0, %1 sc1\n\t" GLDS "%0, %1 offset:1024 sc1\n\t" GLDS "%0, %1 offset:2048 sc1\n\t" GLDS "%0, %1 offset:3072 sc1"
                      ::"v"(vA[0]), "s"(sA), "s"(m) : "memory");
  }
  DI void issueW(unsigned base) const {
    const unsigned m = base + 16384 + lw * C::WB + C0;
    if (C::NSUB == 1) asm volatile("s_mov_b32 m0, %3\n\ts_nop 3\n\t" GLDS "%0, %2\n\t" GLDS "%1, %2 offset:1024" ::"v"(vW[0]), "v"(vW[1]), "s"(sW), "s"(m) : "memory");
    if (C::NSUB == 2) asm volatile("s_mov_b32 m0, %5\n\ts_nop 3\n\t" GLDS "%0, %4\n\t" GLDS "%1, %4 offset:1024\n\t" GLDS "%2, %4 offset:2048\n\t" GLDS "%3, %4 offset:3072"
                                   ::"v"(vW[0]), "v"(vW[1]), "v"(vW[2]), "v"(vW[3]), "s"(sW), "s"(m) : "memory");
    if (C::NSUB == 3) asm volatile("s_mov_b32 m0, %7\n\ts_nop 3\n\t" GLDS "%0, %6 offset:-4096\n\t" GLDS "%1, %6 offset:-3072\n\t" GLDS "%2, %6 offset:-2048\n\t" GLDS "%3, %6 offset:-1024\n\t"
                                   GLDS "%4, %6\n\t" GLDS "%5, %6 offset:1024"
                                   ::"v"(vW[0]), "v"(vW[1]), "v"(vW[2]), "v"(vW[3]), "v"(vW[4]), "v"(vW[5]), "s"(sW), "s"(m) : "memory");
    if (C::HALF) asm volatile(GLDS "%0, %1 offset:2048" ::"v"(vW[6]), "s"(sW) : "memory");
  }
};
template <class C> DI void load_preW(Ld<C>& L, unsigned ring) {
#pragma unroll 1
  for (int s = 0; s < C::NSLOT; ++s) { L.prep(s); L.issueW(ring + s * C::STAGE); }
}
template <class C, bool SPLIT> constexpr int vm_allow(int S) {
  const int N = C::NSLOT, behind = S - 1 - C::LAG > 0 ? S - 1 - C::LAG : 0;
  const int ramp = S + 1 < N - 1 ? S + 1 : N - 1;
  const int last = ramp + behind < C::NK - 1 ? ramp + behind : C::NK - 1;
  return (SPLIT && S < N) ? 4 * (ramp - S) + C::PW * (last - ramp) : C::PW * (last - S);
}
template <class C, bool SPLIT, unsigned POSTM, int S, int SE, class Post> DI void load_fixed(Ld<C>& L, unsigned ring, const Post& post) {
  if constexpr (S < SE) {
    static_assert(C::NSLOT <= C::LAG + 4, "the ring must be full before the first refill");
    wait_vm<vm_allow<C, SPLIT>(S)>();
    bar();
    if constexpr (S + 2 < C::NSLOT) {
      if (!SPLIT) L.issueW(ring + (S + 2) * C::STAGE);
      L.issueA(ring + (S + 2) * C::STAGE);
      L.prep(S + 3);
    }
    constexpr int T = S - 1 - C::LAG + C::NSLOT;
    if constexpr (S - 1 - C::LAG >= 0 && T < C::NK) {
      L.issueW(ring + (T % C::NSLOT) * C::STAGE); L.issueA(ring + (T % C::NSLOT) * C::STAGE);
      if constexpr (T + 1 < C::NK) L.prep(T + 1);
    }
    if constexpr ((POSTM >> S) & 1u) post(S);
    load_fixed<C, SPLIT, POSTM, S + 1, SE>(L, ring, post);
  }
}
template <class C, bool SPLIT, unsigned PREM, unsigned POSTM, class Pre, class Post>
DI void load_all(Ld<C>& L, unsigned ring, const Pre& pre, const Post& post, unsigned long long* dbg = nullptr) {
  constexpr int N = C::NSLOT, S0 = N - 1 > 2 + C::LAG ? N - 1 : 2 + C::LAG, S1 = C::NK - N + C::LAG;
#pragma unroll 1
  for (int s = 0; s < 2; ++s) { L.prep(s); if (!SPLIT) L.issueW(ring + s * C::STAGE); L.issueA(ring + s * C::STAGE); }
  L.prep(2);
  if (PREM >> 31) pre(-1);
  __builtin_amdgcn_s_setprio(3);
  load_fixed<C, SPLIT, POSTM, 0, S0>(L, ring, post);
  static_assert(vm_allow<C, SPLIT>(S0) == (N - 2 - C::LAG) * C::PW && vm_allow<C, SPLIT>(S1) == (N - 2 - C::LAG) * C::PW, "steady count");
  unsigned b = ring + ((S0 - 1 - C::LAG) % N) * C::STAGE;
#pragma unroll 1
  for (int s = S0; s <= S1; ++s) {
    CLK(t0);
    wait_vm<(N - 2 - C::LAG) * C::PW>();
    if ((PREM & 0x7fffffffu) && ((PREM >> s) & 1u)) pre(s);
    CLK(t1);
    bar();
    CLK(t2);
    L.issueW(b);
    L.issueA(b);
    CLK(t3);
    L.prep(min(s - C::LAG + N, C::NK - 1));
    b = (b == ring + (N - 1) * C::STAGE) ? ring : b + C::STAGE;
    if (POSTM && ((POSTM >> s) & 1u)) post(s);
  }
  __builtin_amdgcn_s_setprio(0);
  load_fixed<C, SPLIT, POSTM, S1 + 1, C::NK>(L, ring, post);
  bar();
}

template <int MT, int NWF> struct Fr { u32x4 a[MT]; u32x2 w[NWF][2]; };
DI u32x4 cat(const u32x2 (&w)[2]) { return (u32x4){w[0][0], w[0][1], w[1][0], w[1][1]}; }
struct Adr { unsigned v[6]; };
template <int KIND> struct Shape;
template <> struct Shape<1> { static constexpr int MT = 4, NWF = 6, NMF = 24, NACC = 24, WB = 6144; static constexpr unsigned USE = 0x0f; };
template <> struct Shape<3> { static constexpr int MT = 2, NWF = 4, NMF = 8, NACC = 8, WB = 2048; static constexpr unsigned USE = 0x3d; };
template <> struct Shape<4> { static constexpr int MT = 4, NWF = 8, NMF = 28, NACC = 28, WB = 7168; static constexpr unsigned USE = 0x3f; };
template <> struct Shape<5> { static constexpr int MT = 4, NWF = 4, NMF = 16, NACC = 16, WB = 4096; static constexpr unsigned USE = 0x3f; };
DI lds_ptr lp(unsigned a) { return (lds_ptr)(size_t)a; }
template <int KIND, int J> DI void rd1(Fr<Shape<KIND>::MT, Shape<KIND>::NWF>& f, const Adr& ad, int kb) {
  constexpr int NWF = Shape<KIND>::NWF, MT = Shape<KIND>::MT, WB = Shape<KIND>::WB;
  if constexpr (J < 2 * NWF) {
    constexpr int n = J >> 1, t = J & 1;
    if constexpr (KIND == 4 && n >= 6) f.w[n][t] = lds_tr(lp(ad.v[4 + (n - 6)]) + (2 * kb + t) * WB);
    else if constexpr (KIND == 1 || KIND == 4) f.w[n][t] = lds_tr(lp(ad.v[2 + (n & 1)]) + (n >> 1) * 2048 + (2 * kb + t) * WB);
    else f.w[n][t] = lds_tr(lp(ad.v[2 + n]) + (2 * kb + t) * WB);
  } else if constexpr (J < 2 * NWF + MT) {
    constexpr int m = J - 2 * NWF;
    if constexpr (KIND == 1) f.a[m] = lds128(lp(ad.v[kb]) + m * 2048);
    else f.a[m] = lds128(lp(ad.v[m >> 1]) + (m & 1) * 2048 + kb * 1024);
  }
}
template <int KIND, int I> DI void mm1(f32x4 (&acc)[Shape<KIND>::NACC], const Fr<Shape<KIND>::MT, Shape<KIND>::NWF>& f) {
  if constexpr (KIND == 1) { constexpr int m = I / 6, n = I % 6; if constexpr (n < 4) mfma_ip(acc[I], cat(f.w[n]), f.a[m]); else mfma_ip(acc[I], f.a[m], cat(f.w[n])); }
  else if constexpr (KIND == 4) { if constexpr (I < 24) mfma_ip(acc[I], cat(f.w[I % 6]), f.a[I / 6]); else mfma_ip(acc[I], cat(f.w[6 + ((I - 24) & 1)]), f.a[(I - 24) >> 1]); }
  else mfma_ip(acc[I], cat(f.w[I % 4]), f.a[I / 4]);
}
template <int KIND, int RPG, bool READ, bool MOVE, int I = 0>
DI void step(f32x4 (&acc)[Shape<KIND>::NACC], const Fr<Shape<KIND>::MT, Shape<KIND>::NWF>& cur, Fr<Shape<KIND>::MT, Shape<KIND>::NWF>& nxt, Adr& ad, int kb, int delta) {
  constexpr int NMF = Shape<KIND>::NMF, G0 = (2 * Shape<KIND>::NWF + Shape<KIND>::MT + RPG - 1) / RPG;
  if constexpr (I < NMF) {
    if constexpr (READ) {
      rd1<KIND, I * RPG>(nxt, ad, kb);
      if constexpr (RPG > 1) rd1<KIND, I * RPG + 1>(nxt, ad, kb);
    }
    if constexpr (MOVE && I >= G0) {
      constexpr int per = (6 + (NMF - G0) - 1) / (NMF - G0);
#pragma unroll
      for (int k = (I - G0) * per; k < (I - G0 + 1) * per && k < 6; ++k)
        if ((Shape<KIND>::USE >> k) & 1u) { ad.v[k] += delta; asm volatile("" : "+v"(ad.v[k])); }
    }
    mm1<KIND, I>(acc, cur);
    SB();
    step<KIND, RPG, READ, MOVE, I + 1>(acc, cur, nxt, ad, kb, delta);
  }
}
template <int KIND, int J = 0> DI void rdall(Fr<Shape<KIND>::MT, Shape<KIND>::NWF>& f, const Adr& ad, int kb) {
  if constexpr (J < 2 * Shape<KIND>::NWF + Shape<KIND>::MT) { rd1<KIND, J>(f, ad, kb); rdall<KIND, J + 1>(f, ad, kb); }
}
#define ST4() do {} while (0)
#define ST4ARGS
#define ST4PASS
template <class C, int KIND, int RPG, int CNT>
DI void stages(f32x4 (&acc)[Shape<KIND>::NACC], Fr<Shape<KIND>::MT, Shape<KIND>::NWF>& f0, Fr<Shape<KIND>::MT, Shape<KIND>::NWF>& f1, Adr& ad, int& slot ST4ARGS) {
#pragma unroll 1
  for (int s = 0; s < CNT; ++s) {
    const int delta = (slot + 1 == C::NSLOT) ? -(C::NSLOT - 1) * C::STAGE : C::STAGE;
    slot = (slot + 1 == C::NSLOT) ? 0 : slot + 1;
    SB();
    step<KIND, RPG, true, true>(acc, f0, f1, ad, 1, delta);
    if (!C::LAG) wait_lgkm0();
    ST4();
    bar();
    ST4();
    SB();
    step<KIND, RPG, true, false>(acc, f1, f0, ad, 0, 0);
  }
}
template <class C, int KIND, int RPG, int H0, int H1, int H2, class Hook> DI void consume(f32x4 (&acc)[Shape<KIND>::NACC], Adr& ad, unsigned* dbg4, const Hook& hook) {
  Fr<Shape<KIND>::MT, Shape<KIND>::NWF> f0, f1;
#pragma unroll
  for (int i = 0; i < Shape<KIND>::NACC; ++i) acc[i] = (f32x4){0.f, 0.f, 0.f, 0.f};
  bar();
  rdall<KIND>(f0, ad, 0);
  hook(-1);
  wait_lgkm0();
  int slot = 0;
  if constexpr (H1 > 0) {
    if constexpr (H0 > 0) stages<C, KIND, RPG, H0>(acc, f0, f1, ad, slot ST4PASS);
    hook(0);
    stages<C, KIND, RPG, H1 - H0>(acc, f0, f1, ad, slot ST4PASS);
    hook(1);
    stages<C, KIND, RPG, H2 - H1>(acc, f0, f1, ad, slot ST4PASS);
    hook(2);
  }
  stages<C, KIND, RPG, C::NK - 1 - H2>(acc, f0, f1, ad, slot ST4PASS);
  SB();
  step<KIND, RPG, true, false>(acc, f0, f1, ad, 1, 0);
  wait_lgkm0();
  bar();
  SB();
  step<KIND, RPG, false, false>(acc, f1, f0, ad, 0, 0);
  asm volatile("s_nop 7\n\ts_nop 3");
  SB();
}
template <class C, int KIND, int RPG, class Hook> DI void consume_s(f32x4 (&acc)[Shape<KIND>::NACC], Adr& ad, const Hook& hook) {
  Fr<Shape<KIND>::MT, Shape<KIND>::NWF> f0, f1, g0, g1;
#pragma unroll
  for (int i = 0; i < Shape<KIND>::NACC; ++i) acc[i] = (f32x4){0.f, 0.f, 0.f, 0.f};
  bar();
  rdall<KIND>(f0, ad, 0); rdall<KIND>(f1, ad, 1);
  hook(-1);
#pragma unroll
  for (int k = 0; k < 6; ++k) ad.v[k] += C::STAGE;
  int slot = 1;
#pragma unroll 1
  for (int s = 0; s < C::NK - 2; s += 2) {
    int delta = (slot + 1 == C::NSLOT) ? -(C::NSLOT - 1) * C::STAGE : C::STAGE;
    slot = (slot + 1 == C::NSLOT) ? 0 : slot + 1;
    if (!C::LAG) wait_lgkm0();
    bar(); SB();
    step<KIND, RPG, true, false>(acc, f0, g0, ad, 0, 0);
    step<KIND, RPG, true, true>(acc, f1, g1, ad, 1, delta);
    delta = (slot + 1 == C::NSLOT) ? -(C::NSLOT - 1) * C::STAGE : C::STAGE;
    slot = (slot + 1 == C::NSLOT) ? 0 : slot + 1;
    if (!C::LAG) wait_lgkm0();
    bar(); SB();
    step<KIND, RPG, true, false>(acc, g0, f0, ad, 0, 0);
    step<KIND, RPG, true, true>(acc, g1, f1, ad, 1, delta);
  }
  if (!C::LAG) wait_lgkm0();
  bar(); SB();
  step<KIND, RPG, true, false>(acc, f0, g0, ad, 0, 0);
  step<KIND, RPG, true, false>(acc, f1, g1, ad, 1, 0);
  wait_lgkm0(); bar(); SB();
  step<KIND, RPG, false, false>(acc, g0, f0, ad, 0, 0);
  step<KIND, RPG, false, false>(acc, g1, f1, ad, 1, 0);
  asm volatile("s_nop 7\n\ts_nop 3");
  SB();
}
DI unsigned wlane(int lane, int nt) {
  const int g = lane >> 4, q = (lane >> 2) & 3, p = lane & 3, sw = (2 * g + (q >> 1)) & 3;
  return (4 * g + q) * 128 + ((nt ^ sw) << 5) + p * 8;
}
DI unsigned hlane(int lane, int nt) {
  const int g = lane >> 4, q = (lane >> 2) & 3, p = lane & 3;
  return (4 * g + q) * 64 + ((nt ^ (g & 1)) << 5) + p * 8;
}

template <int OP> DI float op2(float a, float b) { return OP == 0 ? a + b : OP == 1 ? __builtin_elementwise_maximum(a, b) : __builtin_elementwise_minimum(a, b); }
template <int OP> DI float red4(float x0, float x1, float x2, float x3) {
  auto a = __builtin_amdgcn_permlane16_swap(bu(x0), bu(x1), false, false);
  auto b = __builtin_amdgcn_permlane16_swap(bu(x2), bu(x3), false, false);
  const float y0 = op2<OP>(fb(a[0]), fb(a[1])), y1 = op2<OP>(fb(b[0]), fb(b[1]));
  auto c = __builtin_amdgcn_permlane32_swap(bu(y0), bu(y1), false, false);
  return op2<OP>(fb(c[0]), fb(c[1]));
}
DI void spread(float z, float (&o)[4]) {
  auto a = __builtin_amdgcn_permlane32_swap(bu(z), bu(z), false, false);
  auto b = __builtin_amdgcn_permlane16_swap(a[0], a[0], false, false);
  auto c = __builtin_amdgcn_permlane16_swap(a[1], a[1], false, false);
  o[0] = fb(b[0]); o[1] = fb(b[1]); o[2] = fb(c[0]); o[3] = fb(c[1]);
}
DI void dual_softmax4(const f32x4 (&p)[4][2], u32x4 (&pp)[4], u32x4 (&pn)[4]) {
  constexpr float L2E = 1.4426950408889634f;
  float mx[4], mn[4];
#pragma unroll
  for (int m = 0; m < 4; ++m) {
    mx[m] = op2<1>(op2<1>(op2<1>(op2<1>(p[m][0][0], p[m][0][1]), p[m][0][2]), op2<1>(op2<1>(p[m][0][3], p[m][1][0]), p[m][1][1])), op2<1>(p[m][1][2], p[m][1][3]));
    mn[m] = op2<2>(op2<2>(op2<2>(op2<2>(p[m][0][0], p[m][0][1]), p[m][0][2]), op2<2>(op2<2>(p[m][0][3], p[m][1][0]), p[m][1][1])), op2<2>(p[m][1][2], p[m][1][3]));
  }
  spread(-L2E * red4<1>(mx[0], mx[1], mx[2], mx[3]), mx);
  spread(L2E * red4<2>(mn[0], mn[1], mn[2], mn[3]), mn);
  f32x4 ep[4][2], en[4][2];
  float sp[4], sn[4];
#pragma unroll
  for (int m = 0; m < 4; ++m) {
#pragma unroll
    for (int F = 0; F < 2; ++F) {
      const f32x4 a = p[m][F] * L2E + mx[m], b = p[m][F] * (-L2E) + mn[m];
#pragma unroll
      for (int r = 0; r < 4; ++r) { ep[m][F][r] = __builtin_amdgcn_exp2f(a[r]); en[m][F][r] = __builtin_amdgcn_exp2f(b[r]); }
    }
    const f32x4 a = ep[m][0] + ep[m][1], b = en[m][0] + en[m][1];
    sp[m] = (a[0] + a[1]) + (a[2] + a[3]); sn[m] = (b[0] + b[1]) + (b[2] + b[3]);
  }
  spread(__builtin_amdgcn_rcpf(red4<0>(sp[0], sp[1], sp[2], sp[3])), sp);
  spread(__builtin_amdgcn_rcpf(red4<0>(sn[0], sn[1], sn[2], sn[3])), sn);
#pragma unroll
  for (int m = 0; m < 4; ++m) { pp[m] = pack8(ep[m][0] * sp[m], ep[m][1] * sp[m]); pn[m] = pack8(en[m][0] * sn[m], en[m][1] * sn[m]); }
}

DI float dot2(unsigned a, unsigned b, float c) { return __builtin_amdgcn_fdot2_f32_bf16(__builtin_bit_cast(bf16x2, a), __builtin_bit_cast(bf16x2, b), c, false); }
DI unsigned opaque(unsigned c) { asm volatile("" : "+s"(c)); return c; }
template <int NL> DI void colsum_load(u32x4 (&v)[NL], const char* w, int stride, int lane) {
#pragma unroll
  for (int i = 0; i < NL; ++i) v[i] = *(const u32x4*)(w + (size_t)(8 * i + (lane >> 3)) * stride + (lane & 7) * 16);
}
template <int NL> DI void colsum_finish(const u32x4 (&v)[NL], rsrc_t r, unsigned dst, int lane) {
  f32x2 s2[4];
#pragma unroll
  for (int j = 0; j < 4; ++j) s2[j] = (f32x2){bflo(v[0][j]), bfhi(v[0][j])};
#pragma unroll
  for (int i = 1; i < NL; ++i)
#pragma unroll
    for (int j = 0; j < 4; ++j) s2[j] += (f32x2){bflo(v[i][j]), bfhi(v[i][j])};
  const float s[8] = {s2[0][0], s2[0][1], s2[1][0], s2[1][1], s2[2][0], s2[2][1], s2[3][0], s2[3][1]};
  float t[4], u[2];
#pragma unroll
  for (int j = 0; j < 4; ++j) { auto q = __builtin_amdgcn_permlane32_swap(bu(s[j]), bu(s[j + 4]), false, false); t[j] = fb(q[0]) + fb(q[1]); }
#pragma unroll
  for (int j = 0; j < 2; ++j) { auto q = __builtin_amdgcn_permlane16_swap(bu(t[j]), bu(t[j + 2]), false, false); u[j] = fb(q[0]) + fb(q[1]); }
  const bool b3 = lane & 8;
  const float tot = (b3 ? u[1] : u[0]) + dpp<0x128>(b3 ? u[0] : u[1]);
  st4(r, bu(tot), ((lane & 7) * 8 + ((lane >> 5) & 1) * 4 + ((lane >> 4) & 1) * 2 + ((lane >> 3) & 1)) * 4, dst);
}

template <int XN, int TM, int TN> DI void tile_of(int bid, int& tm, int& tn, int& c) {
  const int x = bid & 7, r = bid >> 3, li = r % TM, lj = r / TM;
  tm = (x / XN) * TM + li; tn = (x % XN) * TN + lj; c = (li + lj) & 3;
}

#define RPG1 1
#define RPG4 1
#define RPG5 1
#define W2AT 0
#define PH0 2
#define PH1 6
#define PH2 7
#define PF 10
#define W1M 0u
constexpr int PR = PF + 6 > 15 ? 15 : PF + 6;
constexpr int R1 = 40704, R3 = 40704, R4 = 0, R5 = 0, SYNCW = 163584;
constexpr int S1_ST = 0, S1_CSR = 1024, S1_CSF = 13312, S1_FLG = 14080, S1_WP = 14336, S1_KV = 18432, S1_DUM = 26880;
constexpr int S4_STR = C4::RING, S4_CSR = S4_STR + 16384, S4_STF = S4_CSR + 8192, S4_CSF = S4_STF + 1024, S4_DUM = S4_CSF + 1024;
constexpr int LDS_BYTES = 163840, CODE_BYTES = 54 * 1024;
static_assert(S4_DUM + 256 <= SYNCW && S1_KV + 8448 <= R1 && R1 + C1::RING <= SYNCW && R3 + C3::RING <= SYNCW && C5::RING <= S4_STR, "lds");

template <class St> DI void kv_sum(const Sync& y, Gate& G, lds_ptr smem, int head, int h, int t, bool poller, bool doz, int lane, const St& stamp) {
  u32x4 v[16], zz[4];
  unsigned have = 0;
  f32x2 s2[4] = {{0.f, 0.f}, {0.f, 0.f}, {0.f, 0.f}, {0.f, 0.f}};
#pragma unroll 1
  for (int round = poller ? 2 : 0; round < 3; ++round) {
    gate_slow(y, G, 0u, poller, lane, round == 0 ? 0 : round == 1 ? 13 : 16);
    stamp(round);
    const unsigned fresh = G.mask & ~have;
    have = G.mask;
#pragma unroll
    for (int r = 0; r < 16; ++r) if ((fresh >> r) & 1u) v[r] = ld16(y.r, t * 16, WS_KVP + (head * 16 + r) * 4096);
    if (round == 2 && doz) {
#pragma unroll
      for (int i = 0; i < 4; ++i) zz[i] = ld16(y.r, lane * 16, WS_ZP + head * 4096 + i * 1024);
    }
  }
#pragma unroll
  for (int r = 0; r < 16; ++r)
#pragma unroll
    for (int j = 0; j < 4; ++j) s2[j] += (f32x2){bflo(v[r][j]), bfhi(v[r][j])};
  const float s[8] = {s2[0][0], s2[0][1], s2[1][0], s2[1][1], s2[2][0], s2[2][1], s2[3][0], s2[3][1]};
  *(LAS u32x4*)(smem + S1_KV + h * 4096 + t * 16) = (u32x4){pk2(s[0], s[1]), pk2(s[2], s[3]), pk2(s[4], s[5]), pk2(s[6], s[7])};
  if (doz) {
    f32x4 z = (__builtin_bit_cast(f32x4, zz[0]) + __builtin_bit_cast(f32x4, zz[1])) + (__builtin_bit_cast(f32x4, zz[2]) + __builtin_bit_cast(f32x4, zz[3]));
#pragma unroll
    for (int j = 0; j < 4; ++j) z[j] = xsum32(xsum16(z[j]));
    if (lane < 16) *(LAS u32x2*)(smem + S1_KV + 8192 + h * 128 + lane * 8) = (u32x2){pk2(z[0], z[1]), pk2(z[2], z[3])};
  }
}

__global__ __launch_bounds__(512, 2) void fused(Params P) {
  extern __shared__ __attribute__((aligned(16))) char smem_[];
  lds_ptr smem = (lds_ptr)smem_;
  const unsigned lds0 = (unsigned)(size_t)smem;
  const int wid = __builtin_amdgcn_readfirstlane(threadIdx.x >> 6), lane = threadIdx.x & 63, bid = blockIdx.x;
  {
    const char* pc; asm volatile("s_getpc_b64 %0" : "=s"(pc));
    const unsigned co = ((bid >> 3) & 7) * 8192 + threadIdx.x * 128;
    if (threadIdx.x < 64 && co < CODE_BYTES) dma4<false>(co, pc, lds0 + S1_DUM);
    SB();
  }
  int tm1, tn1, c1, tm3, tn3, c3;
  tile_of<4, 8, 4>(bid, tm1, tn1, c1);
  tile_of<2, 4, 8>(bid, tm3, tn3, c3);
  const int kh = bid & 1, tm5 = ((bid & 7) >> 1) * 4 + ((bid >> 3) & 3), tn5 = bid >> 5, c5 = ((bid >> 3) + tn5) & 3;
  const int par = tn1 & 1, base7 = 7 * (tn1 >> 1), ch0 = base7 + 4 * par;
  const int g = lane >> 4, m16 = lane & 15;
  Sync y;
  y.r = __builtin_amdgcn_make_buffer_rsrc(P.ws, 0, WS_END, 0x00020000);
  y.w = (LAS unsigned*)(smem + SYNCW); y.pb = 0;
  if (wid == 0) TS(0);

  if (wid >= 4) {
    const int lw = wid - 4, lt = threadIdx.x - 256;
    const auto nop = [](int) {};
    {
      Ld<C1> L; L.A = P.x + (size_t)tm1 * 128 * 2048; L.W = P.wqkv + tn1 * 128; L.lw = lw; L.c = c1; L.kt0 = 0; L.init(y, lane, 2048, 0, 0, 0);
      const unsigned frec = (FL_P0 * 256 + (lane < 16 ? tm1 * 16 + lane : lane < 28 ? ((lane - 16) / 3) * 48 + ((lane - 16) % 3) * 16 + tn1 : tn1)) * 128;
      if (wid == 4) TS(31);
      if (lw == 0) dma4<true>(0u * lane, P.ws + WS_CTL, lds0 + S1_FLG);
      load_all<C1, false, (1u << (PF + 3)) | (1u << 31), 0x8005u | W1M | (1u << PF) | (1u << (PF + 3)) | (1u << PR)>(L, lds0 + R1,
        [&](int s) {
          if (s < 0) { if (wid == 4) TS(30); return; }
          if (lw == 0) {
            const u32x4 v = *(volatile LAS u32x4*)(smem + S1_CSR + lane * 16);
            if (!__all(v[0] == y.tag && v[1] == y.tag && v[2] == y.tag && v[3] == y.tag)) {
#pragma unroll 1
              for (int k = 0; k < 4; ++k) poll(y, frec + 4 * k, 64, lane);
            }
          }
        },
        [&](int s) {
          if (s == 0) y.tag = __builtin_amdgcn_readfirstlane(*(volatile LAS unsigned*)(smem + S1_FLG)) + 1u;
          if (s == 2) dma<false>(lane * 16, P.wphi + tn1 * 4096 + lw * 1024, lds0 + S1_WP + lw * 1024);
          if (((W1M >> s) & 1u) && lw == __builtin_popcount(W1M & ((1u << s) - 1u))) dma4<false>(min(lane, 55) * 128, P.w1 + bid * 28672 + lw * 7168, lds0 + S1_DUM);
          if (s == PF && lw == 0) dma<true>(frec, P.ws + WS_FLG, lds0 + S1_CSR);
          if (s == 15 && lw == 1) dma4<false>(lane * 128, P.wo + bid * 8192, lds0 + S1_DUM);
          if (s == PF + 3) {
            if (lw == 3) dma<true>(lane * 16, P.ws + WS_ST1 + tm1 * 1024, lds0 + S1_ST);
#pragma unroll
            for (int i = 0; i < 3; ++i)
              dma<true>((lane >> 4) * 12288 + (lane & 15) * 16, P.ws + WS_CSQ + lw * 49152 + i * 4096 + tn1 * 256, lds0 + S1_CSR + i * 4096 + lw * 1024);
          }
          if (s == PR) {
            if (lt < 192) {
              const LAS float* rawp = (const LAS float*)(smem + S1_CSR) + (lt >> 6) * 1024 + (lt & 63);
              float t = 0.f;
#pragma unroll
              for (int i = 0; i < 16; ++i) t += rawp[i * 64];
              ((LAS float*)(smem + S1_CSF))[lt] = t;
            }
            wait_lgkm0();
          }
        }, DBG(0));
    }
    if (wid == 4) TS(16);
    {
      Ld<C3> L; L.A = P.ws; L.W = P.wo + tn3 * 128; L.lw = lw; L.c = c3; L.kt0 = 0; L.init(y, lane, 0, 0, 0, 9);
      if (bid >= 224 && bid < 224 + NPAGE) {
        if (lw == 1) {
          const unsigned po = bid == 224 ? WS_PRB : (bid - 224) << 21;
          unsigned tmin[2] = {~0u, ~0u};
#pragma unroll 1
          for (int k = 0; k < 6; ++k) {
            const unsigned long long t0 = __builtin_amdgcn_s_memtime();
            if (lane < 8) st16(y.r, (u32x4){0u, 0u, 0u, 0u}, lane * 16, po + (k & 1) * 8192);
            wait_vm<0>();
            const unsigned dt = (unsigned)(__builtin_amdgcn_s_memtime() - t0);
            if (k & 1) tmin[1] = min(tmin[1], dt); else tmin[0] = min(tmin[0], dt);
          }
          if (lane == 0) st4(y.r, (y.tag << 1) | (((unsigned)(tmin[1] < tmin[0]) ^ (unsigned)(bid >> 2)) & 1u), 0, WS_FLG + (FL_CS * 256 + bid) * 128 + 4);
        }
        bar_loaders(y, lw);
      }
      load_preW<C3>(L, lds0 + R3);
      if (lw >= 2 && bid < 224) {
        u32x4 v[16];
        colsum_load<16>(v, P.w1 + (size_t)((bid / 56) * 256 + (lw - 2) * 128) * (FF * 2) + (bid % 56) * 128, FF * 2, lane);
        colsum_finish<16>(v, y.r, WS_CS1 + (((bid / 56) * 2 + lw - 2) * FF + (bid % 56) * 64) * 4, lane);
      }
      publish(y, FL_CS, bid, lane);
      lds_wait(y, 24 + FL_KV, 4u);
      Gate G; G.off = (FL_KV * 256 + m16 * 16 + tn1) * 128; G.mask = 0; G.word = 8;
      const unsigned pbo = WS_FLG + (FL_CS * 256 + 224 + (lane & 31)) * 128 + 4;
      unsigned pbv = 0;
      kv_sum(y, G, smem, 2 * tn1 + 1, 1, lt, lw == 0, lw == 1, lane, [&](int) { if (lw == 0) pbv = __builtin_amdgcn_raw_buffer_load_b32(y.r, pbo, 0, 16); });
      if (lw == 0) {
        const unsigned long long t0 = __builtin_amdgcn_s_memrealtime();
        while (!__all(lane >= NPAGE || (pbv >> 1) == (y.tag & 0x7fffffffu))) {
          if (__builtin_amdgcn_s_memrealtime() - t0 > 4000000ull) { if (lane == 0) st4(y.r, y.tag, 0, WS_CTL + 128); break; }
          pbv = __builtin_amdgcn_raw_buffer_load_b32(y.r, pbo, 0, 16);
        }
        const unsigned raw = (unsigned)__ballot(pbv & 1u), odd = __builtin_popcount((raw ^ 0xaaaaaaaau) & 0x3fffffdu);
        *(volatile LAS unsigned*)(y.w + 13) = odd <= 6 ? 0xaaaaaaaau : odd >= 19 ? 0x55555555u : raw;
      }
      if (wid == 4) TS(17);
      wait_lgkm0(); bar();
      y.pb = __builtin_amdgcn_readfirstlane(*(volatile LAS unsigned*)(y.w + 13));
      L.A += near(y, WS_O + tm3 * 524288, tm3); L.G.off = rec(y, FL_O, tm3 * 16 + m16);
      if (wid == 4) TS(18);
      load_all<C3, true, 0u, 0u>(L, lds0 + R3, nop, nop, DBG(1));
    }
    if (wid == 4) TS(19);
    {
      Ld<C4> L; L.A = P.ws + near(y, WS_H + tm1 * 524288, tm1); L.W = P.w1 + ch0 * 128; L.lw = lw; L.c = c1; L.kt0 = 0;
      L.init(y, lane, 128, par ? -64 : 384, (lane & 16) ? rec(y, FL_CS, (m16 >> 2) * 56 + base7 + 3 * par + (lane & 3)) : rec(y, FL_H, tm1 * 16 + m16), 10);
      lds_wait(y, 24 + FL_H, 4u);
      load_preW<C4>(L, lds0 + R4);
      load_all<C4, true, 0u, 0xa000u | (W2AT == 0 ? 0x550u : W2AT > 0 ? 1u << (W2AT & 31) : 0u)>(L, lds0 + R4, nop,
        [&](int s) {
          if (W2AT == 0 ? (s < 12 && lw == ((s >> 1) & 3)) : s == W2AT) dma4<false>(min(lane, 55) * 128, P.w2 + bid * 28672 + lw * 7168, lds0 + S4_DUM);
          if (s == 13) {
            gate(y, L.G, ~0u, lw == 0, lane);
#pragma unroll
            for (int i = 0; i < 4; ++i) dma<true>(lane * 16, P.ws + near(y, WS_ST2 + tm1 * 32768, tm1) + (lw >> 1) * 16384 + ((lw & 1) * 4 + i) * 1024, lds0 + S4_STR + (lw * 4 + i) * 1024);
#pragma unroll
            for (int i = 0; i < 2; ++i) dma<true>(lane * 16, P.ws + WS_CS1 + ((lw * 2 + i) * FF + (base7 + 3 * par) * 64) * 4, lds0 + S4_CSR + (lw * 2 + i) * 1024);
          }
          if (s == 15) {
            const LAS float* rawp = (const LAS float*)(smem + S4_CSR) + lt;
            float t = 0.f;
#pragma unroll
            for (int i = 0; i < 8; ++i) t += rawp[i * 256];
            ((LAS float*)(smem + S4_CSF))[lt] = t;
            wait_lgkm0();
          }
          if (s == 15 && lw < 2) {
            f32x2 st[16]; float mu = 0.f, m2 = 0.f;
#pragma unroll
            for (int i = 0; i < 16; ++i) { st[i] = *(const LAS f32x2*)(smem + S4_STR + i * 1024 + lt * 8); mu += st[i][0]; }
            mu *= (1.f / 16.f);
#pragma unroll
            for (int i = 0; i < 16; ++i) { const float d = st[i][0] - mu; m2 += st[i][1] + 64.f * d * d; }
            *(LAS f32x2*)(smem + S4_STF + lt * 8) = (f32x2){mu, __builtin_amdgcn_rsqf(m2 * (1.f / D) + 1e-5f)};
            wait_lgkm0();
          }
        }, DBG(2));
    }
    {
      Ld<C5> L; L.A = P.ws + near(y, WS_F + tm5 * 2097152, tm5); L.W = P.w2 + tn5 * 256; L.lw = lw; L.c = c5; L.kt0 = 28 * kh; L.init(y, lane, 128, 0, rec(y, FL_F, tm5 * 16 + 8 * kh + ((lane >> 1) & 7)) + (lane & 1) * 4, 11);
      lds_wait(y, 24 + FL_F, 4u);
      load_preW<C5>(L, lds0 + R5);
      load_all<C5, true, 0u, 0u>(L, lds0 + R5, nop, nop, DBG(3));
    }
    if (wid == 4) { TS(23); TS_FLUSH(); }
    return;
  }

  const int wm = wid >> 1, wn = wid & 1;
  if (wid == 0 && lane < 32) y.w[lane] = 0u;
  {
    f32x4 acc[24];
    Adr ad;
    ad.v[0] = lds0 + R1 + (wm * 64 + m16) * 128 + ((g ^ (m16 >> 1)) << 4); ad.v[1] = lds0 + R1 + (wm * 64 + m16) * 128 + ((g ^ (m16 >> 1) ^ 4) << 4);
    ad.v[2] = lds0 + R1 + 16384 + wlane(lane, 2 * wn); ad.v[3] = lds0 + R1 + 16384 + wlane(lane, 2 * wn + 1); ad.v[4] = 0; ad.v[5] = 0;
    u32x4 px[4], pw[8];
    const int prow = bid * 8 + wid * 2 + (lane >> 5);
    consume<C1, 1, RPG1, PH0, PH1, PH2>(acc, ad, DBG4, [&](int s) {
      if (s < 0) {
        if (wid == 0) TS(1);
        y.tag = __builtin_amdgcn_readfirstlane(*(volatile LAS unsigned*)(smem + S1_FLG)) + 1u;
      }
      if (s == 0) {
#pragma unroll
        for (int i = 0; i < 4; ++i) px[i] = *(const u32x4*)(P.x + (size_t)prow * 2048 + ((lane & 31) + 32 * i) * 16);
        colsum_load<8>(pw, P.wqkv + (size_t)((min(bid, 191) / 48) * 256 + wid * 64) * (3 * D * 2) + (bid % 48) * 128, 3 * D * 2, lane);
      }
      if (s == 1) {
#pragma unroll
        for (int i = 0; i < 4; ++i) asm volatile("" : "+v"(px[i]));
#pragma unroll
        for (int i = 0; i < 8; ++i) asm volatile("" : "+v"(pw[i]));
        const unsigned one2 = opaque(0x3f803f80u);
        float sm = 0.f, sq = 0.f;
#pragma unroll
        for (int i = 0; i < 4; ++i)
#pragma unroll
          for (int j = 0; j < 4; ++j) { sm = dot2(px[i][j], one2, sm); sq = dot2(px[i][j], px[i][j], sq); }
        const float mean = xsum16(row16_sum(sm)) * (1.f / D);
        const float var = fmaxf(xsum16(row16_sum(sq)) * (1.f / D) - mean * mean, 0.f);
        if ((lane & 31) == 0) st8(y.r, (u32x2){bu(mean), bu(__builtin_amdgcn_rsqf(var + 1e-5f))}, prow * 8, WS_ST1);
        if (bid < 192) colsum_finish<8>(pw, y.r, WS_CSQ + (((bid / 48) * 4 + wid) * 3 * D + (bid % 48) * 64) * 4, lane);
      }
      if (s == 2) { wait_vm<0>(); if (lane == 0) st4(y.r, y.tag, 0, WS_FLG + (FL_P0 * 256 + bid) * 128 + wid * 4); }
    });
    if (wid == 0) TS(2);
    const int head = 2 * tn1 + wn;
    f32x4 cs[4]; float csv[2];
#pragma unroll
    for (int n = 0; n < 4; ++n) cs[n] = *(const LAS f32x4*)(smem + S1_CSF + ((n >> 1) * 64 + wn * 32 + (n & 1) * 16 + 4 * g) * 4);
#pragma unroll
    for (int n = 0; n < 2; ++n) csv[n] = *(const LAS float*)(smem + S1_CSF + (128 + wn * 32 + n * 16 + m16) * 4);
    u32x4 wp[2];
#pragma unroll
    for (int F = 0; F < 2; ++F) { lds_ptr a0 = smem + S1_WP + wn * 2048 + (4 * g + (m16 >> 2)) * 64 + F * 32 + (lane & 3) * 8; wp[F] = lds_tr2(a0, a0 + 1024); }
    u32x4 qb[4], vb[2][2];
    f32x4 p[4][2];
    const f32x4 zero = {0.f, 0.f, 0.f, 0.f};
    {
      f32x4 vv[4][2];
#pragma unroll
      for (int m = 0; m < 4; ++m) {
        const f32x2 st = *(const LAS f32x2*)(smem + S1_ST + (wm * 64 + m * 16 + m16) * 8);
        const f32x4 s0 = *(const LAS f32x4*)(smem + S1_ST + (wm * 64 + m * 16 + 4 * g) * 8), s1 = *(const LAS f32x4*)(smem + S1_ST + (wm * 64 + m * 16 + 4 * g) * 8 + 16);
        const f32x4 mu = {s0[0], s0[2], s1[0], s1[2]}, rs = {s0[1], s0[3], s1[1], s1[3]};
        qb[m] = pack8((acc[m * 6] - cs[0] * st[0]) * st[1], (acc[m * 6 + 1] - cs[1] * st[0]) * st[1]);
        const u32x4 kb = pack8((acc[m * 6 + 2] - cs[2] * st[0]) * st[1], (acc[m * 6 + 3] - cs[3] * st[0]) * st[1]);
        p[m][0] = mfma(wp[0], kb, zero); p[m][1] = mfma(wp[1], kb, zero);
        vv[m][0] = (acc[m * 6 + 4] - mu * csv[0]) * rs; vv[m][1] = (acc[m * 6 + 5] - mu * csv[1]) * rs;
      }
#pragma unroll
      for (int u = 0; u < 2; ++u) { vb[u][0] = pack8(vv[2 * u][0], vv[2 * u + 1][0]); vb[u][1] = pack8(vv[2 * u][1], vv[2 * u + 1][1]); }
    }
    u32x4 pp[4], pn[4];
    if (wid == 0) TS(26);
    dual_softmax4(p, pp, pn);
    if (wid == 0) TS(27);
    {
      lds_ptr own = smem + R3 + wid * C3::STAGE, oth = smem + R3 + (wid ^ 2) * C3::STAGE;
#pragma unroll
      for (int m = 0; m < 4; ++m) {
        lds_ptr w = own + (16 * m + m16) * 136 + 8 * g;
        *(LAS u32x2*)(w) = (u32x2){pp[m][0], pp[m][1]}; *(LAS u32x2*)(w + 32) = (u32x2){pp[m][2], pp[m][3]};
        *(LAS u32x2*)(w + 64) = (u32x2){pn[m][0], pn[m][1]}; *(LAS u32x2*)(w + 96) = (u32x2){pn[m][2], pn[m][3]};
      }
#pragma unroll
      for (int i = 0; i < 4; ++i) *(LAS u32x4*)(own + 8704 + i * 1024 + lane * 16) = vb[i >> 1][i & 1];
      wait_lgkm0();
      volatile LAS unsigned* xf = y.w + 16;
      xf[wid] = 1u;
      f32x4 kv[2][2] = {{zero, zero}, {zero, zero}}, z[2] = {zero, zero};
      const u32x4 ones = {0x3f803f80u, 0x3f803f80u, 0x3f803f80u, 0x3f803f80u};
      const unsigned ro = (4 * g + (m16 >> 2)) * 136 + wm * 64 + (lane & 3) * 8;
#pragma unroll
      for (int u = 0; u < 2; ++u)
#pragma unroll
        for (int F = 0; F < 2; ++F) {
          const u32x4 pa = lds_tr2(own + ro + u * 32 * 136 + F * 32, own + ro + u * 32 * 136 + F * 32 + 16 * 136);
          kv[F][0] = mfma(pa, vb[u][0], kv[F][0]); kv[F][1] = mfma(pa, vb[u][1], kv[F][1]); z[F] = mfma(pa, ones, z[F]);
        }
      while (xf[wid ^ 2] == 0u) __builtin_amdgcn_s_sleep(0);
      asm volatile("" ::: "memory");
#pragma unroll
      for (int u = 0; u < 2; ++u) {
        const u32x4 v0 = lds128(oth + 8704 + u * 2048 + lane * 16), v1 = lds128(oth + 8704 + u * 2048 + 1024 + lane * 16);
#pragma unroll
        for (int F = 0; F < 2; ++F) {
          const u32x4 pa = lds_tr2(oth + ro + u * 32 * 136 + F * 32, oth + ro + u * 32 * 136 + F * 32 + 16 * 136);
          kv[F][0] = mfma(pa, v0, kv[F][0]); kv[F][1] = mfma(pa, v1, kv[F][1]); z[F] = mfma(pa, ones, z[F]);
        }
      }
      const unsigned kd = WS_KVP + (head * 16 + tm1) * 4096 + wm * 2048;
      st16(y.r, pack8(kv[0][0], kv[1][0]), lane * 16, kd);
      st16(y.r, pack8(kv[0][1], kv[1][1]), lane * 16, kd + 1024);
      if (m16 == 0) {
        st16(y.r, __builtin_bit_cast(u32x4, z[0]), g * 32, WS_ZP + (head * 16 + tm1) * 256 + wm * 128);
        st16(y.r, __builtin_bit_cast(u32x4, z[1]), g * 32 + 16, WS_ZP + (head * 16 + tm1) * 256 + wm * 128);
      }
      if (wid == 0) TS(25);
      publish(y, FL_KV, tm1 * 16 + tn1, lane);
    }
    if (wid == 0) TS(3);
#pragma unroll
    for (int m = 0; m < 4; ++m) { p[m][0] = mfma(wp[0], qb[m], zero); p[m][1] = mfma(wp[1], qb[m], zero); }
    dual_softmax4(p, pp, pn);
    if (wid == 0) TS(4);
    { Gate G; G.off = 0; G.mask = 0; G.word = 8; kv_sum(y, G, smem, 2 * tn1, 0, threadIdx.x, false, wid == 0, lane, [](int) {}); }
    wait_lgkm0(); bar();
    if (wid == 0) TS(5);
    y.pb = __builtin_amdgcn_readfirstlane(*(volatile LAS unsigned*)(y.w + 13));
    u32x4 ka[2][2], za[2];
#pragma unroll
    for (int ks = 0; ks < 2; ++ks) {
      ka[ks][0] = lds128(smem + S1_KV + wn * 4096 + ks * 2048 + lane * 16); ka[ks][1] = lds128(smem + S1_KV + wn * 4096 + ks * 2048 + 1024 + lane * 16);
      za[ks] = lds128(smem + S1_KV + 8192 + wn * 128 + ks * 64 + g * 16);
    }
#pragma unroll
    for (int m = 0; m < 4; ++m) {
      f32x4 n0 = mfma(ka[0][0], pp[m], zero), n1 = mfma(ka[0][1], pp[m], zero), dn = mfma(za[0], pp[m], zero);
      n0 = mfma(ka[1][0], pn[m], n0); n1 = mfma(ka[1][1], pn[m], n1); dn = mfma(za[1], pn[m], dn);
      const float rd = __builtin_amdgcn_rcpf(dn[0] + 1e-6f);
      st16(y.r, pack8(n0 * rd, n1 * rd), lane * 16, near(y, WS_O + (tm1 * 16 + tn1) * 32768, tm1) + wm * 16384 + (m * 2 + wn) * 1024);
    }
    publish(y, FL_O, tm1 * 16 + tn1, lane);
    if (wid == 0) TS(6);
  }
  {
    u32x4 xr[2][2];
#pragma unroll
    for (int m = 0; m < 2; ++m)
#pragma unroll
      for (int Q = 0; Q < 2; ++Q)
        xr[m][Q] = *(const u32x4*)(P.x + (size_t)(tm3 * 128 + wid * 32 + m * 16 + m16) * 2048 + tn3 * 128 + Q * 64 + (g & 1) * 32 + (g >> 1) * 16);
    f32x4 acc[8];
    Adr ad; ad.v[0] = lds0 + R3 + wid * 4096 + lane * 16; ad.v[1] = 0;
#pragma unroll
    for (int n = 0; n < 4; ++n) ad.v[2 + n] = lds0 + R3 + 16384 + wlane(lane, n);
    consume_s<C3, 3, 2>(acc, ad, [&](int) { if (wid == 0) TS(7); });
    if (wid == 0) TS(8);
#pragma unroll
    for (int m = 0; m < 2; ++m) {
      float hv[16];
#pragma unroll
      for (int Q = 0; Q < 2; ++Q) {
        auto r0 = __builtin_amdgcn_permlane16_swap(xr[m][Q][0], xr[m][Q][2], false, false);
        auto r1 = __builtin_amdgcn_permlane16_swap(xr[m][Q][1], xr[m][Q][3], false, false);
        const f32x4 a = acc[m * 4 + 2 * Q], b = acc[m * 4 + 2 * Q + 1];
        const u32x4 hp = {pk2(a[0] + bflo(r0[0]), a[1] + bfhi(r0[0])), pk2(a[2] + bflo(r1[0]), a[3] + bfhi(r1[0])),
                          pk2(b[0] + bflo(r0[1]), b[1] + bfhi(r0[1])), pk2(b[2] + bflo(r1[1]), b[3] + bfhi(r1[1]))};
        st16(y.r, hp, lane * 16, near(y, WS_H + (tm3 * 16 + tn3) * 32768, tm3) + wm * 16384 + wn * 4096 + (m * 2 + Q) * 1024);
#pragma unroll
        for (int j = 0; j < 4; ++j) { hv[Q * 8 + 2 * j] = bflo(hp[j]); hv[Q * 8 + 2 * j + 1] = bfhi(hp[j]); }
      }
      float s = 0.f;
#pragma unroll
      for (int j = 0; j < 16; ++j) s += hv[j];
      s = xsum32(xsum16(s)) * (1.f / 64.f);
      float q = 0.f;
#pragma unroll
      for (int j = 0; j < 16; ++j) { const float d = hv[j] - s; q += d * d; }
      q = xsum32(xsum16(q));
      if (g == 0) st8(y.r, (u32x2){bu(s), bu(q)}, m16 * 8, near(y, WS_ST2 + tm3 * 32768, tm3) + (tn3 >> 3) * 16384 + (tn3 & 7) * 1024 + (wid * 32 + m * 16) * 8);
    }
    if (wid == 0) TS(29);
    publish(y, FL_H, tm3 * 16 + tn3, lane);
    if (wid == 0) TS(9);
  }
  {
    f32x4 acc[28];
    Adr ad;
    ad.v[0] = lds0 + R4 + wm * 8192 + wn * 4096 + lane * 16; ad.v[1] = lds0 + R4 + wm * 8192 + (wn ^ 1) * 4096 + lane * 16;
    ad.v[2] = lds0 + R4 + 16384 + wlane(lane, wn); ad.v[3] = lds0 + R4 + 16384 + wlane(lane, 2 + wn);
    ad.v[4] = lds0 + R4 + 16384 + 6144 + hlane(lane, 0); ad.v[5] = lds0 + R4 + 16384 + 6144 + hlane(lane, 1);
    consume<C4, 4, RPG4, 0, 0, 0>(acc, ad, DBG4, [&](int s) { if (s < 0 && wid == 0) TS(10); });
    if (wid == 0) TS(11);
    f32x4 cs[8];
#pragma unroll
    for (int n = 0; n < 6; ++n) cs[n] = *(const LAS f32x4*)(smem + S4_CSF + ((par + (n >> 1)) * 64 + (2 * (n & 1) + wn) * 16 + 4 * g) * 4);
#pragma unroll
    for (int n = 0; n < 2; ++n) cs[6 + n] = *(const LAS f32x4*)(smem + S4_CSF + ((par ? 32 : 192) + n * 16 + 4 * g) * 4);
    const f32x4 zero = {0.f, 0.f, 0.f, 0.f};
    f32x2 st[4];
#pragma unroll
    for (int m = 0; m < 4; ++m) st[m] = *(const LAS f32x2*)(smem + S4_STF + ((wm * 4 + ((m + 2 * wn) & 3)) * 16 + m16) * 8);
#pragma unroll
    for (int i = 0; i < 4; ++i)
#pragma unroll
      for (int m = 0; m < (i < 3 ? 4 : 2); ++m) {
        const unsigned fd = near(y, WS_F + tm1 * 2097152, tm1) + wm * 16384 + ((m + 2 * wn) & 3) * 2048 + (i < 3 ? (ch0 + i) * 32768 + wn * 1024 : (base7 + 3) * 32768 + par * 1024);
        const int a = i < 3 ? m * 6 + 2 * i : 24 + 2 * m;
        st16(y.r, pack8(__builtin_elementwise_max((acc[a] - cs[2 * i] * st[m][0]) * st[m][1], zero),
                        __builtin_elementwise_max((acc[a + 1] - cs[2 * i + 1] * st[m][0]) * st[m][1], zero)), lane * 16, fd);
        SB();
      }
    if (wid == 0) TS(28);
    if (lane == 0) __hip_atomic_fetch_add(y.w + 24 + FL_F, 1u, __ATOMIC_RELAXED, __HIP_MEMORY_SCOPE_WORKGROUP);
    wait_vm<10>();
    if (lane == 0 && __hip_atomic_fetch_add(y.w + FL_F, 1u, __ATOMIC_RELAXED, __HIP_MEMORY_SCOPE_WORKGROUP) == 3u) st4(y.r, y.tag, 0, WS_FLG + rec(y, FL_F, tm1 * 16 + tn1));
    if (wid == 0) TS(12);
  }
  {
    f32x4 acc[16];
    Adr ad;
    ad.v[0] = lds0 + R5 + wm * 8192 + kh * 4096 + lane * 16; ad.v[1] = lds0 + R5 + wm * 8192 + (kh ^ 1) * 4096 + lane * 16;
#pragma unroll
    for (int n = 0; n < 4; ++n) ad.v[2 + n] = lds0 + R5 + 16384 + wn * 2048 + wlane(lane, n);
    u32x4 hr[2][2];
    consume_s<C5, 5, 1>(acc, ad, [&](int s) {
      if (s < 0) {
        if (wid == 0) TS(13);
        wait_vm<0>();
        if (lane == 0 && __hip_atomic_fetch_add(y.w + 20, 1u, __ATOMIC_RELAXED, __HIP_MEMORY_SCOPE_WORKGROUP) == 3u) st4(y.r, y.tag, 0, WS_FLG + rec(y, FL_F, tm1 * 16 + tn1) + 4);
#pragma unroll
        for (int m = 0; m < 2; ++m)
#pragma unroll
          for (int Q = 0; Q < 2; ++Q) hr[m][Q] = ld16(y.r, lane * 16, near(y, WS_H + (tm5 * 16 + 2 * tn5 + wn) * 32768, tm5) + wm * 16384 + ((2 * kh + m) * 2 + Q) * 1024);
      }
    });
    if (wid == 0) TS(14);
    const int tile = tm5 * 8 + tn5;
#pragma unroll
    for (int i = 0; i < 4; ++i) st16(y.r, pack8(acc[8 + 2 * i], acc[9 + 2 * i]), lane * 16, near(y, WS_O + (tile * 2 + kh) * 32768, tm5) + wm * 16384 + wn * 4096 + i * 1024);
    wait_vm<0>();
    if (lane == 0) st4(y.r, y.tag, 0, WS_FLG + rec(y, FL_X + wm, tile * 2 + kh) + wn * 64);
    poll(y, rec(y, FL_X + wm, tile * 2 + (kh ^ 1)) + wn * 64, 1, lane);
    if (wid == 0) TS(15);
    const rsrc_t ro = __builtin_amdgcn_make_buffer_rsrc(P.out, 0, 2048 * 2048, 0x00020000);
    u32x4 xv[4];
#pragma unroll
    for (int i = 0; i < 4; ++i) xv[i] = ld16(y.r, lane * 16, near(y, WS_O + (tile * 2 + (kh ^ 1)) * 32768, tm5) + wm * 16384 + wn * 4096 + i * 1024);
#pragma unroll
    for (int m = 0; m < 2; ++m)
#pragma unroll
      for (int Q = 0; Q < 2; ++Q) {
        const u32x4 xx = xv[m * 2 + Q];
        const f32x4 a = acc[m * 4 + 2 * Q] + (f32x4){bflo(xx[0]), bfhi(xx[0]), bflo(xx[1]), bfhi(xx[1])}, b = acc[m * 4 + 2 * Q + 1] + (f32x4){bflo(xx[2]), bfhi(xx[2]), bflo(xx[3]), bfhi(xx[3])};
        const u32x4 hh = hr[m][Q];
        const unsigned a0 = pk2(a[0] + bflo(hh[0]), a[1] + bfhi(hh[0])), a1 = pk2(a[2] + bflo(hh[1]), a[3] + bfhi(hh[1]));
        const unsigned b0 = pk2(b[0] + bflo(hh[2]), b[1] + bfhi(hh[2])), b1 = pk2(b[2] + bflo(hh[3]), b[3] + bfhi(hh[3]));
        auto r0 = __builtin_amdgcn_permlane16_swap(a0, b0, false, false);
        auto r1 = __builtin_amdgcn_permlane16_swap(a1, b1, false, false);
        __builtin_amdgcn_raw_buffer_store_b128((u32x4){r0[0], r1[0], r0[1], r1[1]}, ro, (tm5 * 128 + wm * 64 + (2 * kh + m) * 16 + m16) * 2048 + tn5 * 256 + wn * 128 + Q * 64 + (g & 1) * 32 + (g >> 1) * 16, 0, 16);
      }
    if (bid == 0 && threadIdx.x == 0) st4(y.r, y.tag, 0, WS_CTL);
    if (wid == 0) { TS(24); TS_FLUSH(); }
  }
}

extern "C" void kernel_launch(void* const* d_in, const int* in_sizes, int n_in, void* d_out, int out_size, void* d_ws, size_t ws_size,
                              hipStream_t stream) {
  Params p;
  p.x = (const char*)d_in[0]; p.wqkv = (const char*)d_in[1]; p.wphi = (const char*)d_in[2]; p.wo = (const char*)d_in[3];
  p.w1 = (const char*)d_in[5]; p.w2 = (const char*)d_in[6]; p.ws = (char*)(((size_t)d_ws + 0x1fffff) & ~(size_t)0x1fffff); p.out = (char*)d_out;
  (void)hipFuncSetAttribute((const void*)fused, hipFuncAttributeMaxDynamicSharedMemorySize, LDS_BYTES);
  fused<<<256, 512, LDS_BYTES, stream>>>(p);
}
```

```cpp
#include <hip/hip_runtime.h>
#include <hip/hip_bf16.h>

#define LAS __attribute__((address_space(3)))
#define DI __device__ __forceinline__
typedef __bf16 bf16x8 __attribute__((ext_vector_type(8)));
typedef __bf16 bf16x2 __attribute__((ext_vector_type(2)));
typedef short s16x4 __attribute__((ext_vector_type(4)));
typedef float f32x2 __attribute__((ext_vector_type(2)));
typedef float f32x4 __attribute__((ext_vector_type(4)));
typedef unsigned u32x2 __attribute__((ext_vector_type(2)));
typedef unsigned u32x4 __attribute__((ext_vector_type(4)));
typedef LAS char* lds_ptr;
typedef __amdgpu_buffer_rsrc_t rsrc_t;

constexpr int D = 1024, FF = 3584;
constexpr unsigned WS_CTL = 0;
constexpr unsigned WS_FLG = 4096;
constexpr unsigned WS_ST1 = 320u << 10;
constexpr unsigned WS_CSQ = 384u << 10;
constexpr unsigned WS_CS1 = 576u << 10;
constexpr unsigned WS_ZP = 704u << 10;
constexpr unsigned WS_ST2 = 1024u << 10;
constexpr unsigned WS_FL2 = 1536u << 10;
constexpr unsigned WS_PRB = 1920u << 10;
constexpr unsigned WS_KVP = 2u << 20;
constexpr unsigned WS_O = 4u << 20;
constexpr unsigned WS_H = 12u << 20;
constexpr unsigned WS_F = 20u << 20;
constexpr unsigned WS_END = 52u << 20;
constexpr int NPAGE = 26;
enum { FL_P0 = 0, FL_KV = 1, FL_CS = 2, FL_O = 3, FL_H = 4, FL_F = 5, FL_X = 6   };

struct Params { const char* x; const char* wqkv; const char* wphi; const char* wo; const char* w1; const char* w2; char* ws; char* out; };

DI unsigned pk2(float a, float b) { f32x2 v = {a, b}; return __builtin_bit_cast(unsigned, __builtin_convertvector(v, bf16x2)); }
DI float bflo(unsigned u) { return __uint_as_float(u << 16); }
DI float bfhi(unsigned u) { return __uint_as_float(u & 0xffff0000u); }
DI u32x4 pack8(const f32x4& a, const f32x4& b) { return (u32x4){pk2(a[0], a[1]), pk2(a[2], a[3]), pk2(b[0], b[1]), pk2(b[2], b[3])}; }
DI bf16x8 asbf(const u32x4& v) { return __builtin_bit_cast(bf16x8, v); }
#define SB() __builtin_amdgcn_sched_barrier(0)
DI void bar() { asm volatile("" ::: "memory"); __builtin_amdgcn_s_barrier(); asm volatile("" ::: "memory"); }
DI void wait_lgkm0() { __builtin_amdgcn_s_waitcnt(0xC07F); asm volatile("" ::: "memory"); }
template <int N> DI void wait_vm() { asm volatile("s_waitcnt vmcnt(%0)" ::"i"(N) : "memory"); }
template <bool SC1> DI void dma(unsigned voff, const char* sbase, unsigned lds) {
  if (SC1) asm volatile("s_mov_b32 m0, %2\n\ts_nop 3\n\tglobal_load_lds_dwordx4 %0, %1 sc1" ::"v"(voff), "s"(sbase), "s"(lds) : "memory");
  else asm volatile("s_mov_b32 m0, %2\n\ts_nop 3\n\tglobal_load_lds_dwordx4 %0, %1" ::"v"(voff), "s"(sbase), "s"(lds) : "memory");
}
template <bool SC1> DI void dma4(unsigned voff, const char* sbase, unsigned lds) {
  if (SC1) asm volatile("s_mov_b32 m0, %2\n\ts_nop 3\n\tglobal_load_lds_dword %0, %1 sc1" ::"v"(voff), "s"(sbase), "s"(lds) : "memory");
  else asm volatile("s_mov_b32 m0, %2\n\ts_nop 3\n\tglobal_load_lds_dword %0, %1" ::"v"(voff), "s"(sbase), "s"(lds) : "memory");
}
DI u32x4 lds128(lds_ptr p) { return *(const LAS u32x4*)p; }
DI u32x4 lds_tr2(lds_ptr p0, lds_ptr p1) {
  s16x4 a = __builtin_amdgcn_ds_read_tr16_b64_v4i16((LAS s16x4*)p0);
  s16x4 b = __builtin_amdgcn_ds_read_tr16_b64_v4i16((LAS s16x4*)p1);
  return __builtin_bit_cast(u32x4, __builtin_shufflevector(a, b, 0, 1, 2, 3, 4, 5, 6, 7));
}
DI u32x2 lds_tr(lds_ptr p) { return __builtin_bit_cast(u32x2, __builtin_amdgcn_ds_read_tr16_b64_v4i16((LAS s16x4*)p)); }
DI f32x4 mfma(const u32x4& a, const u32x4& b, const f32x4& c) { return __builtin_amdgcn_mfma_f32_16x16x32_bf16(asbf(a), asbf(b), c, 0, 0, 0); }
DI void mfma_ip(f32x4& acc, const u32x4& a, const u32x4& b) { asm volatile("v_mfma_f32_16x16x32_bf16 %0, %1, %2, %0" : "+v"(acc) : "v"(a), "v"(b)); }
DI u32x4 ld16(rsrc_t r, unsigned voff, unsigned soff) { return __builtin_amdgcn_raw_buffer_load_b128(r, voff, soff, 16); }
DI void st16(rsrc_t r, const u32x4& v, unsigned voff, unsigned soff) { __builtin_amdgcn_raw_buffer_store_b128(v, r, voff + soff, 0, 16);
}
DI void st8(rsrc_t r, const u32x2& v, unsigned voff, unsigned soff) { __builtin_amdgcn_raw_buffer_store_b64(v, r, voff, soff, 16); }
DI void st4(rsrc_t r, unsigned v, unsigned voff, unsigned soff) { __builtin_amdgcn_raw_buffer_store_b32(v, r, voff, soff, 16); }
DI float fb(unsigned u) { return __uint_as_float(u); }
DI unsigned bu(float f) { return __float_as_uint(f); }
DI float xsum16(float x) { auto r = __builtin_amdgcn_permlane16_swap(bu(x), bu(x), false, false); return fb(r[0]) + fb(r[1]); }
DI float xsum32(float x) { auto r = __builtin_amdgcn_permlane32_swap(bu(x), bu(x), false, false); return fb(r[0]) + fb(r[1]); }
template <int CTRL> DI float dpp(float x) { return __builtin_bit_cast(float, __builtin_amdgcn_update_dpp(0, __builtin_bit_cast(int, x), CTRL, 0xf, 0xf, true)); }
DI float row16_sum(float x) { x += dpp<0xB1>(x); x += dpp<0x4E>(x); x += dpp<0x141>(x); x += dpp<0x140>(x); return x; }

struct Sync { rsrc_t r; unsigned tag, pb; LAS unsigned* w; };
DI unsigned near(const Sync& y, unsigned off, int tm) { return off + ((((y.pb >> (off >> 21)) ^ (tm >> 3)) & 1u) << 13); }
DI unsigned rec(const Sync& y, int phase, int idx) {
  return phase < FL_O ? (phase * 256 + idx) * 128 : near(y, WS_FL2 + (phase - FL_O) * 65536 + (idx >> 6) * 16384 + (idx & 63) * 128, idx >> 4) - WS_FLG;
}
DI void publish(const Sync& y, int phase, int idx, int lane) {
  if (lane == 0) __hip_atomic_fetch_add(y.w + 24 + phase, 1u, __ATOMIC_RELAXED, __HIP_MEMORY_SCOPE_WORKGROUP);
  wait_vm<0>();
  if (lane == 0 && __hip_atomic_fetch_add(y.w + phase, 1u, __ATOMIC_RELAXED, __HIP_MEMORY_SCOPE_WORKGROUP) == 3u) st4(y.r, y.tag, 0, WS_FLG + rec(y, phase, idx));
}
DI void poll(const Sync& y, unsigned off, int n, int lane) {
  const unsigned long long t0 = __builtin_amdgcn_s_memrealtime();
  for (unsigned it = 0;; ++it) {
    const unsigned v = __builtin_amdgcn_raw_buffer_load_b32(y.r, WS_FLG + off, 0, 16);
    if (__all(lane >= n || v == y.tag)) break;
    if ((it & 63) == 63) {
      if (__builtin_amdgcn_readfirstlane(__builtin_amdgcn_raw_buffer_load_b32(y.r, 0, WS_CTL + 128, 16)) == y.tag) break;
      if (__builtin_amdgcn_s_memrealtime() - t0 > 4000000ull) { if (lane == 0) st4(y.r, y.tag, 0, WS_CTL + 128); break; }
    }
    __builtin_amdgcn_s_sleep(2);
  }
  asm volatile("" ::: "memory");
}
DI void lds_wait(const Sync& y, int word, unsigned v);
DI void bar_loaders(const Sync& y, int lw) { if (lw == 1) *(volatile LAS unsigned*)(y.w + 14) = 1u; else lds_wait(y, 14, 1u); }
DI void lds_wait(const Sync& y, int word, unsigned v) {
  volatile LAS unsigned* w = y.w + word;
  while (*w < v) __builtin_amdgcn_s_sleep(1);
  asm volatile("" ::: "memory");
}
struct Gate { unsigned off, mask; int word; };
DI void gate_slow(const Sync& y, Gate& G, unsigned need, bool poller, int lane, int cnt = 0) {
  volatile LAS unsigned* w = y.w + G.word;
  if (poller) {
    const unsigned long long t0 = __builtin_amdgcn_s_memrealtime();
    for (unsigned it = 0;; ++it) {
      G.mask = (unsigned)__ballot(__builtin_amdgcn_raw_buffer_load_b32(y.r, WS_FLG + G.off, 0, 16) == y.tag);
      if ((it & 63) == 63) {
        const bool late = __builtin_amdgcn_s_memrealtime() - t0 > 4000000ull;
        if (late && lane == 0) st4(y.r, y.tag, 0, WS_CTL + 128);
        if (late || __builtin_amdgcn_readfirstlane(__builtin_amdgcn_raw_buffer_load_b32(y.r, 0, WS_CTL + 128, 16)) == y.tag) G.mask = ~0u;
      }
      *w = G.mask;
      if ((G.mask & need) == need && __builtin_popcount(G.mask & 0xffffu) >= cnt) break;
      __builtin_amdgcn_s_sleep(1);
    }
  } else {
    for (;;) { G.mask = __builtin_amdgcn_readfirstlane(*w); if ((G.mask & need) == need && __builtin_popcount(G.mask & 0xffffu) >= cnt) break; __builtin_amdgcn_s_sleep(1); }
  }
  asm volatile("" ::: "memory");
}
DI void gate(const Sync& y, Gate& G, unsigned need, bool poller, int lane) { if (__builtin_expect((G.mask & need) != need, 0)) gate_slow(y, G, need, poller, lane); }

template <int NK, bool SPREAD> DI int kmap(int t, int c) {
  const int u = t - 1, q = u & 3, b4 = u & ~3, body = (int)(u < NK - 4);
  const int lead = ((int)(q == c) & body & (int)(u >= 0)) | (int)(SPREAD && t == 0), head = SPREAD ? 0 : -(int)(u < 4);
  const int non = ((b4 + ((c + 1 + q - ((int)(q > c) & body)) & 3)) & ~head) | ((t - (int)(u > c)) & head);
  return non ^ (((b4 + 4 + c) ^ non) & -lead);
}
#define LAG3 1
#define LAG5 1
enum { ORD_NAT = 0, ORD_PAIR = 1, ORD_F = 2 };
template <int NSUB_, bool HALF_, int NSLOT_, int NK_, int ORD_, int WSTRIDE_, bool XROW_, int LAG_> struct Cfg {
  static constexpr int NSUB = NSUB_, NSLOT = NSLOT_, NK = NK_, ORD = ORD_, WSTRIDE = WSTRIDE_;
  static constexpr bool HALF = HALF_, XROW = XROW_;
  static constexpr int LAG = LAG_;
  static constexpr int STAGE = 16384 + NSUB * 8192 + (HALF ? 4096 : 0), PW = 4 + 2 * NSUB + (HALF ? 1 : 0), RING = STAGE * NSLOT;
  static constexpr int WB = NSUB * 2048 + (HALF ? 1024 : 0);
};
typedef Cfg<3, false, 3, 16, ORD_NAT, 3 * D * 2, true, 0> C1;
typedef Cfg<1, false, 5, 16, ORD_PAIR, D * 2, false, LAG3> C3;
typedef Cfg<3, true, 3, 16, ORD_PAIR, FF * 2, false, 0> C4;
typedef Cfg<2, false, 4, 28, ORD_F, D * 2, false, LAG5> C5;

#define GLDS "global_load_lds_dwordx4 "
template <class C> struct Ld {
  const char* A; const char* W; int lw, c, kt0; unsigned vA[4], vW[7];
  const char* sA; const char* sW; unsigned need;
  Gate G; const Sync* y; int lane_;
  static constexpr int C0 = C::WB > 4096 ? 4096 : 0;
  DI void init(const Sync& y_, int lane, int substep, int halfrel, unsigned goff, int gword) {
    y = &y_; lane_ = lane; G.off = goff; G.mask = 0; G.word = gword;
#pragma unroll
    for (int i = 0; i < 4; ++i) vA[i] = C::XROW ? (lane >> 3) * 2048 + ((((lane & 7) ^ (lane >> 4)) << 4) ^ ((i & 1) * 64)) + i * (16384 - 1024) : lane * 16;
    const int rl = (C::ORD == ORD_NAT) ? 8 * (lane >> 5) + ((lane >> 3) & 3) : (lane >> 3);
    const unsigned w0 = rl * C::WSTRIDE + (((lane & 7) ^ (((lane >> 4) & 3) << 1)) << 4) + 4096;
#pragma unroll
    for (int q = 0; q < 2 * C::NSUB; ++q) vW[q] = w0 + (q & 1) * ((C::ORD == ORD_NAT ? 16 : 8) * C::WSTRIDE) + (q >> 1) * substep - (q * 1024 - C0);
    if (C::HALF) vW[6] = (lane >> 2) * C::WSTRIDE + (((lane & 3) ^ (((lane >> 4) & 1) << 1)) << 4) + 4096 + halfrel - (6144 - C0);
    A += C::XROW ? lw * 32 * 2048 : (lw >> 1) * 16384 + (lw & 1) * 4096;
    W -= 4096;
  }
  DI void prep(int s) {
    int kt = kmap<C::NK, C::XROW>(s, c);
    const int kb = lw >> 1, t = lw & 1;
    int ur;
    need = 1u << kt;
    if (C::ORD == ORD_NAT) ur = 32 * kb + 4 * t;
    else if (C::ORD == ORD_F) {
      const int p = kt & 7, q = kt >> 3, sp = -(int)(q == 3);
      need = ((1u << (2 * p + (int)(q > 0))) & ~sp) | ((0xau << (4 * (p & 3))) & sp);
      kt = kt0 + (((7 * (p >> 1) + 4 * (p & 1) + q) & ~sp) | ((7 * p + 3) & sp));
      ur = (16 * lw & sp) | ((16 * kb + 32 * t) & ~sp);
    }
    else ur = 16 * lw;
    sA = A + (unsigned)(kt * (C::XROW ? 128 : 32768));
    sW = W + (unsigned)((kt * 64 + ur) * C::WSTRIDE);
  }
  DI void issueA(unsigned base) {
    if (!C::XROW) gate(*y, G, need, lw == 0, lane_);
    const unsigned m = base + lw * 4096;
    if (C::XROW) asm volatile("s_mov_b32 m0, %5\n\ts_nop 3\n\t" GLDS "%0, %4\n\t" GLDS "%1, %4 offset:1024\n\t" GLDS "%2, %4 offset:2048\n\t" GLDS "%3, %4 offset:3072"
                              ::"v"(vA[0]), "v"(vA[1]), "v"(vA[2]), "v"(vA[3]), "s"(sA), "s"(m) : "memory");
    else asm volatile("s_mov_b32 m0, %2\n\ts_nop 3\n\t" GLDS "%0, %1 sc1\n\t" GLDS "%0, %1 offset:1024 sc1\n\t" GLDS "%0, %1 offset:2048 sc1\n\t" GLDS "%0, %1 offset:3072 sc1"
                      ::"v"(vA[0]), "s"(sA), "s"(m) : "memory");
  }
  DI void issueW(unsigned base) const {
    const unsigned m = base + 16384 + lw * C::WB + C0;
    if (C::NSUB == 1) asm volatile("s_mov_b32 m0, %3\n\ts_nop 3\n\t" GLDS "%0, %2\n\t" GLDS "%1, %2 offset:1024" ::"v"(vW[0]), "v"(vW[1]), "s"(sW), "s"(m) : "memory");
    if (C::NSUB == 2) asm volatile("s_mov_b32 m0, %5\n\ts_nop 3\n\t" GLDS "%0, %4\n\t" GLDS "%1, %4 offset:1024\n\t" GLDS "%2, %4 offset:2048\n\t" GLDS "%3, %4 offset:3072"
                                   ::"v"(vW[0]), "v"(vW[1]), "v"(vW[2]), "v"(vW[3]), "s"(sW), "s"(m) : "memory");
    if (C::NSUB == 3) asm volatile("s_mov_b32 m0, %7\n\ts_nop 3\n\t" GLDS "%0, %6 offset:-4096\n\t" GLDS "%1, %6 offset:-3072\n\t" GLDS "%2, %6 offset:-2048\n\t" GLDS "%3, %6 offset:-1024\n\t"
                                   GLDS "%4, %6\n\t" GLDS "%5, %6 offset:1024"
                                   ::"v"(vW[0]), "v"(vW[1]), "v"(vW[2]), "v"(vW[3]), "v"(vW[4]), "v"(vW[5]), "s"(sW), "s"(m) : "memory");
    if (C::HALF) asm volatile(GLDS "%0, %1 offset:2048" ::"v"(vW[6]), "s"(sW) : "memory");
  }
};
template <class C> DI void load_preW(Ld<C>& L, unsigned ring, int s0 = 0, int s1 = C::NSLOT) {
#pragma unroll 1
  for (int s = s0; s < s1; ++s) { L.prep(s); L.issueW(ring + s * C::STAGE); }
}
template <class C, bool SPLIT> constexpr int vm_allow(int S) {
  const int N = C::NSLOT, behind = S - 1 - C::LAG > 0 ? S - 1 - C::LAG : 0;
  const int ramp = S + 1 < N - 1 ? S + 1 : N - 1;
  const int last = ramp + behind < C::NK - 1 ? ramp + behind : C::NK - 1;
  return (SPLIT && S < N) ? 4 * (ramp - S) + C::PW * (last - ramp) : C::PW * (last - S);
}
template <class C, bool SPLIT, unsigned POSTM, int S, int SE, class Post> DI void load_fixed(Ld<C>& L, unsigned ring, const Post& post) {
  if constexpr (S < SE) {
    static_assert(C::NSLOT <= C::LAG + 4, "the ring must be full before the first refill");
    wait_vm<vm_allow<C, SPLIT>(S)>();
    bar();
    if constexpr (S + 2 < C::NSLOT) {
      if (!SPLIT) L.issueW(ring + (S + 2) * C::STAGE);
      L.issueA(ring + (S + 2) * C::STAGE);
      L.prep(S + 3);
    }
    constexpr int T = S - 1 - C::LAG + C::NSLOT;
    if constexpr (S - 1 - C::LAG >= 0 && T < C::NK) {
      L.issueW(ring + (T % C::NSLOT) * C::STAGE); L.issueA(ring + (T % C::NSLOT) * C::STAGE);
      if constexpr (T + 1 < C::NK) L.prep(T + 1);
    }
    if constexpr ((POSTM >> S) & 1u) post(S);
    load_fixed<C, SPLIT, POSTM, S + 1, SE>(L, ring, post);
  }
}
template <class C, bool SPLIT, unsigned PREM, unsigned POSTM, class Pre, class Post>
DI void load_all(Ld<C>& L, unsigned ring, const Pre& pre, const Post& post) {
  constexpr int N = C::NSLOT, S0 = N - 1 > 2 + C::LAG ? N - 1 : 2 + C::LAG, S1 = C::NK - N + C::LAG;
#pragma unroll 1
  for (int s = 0; s < 2; ++s) { L.prep(s); if (!SPLIT) L.issueW(ring + s * C::STAGE); L.issueA(ring + s * C::STAGE); }
  L.prep(2);
  __builtin_amdgcn_s_setprio(3);
  load_fixed<C, SPLIT, POSTM, 0, S0>(L, ring, post);
  static_assert(vm_allow<C, SPLIT>(S0) == (N - 2 - C::LAG) * C::PW && vm_allow<C, SPLIT>(S1) == (N - 2 - C::LAG) * C::PW, "steady count");
  unsigned b = ring + ((S0 - 1 - C::LAG) % N) * C::STAGE;
#pragma unroll 1
  for (int s = S0; s <= S1; ++s) {
    wait_vm<(N - 2 - C::LAG) * C::PW>();
    if (PREM && ((PREM >> s) & 1u)) pre(s);
    bar();
    L.issueW(b);
    L.issueA(b);
    L.prep(min(s - C::LAG + N, C::NK - 1));
    b = (b == ring + (N - 1) * C::STAGE) ? ring : b + C::STAGE;
    if (POSTM && ((POSTM >> s) & 1u)) post(s);
  }
  __builtin_amdgcn_s_setprio(0);
  load_fixed<C, SPLIT, POSTM, S1 + 1, C::NK>(L, ring, post);
  bar();
}

template <int MT, int NWF> struct Fr { u32x4 a[MT]; u32x2 w[NWF][2]; };
DI u32x4 cat(const u32x2 (&w)[2]) { return (u32x4){w[0][0], w[0][1], w[1][0], w[1][1]}; }
struct Adr { unsigned v[6]; };
template <int KIND> struct Shape;
template <> struct Shape<1> { static constexpr int MT = 4, NWF = 6, NMF = 24, NACC = 24, WB = 6144; static constexpr unsigned USE = 0x0f; };
template <> struct Shape<3> { static constexpr int MT = 2, NWF = 4, NMF = 8, NACC = 8, WB = 2048; static constexpr unsigned USE = 0x3d; };
template <> struct Shape<4> { static constexpr int MT = 4, NWF = 8, NMF = 28, NACC = 28, WB = 7168; static constexpr unsigned USE = 0x3f; };
template <> struct Shape<5> { static constexpr int MT = 4, NWF = 4, NMF = 16, NACC = 16, WB = 4096; static constexpr unsigned USE = 0x3f; };
DI lds_ptr lp(unsigned a) { return (lds_ptr)(size_t)a; }
template <int KIND, int J> DI void rd1(Fr<Shape<KIND>::MT, Shape<KIND>::NWF>& f, const Adr& ad, int kb) {
  constexpr int NWF = Shape<KIND>::NWF, MT = Shape<KIND>::MT, WB = Shape<KIND>::WB;
  if constexpr (J < 2 * NWF) {
    constexpr int n = J >> 1, t = J & 1;
    if constexpr (KIND == 4 && n >= 6) f.w[n][t] = lds_tr(lp(ad.v[4 + (n - 6)]) + (2 * kb + t) * WB);
    else if constexpr (KIND == 1 || KIND == 4) f.w[n][t] = lds_tr(lp(ad.v[2 + (n & 1)]) + (n >> 1) * 2048 + (2 * kb + t) * WB);
    else f.w[n][t] = lds_tr(lp(ad.v[2 + n]) + (2 * kb + t) * WB);
  } else if constexpr (J < 2 * NWF + MT) {
    constexpr int m = J - 2 * NWF;
    if constexpr (KIND == 1) f.a[m] = lds128(lp(ad.v[kb]) + m * 2048);
    else f.a[m] = lds128(lp(ad.v[m >> 1]) + (m & 1) * 2048 + kb * 1024);
  }
}
template <int KIND, int I> DI void mm1(f32x4 (&acc)[Shape<KIND>::NACC], const Fr<Shape<KIND>::MT, Shape<KIND>::NWF>& f) {
  if constexpr (KIND == 1) { constexpr int m = I / 6, n = I % 6; if constexpr (n < 4) mfma_ip(acc[I], cat(f.w[n]), f.a[m]); else mfma_ip(acc[I], f.a[m], cat(f.w[n])); }
  else if constexpr (KIND == 4) { if constexpr (I < 24) mfma_ip(acc[I], cat(f.w[I % 6]), f.a[I / 6]); else mfma_ip(acc[I], cat(f.w[6 + ((I - 24) & 1)]), f.a[(I - 24) >> 1]); }
  else mfma_ip(acc[I], cat(f.w[I % 4]), f.a[I / 4]);
}
template <int KIND, int RPG, bool READ, bool MOVE, int I = 0>
DI void step(f32x4 (&acc)[Shape<KIND>::NACC], const Fr<Shape<KIND>::MT, Shape<KIND>::NWF>& cur, Fr<Shape<KIND>::MT, Shape<KIND>::NWF>& nxt, Adr& ad, int kb, int delta) {
  constexpr int NMF = Shape<KIND>::NMF, G0 = (2 * Shape<KIND>::NWF + Shape<KIND>::MT + RPG - 1) / RPG;
  if constexpr (I < NMF) {
    if constexpr (READ) {
      rd1<KIND, I * RPG>(nxt, ad, kb);
      if constexpr (RPG > 1) rd1<KIND, I * RPG + 1>(nxt, ad, kb);
    }
    if constexpr (MOVE && I >= G0) {
      constexpr int per = (6 + (NMF - G0) - 1) / (NMF - G0);
#pragma unroll
      for (int k = (I - G0) * per; k < (I - G0 + 1) * per && k < 6; ++k)
        if ((Shape<KIND>::USE >> k) & 1u) { ad.v[k] += delta; asm volatile("" : "+v"(ad.v[k])); }
    }
    mm1<KIND, I>(acc, cur);
    SB();
    step<KIND, RPG, READ, MOVE, I + 1>(acc, cur, nxt, ad, kb, delta);
  }
}
template <int KIND, int J = 0> DI void rdall(Fr<Shape<KIND>::MT, Shape<KIND>::NWF>& f, const Adr& ad, int kb) {
  if constexpr (J < 2 * Shape<KIND>::NWF + Shape<KIND>::MT) { rd1<KIND, J>(f, ad, kb); rdall<KIND, J + 1>(f, ad, kb); }
}
template <class C, int KIND, int RPG, int CNT>
DI void stages(f32x4 (&acc)[Shape<KIND>::NACC], Fr<Shape<KIND>::MT, Shape<KIND>::NWF>& f0, Fr<Shape<KIND>::MT, Shape<KIND>::NWF>& f1, Adr& ad, int& slot) {
#pragma unroll 1
  for (int s = 0; s < CNT; ++s) {
    const int delta = (slot + 1 == C::NSLOT) ? -(C::NSLOT - 1) * C::STAGE : C::STAGE;
    slot = (slot + 1 == C::NSLOT) ? 0 : slot + 1;
    SB();
    step<KIND, RPG, true, true>(acc, f0, f1, ad, 1, delta);
    if (!C::LAG) wait_lgkm0();
    bar();
    SB();
    step<KIND, RPG, true, false>(acc, f1, f0, ad, 0, 0);
  }
}
template <class C, int KIND, int RPG, int H0, int H1, int H2, class Hook> DI void consume(f32x4 (&acc)[Shape<KIND>::NACC], Adr& ad, const Hook& hook) {
  Fr<Shape<KIND>::MT, Shape<KIND>::NWF> f0, f1;
#pragma unroll
  for (int i = 0; i < Shape<KIND>::NACC; ++i) acc[i] = (f32x4){0.f, 0.f, 0.f, 0.f};
  bar();
  rdall<KIND>(f0, ad, 0);
  hook(-1);
  wait_lgkm0();
  int slot = 0;
  if constexpr (H1 > 0) {
    if constexpr (H0 > 0) stages<C, KIND, RPG, H0>(acc, f0, f1, ad, slot);
    hook(0);
    stages<C, KIND, RPG, H1 - H0>(acc, f0, f1, ad, slot);
    hook(1);
    stages<C, KIND, RPG, H2 - H1>(acc, f0, f1, ad, slot);
    hook(2);
  }
  stages<C, KIND, RPG, C::NK - 1 - H2>(acc, f0, f1, ad, slot);
  SB();
  step<KIND, RPG, true, false>(acc, f0, f1, ad, 1, 0);
  wait_lgkm0();
  bar();
  SB();
  step<KIND, RPG, false, false>(acc, f1, f0, ad, 0, 0);
  asm volatile("s_nop 7\n\ts_nop 3");
  SB();
}
template <class C, int KIND, int RPG, class Hook> DI void consume_s(f32x4 (&acc)[Shape<KIND>::NACC], Adr& ad, const Hook& hook) {
  Fr<Shape<KIND>::MT, Shape<KIND>::NWF> f0, f1, g0, g1;
#pragma unroll
  for (int i = 0; i < Shape<KIND>::NACC; ++i) acc[i] = (f32x4){0.f, 0.f, 0.f, 0.f};
  bar();
  rdall<KIND>(f0, ad, 0); rdall<KIND>(f1, ad, 1);
  hook(-1);
#pragma unroll
  for (int k = 0; k < 6; ++k) ad.v[k] += C::STAGE;
  int slot = 1;
#pragma unroll 1
  for (int s = 0; s < C::NK - 2; s += 2) {
    int delta = (slot + 1 == C::NSLOT) ? -(C::NSLOT - 1) * C::STAGE : C::STAGE;
    slot = (slot + 1 == C::NSLOT) ? 0 : slot + 1;
    if (!C::LAG) wait_lgkm0();
    bar(); SB();
    step<KIND, RPG, true, false>(acc, f0, g0, ad, 0, 0);
    step<KIND, RPG, true, true>(acc, f1, g1, ad, 1, delta);
    delta = (slot + 1 == C::NSLOT) ? -(C::NSLOT - 1) * C::STAGE : C::STAGE;
    slot = (slot + 1 == C::NSLOT) ? 0 : slot + 1;
    if (!C::LAG) wait_lgkm0();
    bar(); SB();
    step<KIND, RPG, true, false>(acc, g0, f0, ad, 0, 0);
    step<KIND, RPG, true, true>(acc, g1, f1, ad, 1, delta);
  }
  if (!C::LAG) wait_lgkm0();
  bar(); SB();
  step<KIND, RPG, true, false>(acc, f0, g0, ad, 0, 0);
  step<KIND, RPG, true, false>(acc, f1, g1, ad, 1, 0);
  wait_lgkm0(); bar(); SB();
  step<KIND, RPG, false, false>(acc, g0, f0, ad, 0, 0);
  step<KIND, RPG, false, false>(acc, g1, f1, ad, 1, 0);
  asm volatile("s_nop 7\n\ts_nop 3");
  SB();
}
DI unsigned wlane(int lane, int nt) {
  const int g = lane >> 4, q = (lane >> 2) & 3, p = lane & 3, sw = (2 * g + (q >> 1)) & 3;
  return (4 * g + q) * 128 + ((nt ^ sw) << 5) + p * 8;
}
DI unsigned hlane(int lane, int nt) {
  const int g = lane >> 4, q = (lane >> 2) & 3, p = lane & 3;
  return (4 * g + q) * 64 + ((nt ^ (g & 1)) << 5) + p * 8;
}

template <int OP> DI float op2(float a, float b) { return OP == 0 ? a + b : OP == 1 ? __builtin_elementwise_maximum(a, b) : __builtin_elementwise_minimum(a, b); }
template <int OP> DI float red4(float x0, float x1, float x2, float x3) {
  auto a = __builtin_amdgcn_permlane16_swap(bu(x0), bu(x1), false, false);
  auto b = __builtin_amdgcn_permlane16_swap(bu(x2), bu(x3), false, false);
  const float y0 = op2<OP>(fb(a[0]), fb(a[1])), y1 = op2<OP>(fb(b[0]), fb(b[1]));
  auto c = __builtin_amdgcn_permlane32_swap(bu(y0), bu(y1), false, false);
  return op2<OP>(fb(c[0]), fb(c[1]));
}
DI void spread(float z, float (&o)[4]) {
  auto a = __builtin_amdgcn_permlane32_swap(bu(z), bu(z), false, false);
  auto b = __builtin_amdgcn_permlane16_swap(a[0], a[0], false, false);
  auto c = __builtin_amdgcn_permlane16_swap(a[1], a[1], false, false);
  o[0] = fb(b[0]); o[1] = fb(b[1]); o[2] = fb(c[0]); o[3] = fb(c[1]);
}
DI void dual_softmax4(const f32x4 (&p)[4][2], u32x4 (&pp)[4], u32x4 (&pn)[4]) {
  constexpr float L2E = 1.4426950408889634f;
  float mx[4], mn[4];
#pragma unroll
  for (int m = 0; m < 4; ++m) {
    mx[m] = op2<1>(op2<1>(op2<1>(op2<1>(p[m][0][0], p[m][0][1]), p[m][0][2]), op2<1>(op2<1>(p[m][0][3], p[m][1][0]), p[m][1][1])), op2<1>(p[m][1][2], p[m][1][3]));
    mn[m] = op2<2>(op2<2>(op2<2>(op2<2>(p[m][0][0], p[m][0][1]), p[m][0][2]), op2<2>(op2<2>(p[m][0][3], p[m][1][0]), p[m][1][1])), op2<2>(p[m][1][2], p[m][1][3]));
  }
  spread(-L2E * red4<1>(mx[0], mx[1], mx[2], mx[3]), mx);
  spread(L2E * red4<2>(mn[0], mn[1], mn[2], mn[3]), mn);
  f32x4 ep[4][2], en[4][2];
  float sp[4], sn[4];
#pragma unroll
  for (int m = 0; m < 4; ++m) {
#pragma unroll
    for (int F = 0; F < 2; ++F) {
      const f32x4 a = p[m][F] * L2E + mx[m], b = p[m][F] * (-L2E) + mn[m];
#pragma unroll
      for (int r = 0; r < 4; ++r) { ep[m][F][r] = __builtin_amdgcn_exp2f(a[r]); en[m][F][r] = __builtin_amdgcn_exp2f(b[r]); }
    }
    const f32x4 a = ep[m][0] + ep[m][1], b = en[m][0] + en[m][1];
    sp[m] = (a[0] + a[1]) + (a[2] + a[3]); sn[m] = (b[0] + b[1]) + (b[2] + b[3]);
  }
  spread(__builtin_amdgcn_rcpf(red4<0>(sp[0], sp[1], sp[2], sp[3])), sp);
  spread(__builtin_amdgcn_rcpf(red4<0>(sn[0], sn[1], sn[2], sn[3])), sn);
#pragma unroll
  for (int m = 0; m < 4; ++m) { pp[m] = pack8(ep[m][0] * sp[m], ep[m][1] * sp[m]); pn[m] = pack8(en[m][0] * sn[m], en[m][1] * sn[m]); }
}

DI float dot2(unsigned a, unsigned b, float c) { return __builtin_amdgcn_fdot2_f32_bf16(__builtin_bit_cast(bf16x2, a), __builtin_bit_cast(bf16x2, b), c, false); }
DI unsigned opaque(unsigned c) { asm volatile("" : "+s"(c)); return c; }
template <int NL> DI void colsum_load(u32x4 (&v)[NL], const char* w, int stride, int lane) {
#pragma unroll
  for (int i = 0; i < NL; ++i) v[i] = *(const u32x4*)(w + (size_t)(8 * i + (lane >> 3)) * stride + (lane & 7) * 16);
}
template <int NL> DI void colsum_finish(const u32x4 (&v)[NL], rsrc_t r, unsigned dst, int lane) {
  f32x2 s2[4];
#pragma unroll
  for (int j = 0; j < 4; ++j) s2[j] = (f32x2){bflo(v[0][j]), bfhi(v[0][j])};
#pragma unroll
  for (int i = 1; i < NL; ++i)
#pragma unroll
    for (int j = 0; j < 4; ++j) s2[j] += (f32x2){bflo(v[i][j]), bfhi(v[i][j])};
  const float s[8] = {s2[0][0], s2[0][1], s2[1][0], s2[1][1], s2[2][0], s2[2][1], s2[3][0], s2[3][1]};
  float t[4], u[2];
#pragma unroll
  for (int j = 0; j < 4; ++j) { auto q = __builtin_amdgcn_permlane32_swap(bu(s[j]), bu(s[j + 4]), false, false); t[j] = fb(q[0]) + fb(q[1]); }
#pragma unroll
  for (int j = 0; j < 2; ++j) { auto q = __builtin_amdgcn_permlane16_swap(bu(t[j]), bu(t[j + 2]), false, false); u[j] = fb(q[0]) + fb(q[1]); }
  const bool b3 = lane & 8;
  const float tot = (b3 ? u[1] : u[0]) + dpp<0x128>(b3 ? u[0] : u[1]);
  st4(r, bu(tot), ((lane & 7) * 8 + ((lane >> 5) & 1) * 4 + ((lane >> 4) & 1) * 2 + ((lane >> 3) & 1)) * 4, dst);
}

template <int XN, int TM, int TN> DI void tile_of(int bid, int& tm, int& tn, int& c) {
  const int x = bid & 7, r = bid >> 3, li = r % TM, lj = r / TM;
  tm = (x / XN) * TM + li; tn = (x % XN) * TN + lj; c = (li + lj) & 3;
}

#define RPG1 1
#define RPG4 1
#define RPG5 1
#define W2AT 0
#define PH0 2
#define PH1 6
#define PH2 7
#define PF 10
#define W1M 0u
constexpr int PR = PF + 6 > 15 ? 15 : PF + 6;
#define NPROBE 4
#define FAT 1
#define E4 0
constexpr int R1 = 40704, R3 = 40704, R4 = 0, R5 = 0, SYNCW = 163584;
constexpr int S1_ST = 0, S1_CSR = 1024, S1_CSF = 13312, S1_FLG = 14080, S1_WP = 14336, S1_KV = 18432, S1_DUM = 26880;
constexpr int S4_STR = C4::RING, S4_CSR = S4_STR + 16384, S4_STF = S4_CSR + 8192, S4_CSF = S4_STF + 1024, S4_DUM = S4_CSF + 1024;
constexpr int LDS_BYTES = 163840, CODE_BYTES = 54 * 1024;
static_assert(S4_DUM + 256 <= SYNCW && S1_KV + 8448 <= R1 && R1 + C1::RING <= SYNCW && R3 + C3::RING <= SYNCW && C5::RING <= S4_STR, "lds");

template <class St> DI void kv_sum(const Sync& y, Gate& G, lds_ptr smem, int head, int h, int t, bool poller, bool doz, int lane, const St& stamp) {
  u32x4 v[16], zz[4];
  unsigned have = 0;
  f32x2 s2[4] = {{0.f, 0.f}, {0.f, 0.f}, {0.f, 0.f}, {0.f, 0.f}};
#pragma unroll 1
  for (int round = poller ? 2 : 0; round < 3; ++round) {
    gate_slow(y, G, 0u, poller, lane, round == 0 ? 0 : round == 1 ? 13 : 16);
    stamp(round);
    const unsigned fresh = G.mask & ~have;
    have = G.mask;
#pragma unroll
    for (int r = 0; r < 16; ++r) if ((fresh >> r) & 1u) v[r] = ld16(y.r, t * 16, WS_KVP + (head * 16 + r) * 4096);
    if (round == 2 && doz) {
#pragma unroll
      for (int i = 0; i < 4; ++i) zz[i] = ld16(y.r, lane * 16, WS_ZP + head * 4096 + i * 1024);
    }
  }
#pragma unroll
  for (int r = 0; r < 16; ++r)
#pragma unroll
    for (int j = 0; j < 4; ++j) s2[j] += (f32x2){bflo(v[r][j]), bfhi(v[r][j])};
  const float s[8] = {s2[0][0], s2[0][1], s2[1][0], s2[1][1], s2[2][0], s2[2][1], s2[3][0], s2[3][1]};
  *(LAS u32x4*)(smem + S1_KV + h * 4096 + t * 16) = (u32x4){pk2(s[0], s[1]), pk2(s[2], s[3]), pk2(s[4], s[5]), pk2(s[6], s[7])};
  if (doz) {
    f32x4 z = (__builtin_bit_cast(f32x4, zz[0]) + __builtin_bit_cast(f32x4, zz[1])) + (__builtin_bit_cast(f32x4, zz[2]) + __builtin_bit_cast(f32x4, zz[3]));
#pragma unroll
    for (int j = 0; j < 4; ++j) z[j] = xsum32(xsum16(z[j]));
    if (lane < 16) *(LAS u32x2*)(smem + S1_KV + 8192 + h * 128 + lane * 8) = (u32x2){pk2(z[0], z[1]), pk2(z[2], z[3])};
  }
}

__global__ __launch_bounds__(512, 2) void fused(Params P) {
  extern __shared__ __attribute__((aligned(16))) char smem_[];
  lds_ptr smem = (lds_ptr)smem_;
  const unsigned lds0 = (unsigned)(size_t)smem;
  const int wid = __builtin_amdgcn_readfirstlane(threadIdx.x >> 6), lane = threadIdx.x & 63, bid = blockIdx.x;
  {
    const char* pc; asm volatile("s_getpc_b64 %0" : "=s"(pc));
    const unsigned co = ((bid >> 3) & 7) * 8192 + threadIdx.x * 128;
    if (threadIdx.x < 64 && co < CODE_BYTES) dma4<false>(co, pc, lds0 + S1_DUM);
    SB();
  }
  int tm1, tn1, c1, tm3, tn3, c3;
  tile_of<4, 8, 4>(bid, tm1, tn1, c1);
  tile_of<2, 4, 8>(bid, tm3, tn3, c3);
  const int kh = bid & 1, tm5 = ((bid & 7) >> 1) * 4 + ((bid >> 3) & 3), tn5 = bid >> 5, c5 = ((bid >> 3) + tn5) & 3;
  const int par = tn1 & 1, base7 = 7 * (tn1 >> 1), ch0 = base7 + 4 * par;
  const int g = lane >> 4, m16 = lane & 15;
  Sync y;
  y.r = __builtin_amdgcn_make_buffer_rsrc(P.ws, 0, WS_END, 0x00020000);
  y.w = (LAS unsigned*)(smem + SYNCW); y.pb = 0;

  if (wid >= 4) {
    const int lw = wid - 4, lt = threadIdx.x - 256;
    const auto nop = [](int) {};
    {
      Ld<C1> L; L.A = P.x + (size_t)tm1 * 128 * 2048; L.W = P.wqkv + tn1 * 128; L.lw = lw; L.c = c1; L.kt0 = 0; L.init(y, lane, 2048, 0, 0, 0);
      const unsigned frec = (FL_P0 * 256 + (lane < 16 ? tm1 * 16 + lane : lane < 28 ? ((lane - 16) / 3) * 48 + ((lane - 16) % 3) * 16 + tn1 : tn1)) * 128;
      if (lw == 0) dma4<true>(0u * lane, P.ws + WS_CTL, lds0 + S1_FLG);
      load_all<C1, false, (1u << (PF + 3)), 0x8005u | W1M | (1u << PF) | (1u << (PF + 3)) | (1u << PR)>(L, lds0 + R1,
        [&](int s) {
          if (lw == 0) {
            const u32x4 v = *(volatile LAS u32x4*)(smem + S1_CSR + lane * 16);
            if (!__all(v[0] == y.tag && v[1] == y.tag && v[2] == y.tag && v[3] == y.tag)) {
#pragma unroll 1
              for (int k = 0; k < 4; ++k) poll(y, frec + 4 * k, 64, lane);
            }
          }
        },
        [&](int s) {
          if (s == 0) y.tag = __builtin_amdgcn_readfirstlane(*(volatile LAS unsigned*)(smem + S1_FLG)) + 1u;
          if (s == 2) dma<false>(lane * 16, P.wphi + tn1 * 4096 + lw * 1024, lds0 + S1_WP + lw * 1024);
          if (((W1M >> s) & 1u) && lw == __builtin_popcount(W1M & ((1u << s) - 1u))) dma4<false>(min(lane, 55) * 128, P.w1 + bid * 28672 + lw * 7168, lds0 + S1_DUM);
          if (s == PF && lw == 0) dma<true>(frec, P.ws + WS_FLG, lds0 + S1_CSR);
          if (s == 15 && lw == 1) dma4<false>(lane * 128, P.wo + bid * 8192, lds0 + S1_DUM);
          if (s == PF + 3) {
            if (lw == 3) dma<true>(lane * 16, P.ws + WS_ST1 + tm1 * 1024, lds0 + S1_ST);
#pragma unroll
            for (int i = 0; i < 3; ++i)
              dma<true>((lane >> 4) * 12288 + (lane & 15) * 16, P.ws + WS_CSQ + lw * 49152 + i * 4096 + tn1 * 256, lds0 + S1_CSR + i * 4096 + lw * 1024);
          }
          if (s == PR) {
            if (lt < 192) {
              const LAS float* rawp = (const LAS float*)(smem + S1_CSR) + (lt >> 6) * 1024 + (lt & 63);
              float t = 0.f;
#pragma unroll
              for (int i = 0; i < 16; ++i) t += rawp[i * 64];
              ((LAS float*)(smem + S1_CSF))[lt] = t;
            }
            wait_lgkm0();
          }
        });
    }
    {
      Ld<C3> L; L.A = P.ws; L.W = P.wo + tn3 * 128; L.lw = lw; L.c = c3; L.kt0 = 0; L.init(y, lane, 0, 0, 0, 9);
      if (bid >= 224 && bid < 224 + NPAGE) {
        if (lw == 1) {
          const unsigned po = bid == 224 ? WS_PRB : (bid - 224) << 21;
          unsigned tmin[2] = {~0u, ~0u};
#pragma unroll 1
          for (int k = 0; k < NPROBE; ++k) {
            const unsigned long long t0 = __builtin_amdgcn_s_memtime();
            if (lane < 8) st16(y.r, (u32x4){0u, 0u, 0u, 0u}, lane * 16, po + (k & 1) * 8192);
            wait_vm<0>();
            const unsigned dt = (unsigned)(__builtin_amdgcn_s_memtime() - t0);
            if (k & 1) tmin[1] = min(tmin[1], dt); else tmin[0] = min(tmin[0], dt);
          }
          if (lane == 0) st4(y.r, (y.tag << 1) | (((unsigned)(tmin[1] < tmin[0]) ^ (unsigned)(bid >> 2)) & 1u), 0, WS_FLG + (FL_CS * 256 + bid) * 128 + 4);
        }
        bar_loaders(y, lw);
      }
      load_preW<C3>(L, lds0 + R3);
      if (lw >= 2 && bid < 224) {
        u32x4 v[16];
        colsum_load<16>(v, P.w1 + (size_t)((bid / 56) * 256 + (lw - 2) * 128) * (FF * 2) + (bid % 56) * 128, FF * 2, lane);
        colsum_finish<16>(v, y.r, WS_CS1 + (((bid / 56) * 2 + lw - 2) * FF + (bid % 56) * 64) * 4, lane);
      }
      publish(y, FL_CS, bid, lane);
      lds_wait(y, 24 + FL_KV, 4u);
      Gate G; G.off = (FL_KV * 256 + m16 * 16 + tn1) * 128; G.mask = 0; G.word = 8;
      const unsigned pbo = WS_FLG + (FL_CS * 256 + 224 + (lane & 31)) * 128 + 4;
      unsigned pbv = 0;
      kv_sum(y, G, smem, 2 * tn1 + 1, 1, lt, lw == 0, lw == 1, lane, [&](int) { if (lw == 0) pbv = __builtin_amdgcn_raw_buffer_load_b32(y.r, pbo, 0, 16); });
      if (lw == 0) {
        const unsigned long long t0 = __builtin_amdgcn_s_memrealtime();
        while (!__all(lane >= NPAGE || (pbv >> 1) == (y.tag & 0x7fffffffu))) {
          if (__builtin_amdgcn_s_memrealtime() - t0 > 4000000ull) { if (lane == 0) st4(y.r, y.tag, 0, WS_CTL + 128); break; }
          pbv = __builtin_amdgcn_raw_buffer_load_b32(y.r, pbo, 0, 16);
        }
        const unsigned raw = (unsigned)__ballot(pbv & 1u), odd = __builtin_popcount((raw ^ 0xaaaaaaaau) & 0x3fffffdu);
        *(volatile LAS unsigned*)(y.w + 13) = odd <= 6 ? 0xaaaaaaaau : odd >= 19 ? 0x55555555u : raw;
      }
      wait_lgkm0(); bar();
      y.pb = __builtin_amdgcn_readfirstlane(*(volatile LAS unsigned*)(y.w + 13));
      L.A += near(y, WS_O + tm3 * 524288, tm3); L.G.off = rec(y, FL_O, tm3 * 16 + m16);
      load_all<C3, true, 0u, 0u>(L, lds0 + R3, nop, nop);
    }
    {
      Ld<C4> L; L.A = P.ws + near(y, WS_H + tm1 * 524288, tm1); L.W = P.w1 + ch0 * 128; L.lw = lw; L.c = c1; L.kt0 = 0;
      L.init(y, lane, 128, par ? -64 : 384, (lane & 16) ? rec(y, FL_CS, (m16 >> 2) * 56 + base7 + 3 * par + (lane & 3)) : rec(y, FL_H, tm1 * 16 + m16), 10);
      load_preW<C4>(L, lds0 + R4, 0, E4);
      lds_wait(y, 24 + FL_H, 4u);
      load_preW<C4>(L, lds0 + R4, E4);
      load_all<C4, true, 0u, 0xa000u | (W2AT == 0 ? 0x550u : W2AT > 0 ? 1u << (W2AT & 31) : 0u)>(L, lds0 + R4, nop,
        [&](int s) {
          if (W2AT == 0 ? (s < 12 && lw == ((s >> 1) & 3)) : s == W2AT) dma4<false>(min(lane, 55) * 128, P.w2 + bid * 28672 + lw * 7168, lds0 + S4_DUM);
          if (s == 13) {
            gate(y, L.G, ~0u, lw == 0, lane);
#pragma unroll
            for (int i = 0; i < 4; ++i) dma<true>(lane * 16, P.ws + near(y, WS_ST2 + tm1 * 32768, tm1) + (lw >> 1) * 16384 + ((lw & 1) * 4 + i) * 1024, lds0 + S4_STR + (lw * 4 + i) * 1024);
#pragma unroll
            for (int i = 0; i < 2; ++i) dma<true>(lane * 16, P.ws + WS_CS1 + ((lw * 2 + i) * FF + (base7 + 3 * par) * 64) * 4, lds0 + S4_CSR + (lw * 2 + i) * 1024);
          }
          if (s == 15) {
            const LAS float* rawp = (const LAS float*)(smem + S4_CSR) + lt;
            float t = 0.f;
#pragma unroll
            for (int i = 0; i < 8; ++i) t += rawp[i * 256];
            ((LAS float*)(smem + S4_CSF))[lt] = t;
            wait_lgkm0();
          }
          if (s == 15 && lw < 2) {
            f32x2 st[16]; float mu = 0.f, m2 = 0.f;
#pragma unroll
            for (int i = 0; i < 16; ++i) { st[i] = *(const LAS f32x2*)(smem + S4_STR + i * 1024 + lt * 8); mu += st[i][0]; }
            mu *= (1.f / 16.f);
#pragma unroll
            for (int i = 0; i < 16; ++i) { const float d = st[i][0] - mu; m2 += st[i][1] + 64.f * d * d; }
            *(LAS f32x2*)(smem + S4_STF + lt * 8) = (f32x2){mu, __builtin_amdgcn_rsqf(m2 * (1.f / D) + 1e-5f)};
            wait_lgkm0();
          }
        });
    }
    {
      Ld<C5> L; L.A = P.ws + near(y, WS_F + tm5 * 2097152, tm5); L.W = P.w2 + tn5 * 256; L.lw = lw; L.c = c5; L.kt0 = 28 * kh; L.init(y, lane, 128, 0, rec(y, FL_F, tm5 * 16 + 8 * kh + ((lane >> 1) & 7)) + (lane & 1) * 4, 11);
      load_preW<C5>(L, lds0 + R5);

      load_all<C5, true, 0u, 0u>(L, lds0 + R5, nop, nop);
    }
    return;
  }

  const int wm = wid >> 1, wn = wid & 1;
  if (wid == 0 && lane < 32) y.w[lane] = 0u;
  {
    f32x4 acc[24];
    Adr ad;
    ad.v[0] = lds0 + R1 + (wm * 64 + m16) * 128 + ((g ^ (m16 >> 1)) << 4); ad.v[1] = lds0 + R1 + (wm * 64 + m16) * 128 + ((g ^ (m16 >> 1) ^ 4) << 4);
    ad.v[2] = lds0 + R1 + 16384 + wlane(lane, 2 * wn); ad.v[3] = lds0 + R1 + 16384 + wlane(lane, 2 * wn + 1); ad.v[4] = 0; ad.v[5] = 0;
    u32x4 px[4], pw[8];
    const int prow = bid * 8 + wid * 2 + (lane >> 5);
    consume<C1, 1, RPG1, PH0, PH1, PH2>(acc, ad, [&](int s) {
      if (s < 0) {
        y.tag = __builtin_amdgcn_readfirstlane(*(volatile LAS unsigned*)(smem + S1_FLG)) + 1u;
      }
      if (s == 0) {
#pragma unroll
        for (int i = 0; i < 4; ++i) px[i] = *(const u32x4*)(P.x + (size_t)prow * 2048 + ((lane & 31) + 32 * i) * 16);
        colsum_load<8>(pw, P.wqkv + (size_t)((min(bid, 191) / 48) * 256 + wid * 64) * (3 * D * 2) + (bid % 48) * 128, 3 * D * 2, lane);
      }
      if (s == 1) {
#pragma unroll
        for (int i = 0; i < 4; ++i) asm volatile("" : "+v"(px[i]));
#pragma unroll
        for (int i = 0; i < 8; ++i) asm volatile("" : "+v"(pw[i]));
        const unsigned one2 = opaque(0x3f803f80u);
        float sm = 0.f, sq = 0.f;
#pragma unroll
        for (int i = 0; i < 4; ++i)
#pragma unroll
          for (int j = 0; j < 4; ++j) { sm = dot2(px[i][j], one2, sm); sq = dot2(px[i][j], px[i][j], sq); }
        const float mean = xsum16(row16_sum(sm)) * (1.f / D);
        const float var = fmaxf(xsum16(row16_sum(sq)) * (1.f / D) - mean * mean, 0.f);
        if ((lane & 31) == 0) st8(y.r, (u32x2){bu(mean), bu(__builtin_amdgcn_rsqf(var + 1e-5f))}, prow * 8, WS_ST1);
        if (bid < 192) colsum_finish<8>(pw, y.r, WS_CSQ + (((bid / 48) * 4 + wid) * 3 * D + (bid % 48) * 64) * 4, lane);
      }
      if (s == 2) { wait_vm<0>(); if (lane == 0) st4(y.r, y.tag, 0, WS_FLG + (FL_P0 * 256 + bid) * 128 + wid * 4); }
    });
    const int head = 2 * tn1 + wn;
    f32x4 cs[4]; float csv[2];
#pragma unroll
    for (int n = 0; n < 4; ++n) cs[n] = *(const LAS f32x4*)(smem + S1_CSF + ((n >> 1) * 64 + wn * 32 + (n & 1) * 16 + 4 * g) * 4);
#pragma unroll
    for (int n = 0; n < 2; ++n) csv[n] = *(const LAS float*)(smem + S1_CSF + (128 + wn * 32 + n * 16 + m16) * 4);
    u32x4 wp[2];
#pragma unroll
    for (int F = 0; F < 2; ++F) { lds_ptr a0 = smem + S1_WP + wn * 2048 + (4 * g + (m16 >> 2)) * 64 + F * 32 + (lane & 3) * 8; wp[F] = lds_tr2(a0, a0 + 1024); }
    u32x4 qb[4], vb[2][2];
    f32x4 p[4][2];
    const f32x4 zero = {0.f, 0.f, 0.f, 0.f};
    {
      f32x4 vv[4][2];
#pragma unroll
      for (int m = 0; m < 4; ++m) {
        const f32x2 st = *(const LAS f32x2*)(smem + S1_ST + (wm * 64 + m * 16 + m16) * 8);
        const f32x4 s0 = *(const LAS f32x4*)(smem + S1_ST + (wm * 64 + m * 16 + 4 * g) * 8), s1 = *(const LAS f32x4*)(smem + S1_ST + (wm * 64 + m * 16 + 4 * g) * 8 + 16);
        const f32x4 mu = {s0[0], s0[2], s1[0], s1[2]}, rs = {s0[1], s0[3], s1[1], s1[3]};
        qb[m] = pack8((acc[m * 6] - cs[0] * st[0]) * st[1], (acc[m * 6 + 1] - cs[1] * st[0]) * st[1]);
        const u32x4 kb = pack8((acc[m * 6 + 2] - cs[2] * st[0]) * st[1], (acc[m * 6 + 3] - cs[3] * st[0]) * st[1]);
        p[m][0] = mfma(wp[0], kb, zero); p[m][1] = mfma(wp[1], kb, zero);
        vv[m][0] = (acc[m * 6 + 4] - mu * csv[0]) * rs; vv[m][1] = (acc[m * 6 + 5] - mu * csv[1]) * rs;
      }
#pragma unroll
      for (int u = 0; u < 2; ++u) { vb[u][0] = pack8(vv[2 * u][0], vv[2 * u + 1][0]); vb[u][1] = pack8(vv[2 * u][1], vv[2 * u + 1][1]); }
    }
    u32x4 pp[4], pn[4];
    dual_softmax4(p, pp, pn);
    {
      lds_ptr own = smem + R3 + wid * C3::STAGE, oth = smem + R3 + (wid ^ 2) * C3::STAGE;
#pragma unroll
      for (int m = 0; m < 4; ++m) {
        lds_ptr w = own + (16 * m + m16) * 136 + 8 * g;
        *(LAS u32x2*)(w) = (u32x2){pp[m][0], pp[m][1]}; *(LAS u32x2*)(w + 32) = (u32x2){pp[m][2], pp[m][3]};
        *(LAS u32x2*)(w + 64) = (u32x2){pn[m][0], pn[m][1]}; *(LAS u32x2*)(w + 96) = (u32x2){pn[m][2], pn[m][3]};
      }
#pragma unroll
      for (int i = 0; i < 4; ++i) *(LAS u32x4*)(own + 8704 + i * 1024 + lane * 16) = vb[i >> 1][i & 1];
      wait_lgkm0();
      volatile LAS unsigned* xf = y.w + 16;
      xf[wid] = 1u;
      f32x4 kv[2][2] = {{zero, zero}, {zero, zero}}, z[2] = {zero, zero};
      const u32x4 ones = {0x3f803f80u, 0x3f803f80u, 0x3f803f80u, 0x3f803f80u};
      const unsigned ro = (4 * g + (m16 >> 2)) * 136 + wm * 64 + (lane & 3) * 8;
#pragma unroll
      for (int u = 0; u < 2; ++u)
#pragma unroll
        for (int F = 0; F < 2; ++F) {
          const u32x4 pa = lds_tr2(own + ro + u * 32 * 136 + F * 32, own + ro + u * 32 * 136 + F * 32 + 16 * 136);
          kv[F][0] = mfma(pa, vb[u][0], kv[F][0]); kv[F][1] = mfma(pa, vb[u][1], kv[F][1]); z[F] = mfma(pa, ones, z[F]);
        }
      while (xf[wid ^ 2] == 0u) __builtin_amdgcn_s_sleep(0);
      asm volatile("" ::: "memory");
#pragma unroll
      for (int u = 0; u < 2; ++u) {
        const u32x4 v0 = lds128(oth + 8704 + u * 2048 + lane * 16), v1 = lds128(oth + 8704 + u * 2048 + 1024 + lane * 16);
#pragma unroll
        for (int F = 0; F < 2; ++F) {
          const u32x4 pa = lds_tr2(oth + ro + u * 32 * 136 + F * 32, oth + ro + u * 32 * 136 + F * 32 + 16 * 136);
          kv[F][0] = mfma(pa, v0, kv[F][0]); kv[F][1] = mfma(pa, v1, kv[F][1]); z[F] = mfma(pa, ones, z[F]);
        }
      }
      const unsigned kd = WS_KVP + (head * 16 + tm1) * 4096 + wm * 2048;
      st16(y.r, pack8(kv[0][0], kv[1][0]), lane * 16, kd);
      st16(y.r, pack8(kv[0][1], kv[1][1]), lane * 16, kd + 1024);
      if (m16 == 0) {
        st16(y.r, __builtin_bit_cast(u32x4, z[0]), g * 32, WS_ZP + (head * 16 + tm1) * 256 + wm * 128);
        st16(y.r, __builtin_bit_cast(u32x4, z[1]), g * 32 + 16, WS_ZP + (head * 16 + tm1) * 256 + wm * 128);
      }
      publish(y, FL_KV, tm1 * 16 + tn1, lane);
    }
#pragma unroll
    for (int m = 0; m < 4; ++m) { p[m][0] = mfma(wp[0], qb[m], zero); p[m][1] = mfma(wp[1], qb[m], zero); }
    dual_softmax4(p, pp, pn);
    { Gate G; G.off = 0; G.mask = 0; G.word = 8; kv_sum(y, G, smem, 2 * tn1, 0, threadIdx.x, false, wid == 0, lane, [](int) {}); }
    wait_lgkm0(); bar();
    y.pb = __builtin_amdgcn_readfirstlane(*(volatile LAS unsigned*)(y.w + 13));
    u32x4 ka[2][2], za[2];
#pragma unroll
    for (int ks = 0; ks < 2; ++ks) {
      ka[ks][0] = lds128(smem + S1_KV + wn * 4096 + ks * 2048 + lane * 16); ka[ks][1] = lds128(smem + S1_KV + wn * 4096 + ks * 2048 + 1024 + lane * 16);
      za[ks] = lds128(smem + S1_KV + 8192 + wn * 128 + ks * 64 + g * 16);
    }
#pragma unroll
    for (int m = 0; m < 4; ++m) {
      f32x4 n0 = mfma(ka[0][0], pp[m], zero), n1 = mfma(ka[0][1], pp[m], zero), dn = mfma(za[0], pp[m], zero);
      n0 = mfma(ka[1][0], pn[m], n0); n1 = mfma(ka[1][1], pn[m], n1); dn = mfma(za[1], pn[m], dn);
      const float rd = __builtin_amdgcn_rcpf(dn[0] + 1e-6f);
      st16(y.r, pack8(n0 * rd, n1 * rd), lane * 16, near(y, WS_O + (tm1 * 16 + tn1) * 32768, tm1) + wm * 16384 + (m * 2 + wn) * 1024);
    }
    publish(y, FL_O, tm1 * 16 + tn1, lane);
  }
  {
    u32x4 xr[2][2];
#pragma unroll
    for (int m = 0; m < 2; ++m)
#pragma unroll
      for (int Q = 0; Q < 2; ++Q)
        xr[m][Q] = *(const u32x4*)(P.x + (size_t)(tm3 * 128 + wid * 32 + m * 16 + m16) * 2048 + tn3 * 128 + Q * 64 + (g & 1) * 32 + (g >> 1) * 16);
    f32x4 acc[8];
    Adr ad; ad.v[0] = lds0 + R3 + wid * 4096 + lane * 16; ad.v[1] = 0;
#pragma unroll
    for (int n = 0; n < 4; ++n) ad.v[2 + n] = lds0 + R3 + 16384 + wlane(lane, n);
    consume_s<C3, 3, 2>(acc, ad, [](int) {});
#pragma unroll
    for (int m = 0; m < 2; ++m) {
      float hv[16];
#pragma unroll
      for (int Q = 0; Q < 2; ++Q) {
        auto r0 = __builtin_amdgcn_permlane16_swap(xr[m][Q][0], xr[m][Q][2], false, false);
        auto r1 = __builtin_amdgcn_permlane16_swap(xr[m][Q][1], xr[m][Q][3], false, false);
        const f32x4 a = acc[m * 4 + 2 * Q], b = acc[m * 4 + 2 * Q + 1];
        const u32x4 hp = {pk2(a[0] + bflo(r0[0]), a[1] + bfhi(r0[0])), pk2(a[2] + bflo(r1[0]), a[3] + bfhi(r1[0])),
                          pk2(b[0] + bflo(r0[1]), b[1] + bfhi(r0[1])), pk2(b[2] + bflo(r1[1]), b[3] + bfhi(r1[1]))};
        st16(y.r, hp, lane * 16, near(y, WS_H + (tm3 * 16 + tn3) * 32768, tm3) + wm * 16384 + wn * 4096 + (m * 2 + Q) * 1024);
#pragma unroll
        for (int j = 0; j < 4; ++j) { hv[Q * 8 + 2 * j] = bflo(hp[j]); hv[Q * 8 + 2 * j + 1] = bfhi(hp[j]); }
      }
      float s = 0.f;
#pragma unroll
      for (int j = 0; j < 16; ++j) s += hv[j];
      s = xsum32(xsum16(s)) * (1.f / 64.f);
      float q = 0.f;
#pragma unroll
      for (int j = 0; j < 16; ++j) { const float d = hv[j] - s; q += d * d; }
      q = xsum32(xsum16(q));
      if (g == 0) st8(y.r, (u32x2){bu(s), bu(q)}, m16 * 8, near(y, WS_ST2 + tm3 * 32768, tm3) + (tn3 >> 3) * 16384 + (tn3 & 7) * 1024 + (wid * 32 + m * 16) * 8);
    }
    publish(y, FL_H, tm3 * 16 + tn3, lane);
  }
  {
    f32x4 acc[28];
    Adr ad;
    ad.v[0] = lds0 + R4 + wm * 8192 + wn * 4096 + lane * 16; ad.v[1] = lds0 + R4 + wm * 8192 + (wn ^ 1) * 4096 + lane * 16;
    ad.v[2] = lds0 + R4 + 16384 + wlane(lane, wn); ad.v[3] = lds0 + R4 + 16384 + wlane(lane, 2 + wn);
    ad.v[4] = lds0 + R4 + 16384 + 6144 + hlane(lane, 0); ad.v[5] = lds0 + R4 + 16384 + 6144 + hlane(lane, 1);
    consume<C4, 4, RPG4, 0, 0, 0>(acc, ad, [](int) {});
    f32x4 cs[8];
#pragma unroll
    for (int n = 0; n < 6; ++n) cs[n] = *(const LAS f32x4*)(smem + S4_CSF + ((par + (n >> 1)) * 64 + (2 * (n & 1) + wn) * 16 + 4 * g) * 4);
#pragma unroll
    for (int n = 0; n < 2; ++n) cs[6 + n] = *(const LAS f32x4*)(smem + S4_CSF + ((par ? 32 : 192) + n * 16 + 4 * g) * 4);
    const f32x4 zero = {0.f, 0.f, 0.f, 0.f};
    f32x2 st[4];
#pragma unroll
    for (int m = 0; m < 4; ++m) st[m] = *(const LAS f32x2*)(smem + S4_STF + ((wm * 4 + ((m + 2 * wn) & 3)) * 16 + m16) * 8);
#pragma unroll
    for (int i = 0; i < 4; ++i) {
#pragma unroll
      for (int m = 0; m < (i < 3 ? 4 : 2); ++m) {
        const unsigned fd = near(y, WS_F + tm1 * 2097152, tm1) + wm * 16384 + ((m + 2 * wn) & 3) * 2048 + (i < 3 ? (ch0 + i) * 32768 + wn * 1024 : (base7 + 3) * 32768 + par * 1024);
        const int a = i < 3 ? m * 6 + 2 * i : 24 + 2 * m;
        st16(y.r, pack8(__builtin_elementwise_max((acc[a] - cs[2 * i] * st[m][0]) * st[m][1], zero),
                        __builtin_elementwise_max((acc[a + 1] - cs[2 * i + 1] * st[m][0]) * st[m][1], zero)), lane * 16, fd);
        SB();
      }
      if (i == FAT) {
        wait_vm<FAT == 3 ? 10 : 4 * FAT>();
        if (lane == 0 && __hip_atomic_fetch_add(y.w + FL_F, 1u, __ATOMIC_RELAXED, __HIP_MEMORY_SCOPE_WORKGROUP) == 3u) st4(y.r, y.tag, 0, WS_FLG + rec(y, FL_F, tm1 * 16 + tn1));
        SB();
      }
    }
  }
  {
    f32x4 acc[16];
    Adr ad;
    ad.v[0] = lds0 + R5 + wm * 8192 + kh * 4096 + lane * 16; ad.v[1] = lds0 + R5 + wm * 8192 + (kh ^ 1) * 4096 + lane * 16;
#pragma unroll
    for (int n = 0; n < 4; ++n) ad.v[2 + n] = lds0 + R5 + 16384 + wn * 2048 + wlane(lane, n);
    u32x4 hr[2][2];
    consume_s<C5, 5, 1>(acc, ad, [&](int s) {
      if (s < 0) {
        wait_vm<0>();
        if (lane == 0 && __hip_atomic_fetch_add(y.w + 20, 1u, __ATOMIC_RELAXED, __HIP_MEMORY_SCOPE_WORKGROUP) == 3u) st4(y.r, y.tag, 0, WS_FLG + rec(y, FL_F, tm1 * 16 + tn1) + 4);
#pragma unroll
        for (int m = 0; m < 2; ++m)
#pragma unroll
          for (int Q = 0; Q < 2; ++Q) hr[m][Q] = ld16(y.r, lane * 16, near(y, WS_H + (tm5 * 16 + 2 * tn5 + wn) * 32768, tm5) + wm * 16384 + ((2 * kh + m) * 2 + Q) * 1024);
      }
    });
    const int tile = tm5 * 8 + tn5;
#pragma unroll
    for (int i = 0; i < 4; ++i) st16(y.r, pack8(acc[8 + 2 * i], acc[9 + 2 * i]), lane * 16, near(y, WS_O + (tile * 2 + kh) * 32768, tm5) + wm * 16384 + wn * 4096 + i * 1024);
    wait_vm<0>();
    if (lane == 0) st4(y.r, y.tag, 0, WS_FLG + rec(y, FL_X + wm, tile * 2 + kh) + wn * 64);
    poll(y, rec(y, FL_X + wm, tile * 2 + (kh ^ 1)) + wn * 64, 1, lane);
    const rsrc_t ro = __builtin_amdgcn_make_buffer_rsrc(P.out, 0, 2048 * 2048, 0x00020000);
    u32x4 xv[4];
#pragma unroll
    for (int i = 0; i < 4; ++i) xv[i] = ld16(y.r, lane * 16, near(y, WS_O + (tile * 2 + (kh ^ 1)) * 32768, tm5) + wm * 16384 + wn * 4096 + i * 1024);
#pragma unroll
    for (int m = 0; m < 2; ++m)
#pragma unroll
      for (int Q = 0; Q < 2; ++Q) {
        const u32x4 xx = xv[m * 2 + Q];
        const f32x4 a = acc[m * 4 + 2 * Q] + (f32x4){bflo(xx[0]), bfhi(xx[0]), bflo(xx[1]), bfhi(xx[1])}, b = acc[m * 4 + 2 * Q + 1] + (f32x4){bflo(xx[2]), bfhi(xx[2]), bflo(xx[3]), bfhi(xx[3])};
        const u32x4 hh = hr[m][Q];
        const unsigned a0 = pk2(a[0] + bflo(hh[0]), a[1] + bfhi(hh[0])), a1 = pk2(a[2] + bflo(hh[1]), a[3] + bfhi(hh[1]));
        const unsigned b0 = pk2(b[0] + bflo(hh[2]), b[1] + bfhi(hh[2])), b1 = pk2(b[2] + bflo(hh[3]), b[3] + bfhi(hh[3]));
        auto r0 = __builtin_amdgcn_permlane16_swap(a0, b0, false, false);
        auto r1 = __builtin_amdgcn_permlane16_swap(a1, b1, false, false);
        __builtin_amdgcn_raw_buffer_store_b128((u32x4){r0[0], r1[0], r0[1], r1[1]}, ro, (tm5 * 128 + wm * 64 + (2 * kh + m) * 16 + m16) * 2048 + tn5 * 256 + wn * 128 + Q * 64 + (g & 1) * 32 + (g >> 1) * 16, 0, 16);
      }
    if (bid == 0 && threadIdx.x == 0) st4(y.r, y.tag, 0, WS_CTL);
  }
}

extern "C" void kernel_launch(void* const* d_in, const int* in_sizes, int n_in, void* d_out, int out_size, void* d_ws, size_t ws_size,
                              hipStream_t stream) {
  Params p;
  p.x = (const char*)d_in[0]; p.wqkv = (const char*)d_in[1]; p.wphi = (const char*)d_in[2]; p.wo = (const char*)d_in[3];
  p.w1 = (const char*)d_in[5]; p.w2 = (const char*)d_in[6]; p.ws = (char*)(((size_t)d_ws + 0x1fffff) & ~(size_t)0x1fffff); p.out = (char*)d_out;
  (void)hipFuncSetAttribute((const void*)fused, hipFuncAttributeMaxDynamicSharedMemorySize, LDS_BYTES);
  fused<<<256, 512, LDS_BYTES, stream>>>(p);
}
```

```cpp
#include <hip/hip_runtime.h>
#include <hip/hip_bf16.h>

#define LAS __attribute__((address_space(3)))
#define DI __device__ __forceinline__
typedef __bf16 bf16x8 __attribute__((ext_vector_type(8)));
typedef __bf16 bf16x2 __attribute__((ext_vector_type(2)));
typedef short s16x4 __attribute__((ext_vector_type(4)));
typedef float f32x2 __attribute__((ext_vector_type(2)));
typedef float f32x4 __attribute__((ext_vector_type(4)));
typedef unsigned u32x2 __attribute__((ext_vector_type(2)));
typedef unsigned u32x4 __attribute__((ext_vector_type(4)));
typedef LAS char* lds_ptr;
typedef __amdgpu_buffer_rsrc_t rsrc_t;

constexpr int D = 1024, FF = 3584;
constexpr unsigned WS_CTL = 0;
constexpr unsigned WS_FLG = 4096;
constexpr unsigned WS_ST1 = 320u << 10;
constexpr unsigned WS_CSQ = 384u << 10;
constexpr unsigned WS_CS1 = 576u << 10;
constexpr unsigned WS_ZP = 704u << 10;
constexpr unsigned WS_ST2 = 1024u << 10;
constexpr unsigned WS_FL2 = 1536u << 10;
constexpr unsigned WS_PRB = 1920u << 10;
constexpr unsigned WS_KVP = 2u << 20;
constexpr unsigned WS_O = 4u << 20;
constexpr unsigned WS_H = 12u << 20;
constexpr unsigned WS_F = 20u << 20;
constexpr unsigned WS_END = 52u << 20;
constexpr int NPAGE = 26;
enum { FL_P0 = 0, FL_KV = 1, FL_CS = 2, FL_O = 3, FL_H = 4, FL_F = 5, FL_X = 6   };

struct Params { const char* x; const char* wqkv; const char* wphi; const char* wo; const char* w1; const char* w2; char* ws; char* out; };

DI unsigned pk2(float a, float b) { f32x2 v = {a, b}; return __builtin_bit_cast(unsigned, __builtin_convertvector(v, bf16x2)); }
DI float bflo(unsigned u) { return __uint_as_float(u << 16); }
DI float bfhi(unsigned u) { return __uint_as_float(u & 0xffff0000u); }
DI u32x4 pack8(const f32x4& a, const f32x4& b) { return (u32x4){pk2(a[0], a[1]), pk2(a[2], a[3]), pk2(b[0], b[1]), pk2(b[2], b[3])}; }
DI bf16x8 asbf(const u32x4& v) { return __builtin_bit_cast(bf16x8, v); }
#define SB() __builtin_amdgcn_sched_barrier(0)
DI void bar() { asm volatile("" ::: "memory"); __builtin_amdgcn_s_barrier(); asm volatile("" ::: "memory"); }
DI void wait_lgkm0() { __builtin_amdgcn_s_waitcnt(0xC07F); asm volatile("" ::: "memory"); }
template <int N> DI void wait_vm() { asm volatile("s_waitcnt vmcnt(%0)" ::"i"(N) : "memory"); }
template <bool SC1> DI void dma(unsigned voff, const char* sbase, unsigned lds) {
  if (SC1) asm volatile("s_mov_b32 m0, %2\n\ts_nop 3\n\tglobal_load_lds_dwordx4 %0, %1 sc1" ::"v"(voff), "s"(sbase), "s"(lds) : "memory");
  else asm volatile("s_mov_b32 m0, %2\n\ts_nop 3\n\tglobal_load_lds_dwordx4 %0, %1" ::"v"(voff), "s"(sbase), "s"(lds) : "memory");
}
template <bool SC1> DI void dma4(unsigned voff, const char* sbase, unsigned lds) {
  if (SC1) asm volatile("s_mov_b32 m0, %2\n\ts_nop 3\n\tglobal_load_lds_dword %0, %1 sc1" ::"v"(voff), "s"(sbase), "s"(lds) : "memory");
  else asm volatile("s_mov_b32 m0, %2\n\ts_nop 3\n\tglobal_load_lds_dword %0, %1" ::"v"(voff), "s"(sbase), "s"(lds) : "memory");
}
DI u32x4 lds128(lds_ptr p) { return *(const LAS u32x4*)p; }
DI u32x4 lds_tr2(lds_ptr p0, lds_ptr p1) {
  s16x4 a = __builtin_amdgcn_ds_read_tr16_b64_v4i16((LAS s16x4*)p0);
  s16x4 b = __builtin_amdgcn_ds_read_tr16_b64_v4i16((LAS s16x4*)p1);
  return __builtin_bit_cast(u32x4, __builtin_shufflevector(a, b, 0, 1, 2, 3, 4, 5, 6, 7));
}
DI u32x2 lds_tr(lds_ptr p) { return __builtin_bit_cast(u32x2, __builtin_amdgcn_ds_read_tr16_b64_v4i16((LAS s16x4*)p)); }
DI f32x4 mfma(const u32x4& a, const u32x4& b, const f32x4& c) { return __builtin_amdgcn_mfma_f32_16x16x32_bf16(asbf(a), asbf(b), c, 0, 0, 0); }
DI void mfma_ip(f32x4& acc, const u32x4& a, const u32x4& b) { asm volatile("v_mfma_f32_16x16x32_bf16 %0, %1, %2, %0" : "+v"(acc) : "v"(a), "v"(b)); }
DI u32x4 ld16(rsrc_t r, unsigned voff, unsigned soff) { return __builtin_amdgcn_raw_buffer_load_b128(r, voff, soff, 16); }
DI void st16(rsrc_t r, const u32x4& v, unsigned voff, unsigned soff) { __builtin_amdgcn_raw_buffer_store_b128(v, r, voff + soff, 0, 16);
}
DI void st8(rsrc_t r, const u32x2& v, unsigned voff, unsigned soff) { __builtin_amdgcn_raw_buffer_store_b64(v, r, voff, soff, 16); }
DI void st4(rsrc_t r, unsigned v, unsigned voff, unsigned soff) { __builtin_amdgcn_raw_buffer_store_b32(v, r, voff, soff, 16); }
DI float fb(unsigned u) { return __uint_as_float(u); }
DI unsigned bu(float f) { return __float_as_uint(f); }
DI float xsum16(float x) { auto r = __builtin_amdgcn_permlane16_swap(bu(x), bu(x), false, false); return fb(r[0]) + fb(r[1]); }
DI float xsum32(float x) { auto r = __builtin_amdgcn_permlane32_swap(bu(x), bu(x), false, false); return fb(r[0]) + fb(r[1]); }
template <int CTRL> DI float dpp(float x) { return __builtin_bit_cast(float, __builtin_amdgcn_update_dpp(0, __builtin_bit_cast(int, x), CTRL, 0xf, 0xf, true)); }
DI float row16_sum(float x) { x += dpp<0xB1>(x); x += dpp<0x4E>(x); x += dpp<0x141>(x); x += dpp<0x140>(x); return x; }

struct Sync { rsrc_t r; unsigned tag, pb; LAS unsigned* w; };
DI unsigned near(const Sync& y, unsigned off, int tm) { return off + ((((y.pb >> (off >> 21)) ^ (tm >> 3)) & 1u) << 13); }
DI unsigned rec(const Sync& y, int phase, int idx) {
  return phase < FL_O ? (phase * 256 + idx) * 128 : near(y, WS_FL2 + (phase - FL_O) * 65536 + (idx >> 6) * 16384 + (idx & 63) * 128, idx >> 4) - WS_FLG;
}
DI void publish(const Sync& y, int phase, int idx, int lane) {
  if (lane == 0) __hip_atomic_fetch_add(y.w + 24 + phase, 1u, __ATOMIC_RELAXED, __HIP_MEMORY_SCOPE_WORKGROUP);
  wait_vm<0>();
  if (lane == 0 && __hip_atomic_fetch_add(y.w + phase, 1u, __ATOMIC_RELAXED, __HIP_MEMORY_SCOPE_WORKGROUP) == 3u) st4(y.r, y.tag, 0, WS_FLG + rec(y, phase, idx));
}
DI void poll(const Sync& y, unsigned off, int n, int lane) {
  const unsigned long long t0 = __builtin_amdgcn_s_memrealtime();
  for (unsigned it = 0;; ++it) {
    const unsigned v = __builtin_amdgcn_raw_buffer_load_b32(y.r, WS_FLG + off, 0, 16);
    if (__all(lane >= n || v == y.tag)) break;
    if ((it & 63) == 63) {
      if (__builtin_amdgcn_readfirstlane(__builtin_amdgcn_raw_buffer_load_b32(y.r, 0, WS_CTL + 128, 16)) == y.tag) break;
      if (__builtin_amdgcn_s_memrealtime() - t0 > 4000000ull) { if (lane == 0) st4(y.r, y.tag, 0, WS_CTL + 128); break; }
    }
    __builtin_amdgcn_s_sleep(2);
  }
  asm volatile("" ::: "memory");
}
DI void lds_wait(const Sync& y, int word, unsigned v);
DI void bar_loaders(const Sync& y, int lw) { if (lw == 1) *(volatile LAS unsigned*)(y.w + 14) = 1u; else lds_wait(y, 14, 1u); }
DI void lds_wait(const Sync& y, int word, unsigned v) {
  volatile LAS unsigned* w = y.w + word;
  while (*w < v) __builtin_amdgcn_s_sleep(1);
  asm volatile("" ::: "memory");
}
struct Gate { unsigned off, mask; int word; };
DI void gate_slow(const Sync& y, Gate& G, unsigned need, bool poller, int lane, int cnt = 0) {
  volatile LAS unsigned* w = y.w + G.word;
  if (poller) {
    const unsigned long long t0 = __builtin_amdgcn_s_memrealtime();
    for (unsigned it = 0;; ++it) {
      G.mask = (unsigned)__ballot(__builtin_amdgcn_raw_buffer_load_b32(y.r, WS_FLG + G.off, 0, 16) == y.tag);
      if ((it & 63) == 63) {
        const bool late = __builtin_amdgcn_s_memrealtime() - t0 > 4000000ull;
        if (late && lane == 0) st4(y.r, y.tag, 0, WS_CTL + 128);
        if (late || __builtin_amdgcn_readfirstlane(__builtin_amdgcn_raw_buffer_load_b32(y.r, 0, WS_CTL + 128, 16)) == y.tag) G.mask = ~0u;
      }
      *w = G.mask;
      if ((G.mask & need) == need && __builtin_popcount(G.mask & 0xffffu) >= cnt) break;
      __builtin_amdgcn_s_sleep(1);
    }
  } else {
    for (;;) { G.mask = __builtin_amdgcn_readfirstlane(*w); if ((G.mask & need) == need && __builtin_popcount(G.mask & 0xffffu) >= cnt) break; __builtin_amdgcn_s_sleep(1); }
  }
  asm volatile("" ::: "memory");
}
DI void gate(const Sync& y, Gate& G, unsigned need, bool poller, int lane) { if (__builtin_expect((G.mask & need) != need, 0)) gate_slow(y, G, need, poller, lane); }

template <int NK, bool SPREAD> DI int kmap(int t, int c) {
  const int u = t - 1, q = u & 3, b4 = u & ~3, body = (int)(u < NK - 4);
  const int lead = ((int)(q == c) & body & (int)(u >= 0)) | (int)(SPREAD && t == 0), head = SPREAD ? 0 : -(int)(u < 4);
  const int non = ((b4 + ((c + 1 + q - ((int)(q > c) & body)) & 3)) & ~head) | ((t - (int)(u > c)) & head);
  return non ^ (((b4 + 4 + c) ^ non) & -lead);
}
#define LAG3 1
#define LAG5 1
enum { ORD_NAT = 0, ORD_PAIR = 1, ORD_F = 2 };
template <int NSUB_, bool HALF_, int NSLOT_, int NK_, int ORD_, int WSTRIDE_, bool XROW_, int LAG_> struct Cfg {
  static constexpr int NSUB = NSUB_, NSLOT = NSLOT_, NK = NK_, ORD = ORD_, WSTRIDE = WSTRIDE_;
  static constexpr bool HALF = HALF_, XROW = XROW_;
  static constexpr int LAG = LAG_;
  static constexpr int STAGE = 16384 + NSUB * 8192 + (HALF ? 4096 : 0), PW = 4 + 2 * NSUB + (HALF ? 1 : 0), RING = STAGE * NSLOT;
  static constexpr int WB = NSUB * 2048 + (HALF ? 1024 : 0);
};
typedef Cfg<3, false, 3, 16, ORD_NAT, 3 * D * 2, true, 0> C1;
typedef Cfg<1, false, 5, 16, ORD_PAIR, D * 2, false, LAG3> C3;
typedef Cfg<3, true, 3, 16, ORD_PAIR, FF * 2, false, 0> C4;
typedef Cfg<2, false, 4, 28, ORD_F, D * 2, false, LAG5> C5;

#define GLDS "global_load_lds_dwordx4 "
template <class C> struct Ld {
  const char* A; const char* W; int lw, c, kt0; unsigned vA[4], vW[7];
  const char* sA; const char* sW; unsigned need;
  Gate G; const Sync* y; int lane_;
  static constexpr int C0 = C::WB > 4096 ? 4096 : 0;
  DI void init(const Sync& y_, int lane, int substep, int halfrel, unsigned goff, int gword) {
    y = &y_; lane_ = lane; G.off = goff; G.mask = 0; G.word = gword;
#pragma unroll
    for (int i = 0; i < 4; ++i) vA[i] = C::XROW ? (lane >> 3) * 2048 + ((((lane & 7) ^ (lane >> 4)) << 4) ^ ((i & 1) * 64)) + i * (16384 - 1024) : lane * 16;
    const int rl = (C::ORD == ORD_NAT) ? 8 * (lane >> 5) + ((lane >> 3) & 3) : (lane >> 3);
    const unsigned w0 = rl * C::WSTRIDE + (((lane & 7) ^ (((lane >> 4) & 3) << 1)) << 4) + 4096;
#pragma unroll
    for (int q = 0; q < 2 * C::NSUB; ++q) vW[q] = w0 + (q & 1) * ((C::ORD == ORD_NAT ? 16 : 8) * C::WSTRIDE) + (q >> 1) * substep - (q * 1024 - C0);
    if (C::HALF) vW[6] = (lane >> 2) * C::WSTRIDE + (((lane & 3) ^ (((lane >> 4) & 1) << 1)) << 4) + 4096 + halfrel - (6144 - C0);
    A += C::XROW ? lw * 32 * 2048 : (lw >> 1) * 16384 + (lw & 1) * 4096;
    W -= 4096;
  }
  DI void prep(int s) {
    int kt = kmap<C::NK, C::XROW>(s, c);
    const int kb = lw >> 1, t = lw & 1;
    int ur;
    need = 1u << kt;
    if (C::ORD == ORD_NAT) ur = 32 * kb + 4 * t;
    else if (C::ORD == ORD_F) {
      const int p = kt & 7, q = kt >> 3, sp = -(int)(q == 3);
      need = ((1u << (2 * p + (int)(q > 0))) & ~sp) | ((0xau << (4 * (p & 3))) & sp);
      kt = kt0 + (((7 * (p >> 1) + 4 * (p & 1) + q) & ~sp) | ((7 * p + 3) & sp));
      ur = (16 * lw & sp) | ((16 * kb + 32 * t) & ~sp);
    }
    else ur = 16 * lw;
    sA = A + (unsigned)(kt * (C::XROW ? 128 : 32768));
    sW = W + (unsigned)((kt * 64 + ur) * C::WSTRIDE);
  }
  DI void issueA(unsigned base) {
    if (!C::XROW) gate(*y, G, need, lw == 0, lane_);
    const unsigned m = base + lw * 4096;
    if (C::XROW) asm volatile("s_mov_b32 m0, %5\n\ts_nop 3\n\t" GLDS "%0, %4\n\t" GLDS "%1, %4 offset:1024\n\t" GLDS "%2, %4 offset:2048\n\t" GLDS "%3, %4 offset:3072"
                              ::"v"(vA[0]), "v"(vA[1]), "v"(vA[2]), "v"(vA[3]), "s"(sA), "s"(m) : "memory");
    else asm volatile("s_mov_b32 m0, %2\n\ts_nop 3\n\t" GLDS "%0, %1 sc1\n\t" GLDS "%0, %1 offset:1024 sc1\n\t" GLDS "%0, %1 offset:2048 sc1\n\t" GLDS "%0, %1 offset:3072 sc1"
                      ::"v"(vA[0]), "s"(sA), "s"(m) : "memory");
  }
  DI void issueW(unsigned base) const {
    const unsigned m = base + 16384 + lw * C::WB + C0;
    if (C::NSUB == 1) asm volatile("s_mov_b32 m0, %3\n\ts_nop 3\n\t" GLDS "%0, %2\n\t" GLDS "%1, %2 offset:1024" ::"v"(vW[0]), "v"(vW[1]), "s"(sW), "s"(m) : "memory");
    if (C::NSUB == 2) asm volatile("s_mov_b32 m0, %5\n\ts_nop 3\n\t" GLDS "%0, %4\n\t" GLDS "%1, %4 offset:1024\n\t" GLDS "%2, %4 offset:2048\n\t" GLDS "%3, %4 offset:3072"
                                   ::"v"(vW[0]), "v"(vW[1]), "v"(vW[2]), "v"(vW[3]), "s"(sW), "s"(m) : "memory");
    if (C::NSUB == 3) asm volatile("s_mov_b32 m0, %7\n\ts_nop 3\n\t" GLDS "%0, %6 offset:-4096\n\t" GLDS "%1, %6 offset:-3072\n\t" GLDS "%2, %6 offset:-2048\n\t" GLDS "%3, %6 offset:-1024\n\t"
                                   GLDS "%4, %6\n\t" GLDS "%5, %6 offset:1024"
                                   ::"v"(vW[0]), "v"(vW[1]), "v"(vW[2]), "v"(vW[3]), "v"(vW[4]), "v"(vW[5]), "s"(sW), "s"(m) : "memory");
    if (C::HALF) asm volatile(GLDS "%0, %1 offset:2048" ::"v"(vW[6]), "s"(sW) : "memory");
  }
};
template <class C> DI void load_preW(Ld<C>& L, unsigned ring, int s0 = 0, int s1 = C::NSLOT) {
#pragma unroll 1
  for (int s = s0; s < s1; ++s) { L.prep(s); L.issueW(ring + s * C::STAGE); }
}
template <class C, bool SPLIT> constexpr int vm_allow(int S) {
  const int N = C::NSLOT, behind = S - 1 - C::LAG > 0 ? S - 1 - C::LAG : 0;
  const int ramp = S + 1 < N - 1 ? S + 1 : N - 1;
  const int last = ramp + behind < C::NK - 1 ? ramp + behind : C::NK - 1;
  return (SPLIT && S < N) ? 4 * (ramp - S) + C::PW * (last - ramp) : C::PW * (last - S);
}
template <class C, bool SPLIT, unsigned POSTM, int S, int SE, class Post> DI void load_fixed(Ld<C>& L, unsigned ring, const Post& post) {
  if constexpr (S < SE) {
    static_assert(C::NSLOT <= C::LAG + 4, "the ring must be full before the first refill");
    wait_vm<vm_allow<C, SPLIT>(S)>();
    if constexpr (S == 0) { if (L.lw == 0) *(volatile LAS unsigned*)(L.y->w + 40) = C::STAGE; }
    bar();
    if constexpr (S + 2 < C::NSLOT) {
      if (!SPLIT) L.issueW(ring + (S + 2) * C::STAGE);
      L.issueA(ring + (S + 2) * C::STAGE);
      L.prep(S + 3);
    }
    constexpr int T = S - 1 - C::LAG + C::NSLOT;
    if constexpr (S - 1 - C::LAG >= 0 && T < C::NK) {
      L.issueW(ring + (T % C::NSLOT) * C::STAGE); L.issueA(ring + (T % C::NSLOT) * C::STAGE);
      if constexpr (T + 1 < C::NK) L.prep(T + 1);
    }
    if constexpr ((POSTM >> S) & 1u) post(S);
    load_fixed<C, SPLIT, POSTM, S + 1, SE>(L, ring, post);
  }
}
template <class C, bool SPLIT, unsigned PREM, unsigned POSTM, class Pre, class Post>
DI void load_all(Ld<C>& L, unsigned ring, const Pre& pre, const Post& post) {
  constexpr int N = C::NSLOT, S0 = N - 1 > 2 + C::LAG ? N - 1 : 2 + C::LAG, S1 = C::NK - N + C::LAG;
#pragma unroll 1
  for (int s = 0; s < 2; ++s) { L.prep(s); if (!SPLIT) L.issueW(ring + s * C::STAGE); L.issueA(ring + s * C::STAGE); }
  L.prep(2);
  __builtin_amdgcn_s_setprio(3);
  load_fixed<C, SPLIT, POSTM, 0, S0>(L, ring, post);
  static_assert(vm_allow<C, SPLIT>(S0) == (N - 2 - C::LAG) * C::PW && vm_allow<C, SPLIT>(S1) == (N - 2 - C::LAG) * C::PW, "steady count");
  unsigned b = ring + ((S0 - 1 - C::LAG) % N) * C::STAGE;
#pragma unroll 1
  for (int s = S0; s <= S1; ++s) {
    wait_vm<(N - 2 - C::LAG) * C::PW>();
    if (PREM && ((PREM >> s) & 1u)) pre(s);
    bar();
    L.issueW(b);
    L.issueA(b);
    L.prep(min(s - C::LAG + N, C::NK - 1));
    b = (b == ring + (N - 1) * C::STAGE) ? ring : b + C::STAGE;
    if (POSTM && ((POSTM >> s) & 1u)) post(s);
  }
  __builtin_amdgcn_s_setprio(0);
  load_fixed<C, SPLIT, POSTM, S1 + 1, C::NK>(L, ring, post);
  bar();
}

template <int MT, int NWF> struct Fr { u32x4 a[MT]; u32x2 w[NWF][2]; };
DI u32x4 cat(const u32x2 (&w)[2]) { return (u32x4){w[0][0], w[0][1], w[1][0], w[1][1]}; }
struct Adr { unsigned v[6]; };
template <int KIND> struct Shape;
template <> struct Shape<1> { static constexpr int MT = 4, NWF = 6, NMF = 24, NACC = 24, WB = 6144; static constexpr unsigned USE = 0x0f; };
template <> struct Shape<3> { static constexpr int MT = 2, NWF = 4, NMF = 8, NACC = 8, WB = 2048; static constexpr unsigned USE = 0x3d; };
template <> struct Shape<4> { static constexpr int MT = 4, NWF = 8, NMF = 28, NACC = 28, WB = 7168; static constexpr unsigned USE = 0x3f; };
template <> struct Shape<5> { static constexpr int MT = 4, NWF = 4, NMF = 16, NACC = 16, WB = 4096; static constexpr unsigned USE = 0x3f; };
DI lds_ptr lp(unsigned a) { return (lds_ptr)(size_t)a; }
template <int KIND, int J> DI void rd1(Fr<Shape<KIND>::MT, Shape<KIND>::NWF>& f, const Adr& ad, int kb) {
  constexpr int NWF = Shape<KIND>::NWF, MT = Shape<KIND>::MT, WB = Shape<KIND>::WB;
  if constexpr (J < 2 * NWF) {
    constexpr int n = J >> 1, t = J & 1;
    if constexpr (KIND == 4 && n >= 6) f.w[n][t] = lds_tr(lp(ad.v[4 + (n - 6)]) + (2 * kb + t) * WB);
    else if constexpr (KIND == 1 || KIND == 4) f.w[n][t] = lds_tr(lp(ad.v[2 + (n & 1)]) + (n >> 1) * 2048 + (2 * kb + t) * WB);
    else f.w[n][t] = lds_tr(lp(ad.v[2 + n]) + (2 * kb + t) * WB);
  } else if constexpr (J < 2 * NWF + MT) {
    constexpr int m = J - 2 * NWF;
    if constexpr (KIND == 1) f.a[m] = lds128(lp(ad.v[kb]) + m * 2048);
    else f.a[m] = lds128(lp(ad.v[m >> 1]) + (m & 1) * 2048 + kb * 1024);
  }
}
template <int KIND, int I> DI void mm1(f32x4 (&acc)[Shape<KIND>::NACC], const Fr<Shape<KIND>::MT, Shape<KIND>::NWF>& f) {
  if constexpr (KIND == 1) { constexpr int m = I / 6, n = I % 6; if constexpr (n < 4) mfma_ip(acc[I], cat(f.w[n]), f.a[m]); else mfma_ip(acc[I], f.a[m], cat(f.w[n])); }
  else if constexpr (KIND == 4) { if constexpr (I < 24) mfma_ip(acc[I], cat(f.w[I % 6]), f.a[I / 6]); else mfma_ip(acc[I], cat(f.w[6 + ((I - 24) & 1)]), f.a[(I - 24) >> 1]); }
  else mfma_ip(acc[I], cat(f.w[I % 4]), f.a[I / 4]);
}
template <int KIND, int RPG, bool READ, bool MOVE, int I = 0>
DI void step(f32x4 (&acc)[Shape<KIND>::NACC], const Fr<Shape<KIND>::MT, Shape<KIND>::NWF>& cur, Fr<Shape<KIND>::MT, Shape<KIND>::NWF>& nxt, Adr& ad, int kb, int delta) {
  constexpr int NMF = Shape<KIND>::NMF, G0 = (2 * Shape<KIND>::NWF + Shape<KIND>::MT + RPG - 1) / RPG;
  if constexpr (I < NMF) {
    if constexpr (READ) {
      rd1<KIND, I * RPG>(nxt, ad, kb);
      if constexpr (RPG > 1) rd1<KIND, I * RPG + 1>(nxt, ad, kb);
    }
    if constexpr (MOVE && I >= G0) {
      constexpr int per = (6 + (NMF - G0) - 1) / (NMF - G0);
#pragma unroll
      for (int k = (I - G0) * per; k < (I - G0 + 1) * per && k < 6; ++k)
        if ((Shape<KIND>::USE >> k) & 1u) { ad.v[k] += delta; asm volatile("" : "+v"(ad.v[k])); }
    }
    mm1<KIND, I>(acc, cur);
    SB();
    step<KIND, RPG, READ, MOVE, I + 1>(acc, cur, nxt, ad, kb, delta);
  }
}
template <int KIND, int J = 0> DI void rdall(Fr<Shape<KIND>::MT, Shape<KIND>::NWF>& f, const Adr& ad, int kb) {
  if constexpr (J < 2 * Shape<KIND>::NWF + Shape<KIND>::MT) { rd1<KIND, J>(f, ad, kb); rdall<KIND, J + 1>(f, ad, kb); }
}
template <class C> constexpr bool WARMSEL() { return C::XROW; }
template <class C> DI void keep_warm(LAS unsigned* w) {
  f32x4 d0 = {0.f, 0.f, 0.f, 0.f}, d1 = d0, d2 = d0, d3 = d0;
  const unsigned h = threadIdx.x * 2654435761u;
  const u32x4 a = {(h & 0x807f807fu) | 0x3f003f00u, ((h >> 3) & 0x807f807fu) | 0x3e803f00u, ((h * 7) & 0x807f807fu) | 0x3f003e80u, ((h * 13) & 0x807f807fu) | 0x3f003f00u};
  const u32x4 b = {a[2] ^ 0x80150033u, a[3] ^ 0x00458051u, a[0] ^ 0x80220011u, a[1] ^ 0x0071804fu};
  while ((unsigned)__builtin_amdgcn_readfirstlane(*(volatile LAS unsigned*)(w + 40)) != (unsigned)C::STAGE) {
#pragma unroll
    for (int i = 0; i < 2; ++i) { mfma_ip(d0, a, b); mfma_ip(d1, b, a); mfma_ip(d2, a, a); mfma_ip(d3, b, b); }
  }
  asm volatile("s_nop 7" :: "v"(d0), "v"(d1), "v"(d2), "v"(d3));
}
template <class C, int KIND, int RPG, int CNT>
DI void stages(f32x4 (&acc)[Shape<KIND>::NACC], Fr<Shape<KIND>::MT, Shape<KIND>::NWF>& f0, Fr<Shape<KIND>::MT, Shape<KIND>::NWF>& f1, Adr& ad, int& slot) {
#pragma unroll 1
  for (int s = 0; s < CNT; ++s) {
    const int delta = (slot + 1 == C::NSLOT) ? -(C::NSLOT - 1) * C::STAGE : C::STAGE;
    slot = (slot + 1 == C::NSLOT) ? 0 : slot + 1;
    SB();
    step<KIND, RPG, true, true>(acc, f0, f1, ad, 1, delta);
    if (!C::LAG) wait_lgkm0();
    bar();
    SB();
    step<KIND, RPG, true, false>(acc, f1, f0, ad, 0, 0);
  }
}
template <class C, int KIND, int RPG, int H0, int H1, int H2, class Hook> DI void consume(f32x4 (&acc)[Shape<KIND>::NACC], Adr& ad, LAS unsigned* sw, const Hook& hook) {
  Fr<Shape<KIND>::MT, Shape<KIND>::NWF> f0, f1;
#pragma unroll
  for (int i = 0; i < Shape<KIND>::NACC; ++i) acc[i] = (f32x4){0.f, 0.f, 0.f, 0.f};
  if (WARMSEL<C>()) keep_warm<C>(sw);
  bar();
  rdall<KIND>(f0, ad, 0);
  hook(-1);
  wait_lgkm0();
  int slot = 0;
  if constexpr (H1 > 0) {
    if constexpr (H0 > 0) stages<C, KIND, RPG, H0>(acc, f0, f1, ad, slot);
    hook(0);
    stages<C, KIND, RPG, H1 - H0>(acc, f0, f1, ad, slot);
    hook(1);
    stages<C, KIND, RPG, H2 - H1>(acc, f0, f1, ad, slot);
    hook(2);
  }
  stages<C, KIND, RPG, C::NK - 1 - H2>(acc, f0, f1, ad, slot);
  SB();
  step<KIND, RPG, true, false>(acc, f0, f1, ad, 1, 0);
  wait_lgkm0();
  bar();
  SB();
  step<KIND, RPG, false, false>(acc, f1, f0, ad, 0, 0);
  asm volatile("s_nop 7\n\ts_nop 3");
  SB();
}
template <class C, int KIND, int RPG, class Hook> DI void consume_s(f32x4 (&acc)[Shape<KIND>::NACC], Adr& ad, LAS unsigned* sw, const Hook& hook) {
  Fr<Shape<KIND>::MT, Shape<KIND>::NWF> f0, f1, g0, g1;
#pragma unroll
  for (int i = 0; i < Shape<KIND>::NACC; ++i) acc[i] = (f32x4){0.f, 0.f, 0.f, 0.f};
  if (WARMSEL<C>()) keep_warm<C>(sw);
  bar();
  rdall<KIND>(f0, ad, 0); rdall<KIND>(f1, ad, 1);
  hook(-1);
#pragma unroll
  for (int k = 0; k < 6; ++k) ad.v[k] += C::STAGE;
  int slot = 1;
#pragma unroll 1
  for (int s = 0; s < C::NK - 2; s += 2) {
    int delta = (slot + 1 == C::NSLOT) ? -(C::NSLOT - 1) * C::STAGE : C::STAGE;
    slot = (slot + 1 == C::NSLOT) ? 0 : slot + 1;
    if (!C::LAG) wait_lgkm0();
    bar(); SB();
    step<KIND, RPG, true, false>(acc, f0, g0, ad, 0, 0);
    step<KIND, RPG, true, true>(acc, f1, g1, ad, 1, delta);
    delta = (slot + 1 == C::NSLOT) ? -(C::NSLOT - 1) * C::STAGE : C::STAGE;
    slot = (slot + 1 == C::NSLOT) ? 0 : slot + 1;
    if (!C::LAG) wait_lgkm0();
    bar(); SB();
    step<KIND, RPG, true, false>(acc, g0, f0, ad, 0, 0);
    step<KIND, RPG, true, true>(acc, g1, f1, ad, 1, delta);
  }
  if (!C::LAG) wait_lgkm0();
  bar(); SB();
  step<KIND, RPG, true, false>(acc, f0, g0, ad, 0, 0);
  step<KIND, RPG, true, false>(acc, f1, g1, ad, 1, 0);
  wait_lgkm0(); bar(); SB();
  step<KIND, RPG, false, false>(acc, g0, f0, ad, 0, 0);
  step<KIND, RPG, false, false>(acc, g1, f1, ad, 1, 0);
  asm volatile("s_nop 7\n\ts_nop 3");
  SB();
}
DI unsigned wlane(int lane, int nt) {
  const int g = lane >> 4, q = (lane >> 2) & 3, p = lane & 3, sw = (2 * g + (q >> 1)) & 3;
  return (4 * g + q) * 128 + ((nt ^ sw) << 5) + p * 8;
}
DI unsigned hlane(int lane, int nt) {
  const int g = lane >> 4, q = (lane >> 2) & 3, p = lane & 3;
  return (4 * g + q) * 64 + ((nt ^ (g & 1)) << 5) + p * 8;
}

template <int OP> DI float op2(float a, float b) { return OP == 0 ? a + b : OP == 1 ? __builtin_elementwise_maximum(a, b) : __builtin_elementwise_minimum(a, b); }
template <int OP> DI float red4(float x0, float x1, float x2, float x3) {
  auto a = __builtin_amdgcn_permlane16_swap(bu(x0), bu(x1), false, false);
  auto b = __builtin_amdgcn_permlane16_swap(bu(x2), bu(x3), false, false);
  const float y0 = op2<OP>(fb(a[0]), fb(a[1])), y1 = op2<OP>(fb(b[0]), fb(b[1]));
  auto c = __builtin_amdgcn_permlane32_swap(bu(y0), bu(y1), false, false);
  return op2<OP>(fb(c[0]), fb(c[1]));
}
DI void spread(float z, float (&o)[4]) {
  auto a = __builtin_amdgcn_permlane32_swap(bu(z), bu(z), false, false);
  auto b = __builtin_amdgcn_permlane16_swap(a[0], a[0], false, false);
  auto c = __builtin_amdgcn_permlane16_swap(a[1], a[1], false, false);
  o[0] = fb(b[0]); o[1] = fb(b[1]); o[2] = fb(c[0]); o[3] = fb(c[1]);
}
DI void dual_softmax4(const f32x4 (&p)[4][2], u32x4 (&pp)[4], u32x4 (&pn)[4]) {
  constexpr float L2E = 1.4426950408889634f;
  float mx[4], mn[4];
#pragma unroll
  for (int m = 0; m < 4; ++m) {
    mx[m] = op2<1>(op2<1>(op2<1>(op2<1>(p[m][0][0], p[m][0][1]), p[m][0][2]), op2<1>(op2<1>(p[m][0][3], p[m][1][0]), p[m][1][1])), op2<1>(p[m][1][2], p[m][1][3]));
    mn[m] = op2<2>(op2<2>(op2<2>(op2<2>(p[m][0][0], p[m][0][1]), p[m][0][2]), op2<2>(op2<2>(p[m][0][3], p[m][1][0]), p[m][1][1])), op2<2>(p[m][1][2], p[m][1][3]));
  }
  spread(-L2E * red4<1>(mx[0], mx[1], mx[2], mx[3]), mx);
  spread(L2E * red4<2>(mn[0], mn[1], mn[2], mn[3]), mn);
  f32x4 ep[4][2], en[4][2];
  float sp[4], sn[4];
#pragma unroll
  for (int m = 0; m < 4; ++m) {
#pragma unroll
    for (int F = 0; F < 2; ++F) {
      const f32x4 a = p[m][F] * L2E + mx[m], b = p[m][F] * (-L2E) + mn[m];
#pragma unroll
      for (int r = 0; r < 4; ++r) { ep[m][F][r] = __builtin_amdgcn_exp2f(a[r]); en[m][F][r] = __builtin_amdgcn_exp2f(b[r]); }
    }
    const f32x4 a = ep[m][0] + ep[m][1], b = en[m][0] + en[m][1];
    sp[m] = (a[0] + a[1]) + (a[2] + a[3]); sn[m] = (b[0] + b[1]) + (b[2] + b[3]);
  }
  spread(__builtin_amdgcn_rcpf(red4<0>(sp[0], sp[1], sp[2], sp[3])), sp);
  spread(__builtin_amdgcn_rcpf(red4<0>(sn[0], sn[1], sn[2], sn[3])), sn);
#pragma unroll
  for (int m = 0; m < 4; ++m) { pp[m] = pack8(ep[m][0] * sp[m], ep[m][1] * sp[m]); pn[m] = pack8(en[m][0] * sn[m], en[m][1] * sn[m]); }
}

DI float dot2(unsigned a, unsigned b, float c) { return __builtin_amdgcn_fdot2_f32_bf16(__builtin_bit_cast(bf16x2, a), __builtin_bit_cast(bf16x2, b), c, false); }
DI unsigned opaque(unsigned c) { asm volatile("" : "+s"(c)); return c; }
template <int NL> DI void colsum_load(u32x4 (&v)[NL], const char* w, int stride, int lane) {
#pragma unroll
  for (int i = 0; i < NL; ++i) v[i] = *(const u32x4*)(w + (size_t)(8 * i + (lane >> 3)) * stride + (lane & 7) * 16);
}
template <int NL> DI void colsum_finish(const u32x4 (&v)[NL], rsrc_t r, unsigned dst, int lane) {
  f32x2 s2[4];
#pragma unroll
  for (int j = 0; j < 4; ++j) s2[j] = (f32x2){bflo(v[0][j]), bfhi(v[0][j])};
#pragma unroll
  for (int i = 1; i < NL; ++i)
#pragma unroll
    for (int j = 0; j < 4; ++j) s2[j] += (f32x2){bflo(v[i][j]), bfhi(v[i][j])};
  const float s[8] = {s2[0][0], s2[0][1], s2[1][0], s2[1][1], s2[2][0], s2[2][1], s2[3][0], s2[3][1]};
  float t[4], u[2];
#pragma unroll
  for (int j = 0; j < 4; ++j) { auto q = __builtin_amdgcn_permlane32_swap(bu(s[j]), bu(s[j + 4]), false, false); t[j] = fb(q[0]) + fb(q[1]); }
#pragma unroll
  for (int j = 0; j < 2; ++j) { auto q = __builtin_amdgcn_permlane16_swap(bu(t[j]), bu(t[j + 2]), false, false); u[j] = fb(q[0]) + fb(q[1]); }
  const bool b3 = lane & 8;
  const float tot = (b3 ? u[1] : u[0]) + dpp<0x128>(b3 ? u[0] : u[1]);
  st4(r, bu(tot), ((lane & 7) * 8 + ((lane >> 5) & 1) * 4 + ((lane >> 4) & 1) * 2 + ((lane >> 3) & 1)) * 4, dst);
}

template <int XN, int TM, int TN> DI void tile_of(int bid, int& tm, int& tn, int& c) {
  const int x = bid & 7, r = bid >> 3, li = r % TM, lj = r / TM;
  tm = (x / XN) * TM + li; tn = (x % XN) * TN + lj; c = (li + lj) & 3;
}

#define RPG1 1
#define RPG4 1
#define RPG5 1
#define W2AT 0
#define PH0 2
#define PH1 6
#define PH2 7
#define PF 10
#define W1M 0u
constexpr int PR = PF + 6 > 15 ? 15 : PF + 6;
#define NPROBE 4
#define FAT 1
#define E4 0
constexpr int R1 = 40704, R3 = 40704, R4 = 0, R5 = 0, SYNCW = 163584;
constexpr int S1_ST = 0, S1_CSR = 1024, S1_CSF = 13312, S1_FLG = 14080, S1_WP = 14336, S1_KV = 18432, S1_DUM = 26880;
constexpr int S4_STR = C4::RING, S4_CSR = S4_STR + 16384, S4_STF = S4_CSR + 8192, S4_CSF = S4_STF + 1024, S4_DUM = S4_CSF + 1024;
constexpr int LDS_BYTES = 163840, CODE_BYTES = 54 * 1024;
static_assert(S4_DUM + 256 <= SYNCW && S1_KV + 8448 <= R1 && R1 + C1::RING <= SYNCW && R3 + C3::RING <= SYNCW && C5::RING <= S4_STR, "lds");

template <class St> DI void kv_sum(const Sync& y, Gate& G, lds_ptr smem, int head, int h, int t, bool poller, bool doz, int lane, const St& stamp) {
  u32x4 v[16], zz[4];
  unsigned have = 0;
  f32x2 s2[4] = {{0.f, 0.f}, {0.f, 0.f}, {0.f, 0.f}, {0.f, 0.f}};
#pragma unroll 1
  for (int round = poller ? 2 : 0; round < 3; ++round) {
    gate_slow(y, G, 0u, poller, lane, round == 0 ? 0 : round == 1 ? 13 : 16);
    stamp(round);
    const unsigned fresh = G.mask & ~have;
    have = G.mask;
#pragma unroll
    for (int r = 0; r < 16; ++r) if ((fresh >> r) & 1u) v[r] = ld16(y.r, t * 16, WS_KVP + (head * 16 + r) * 4096);
    if (round == 2 && doz) {
#pragma unroll
      for (int i = 0; i < 4; ++i) zz[i] = ld16(y.r, lane * 16, WS_ZP + head * 4096 + i * 1024);
    }
  }
#pragma unroll
  for (int r = 0; r < 16; ++r)
#pragma unroll
    for (int j = 0; j < 4; ++j) s2[j] += (f32x2){bflo(v[r][j]), bfhi(v[r][j])};
  const float s[8] = {s2[0][0], s2[0][1], s2[1][0], s2[1][1], s2[2][0], s2[2][1], s2[3][0], s2[3][1]};
  *(LAS u32x4*)(smem + S1_KV + h * 4096 + t * 16) = (u32x4){pk2(s[0], s[1]), pk2(s[2], s[3]), pk2(s[4], s[5]), pk2(s[6], s[7])};
  if (doz) {
    f32x4 z = (__builtin_bit_cast(f32x4, zz[0]) + __builtin_bit_cast(f32x4, zz[1])) + (__builtin_bit_cast(f32x4, zz[2]) + __builtin_bit_cast(f32x4, zz[3]));
#pragma unroll
    for (int j = 0; j < 4; ++j) z[j] = xsum32(xsum16(z[j]));
    if (lane < 16) *(LAS u32x2*)(smem + S1_KV + 8192 + h * 128 + lane * 8) = (u32x2){pk2(z[0], z[1]), pk2(z[2], z[3])};
  }
}

__global__ __launch_bounds__(512, 2) void fused(Params P) {
  extern __shared__ __attribute__((aligned(16))) char smem_[];
  lds_ptr smem = (lds_ptr)smem_;
  const unsigned lds0 = (unsigned)(size_t)smem;
  const int wid = __builtin_amdgcn_readfirstlane(threadIdx.x >> 6), lane = threadIdx.x & 63, bid = blockIdx.x;
  {
    const char* pc; asm volatile("s_getpc_b64 %0" : "=s"(pc));
    const unsigned co = ((bid >> 3) & 7) * 8192 + threadIdx.x * 128;
    if (threadIdx.x < 64 && co < CODE_BYTES) dma4<false>(co, pc, lds0 + S1_DUM);
    SB();
  }
  int tm1, tn1, c1, tm3, tn3, c3;
  tile_of<4, 8, 4>(bid, tm1, tn1, c1);
  tile_of<2, 4, 8>(bid, tm3, tn3, c3);
  const int kh = bid & 1, tm5 = ((bid & 7) >> 1) * 4 + ((bid >> 3) & 3), tn5 = bid >> 5, c5 = ((bid >> 3) + tn5) & 3;
  const int par = tn1 & 1, base7 = 7 * (tn1 >> 1), ch0 = base7 + 4 * par;
  const int g = lane >> 4, m16 = lane & 15;
  Sync y;
  y.r = __builtin_amdgcn_make_buffer_rsrc(P.ws, 0, WS_END, 0x00020000);
  y.w = (LAS unsigned*)(smem + SYNCW); y.pb = 0;

  if (wid >= 4) {
    const int lw = wid - 4, lt = threadIdx.x - 256;
    const auto nop = [](int) {};
    {
      Ld<C1> L; L.A = P.x + (size_t)tm1 * 128 * 2048; L.W = P.wqkv + tn1 * 128; L.lw = lw; L.c = c1; L.kt0 = 0; L.init(y, lane, 2048, 0, 0, 0);
      const unsigned frec = (FL_P0 * 256 + (lane < 16 ? tm1 * 16 + lane : lane < 28 ? ((lane - 16) / 3) * 48 + ((lane - 16) % 3) * 16 + tn1 : tn1)) * 128;
      if (lw == 0) dma4<true>(0u * lane, P.ws + WS_CTL, lds0 + S1_FLG);
      load_all<C1, false, (1u << (PF + 3)), 0x8005u | W1M | (1u << PF) | (1u << (PF + 3)) | (1u << PR)>(L, lds0 + R1,
        [&](int s) {
          if (lw == 0) {
            const u32x4 v = *(volatile LAS u32x4*)(smem + S1_CSR + lane * 16);
            if (!__all(v[0] == y.tag && v[1] == y.tag && v[2] == y.tag && v[3] == y.tag)) {
#pragma unroll 1
              for (int k = 0; k < 4; ++k) poll(y, frec + 4 * k, 64, lane);
            }
          }
        },
        [&](int s) {
          if (s == 0) y.tag = __builtin_amdgcn_readfirstlane(*(volatile LAS unsigned*)(smem + S1_FLG)) + 1u;
          if (s == 2) dma<false>(lane * 16, P.wphi + tn1 * 4096 + lw * 1024, lds0 + S1_WP + lw * 1024);
          if (((W1M >> s) & 1u) && lw == __builtin_popcount(W1M & ((1u << s) - 1u))) dma4<false>(min(lane, 55) * 128, P.w1 + bid * 28672 + lw * 7168, lds0 + S1_DUM);
          if (s == PF && lw == 0) dma<true>(frec, P.ws + WS_FLG, lds0 + S1_CSR);
          if (s == 15 && lw == 1) dma4<false>(lane * 128, P.wo + bid * 8192, lds0 + S1_DUM);
          if (s == PF + 3) {
            if (lw == 3) dma<true>(lane * 16, P.ws + WS_ST1 + tm1 * 1024, lds0 + S1_ST);
#pragma unroll
            for (int i = 0; i < 3; ++i)
              dma<true>((lane >> 4) * 12288 + (lane & 15) * 16, P.ws + WS_CSQ + lw * 49152 + i * 4096 + tn1 * 256, lds0 + S1_CSR + i * 4096 + lw * 1024);
          }
          if (s == PR) {
            if (lt < 192) {
              const LAS float* rawp = (const LAS float*)(smem + S1_CSR) + (lt >> 6) * 1024 + (lt & 63);
              float t = 0.f;
#pragma unroll
              for (int i = 0; i < 16; ++i) t += rawp[i * 64];
              ((LAS float*)(smem + S1_CSF))[lt] = t;
            }
            wait_lgkm0();
          }
        });
    }
    {
      Ld<C3> L; L.A = P.ws; L.W = P.wo + tn3 * 128; L.lw = lw; L.c = c3; L.kt0 = 0; L.init(y, lane, 0, 0, 0, 9);
      if (bid >= 224 && bid < 224 + NPAGE) {
        if (lw == 1) {
          const unsigned po = bid == 224 ? WS_PRB : (bid - 224) << 21;
          unsigned tmin[2] = {~0u, ~0u};
#pragma unroll 1
          for (int k = 0; k < NPROBE; ++k) {
            const unsigned long long t0 = __builtin_amdgcn_s_memtime();
            if (lane < 8) st16(y.r, (u32x4){0u, 0u, 0u, 0u}, lane * 16, po + (k & 1) * 8192);
            wait_vm<0>();
            const unsigned dt = (unsigned)(__builtin_amdgcn_s_memtime() - t0);
            if (k & 1) tmin[1] = min(tmin[1], dt); else tmin[0] = min(tmin[0], dt);
          }
          if (lane == 0) st4(y.r, (y.tag << 1) | (((unsigned)(tmin[1] < tmin[0]) ^ (unsigned)(bid >> 2)) & 1u), 0, WS_FLG + (FL_CS * 256 + bid) * 128 + 4);
        }
        bar_loaders(y, lw);
      }
      load_preW<C3>(L, lds0 + R3);
      if (lw >= 2 && bid < 224) {
        u32x4 v[16];
        colsum_load<16>(v, P.w1 + (size_t)((bid / 56) * 256 + (lw - 2) * 128) * (FF * 2) + (bid % 56) * 128, FF * 2, lane);
        colsum_finish<16>(v, y.r, WS_CS1 + (((bid / 56) * 2 + lw - 2) * FF + (bid % 56) * 64) * 4, lane);
      }
      publish(y, FL_CS, bid, lane);
      lds_wait(y, 24 + FL_KV, 4u);
      Gate G; G.off = (FL_KV * 256 + m16 * 16 + tn1) * 128; G.mask = 0; G.word = 8;
      const unsigned pbo = WS_FLG + (FL_CS * 256 + 224 + (lane & 31)) * 128 + 4;
      unsigned pbv = 0;
      kv_sum(y, G, smem, 2 * tn1 + 1, 1, lt, lw == 0, lw == 1, lane, [&](int) { if (lw == 0) pbv = __builtin_amdgcn_raw_buffer_load_b32(y.r, pbo, 0, 16); });
      if (lw == 0) {
        const unsigned long long t0 = __builtin_amdgcn_s_memrealtime();
        while (!__all(lane >= NPAGE || (pbv >> 1) == (y.tag & 0x7fffffffu))) {
          if (__builtin_amdgcn_s_memrealtime() - t0 > 4000000ull) { if (lane == 0) st4(y.r, y.tag, 0, WS_CTL + 128); break; }
          pbv = __builtin_amdgcn_raw_buffer_load_b32(y.r, pbo, 0, 16);
        }
        const unsigned raw = (unsigned)__ballot(pbv & 1u), odd = __builtin_popcount((raw ^ 0xaaaaaaaau) & 0x3fffffdu);
        *(volatile LAS unsigned*)(y.w + 13) = odd <= 6 ? 0xaaaaaaaau : odd >= 19 ? 0x55555555u : raw;
      }
      wait_lgkm0(); bar();
      y.pb = __builtin_amdgcn_readfirstlane(*(volatile LAS unsigned*)(y.w + 13));
      L.A += near(y, WS_O + tm3 * 524288, tm3); L.G.off = rec(y, FL_O, tm3 * 16 + m16);
      load_all<C3, true, 0u, 0u>(L, lds0 + R3, nop, nop);
    }
    {
      Ld<C4> L; L.A = P.ws + near(y, WS_H + tm1 * 524288, tm1); L.W = P.w1 + ch0 * 128; L.lw = lw; L.c = c1; L.kt0 = 0;
      L.init(y, lane, 128, par ? -64 : 384, (lane & 16) ? rec(y, FL_CS, (m16 >> 2) * 56 + base7 + 3 * par + (lane & 3)) : rec(y, FL_H, tm1 * 16 + m16), 10);
      load_preW<C4>(L, lds0 + R4, 0, E4);
      lds_wait(y, 24 + FL_H, 4u);
      load_preW<C4>(L, lds0 + R4, E4);
      load_all<C4, true, 0u, 0xa000u | (W2AT == 0 ? 0x550u : W2AT > 0 ? 1u << (W2AT & 31) : 0u)>(L, lds0 + R4, nop,
        [&](int s) {
          if (W2AT == 0 ? (s < 12 && lw == ((s >> 1) & 3)) : s == W2AT) dma4<false>(min(lane, 55) * 128, P.w2 + bid * 28672 + lw * 7168, lds0 + S4_DUM);
          if (s == 13) {
            gate(y, L.G, ~0u, lw == 0, lane);
#pragma unroll
            for (int i = 0; i < 4; ++i) dma<true>(lane * 16, P.ws + near(y, WS_ST2 + tm1 * 32768, tm1) + (lw >> 1) * 16384 + ((lw & 1) * 4 + i) * 1024, lds0 + S4_STR + (lw * 4 + i) * 1024);
#pragma unroll
            for (int i = 0; i < 2; ++i) dma<true>(lane * 16, P.ws + WS_CS1 + ((lw * 2 + i) * FF + (base7 + 3 * par) * 64) * 4, lds0 + S4_CSR + (lw * 2 + i) * 1024);
          }
          if (s == 15) {
            const LAS float* rawp = (const LAS float*)(smem + S4_CSR) + lt;
            float t = 0.f;
#pragma unroll
            for (int i = 0; i < 8; ++i) t += rawp[i * 256];
            ((LAS float*)(smem + S4_CSF))[lt] = t;
            wait_lgkm0();
          }
          if (s == 15 && lw < 2) {
            f32x2 st[16]; float mu = 0.f, m2 = 0.f;
#pragma unroll
            for (int i = 0; i < 16; ++i) { st[i] = *(const LAS f32x2*)(smem + S4_STR + i * 1024 + lt * 8); mu += st[i][0]; }
            mu *= (1.f / 16.f);
#pragma unroll
            for (int i = 0; i < 16; ++i) { const float d = st[i][0] - mu; m2 += st[i][1] + 64.f * d * d; }
            *(LAS f32x2*)(smem + S4_STF + lt * 8) = (f32x2){mu, __builtin_amdgcn_rsqf(m2 * (1.f / D) + 1e-5f)};
            wait_lgkm0();
          }
        });
    }
    {
      Ld<C5> L; L.A = P.ws + near(y, WS_F + tm5 * 2097152, tm5); L.W = P.w2 + tn5 * 256; L.lw = lw; L.c = c5; L.kt0 = 28 * kh; L.init(y, lane, 128, 0, rec(y, FL_F, tm5 * 16 + 8 * kh + ((lane >> 1) & 7)) + (lane & 1) * 4, 11);
      load_preW<C5>(L, lds0 + R5);

      load_all<C5, true, 0u, 0u>(L, lds0 + R5, nop, nop);
    }
    return;
  }

  const int wm = wid >> 1, wn = wid & 1;
  if (wid == 0 && lane < 32) y.w[lane] = 0u;
  {
    f32x4 acc[24];
    Adr ad;
    ad.v[0] = lds0 + R1 + (wm * 64 + m16) * 128 + ((g ^ (m16 >> 1)) << 4); ad.v[1] = lds0 + R1 + (wm * 64 + m16) * 128 + ((g ^ (m16 >> 1) ^ 4) << 4);
    ad.v[2] = lds0 + R1 + 16384 + wlane(lane, 2 * wn); ad.v[3] = lds0 + R1 + 16384 + wlane(lane, 2 * wn + 1); ad.v[4] = 0; ad.v[5] = 0;
    u32x4 px[4], pw[8];
    const int prow = bid * 8 + wid * 2 + (lane >> 5);
    consume<C1, 1, RPG1, PH0, PH1, PH2>(acc, ad, y.w, [&](int s) {
      if (s < 0) {
        y.tag = __builtin_amdgcn_readfirstlane(*(volatile LAS unsigned*)(smem + S1_FLG)) + 1u;
      }
      if (s == 0) {
#pragma unroll
        for (int i = 0; i < 4; ++i) px[i] = *(const u32x4*)(P.x + (size_t)prow * 2048 + ((lane & 31) + 32 * i) * 16);
        colsum_load<8>(pw, P.wqkv + (size_t)((min(bid, 191) / 48) * 256 + wid * 64) * (3 * D * 2) + (bid % 48) * 128, 3 * D * 2, lane);
      }
      if (s == 1) {
#pragma unroll
        for (int i = 0; i < 4; ++i) asm volatile("" : "+v"(px[i]));
#pragma unroll
        for (int i = 0; i < 8; ++i) asm volatile("" : "+v"(pw[i]));
        const unsigned one2 = opaque(0x3f803f80u);
        float sm = 0.f, sq = 0.f;
#pragma unroll
        for (int i = 0; i < 4; ++i)
#pragma unroll
          for (int j = 0; j < 4; ++j) { sm = dot2(px[i][j], one2, sm); sq = dot2(px[i][j], px[i][j], sq); }
        const float mean = xsum16(row16_sum(sm)) * (1.f / D);
        const float var = fmaxf(xsum16(row16_sum(sq)) * (1.f / D) - mean * mean, 0.f);
        if ((lane & 31) == 0) st8(y.r, (u32x2){bu(mean), bu(__builtin_amdgcn_rsqf(var + 1e-5f))}, prow * 8, WS_ST1);
        if (bid < 192) colsum_finish<8>(pw, y.r, WS_CSQ + (((bid / 48) * 4 + wid) * 3 * D + (bid % 48) * 64) * 4, lane);
      }
      if (s == 2) { wait_vm<0>(); if (lane == 0) st4(y.r, y.tag, 0, WS_FLG + (FL_P0 * 256 + bid) * 128 + wid * 4); }
    });
    const int head = 2 * tn1 + wn;
    f32x4 cs[4]; float csv[2];
#pragma unroll
    for (int n = 0; n < 4; ++n) cs[n] = *(const LAS f32x4*)(smem + S1_CSF + ((n >> 1) * 64 + wn * 32 + (n & 1) * 16 + 4 * g) * 4);
#pragma unroll
    for (int n = 0; n < 2; ++n) csv[n] = *(const LAS float*)(smem + S1_CSF + (128 + wn * 32 + n * 16 + m16) * 4);
    u32x4 wp[2];
#pragma unroll
    for (int F = 0; F < 2; ++F) { lds_ptr a0 = smem + S1_WP + wn * 2048 + (4 * g + (m16 >> 2)) * 64 + F * 32 + (lane & 3) * 8; wp[F] = lds_tr2(a0, a0 + 1024); }
    u32x4 qb[4], vb[2][2];
    f32x4 p[4][2];
    const f32x4 zero = {0.f, 0.f, 0.f, 0.f};
    {
      f32x4 vv[4][2];
#pragma unroll
      for (int m = 0; m < 4; ++m) {
        const f32x2 st = *(const LAS f32x2*)(smem + S1_ST + (wm * 64 + m * 16 + m16) * 8);
        const f32x4 s0 = *(const LAS f32x4*)(smem + S1_ST + (wm * 64 + m * 16 + 4 * g) * 8), s1 = *(const LAS f32x4*)(smem + S1_ST + (wm * 64 + m * 16 + 4 * g) * 8 + 16);
        const f32x4 mu = {s0[0], s0[2], s1[0], s1[2]}, rs = {s0[1], s0[3], s1[1], s1[3]};
        qb[m] = pack8((acc[m * 6] - cs[0] * st[0]) * st[1], (acc[m * 6 + 1] - cs[1] * st[0]) * st[1]);
        const u32x4 kb = pack8((acc[m * 6 + 2] - cs[2] * st[0]) * st[1], (acc[m * 6 + 3] - cs[3] * st[0]) * st[1]);
        p[m][0] = mfma(wp[0], kb, zero); p[m][1] = mfma(wp[1], kb, zero);
        vv[m][0] = (acc[m * 6 + 4] - mu * csv[0]) * rs; vv[m][1] = (acc[m * 6 + 5] - mu * csv[1]) * rs;
      }
#pragma unroll
      for (int u = 0; u < 2; ++u) { vb[u][0] = pack8(vv[2 * u][0], vv[2 * u + 1][0]); vb[u][1] = pack8(vv[2 * u][1], vv[2 * u + 1][1]); }
    }
    u32x4 pp[4], pn[4];
    dual_softmax4(p, pp, pn);
    {
      lds_ptr own = smem + R3 + wid * C3::STAGE, oth = smem + R3 + (wid ^ 2) * C3::STAGE;
#pragma unroll
      for (int m = 0; m < 4; ++m) {
        lds_ptr w = own + (16 * m + m16) * 136 + 8 * g;
        *(LAS u32x2*)(w) = (u32x2){pp[m][0], pp[m][1]}; *(LAS u32x2*)(w + 32) = (u32x2){pp[m][2], pp[m][3]};
        *(LAS u32x2*)(w + 64) = (u32x2){pn[m][0], pn[m][1]}; *(LAS u32x2*)(w + 96) = (u32x2){pn[m][2], pn[m][3]};
      }
#pragma unroll
      for (int i = 0; i < 4; ++i) *(LAS u32x4*)(own + 8704 + i * 1024 + lane * 16) = vb[i >> 1][i & 1];
      wait_lgkm0();
      volatile LAS unsigned* xf = y.w + 16;
      xf[wid] = 1u;
      f32x4 kv[2][2] = {{zero, zero}, {zero, zero}}, z[2] = {zero, zero};
      const u32x4 ones = {0x3f803f80u, 0x3f803f80u, 0x3f803f80u, 0x3f803f80u};
      const unsigned ro = (4 * g + (m16 >> 2)) * 136 + wm * 64 + (lane & 3) * 8;
#pragma unroll
      for (int u = 0; u < 2; ++u)
#pragma unroll
        for (int F = 0; F < 2; ++F) {
          const u32x4 pa = lds_tr2(own + ro + u * 32 * 136 + F * 32, own + ro + u * 32 * 136 + F * 32 + 16 * 136);
          kv[F][0] = mfma(pa, vb[u][0], kv[F][0]); kv[F][1] = mfma(pa, vb[u][1], kv[F][1]); z[F] = mfma(pa, ones, z[F]);
        }
      while (xf[wid ^ 2] == 0u) __builtin_amdgcn_s_sleep(0);
      asm volatile("" ::: "memory");
#pragma unroll
      for (int u = 0; u < 2; ++u) {
        const u32x4 v0 = lds128(oth + 8704 + u * 2048 + lane * 16), v1 = lds128(oth + 8704 + u * 2048 + 1024 + lane * 16);
#pragma unroll
        for (int F = 0; F < 2; ++F) {
          const u32x4 pa = lds_tr2(oth + ro + u * 32 * 136 + F * 32, oth + ro + u * 32 * 136 + F * 32 + 16 * 136);
          kv[F][0] = mfma(pa, v0, kv[F][0]); kv[F][1] = mfma(pa, v1, kv[F][1]); z[F] = mfma(pa, ones, z[F]);
        }
      }
      const unsigned kd = WS_KVP + (head * 16 + tm1) * 4096 + wm * 2048;
      st16(y.r, pack8(kv[0][0], kv[1][0]), lane * 16, kd);
      st16(y.r, pack8(kv[0][1], kv[1][1]), lane * 16, kd + 1024);
      if (m16 == 0) {
        st16(y.r, __builtin_bit_cast(u32x4, z[0]), g * 32, WS_ZP + (head * 16 + tm1) * 256 + wm * 128);
        st16(y.r, __builtin_bit_cast(u32x4, z[1]), g * 32 + 16, WS_ZP + (head * 16 + tm1) * 256 + wm * 128);
      }
      publish(y, FL_KV, tm1 * 16 + tn1, lane);
    }
#pragma unroll
    for (int m = 0; m < 4; ++m) { p[m][0] = mfma(wp[0], qb[m], zero); p[m][1] = mfma(wp[1], qb[m], zero); }
    dual_softmax4(p, pp, pn);
    { Gate G; G.off = 0; G.mask = 0; G.word = 8; kv_sum(y, G, smem, 2 * tn1, 0, threadIdx.x, false, wid == 0, lane, [](int) {}); }
    wait_lgkm0(); bar();
    y.pb = __builtin_amdgcn_readfirstlane(*(volatile LAS unsigned*)(y.w + 13));
    u32x4 ka[2][2], za[2];
#pragma unroll
    for (int ks = 0; ks < 2; ++ks) {
      ka[ks][0] = lds128(smem + S1_KV + wn * 4096 + ks * 2048 + lane * 16); ka[ks][1] = lds128(smem + S1_KV + wn * 4096 + ks * 2048 + 1024 + lane * 16);
      za[ks] = lds128(smem + S1_KV + 8192 + wn * 128 + ks * 64 + g * 16);
    }
#pragma unroll
    for (int m = 0; m < 4; ++m) {
      f32x4 n0 = mfma(ka[0][0], pp[m], zero), n1 = mfma(ka[0][1], pp[m], zero), dn = mfma(za[0], pp[m], zero);
      n0 = mfma(ka[1][0], pn[m], n0); n1 = mfma(ka[1][1], pn[m], n1); dn = mfma(za[1], pn[m], dn);
      const float rd = __builtin_amdgcn_rcpf(dn[0] + 1e-6f);
      st16(y.r, pack8(n0 * rd, n1 * rd), lane * 16, near(y, WS_O + (tm1 * 16 + tn1) * 32768, tm1) + wm * 16384 + (m * 2 + wn) * 1024);
    }
    publish(y, FL_O, tm1 * 16 + tn1, lane);
  }
  {
    u32x4 xr[2][2];
#pragma unroll
    for (int m = 0; m < 2; ++m)
#pragma unroll
      for (int Q = 0; Q < 2; ++Q)
        xr[m][Q] = *(const u32x4*)(P.x + (size_t)(tm3 * 128 + wid * 32 + m * 16 + m16) * 2048 + tn3 * 128 + Q * 64 + (g & 1) * 32 + (g >> 1) * 16);
    f32x4 acc[8];
    Adr ad; ad.v[0] = lds0 + R3 + wid * 4096 + lane * 16; ad.v[1] = 0;
#pragma unroll
    for (int n = 0; n < 4; ++n) ad.v[2 + n] = lds0 + R3 + 16384 + wlane(lane, n);
    consume_s<C3, 3, 2>(acc, ad, y.w, [](int) {});
#pragma unroll
    for (int m = 0; m < 2; ++m) {
      float hv[16];
#pragma unroll
      for (int Q = 0; Q < 2; ++Q) {
        auto r0 = __builtin_amdgcn_permlane16_swap(xr[m][Q][0], xr[m][Q][2], false, false);
        auto r1 = __builtin_amdgcn_permlane16_swap(xr[m][Q][1], xr[m][Q][3], false, false);
        const f32x4 a = acc[m * 4 + 2 * Q], b = acc[m * 4 + 2 * Q + 1];
        const u32x4 hp = {pk2(a[0] + bflo(r0[0]), a[1] + bfhi(r0[0])), pk2(a[2] + bflo(r1[0]), a[3] + bfhi(r1[0])),
                          pk2(b[0] + bflo(r0[1]), b[1] + bfhi(r0[1])), pk2(b[2] + bflo(r1[1]), b[3] + bfhi(r1[1]))};
        st16(y.r, hp, lane * 16, near(y, WS_H + (tm3 * 16 + tn3) * 32768, tm3) + wm * 16384 + wn * 4096 + (m * 2 + Q) * 1024);
#pragma unroll
        for (int j = 0; j < 4; ++j) { hv[Q * 8 + 2 * j] = bflo(hp[j]); hv[Q * 8 + 2 * j + 1] = bfhi(hp[j]); }
      }
      float s = 0.f;
#pragma unroll
      for (int j = 0; j < 16; ++j) s += hv[j];
      s = xsum32(xsum16(s)) * (1.f / 64.f);
      float q = 0.f;
#pragma unroll
      for (int j = 0; j < 16; ++j) { const float d = hv[j] - s; q += d * d; }
      q = xsum32(xsum16(q));
      if (g == 0) st8(y.r, (u32x2){bu(s), bu(q)}, m16 * 8, near(y, WS_ST2 + tm3 * 32768, tm3) + (tn3 >> 3) * 16384 + (tn3 & 7) * 1024 + (wid * 32 + m * 16) * 8);
    }
    publish(y, FL_H, tm3 * 16 + tn3, lane);
  }
  {
    f32x4 acc[28];
    Adr ad;
    ad.v[0] = lds0 + R4 + wm * 8192 + wn * 4096 + lane * 16; ad.v[1] = lds0 + R4 + wm * 8192 + (wn ^ 1) * 4096 + lane * 16;
    ad.v[2] = lds0 + R4 + 16384 + wlane(lane, wn); ad.v[3] = lds0 + R4 + 16384 + wlane(lane, 2 + wn);
    ad.v[4] = lds0 + R4 + 16384 + 6144 + hlane(lane, 0); ad.v[5] = lds0 + R4 + 16384 + 6144 + hlane(lane, 1);
    consume<C4, 4, RPG4, 0, 0, 0>(acc, ad, y.w, [](int) {});
    f32x4 cs[8];
#pragma unroll
    for (int n = 0; n < 6; ++n) cs[n] = *(const LAS f32x4*)(smem + S4_CSF + ((par + (n >> 1)) * 64 + (2 * (n & 1) + wn) * 16 + 4 * g) * 4);
#pragma unroll
    for (int n = 0; n < 2; ++n) cs[6 + n] = *(const LAS f32x4*)(smem + S4_CSF + ((par ? 32 : 192) + n * 16 + 4 * g) * 4);
    const f32x4 zero = {0.f, 0.f, 0.f, 0.f};
    f32x2 st[4];
#pragma unroll
    for (int m = 0; m < 4; ++m) st[m] = *(const LAS f32x2*)(smem + S4_STF + ((wm * 4 + ((m + 2 * wn) & 3)) * 16 + m16) * 8);
#pragma unroll
    for (int i = 0; i < 4; ++i) {
#pragma unroll
      for (int m = 0; m < (i < 3 ? 4 : 2); ++m) {
        const unsigned fd = near(y, WS_F + tm1 * 2097152, tm1) + wm * 16384 + ((m + 2 * wn) & 3) * 2048 + (i < 3 ? (ch0 + i) * 32768 + wn * 1024 : (base7 + 3) * 32768 + par * 1024);
        const int a = i < 3 ? m * 6 + 2 * i : 24 + 2 * m;
        st16(y.r, pack8(__builtin_elementwise_max((acc[a] - cs[2 * i] * st[m][0]) * st[m][1], zero),
                        __builtin_elementwise_max((acc[a + 1] - cs[2 * i + 1] * st[m][0]) * st[m][1], zero)), lane * 16, fd);
        SB();
      }
      if (i == FAT) {
        wait_vm<FAT == 3 ? 10 : 4 * FAT>();
        if (lane == 0 && __hip_atomic_fetch_add(y.w + FL_F, 1u, __ATOMIC_RELAXED, __HIP_MEMORY_SCOPE_WORKGROUP) == 3u) st4(y.r, y.tag, 0, WS_FLG + rec(y, FL_F, tm1 * 16 + tn1));
        SB();
      }
    }
  }
  {
    f32x4 acc[16];
    Adr ad;
    ad.v[0] = lds0 + R5 + wm * 8192 + kh * 4096 + lane * 16; ad.v[1] = lds0 + R5 + wm * 8192 + (kh ^ 1) * 4096 + lane * 16;
#pragma unroll
    for (int n = 0; n < 4; ++n) ad.v[2 + n] = lds0 + R5 + 16384 + wn * 2048 + wlane(lane, n);
    u32x4 hr[2][2];
    consume_s<C5, 5, 1>(acc, ad, y.w, [&](int s) {
      if (s < 0) {
        wait_vm<0>();
        if (lane == 0 && __hip_atomic_fetch_add(y.w + 20, 1u, __ATOMIC_RELAXED, __HIP_MEMORY_SCOPE_WORKGROUP) == 3u) st4(y.r, y.tag, 0, WS_FLG + rec(y, FL_F, tm1 * 16 + tn1) + 4);
#pragma unroll
        for (int m = 0; m < 2; ++m)
#pragma unroll
          for (int Q = 0; Q < 2; ++Q) hr[m][Q] = ld16(y.r, lane * 16, near(y, WS_H + (tm5 * 16 + 2 * tn5 + wn) * 32768, tm5) + wm * 16384 + ((2 * kh + m) * 2 + Q) * 1024);
      }
    });
    const int tile = tm5 * 8 + tn5;
#pragma unroll
    for (int i = 0; i < 4; ++i) st16(y.r, pack8(acc[8 + 2 * i], acc[9 + 2 * i]), lane * 16, near(y, WS_O + (tile * 2 + kh) * 32768, tm5) + wm * 16384 + wn * 4096 + i * 1024);
    wait_vm<0>();
    if (lane == 0) st4(y.r, y.tag, 0, WS_FLG + rec(y, FL_X + wm, tile * 2 + kh) + wn * 64);
    poll(y, rec(y, FL_X + wm, tile * 2 + (kh ^ 1)) + wn * 64, 1, lane);
    const rsrc_t ro = __builtin_amdgcn_make_buffer_rsrc(P.out, 0, 2048 * 2048, 0x00020000);
    u32x4 xv[4];
#pragma unroll
    for (int i = 0; i < 4; ++i) xv[i] = ld16(y.r, lane * 16, near(y, WS_O + (tile * 2 + (kh ^ 1)) * 32768, tm5) + wm * 16384 + wn * 4096 + i * 1024);
#pragma unroll
    for (int m = 0; m < 2; ++m)
#pragma unroll
      for (int Q = 0; Q < 2; ++Q) {
        const u32x4 xx = xv[m * 2 + Q];
        const f32x4 a = acc[m * 4 + 2 * Q] + (f32x4){bflo(xx[0]), bfhi(xx[0]), bflo(xx[1]), bfhi(xx[1])}, b = acc[m * 4 + 2 * Q + 1] + (f32x4){bflo(xx[2]), bfhi(xx[2]), bflo(xx[3]), bfhi(xx[3])};
        const u32x4 hh = hr[m][Q];
        const unsigned a0 = pk2(a[0] + bflo(hh[0]), a[1] + bfhi(hh[0])), a1 = pk2(a[2] + bflo(hh[1]), a[3] + bfhi(hh[1]));
        const unsigned b0 = pk2(b[0] + bflo(hh[2]), b[1] + bfhi(hh[2])), b1 = pk2(b[2] + bflo(hh[3]), b[3] + bfhi(hh[3]));
        auto r0 = __builtin_amdgcn_permlane16_swap(a0, b0, false, false);
        auto r1 = __builtin_amdgcn_permlane16_swap(a1, b1, false, false);
        __builtin_amdgcn_raw_buffer_store_b128((u32x4){r0[0], r1[0], r0[1], r1[1]}, ro, (tm5 * 128 + wm * 64 + (2 * kh + m) * 16 + m16) * 2048 + tn5 * 256 + wn * 128 + Q * 64 + (g & 1) * 32 + (g >> 1) * 16, 0, 16);
      }
    if (bid == 0 && threadIdx.x == 0) st4(y.r, y.tag, 0, WS_CTL);
  }
}

extern "C" void kernel_launch(void* const* d_in, const int* in_sizes, int n_in, void* d_out, int out_size, void* d_ws, size_t ws_size,
                              hipStream_t stream) {
  Params p;
  p.x = (const char*)d_in[0]; p.wqkv = (const char*)d_in[1]; p.wphi = (const char*)d_in[2]; p.wo = (const char*)d_in[3];
  p.w1 = (const char*)d_in[5]; p.w2 = (const char*)d_in[6]; p.ws = (char*)(((size_t)d_ws + 0x1fffff) & ~(size_t)0x1fffff); p.out = (char*)d_out;
  (void)hipFuncSetAttribute((const void*)fused, hipFuncAttributeMaxDynamicSharedMemorySize, LDS_BYTES);
  fused<<<256, 512, LDS_BYTES, stream>>>(p);
}
```

```cpp
#include <hip/hip_runtime.h>
#include <hip/hip_bf16.h>

#define LAS __attribute__((address_space(3)))
#define DI __device__ __forceinline__
typedef __bf16 bf16x8 __attribute__((ext_vector_type(8)));
typedef __bf16 bf16x2 __attribute__((ext_vector_type(2)));
typedef short s16x4 __attribute__((ext_vector_type(4)));
typedef float f32x2 __attribute__((ext_vector_type(2)));
typedef float f32x4 __attribute__((ext_vector_type(4)));
typedef unsigned u32x2 __attribute__((ext_vector_type(2)));
typedef unsigned u32x4 __attribute__((ext_vector_type(4)));
typedef LAS char* lds_ptr;
typedef __amdgpu_buffer_rsrc_t rsrc_t;

constexpr int D = 1024, FF = 3584;
constexpr unsigned WS_CTL = 0;
constexpr unsigned WS_FLG = 4096;
constexpr unsigned WS_ST1 = 320u << 10;
constexpr unsigned WS_CSQ = 384u << 10;
constexpr unsigned WS_CS1 = 576u << 10;
constexpr unsigned WS_ZP = 704u << 10;
constexpr unsigned WS_ST2 = 1024u << 10;
constexpr unsigned WS_FL2 = 1536u << 10;
constexpr unsigned WS_PRB = 1920u << 10;
constexpr unsigned WS_KVP = 2u << 20;
constexpr unsigned WS_O = 4u << 20;
constexpr unsigned WS_H = 12u << 20;
constexpr unsigned WS_F = 20u << 20;
constexpr unsigned WS_END = 52u << 20;
constexpr int NPAGE = 26;
enum { FL_P0 = 0, FL_KV = 1, FL_CS = 2, FL_O = 3, FL_H = 4, FL_F = 5, FL_X = 6   };

#define TS(I) do { if ((I) == 0) tx = __builtin_amdgcn_s_memrealtime(); } while (0)
#define TS_FLUSH() do {} while (0)

#define DBG(k) nullptr
#define CLK(v)
#define DBG4 nullptr
struct Params { const char* x; const char* wqkv; const char* wphi; const char* wo; const char* w1; const char* w2; char* ws; char* out; };

DI unsigned pk2(float a, float b) { f32x2 v = {a, b}; return __builtin_bit_cast(unsigned, __builtin_convertvector(v, bf16x2)); }
DI float bflo(unsigned u) { return __uint_as_float(u << 16); }
DI float bfhi(unsigned u) { return __uint_as_float(u & 0xffff0000u); }
DI u32x4 pack8(const f32x4& a, const f32x4& b) { return (u32x4){pk2(a[0], a[1]), pk2(a[2], a[3]), pk2(b[0], b[1]), pk2(b[2], b[3])}; }
DI bf16x8 asbf(const u32x4& v) { return __builtin_bit_cast(bf16x8, v); }
#define SB() __builtin_amdgcn_sched_barrier(0)
DI void bar() { asm volatile("" ::: "memory"); __builtin_amdgcn_s_barrier(); asm volatile("" ::: "memory"); }
DI void wait_lgkm0() { __builtin_amdgcn_s_waitcnt(0xC07F); asm volatile("" ::: "memory"); }
template <int N> DI void wait_vm() { asm volatile("s_waitcnt vmcnt(%0)" ::"i"(N) : "memory"); }
template <bool SC1> DI void dma(unsigned voff, const char* sbase, unsigned lds) {
  if (SC1) asm volatile("s_mov_b32 m0, %2\n\ts_nop 3\n\tglobal_load_lds_dwordx4 %0, %1 sc1" ::"v"(voff), "s"(sbase), "s"(lds) : "memory");
  else asm volatile("s_mov_b32 m0, %2\n\ts_nop 3\n\tglobal_load_lds_dwordx4 %0, %1" ::"v"(voff), "s"(sbase), "s"(lds) : "memory");
}
template <bool SC1> DI void dma4(unsigned voff, const char* sbase, unsigned lds) {
  if (SC1) asm volatile("s_mov_b32 m0, %2\n\ts_nop 3\n\tglobal_load_lds_dword %0, %1 sc1" ::"v"(voff), "s"(sbase), "s"(lds) : "memory");
  else asm volatile("s_mov_b32 m0, %2\n\ts_nop 3\n\tglobal_load_lds_dword %0, %1" ::"v"(voff), "s"(sbase), "s"(lds) : "memory");
}
DI u32x4 lds128(lds_ptr p) { return *(const LAS u32x4*)p; }
DI u32x4 lds_tr2(lds_ptr p0, lds_ptr p1) {
  s16x4 a = __builtin_amdgcn_ds_read_tr16_b64_v4i16((LAS s16x4*)p0);
  s16x4 b = __builtin_amdgcn_ds_read_tr16_b64_v4i16((LAS s16x4*)p1);
  return __builtin_bit_cast(u32x4, __builtin_shufflevector(a, b, 0, 1, 2, 3, 4, 5, 6, 7));
}
DI u32x2 lds_tr(lds_ptr p) { return __builtin_bit_cast(u32x2, __builtin_amdgcn_ds_read_tr16_b64_v4i16((LAS s16x4*)p)); }
DI f32x4 mfma(const u32x4& a, const u32x4& b, const f32x4& c) { return __builtin_amdgcn_mfma_f32_16x16x32_bf16(asbf(a), asbf(b), c, 0, 0, 0); }
DI void mfma_ip(f32x4& acc, const u32x4& a, const u32x4& b) { asm volatile("v_mfma_f32_16x16x32_bf16 %0, %1, %2, %0" : "+v"(acc) : "v"(a), "v"(b)); }
DI u32x4 ld16(rsrc_t r, unsigned voff, unsigned soff) { return __builtin_amdgcn_raw_buffer_load_b128(r, voff, soff, 16); }
DI void st16(rsrc_t r, const u32x4& v, unsigned voff, unsigned soff) { __builtin_amdgcn_raw_buffer_store_b128(v, r, voff + soff, 0, 16);
}
DI void st8(rsrc_t r, const u32x2& v, unsigned voff, unsigned soff) { __builtin_amdgcn_raw_buffer_store_b64(v, r, voff, soff, 16); }
DI void st4(rsrc_t r, unsigned v, unsigned voff, unsigned soff) { __builtin_amdgcn_raw_buffer_store_b32(v, r, voff, soff, 16); }
DI float fb(unsigned u) { return __uint_as_float(u); }
DI unsigned bu(float f) { return __float_as_uint(f); }
DI float xsum16(float x) { auto r = __builtin_amdgcn_permlane16_swap(bu(x), bu(x), false, false); return fb(r[0]) + fb(r[1]); }
DI float xsum32(float x) { auto r = __builtin_amdgcn_permlane32_swap(bu(x), bu(x), false, false); return fb(r[0]) + fb(r[1]); }
template <int CTRL> DI float dpp(float x) { return __builtin_bit_cast(float, __builtin_amdgcn_update_dpp(0, __builtin_bit_cast(int, x), CTRL, 0xf, 0xf, true)); }
DI float row16_sum(float x) { x += dpp<0xB1>(x); x += dpp<0x4E>(x); x += dpp<0x141>(x); x += dpp<0x140>(x); return x; }

struct Sync { rsrc_t r; unsigned tag, pb; LAS unsigned* w; };
DI unsigned near(const Sync& y, unsigned off, int tm) { return off + ((((y.pb >> (off >> 21)) ^ (tm >> 3)) & 1u) << 13); }
DI unsigned rec(const Sync& y, int phase, int idx) {
  return phase < FL_O ? (phase * 256 + idx) * 128 : near(y, WS_FL2 + (phase - FL_O) * 65536 + (idx >> 6) * 16384 + (idx & 63) * 128, idx >> 4) - WS_FLG;
}
DI void publish(const Sync& y, int phase, int idx, int lane) {
  if (lane == 0) __hip_atomic_fetch_add(y.w + 24 + phase, 1u, __ATOMIC_RELAXED, __HIP_MEMORY_SCOPE_WORKGROUP);
  wait_vm<0>();
  if (lane == 0 && __hip_atomic_fetch_add(y.w + phase, 1u, __ATOMIC_RELAXED, __HIP_MEMORY_SCOPE_WORKGROUP) == 3u) st4(y.r, y.tag, 0, WS_FLG + rec(y, phase, idx));
}
DI void poll(const Sync& y, unsigned off, int n, int lane) {
  const unsigned long long t0 = __builtin_amdgcn_s_memrealtime();
  for (unsigned it = 0;; ++it) {
    const unsigned v = __builtin_amdgcn_raw_buffer_load_b32(y.r, WS_FLG + off, 0, 16);
    if (__all(lane >= n || v == y.tag)) break;
    if ((it & 63) == 63) {
      if (__builtin_amdgcn_readfirstlane(__builtin_amdgcn_raw_buffer_load_b32(y.r, 0, WS_CTL + 128, 16)) == y.tag) break;
      if (__builtin_amdgcn_s_memrealtime() - t0 > 4000000ull) { if (lane == 0) st4(y.r, y.tag, 0, WS_CTL + 128); break; }
    }
    __builtin_amdgcn_s_sleep(2);
  }
  asm volatile("" ::: "memory");
}
DI void lds_wait(const Sync& y, int word, unsigned v);
DI void bar_loaders(const Sync& y, int lw) { if (lw == 1) *(volatile LAS unsigned*)(y.w + 14) = 1u; else lds_wait(y, 14, 1u); }
DI void lds_wait(const Sync& y, int word, unsigned v) {
  volatile LAS unsigned* w = y.w + word;
  while (*w < v) __builtin_amdgcn_s_sleep(1);
  asm volatile("" ::: "memory");
}
struct Gate { unsigned off, mask; int word; };
DI void gate_slow(const Sync& y, Gate& G, unsigned need, bool poller, int lane, int cnt = 0) {
  volatile LAS unsigned* w = y.w + G.word;
  if (poller) {
    const unsigned long long t0 = __builtin_amdgcn_s_memrealtime();
    for (unsigned it = 0;; ++it) {
      G.mask = (unsigned)__ballot(__builtin_amdgcn_raw_buffer_load_b32(y.r, WS_FLG + G.off, 0, 16) == y.tag);
      if ((it & 63) == 63) {
        const bool late = __builtin_amdgcn_s_memrealtime() - t0 > 4000000ull;
        if (late && lane == 0) st4(y.r, y.tag, 0, WS_CTL + 128);
        if (late || __builtin_amdgcn_readfirstlane(__builtin_amdgcn_raw_buffer_load_b32(y.r, 0, WS_CTL + 128, 16)) == y.tag) G.mask = ~0u;
      }
      *w = G.mask;
      if ((G.mask & need) == need && __builtin_popcount(G.mask & 0xffffu) >= cnt) break;
      __builtin_amdgcn_s_sleep(1);
    }
  } else {
    for (;;) { G.mask = __builtin_amdgcn_readfirstlane(*w); if ((G.mask & need) == need && __builtin_popcount(G.mask & 0xffffu) >= cnt) break; __builtin_amdgcn_s_sleep(1); }
  }
  asm volatile("" ::: "memory");
}
DI void gate(const Sync& y, Gate& G, unsigned need, bool poller, int lane) { if (__builtin_expect((G.mask & need) != need, 0)) gate_slow(y, G, need, poller, lane); }

template <int NK, bool SPREAD> DI int kmap(int t, int c) {
  const int u = t - 1, q = u & 3, b4 = u & ~3, body = (int)(u < NK - 4);
  const int lead = ((int)(q == c) & body & (int)(u >= 0)) | (int)(SPREAD && t == 0), head = SPREAD ? 0 : -(int)(u < 4);
  const int non = ((b4 + ((c + 1 + q - ((int)(q > c) & body)) & 3)) & ~head) | ((t - (int)(u > c)) & head);
  return non ^ (((b4 + 4 + c) ^ non) & -lead);
}
#define LAG3 1
#define LAG5 1
enum { ORD_NAT = 0, ORD_PAIR = 1, ORD_F = 2 };
template <int NSUB_, bool HALF_, int NSLOT_, int NK_, int ORD_, int WSTRIDE_, bool XROW_, int LAG_> struct Cfg {
  static constexpr int NSUB = NSUB_, NSLOT = NSLOT_, NK = NK_, ORD = ORD_, WSTRIDE = WSTRIDE_;
  static constexpr bool HALF = HALF_, XROW = XROW_;
  static constexpr int LAG = LAG_;
  static constexpr int STAGE = 16384 + NSUB * 8192 + (HALF ? 4096 : 0), PW = 4 + 2 * NSUB + (HALF ? 1 : 0), RING = STAGE * NSLOT;
  static constexpr int WB = NSUB * 2048 + (HALF ? 1024 : 0);
};
typedef Cfg<3, false, 3, 16, ORD_NAT, 3 * D * 2, true, 0> C1;
typedef Cfg<1, false, 5, 16, ORD_PAIR, D * 2, false, LAG3> C3;
typedef Cfg<3, true, 3, 16, ORD_PAIR, FF * 2, false, 0> C4;
typedef Cfg<2, false, 4, 28, ORD_F, D * 2, false, LAG5> C5;

#define GLDS "global_load_lds_dwordx4 "
template <class C> struct Ld {
  const char* A; const char* W; int lw, c, kt0; unsigned vA[4], vW[7];
  const char* sA; const char* sW; unsigned need;
  Gate G; const Sync* y; int lane_;
  static constexpr int C0 = C::WB > 4096 ? 4096 : 0;
  DI void init(const Sync& y_, int lane, int substep, int halfrel, unsigned goff, int gword) {
    y = &y_; lane_ = lane; G.off = goff; G.mask = 0; G.word = gword;
#pragma unroll
    for (int i = 0; i < 4; ++i) vA[i] = C::XROW ? (lane >> 3) * 2048 + ((((lane & 7) ^ (lane >> 4)) << 4) ^ ((i & 1) * 64)) + i * (16384 - 1024) : lane * 16;
    const int rl = (C::ORD == ORD_NAT) ? 8 * (lane >> 5) + ((lane >> 3) & 3) : (lane >> 3);
    const unsigned w0 = rl * C::WSTRIDE + (((lane & 7) ^ (((lane >> 4) & 3) << 1)) << 4) + 4096;
#pragma unroll
    for (int q = 0; q < 2 * C::NSUB; ++q) vW[q] = w0 + (q & 1) * ((C::ORD == ORD_NAT ? 16 : 8) * C::WSTRIDE) + (q >> 1) * substep - (q * 1024 - C0);
    if (C::HALF) vW[6] = (lane >> 2) * C::WSTRIDE + (((lane & 3) ^ (((lane >> 4) & 1) << 1)) << 4) + 4096 + halfrel - (6144 - C0);
    A += C::XROW ? lw * 32 * 2048 : (lw >> 1) * 16384 + (lw & 1) * 4096;
    W -= 4096;
  }
  DI void prep(int s) {
    int kt = kmap<C::NK, C::XROW>(s, c);
    const int kb = lw >> 1, t = lw & 1;
    int ur;
    need = 1u << kt;
    if (C::ORD == ORD_NAT) ur = 32 * kb + 4 * t;
    else if (C::ORD == ORD_F) {
      const int p = kt & 7, q = kt >> 3, sp = -(int)(q == 3);
      need = ((1u << (2 * p + (int)(q > 0))) & ~sp) | ((0xau << (4 * (p & 3))) & sp);
      kt = kt0 + (((7 * (p >> 1) + 4 * (p & 1) + q) & ~sp) | ((7 * p + 3) & sp));
      ur = (16 * lw & sp) | ((16 * kb + 32 * t) & ~sp);
    }
    else ur = 16 * lw;
    sA = A + (unsigned)(kt * (C::XROW ? 128 : 32768));
    sW = W + (unsigned)((kt * 64 + ur) * C::WSTRIDE);
  }
  DI void issueA(unsigned base) {
    if (!C::XROW) gate(*y, G, need, lw == 0, lane_);
    const unsigned m = base + lw * 4096;
    if (C::XROW) asm volatile("s_mov_b32 m0, %5\n\ts_nop 3\n\t" GLDS "%0, %4\n\t" GLDS "%1, %4 offset:1024\n\t" GLDS "%2, %4 offset:2048\n\t" GLDS "%3, %4 offset:3072"
                              ::"v"(vA[0]), "v"(vA[1]), "v"(vA[2]), "v"(vA[3]), "s"(sA), "s"(m) : "memory");
    else asm volatile("s_mov_b32 m0, %2\n\ts_nop 3\n\t" GLDS "%0, %1 sc1\n\t" GLDS "%0, %1 offset:1024 sc1\n\t" GLDS "%0, %1 offset:2048 sc1\n\t" GLDS "%0, %1 offset:3072 sc1"
                      ::"v"(vA[0]), "s"(sA), "s"(m) : "memory");
  }
  DI void issueW(unsigned base) const {
    const unsigned m = base + 16384 + lw * C::WB + C0;
    if (C::NSUB == 1) asm volatile("s_mov_b32 m0, %3\n\ts_nop 3\n\t" GLDS "%0, %2\n\t" GLDS "%1, %2 offset:1024" ::"v"(vW[0]), "v"(vW[1]), "s"(sW), "s"(m) : "memory");
    if (C::NSUB == 2) asm volatile("s_mov_b32 m0, %5\n\ts_nop 3\n\t" GLDS "%0, %4\n\t" GLDS "%1, %4 offset:1024\n\t" GLDS "%2, %4 offset:2048\n\t" GLDS "%3, %4 offset:3072"
                                   ::"v"(vW[0]), "v"(vW[1]), "v"(vW[2]), "v"(vW[3]), "s"(sW), "s"(m) : "memory");
    if (C::NSUB == 3) asm volatile("s_mov_b32 m0, %7\n\ts_nop 3\n\t" GLDS "%0, %6 offset:-4096\n\t" GLDS "%1, %6 offset:-3072\n\t" GLDS "%2, %6 offset:-2048\n\t" GLDS "%3, %6 offset:-1024\n\t"
                                   GLDS "%4, %6\n\t" GLDS "%5, %6 offset:1024"
                                   ::"v"(vW[0]), "v"(vW[1]), "v"(vW[2]), "v"(vW[3]), "v"(vW[4]), "v"(vW[5]), "s"(sW), "s"(m) : "memory");
    if (C::HALF) asm volatile(GLDS "%0, %1 offset:2048" ::"v"(vW[6]), "s"(sW) : "memory");
  }
};
template <class C> DI void load_preW(Ld<C>& L, unsigned ring, int s0 = 0, int s1 = C::NSLOT) {
#pragma unroll 1
  for (int s = s0; s < s1; ++s) { L.prep(s); L.issueW(ring + s * C::STAGE); }
}
template <class C, bool SPLIT> constexpr int vm_allow(int S) {
  const int N = C::NSLOT, behind = S - 1 - C::LAG > 0 ? S - 1 - C::LAG : 0;
  const int ramp = S + 1 < N - 1 ? S + 1 : N - 1;
  const int last = ramp + behind < C::NK - 1 ? ramp + behind : C::NK - 1;
  return (SPLIT && S < N) ? 4 * (ramp - S) + C::PW * (last - ramp) : C::PW * (last - S);
}
template <class C, bool SPLIT, unsigned POSTM, int S, int SE, class Post> DI void load_fixed(Ld<C>& L, unsigned ring, const Post& post) {
  if constexpr (S < SE) {
    static_assert(C::NSLOT <= C::LAG + 4, "the ring must be full before the first refill");
    wait_vm<vm_allow<C, SPLIT>(S)>();
    bar();
    if constexpr (S + 2 < C::NSLOT) {
      if (!SPLIT) L.issueW(ring + (S + 2) * C::STAGE);
      L.issueA(ring + (S + 2) * C::STAGE);
      L.prep(S + 3);
    }
    constexpr int T = S - 1 - C::LAG + C::NSLOT;
    if constexpr (S - 1 - C::LAG >= 0 && T < C::NK) {
      L.issueW(ring + (T % C::NSLOT) * C::STAGE); L.issueA(ring + (T % C::NSLOT) * C::STAGE);
      if constexpr (T + 1 < C::NK) L.prep(T + 1);
    }
    if constexpr ((POSTM >> S) & 1u) post(S);
    load_fixed<C, SPLIT, POSTM, S + 1, SE>(L, ring, post);
  }
}
template <class C, bool SPLIT, unsigned PREM, unsigned POSTM, class Pre, class Post>
DI void load_all(Ld<C>& L, unsigned ring, const Pre& pre, const Post& post, unsigned long long* dbg = nullptr) {
  constexpr int N = C::NSLOT, S0 = N - 1 > 2 + C::LAG ? N - 1 : 2 + C::LAG, S1 = C::NK - N + C::LAG;
#pragma unroll 1
  for (int s = 0; s < 2; ++s) { L.prep(s); if (!SPLIT) L.issueW(ring + s * C::STAGE); L.issueA(ring + s * C::STAGE); }
  L.prep(2);
  if (PREM >> 31) pre(-1);
  __builtin_amdgcn_s_setprio(3);
  load_fixed<C, SPLIT, POSTM, 0, S0>(L, ring, post);
  static_assert(vm_allow<C, SPLIT>(S0) == (N - 2 - C::LAG) * C::PW && vm_allow<C, SPLIT>(S1) == (N - 2 - C::LAG) * C::PW, "steady count");
  unsigned b = ring + ((S0 - 1 - C::LAG) % N) * C::STAGE;
#pragma unroll 1
  for (int s = S0; s <= S1; ++s) {
    CLK(t0);
    wait_vm<(N - 2 - C::LAG) * C::PW>();
    if ((PREM & 0x7fffffffu) && ((PREM >> s) & 1u)) pre(s);
    CLK(t1);
    bar();
    CLK(t2);
    L.issueW(b);
    L.issueA(b);
    CLK(t3);
    L.prep(min(s - C::LAG + N, C::NK - 1));
    b = (b == ring + (N - 1) * C::STAGE) ? ring : b + C::STAGE;
    if (POSTM && ((POSTM >> s) & 1u)) post(s);
  }
  __builtin_amdgcn_s_setprio(0);
  load_fixed<C, SPLIT, POSTM, S1 + 1, C::NK>(L, ring, post);
  bar();
}

template <int MT, int NWF> struct Fr { u32x4 a[MT]; u32x2 w[NWF][2]; };
DI u32x4 cat(const u32x2 (&w)[2]) { return (u32x4){w[0][0], w[0][1], w[1][0], w[1][1]}; }
struct Adr { unsigned v[6]; };
template <int KIND> struct Shape;
template <> struct Shape<1> { static constexpr int MT = 4, NWF = 6, NMF = 24, NACC = 24, WB = 6144; static constexpr unsigned USE = 0x0f; };
template <> struct Shape<3> { static constexpr int MT = 2, NWF = 4, NMF = 8, NACC = 8, WB = 2048; static constexpr unsigned USE = 0x3d; };
template <> struct Shape<4> { static constexpr int MT = 4, NWF = 8, NMF = 28, NACC = 28, WB = 7168; static constexpr unsigned USE = 0x3f; };
template <> struct Shape<5> { static constexpr int MT = 4, NWF = 4, NMF = 16, NACC = 16, WB = 4096; static constexpr unsigned USE = 0x3f; };
DI lds_ptr lp(unsigned a) { return (lds_ptr)(size_t)a; }
template <int KIND, int J> DI void rd1(Fr<Shape<KIND>::MT, Shape<KIND>::NWF>& f, const Adr& ad, int kb) {
  constexpr int NWF = Shape<KIND>::NWF, MT = Shape<KIND>::MT, WB = Shape<KIND>::WB;
  if constexpr (J < 2 * NWF) {
    constexpr int n = J >> 1, t = J & 1;
    if constexpr (KIND == 4 && n >= 6) f.w[n][t] = lds_tr(lp(ad.v[4 + (n - 6)]) + (2 * kb + t) * WB);
    else if constexpr (KIND == 1 || KIND == 4) f.w[n][t] = lds_tr(lp(ad.v[2 + (n & 1)]) + (n >> 1) * 2048 + (2 * kb + t) * WB);
    else f.w[n][t] = lds_tr(lp(ad.v[2 + n]) + (2 * kb + t) * WB);
  } else if constexpr (J < 2 * NWF + MT) {
    constexpr int m = J - 2 * NWF;
    if constexpr (KIND == 1) f.a[m] = lds128(lp(ad.v[kb]) + m * 2048);
    else f.a[m] = lds128(lp(ad.v[m >> 1]) + (m & 1) * 2048 + kb * 1024);
  }
}
template <int KIND, int I> DI void mm1(f32x4 (&acc)[Shape<KIND>::NACC], const Fr<Shape<KIND>::MT, Shape<KIND>::NWF>& f) {
  if constexpr (KIND == 1) { constexpr int m = I / 6, n = I % 6; if constexpr (n < 4) mfma_ip(acc[I], cat(f.w[n]), f.a[m]); else mfma_ip(acc[I], f.a[m], cat(f.w[n])); }
  else if constexpr (KIND == 4) { if constexpr (I < 24) mfma_ip(acc[I], cat(f.w[I % 6]), f.a[I / 6]); else mfma_ip(acc[I], cat(f.w[6 + ((I - 24) & 1)]), f.a[(I - 24) >> 1]); }
  else mfma_ip(acc[I], cat(f.w[I % 4]), f.a[I / 4]);
}
template <int KIND, int RPG, bool READ, bool MOVE, int I = 0>
DI void step(f32x4 (&acc)[Shape<KIND>::NACC], const Fr<Shape<KIND>::MT, Shape<KIND>::NWF>& cur, Fr<Shape<KIND>::MT, Shape<KIND>::NWF>& nxt, Adr& ad, int kb, int delta) {
  constexpr int NMF = Shape<KIND>::NMF, G0 = (2 * Shape<KIND>::NWF + Shape<KIND>::MT + RPG - 1) / RPG;
  if constexpr (I < NMF) {
    if constexpr (READ) {
      rd1<KIND, I * RPG>(nxt, ad, kb);
      if constexpr (RPG > 1) rd1<KIND, I * RPG + 1>(nxt, ad, kb);
    }
    if constexpr (MOVE && I >= G0) {
      constexpr int per = (6 + (NMF - G0) - 1) / (NMF - G0);
#pragma unroll
      for (int k = (I - G0) * per; k < (I - G0 + 1) * per && k < 6; ++k)
        if ((Shape<KIND>::USE >> k) & 1u) { ad.v[k] += delta; asm volatile("" : "+v"(ad.v[k])); }
    }
    mm1<KIND, I>(acc, cur);
    SB();
    step<KIND, RPG, READ, MOVE, I + 1>(acc, cur, nxt, ad, kb, delta);
  }
}
template <int KIND, int J = 0> DI void rdall(Fr<Shape<KIND>::MT, Shape<KIND>::NWF>& f, const Adr& ad, int kb) {
  if constexpr (J < 2 * Shape<KIND>::NWF + Shape<KIND>::MT) { rd1<KIND, J>(f, ad, kb); rdall<KIND, J + 1>(f, ad, kb); }
}
#define ST4() do {} while (0)
#define ST4ARGS
#define ST4PASS
template <class C, int KIND, int RPG, int CNT>
DI void stages(f32x4 (&acc)[Shape<KIND>::NACC], Fr<Shape<KIND>::MT, Shape<KIND>::NWF>& f0, Fr<Shape<KIND>::MT, Shape<KIND>::NWF>& f1, Adr& ad, int& slot ST4ARGS) {
#pragma unroll 1
  for (int s = 0; s < CNT; ++s) {
    const int delta = (slot + 1 == C::NSLOT) ? -(C::NSLOT - 1) * C::STAGE : C::STAGE;
    slot = (slot + 1 == C::NSLOT) ? 0 : slot + 1;
    SB();
    step<KIND, RPG, true, true>(acc, f0, f1, ad, 1, delta);
    if (!C::LAG) wait_lgkm0();
    ST4();
    bar();
    ST4();
    SB();
    step<KIND, RPG, true, false>(acc, f1, f0, ad, 0, 0);
  }
}
template <class C, int KIND, int RPG, int H0, int H1, int H2, class Hook> DI void consume(f32x4 (&acc)[Shape<KIND>::NACC], Adr& ad, unsigned* dbg4, const Hook& hook) {
  Fr<Shape<KIND>::MT, Shape<KIND>::NWF> f0, f1;
#pragma unroll
  for (int i = 0; i < Shape<KIND>::NACC; ++i) acc[i] = (f32x4){0.f, 0.f, 0.f, 0.f};
  bar();
  rdall<KIND>(f0, ad, 0);
  hook(-1);
  wait_lgkm0();
  int slot = 0;
  if constexpr (H1 > 0) {
    if constexpr (H0 > 0) stages<C, KIND, RPG, H0>(acc, f0, f1, ad, slot ST4PASS);
    hook(0);
    stages<C, KIND, RPG, H1 - H0>(acc, f0, f1, ad, slot ST4PASS);
    hook(1);
    stages<C, KIND, RPG, H2 - H1>(acc, f0, f1, ad, slot ST4PASS);
    hook(2);
  }
  stages<C, KIND, RPG, C::NK - 1 - H2>(acc, f0, f1, ad, slot ST4PASS);
  SB();
  step<KIND, RPG, true, false>(acc, f0, f1, ad, 1, 0);
  wait_lgkm0();
  bar();
  SB();
  step<KIND, RPG, false, false>(acc, f1, f0, ad, 0, 0);
  asm volatile("s_nop 7\n\ts_nop 3");
  SB();
}
template <class C, int KIND, int RPG, class Hook> DI void consume_s(f32x4 (&acc)[Shape<KIND>::NACC], Adr& ad, const Hook& hook) {
  Fr<Shape<KIND>::MT, Shape<KIND>::NWF> f0, f1, g0, g1;
#pragma unroll
  for (int i = 0; i < Shape<KIND>::NACC; ++i) acc[i] = (f32x4){0.f, 0.f, 0.f, 0.f};
  bar();
  rdall<KIND>(f0, ad, 0); rdall<KIND>(f1, ad, 1);
  hook(-1);
#pragma unroll
  for (int k = 0; k < 6; ++k) ad.v[k] += C::STAGE;
  int slot = 1;
#pragma unroll 1
  for (int s = 0; s < C::NK - 2; s += 2) {
    int delta = (slot + 1 == C::NSLOT) ? -(C::NSLOT - 1) * C::STAGE : C::STAGE;
    slot = (slot + 1 == C::NSLOT) ? 0 : slot + 1;
    if (!C::LAG) wait_lgkm0();
    bar(); SB();
    step<KIND, RPG, true, false>(acc, f0, g0, ad, 0, 0);
    step<KIND, RPG, true, true>(acc, f1, g1, ad, 1, delta);
    delta = (slot + 1 == C::NSLOT) ? -(C::NSLOT - 1) * C::STAGE : C::STAGE;
    slot = (slot + 1 == C::NSLOT) ? 0 : slot + 1;
    if (!C::LAG) wait_lgkm0();
    bar(); SB();
    step<KIND, RPG, true, false>(acc, g0, f0, ad, 0, 0);
    step<KIND, RPG, true, true>(acc, g1, f1, ad, 1, delta);
  }
  if (!C::LAG) wait_lgkm0();
  bar(); SB();
  step<KIND, RPG, true, false>(acc, f0, g0, ad, 0, 0);
  step<KIND, RPG, true, false>(acc, f1, g1, ad, 1, 0);
  wait_lgkm0(); bar(); SB();
  step<KIND, RPG, false, false>(acc, g0, f0, ad, 0, 0);
  step<KIND, RPG, false, false>(acc, g1, f1, ad, 1, 0);
  asm volatile("s_nop 7\n\ts_nop 3");
  SB();
}
DI unsigned wlane(int lane, int nt) {
  const int g = lane >> 4, q = (lane >> 2) & 3, p = lane & 3, sw = (2 * g + (q >> 1)) & 3;
  return (4 * g + q) * 128 + ((nt ^ sw) << 5) + p * 8;
}
DI unsigned hlane(int lane, int nt) {
  const int g = lane >> 4, q = (lane >> 2) & 3, p = lane & 3;
  return (4 * g + q) * 64 + ((nt ^ (g & 1)) << 5) + p * 8;
}

template <int OP> DI float op2(float a, float b) { return OP == 0 ? a + b : OP == 1 ? __builtin_elementwise_maximum(a, b) : __builtin_elementwise_minimum(a, b); }
template <int OP> DI float red4(float x0, float x1, float x2, float x3) {
  auto a = __builtin_amdgcn_permlane16_swap(bu(x0), bu(x1), false, false);
  auto b = __builtin_amdgcn_permlane16_swap(bu(x2), bu(x3), false, false);
  const float y0 = op2<OP>(fb(a[0]), fb(a[1])), y1 = op2<OP>(fb(b[0]), fb(b[1]));
  auto c = __builtin_amdgcn_permlane32_swap(bu(y0), bu(y1), false, false);
  return op2<OP>(fb(c[0]), fb(c[1]));
}
DI void spread(float z, float (&o)[4]) {
  auto a = __builtin_amdgcn_permlane32_swap(bu(z), bu(z), false, false);
  auto b = __builtin_amdgcn_permlane16_swap(a[0], a[0], false, false);
  auto c = __builtin_amdgcn_permlane16_swap(a[1], a[1], false, false);
  o[0] = fb(b[0]); o[1] = fb(b[1]); o[2] = fb(c[0]); o[3] = fb(c[1]);
}
DI void dual_softmax4(const f32x4 (&p)[4][2], u32x4 (&pp)[4], u32x4 (&pn)[4]) {
  constexpr float L2E = 1.4426950408889634f;
  float mx[4], mn[4];
#pragma unroll
  for (int m = 0; m < 4; ++m) {
    mx[m] = op2<1>(op2<1>(op2<1>(op2<1>(p[m][0][0], p[m][0][1]), p[m][0][2]), op2<1>(op2<1>(p[m][0][3], p[m][1][0]), p[m][1][1])), op2<1>(p[m][1][2], p[m][1][3]));
    mn[m] = op2<2>(op2<2>(op2<2>(op2<2>(p[m][0][0], p[m][0][1]), p[m][0][2]), op2<2>(op2<2>(p[m][0][3], p[m][1][0]), p[m][1][1])), op2<2>(p[m][1][2], p[m][1][3]));
  }
  spread(-L2E * red4<1>(mx[0], mx[1], mx[2], mx[3]), mx);
  spread(L2E * red4<2>(mn[0], mn[1], mn[2], mn[3]), mn);
  f32x4 ep[4][2], en[4][2];
  float sp[4], sn[4];
#pragma unroll
  for (int m = 0; m < 4; ++m) {
#pragma unroll
    for (int F = 0; F < 2; ++F) {
      const f32x4 a = p[m][F] * L2E + mx[m], b = p[m][F] * (-L2E) + mn[m];
#pragma unroll
      for (int r = 0; r < 4; ++r) { ep[m][F][r] = __builtin_amdgcn_exp2f(a[r]); en[m][F][r] = __builtin_amdgcn_exp2f(b[r]); }
    }
    const f32x4 a = ep[m][0] + ep[m][1], b = en[m][0] + en[m][1];
    sp[m] = (a[0] + a[1]) + (a[2] + a[3]); sn[m] = (b[0] + b[1]) + (b[2] + b[3]);
  }
  spread(__builtin_amdgcn_rcpf(red4<0>(sp[0], sp[1], sp[2], sp[3])), sp);
  spread(__builtin_amdgcn_rcpf(red4<0>(sn[0], sn[1], sn[2], sn[3])), sn);
#pragma unroll
  for (int m = 0; m < 4; ++m) { pp[m] = pack8(ep[m][0] * sp[m], ep[m][1] * sp[m]); pn[m] = pack8(en[m][0] * sn[m], en[m][1] * sn[m]); }
}

DI float dot2(unsigned a, unsigned b, float c) { return __builtin_amdgcn_fdot2_f32_bf16(__builtin_bit_cast(bf16x2, a), __builtin_bit_cast(bf16x2, b), c, false); }
DI unsigned opaque(unsigned c) { asm volatile("" : "+s"(c)); return c; }
template <int NL> DI void colsum_load(u32x4 (&v)[NL], const char* w, int stride, int lane) {
#pragma unroll
  for (int i = 0; i < NL; ++i) v[i] = *(const u32x4*)(w + (size_t)(8 * i + (lane >> 3)) * stride + (lane & 7) * 16);
}
template <int NL> DI void colsum_finish(const u32x4 (&v)[NL], rsrc_t r, unsigned dst, int lane) {
  f32x2 s2[4];
#pragma unroll
  for (int j = 0; j < 4; ++j) s2[j] = (f32x2){bflo(v[0][j]), bfhi(v[0][j])};
#pragma unroll
  for (int i = 1; i < NL; ++i)
#pragma unroll
    for (int j = 0; j < 4; ++j) s2[j] += (f32x2){bflo(v[i][j]), bfhi(v[i][j])};
  const float s[8] = {s2[0][0], s2[0][1], s2[1][0], s2[1][1], s2[2][0], s2[2][1], s2[3][0], s2[3][1]};
  float t[4], u[2];
#pragma unroll
  for (int j = 0; j < 4; ++j) { auto q = __builtin_amdgcn_permlane32_swap(bu(s[j]), bu(s[j + 4]), false, false); t[j] = fb(q[0]) + fb(q[1]); }
#pragma unroll
  for (int j = 0; j < 2; ++j) { auto q = __builtin_amdgcn_permlane16_swap(bu(t[j]), bu(t[j + 2]), false, false); u[j] = fb(q[0]) + fb(q[1]); }
  const bool b3 = lane & 8;
  const float tot = (b3 ? u[1] : u[0]) + dpp<0x128>(b3 ? u[0] : u[1]);
  st4(r, bu(tot), ((lane & 7) * 8 + ((lane >> 5) & 1) * 4 + ((lane >> 4) & 1) * 2 + ((lane >> 3) & 1)) * 4, dst);
}

template <int XN, int TM, int TN> DI void tile_of(int bid, int& tm, int& tn, int& c) {
  const int x = bid & 7, r = bid >> 3, li = r % TM, lj = r / TM;
  tm = (x / XN) * TM + li; tn = (x % XN) * TN + lj; c = (li + lj) & 3;
}

#define RPG1 1
#define RPG4 1
#define RPG5 1
#define W2AT 0
#define PH0 2
#define PH1 6
#define PH2 7
#define PF 10
#define W1M 0u
constexpr int PR = PF + 6 > 15 ? 15 : PF + 6;
#define NPROBE 4
#define FAT 1
#define E4 0
constexpr int R1 = 40704, R3 = 40704, R4 = 0, R5 = 0, SYNCW = 163584;
constexpr int S1_ST = 0, S1_CSR = 1024, S1_CSF = 13312, S1_FLG = 14080, S1_WP = 14336, S1_KV = 18432, S1_DUM = 26880;
constexpr int S4_STR = C4::RING, S4_CSR = S4_STR + 16384, S4_STF = S4_CSR + 8192, S4_CSF = S4_STF + 1024, S4_DUM = S4_CSF + 1024;
constexpr int LDS_BYTES = 163840, CODE_BYTES = 54 * 1024;
static_assert(S4_DUM + 256 <= SYNCW && S1_KV + 8448 <= R1 && R1 + C1::RING <= SYNCW && R3 + C3::RING <= SYNCW && C5::RING <= S4_STR, "lds");

template <class St> DI void kv_sum(const Sync& y, Gate& G, lds_ptr smem, int head, int h, int t, bool poller, bool doz, int lane, const St& stamp) {
  u32x4 v[16], zz[4];
  unsigned have = 0;
  f32x2 s2[4] = {{0.f, 0.f}, {0.f, 0.f}, {0.f, 0.f}, {0.f, 0.f}};
#pragma unroll 1
  for (int round = poller ? 2 : 0; round < 3; ++round) {
    gate_slow(y, G, 0u, poller, lane, round == 0 ? 0 : round == 1 ? 13 : 16);
    stamp(round);
    const unsigned fresh = G.mask & ~have;
    have = G.mask;
#pragma unroll
    for (int r = 0; r < 16; ++r) if ((fresh >> r) & 1u) v[r] = ld16(y.r, t * 16, WS_KVP + (head * 16 + r) * 4096);
    if (round == 2 && doz) {
#pragma unroll
      for (int i = 0; i < 4; ++i) zz[i] = ld16(y.r, lane * 16, WS_ZP + head * 4096 + i * 1024);
    }
  }
#pragma unroll
  for (int r = 0; r < 16; ++r)
#pragma unroll
    for (int j = 0; j < 4; ++j) s2[j] += (f32x2){bflo(v[r][j]), bfhi(v[r][j])};
  const float s[8] = {s2[0][0], s2[0][1], s2[1][0], s2[1][1], s2[2][0], s2[2][1], s2[3][0], s2[3][1]};
  *(LAS u32x4*)(smem + S1_KV + h * 4096 + t * 16) = (u32x4){pk2(s[0], s[1]), pk2(s[2], s[3]), pk2(s[4], s[5]), pk2(s[6], s[7])};
  if (doz) {
    f32x4 z = (__builtin_bit_cast(f32x4, zz[0]) + __builtin_bit_cast(f32x4, zz[1])) + (__builtin_bit_cast(f32x4, zz[2]) + __builtin_bit_cast(f32x4, zz[3]));
#pragma unroll
    for (int j = 0; j < 4; ++j) z[j] = xsum32(xsum16(z[j]));
    if (lane < 16) *(LAS u32x2*)(smem + S1_KV + 8192 + h * 128 + lane * 8) = (u32x2){pk2(z[0], z[1]), pk2(z[2], z[3])};
  }
}

__global__ __launch_bounds__(512, 2) void fused(Params P) {
  extern __shared__ __attribute__((aligned(16))) char smem_[];
  lds_ptr smem = (lds_ptr)smem_;
  const unsigned lds0 = (unsigned)(size_t)smem;
  const int wid = __builtin_amdgcn_readfirstlane(threadIdx.x >> 6), lane = threadIdx.x & 63, bid = blockIdx.x;
  {
    const char* pc; asm volatile("s_getpc_b64 %0" : "=s"(pc));
    const unsigned co = ((bid >> 3) & 7) * 8192 + threadIdx.x * 128;
    if (threadIdx.x < 64 && co < CODE_BYTES) dma4<false>(co, pc, lds0 + S1_DUM);
    SB();
  }
  const unsigned long long tstart = __builtin_amdgcn_s_memrealtime(); unsigned long long tx = tstart, cA = 0, cB = 0; (void)cA; (void)cB; (void)tx;
  int tm1, tn1, c1, tm3, tn3, c3;
  tile_of<4, 8, 4>(bid, tm1, tn1, c1);
  tile_of<2, 4, 8>(bid, tm3, tn3, c3);
  const int kh = bid & 1, tm5 = ((bid & 7) >> 1) * 4 + ((bid >> 3) & 3), tn5 = bid >> 5, c5 = ((bid >> 3) + tn5) & 3;
  const int par = tn1 & 1, base7 = 7 * (tn1 >> 1), ch0 = base7 + 4 * par;
  const int g = lane >> 4, m16 = lane & 15;
  Sync y;
  y.r = __builtin_amdgcn_make_buffer_rsrc(P.ws, 0, WS_END, 0x00020000);
  y.w = (LAS unsigned*)(smem + SYNCW); y.pb = 0;
  if (wid == 0) TS(0);

  if (wid >= 4) {
    const int lw = wid - 4, lt = threadIdx.x - 256;
    const auto nop = [](int) {};
    {
      Ld<C1> L; L.A = P.x + (size_t)tm1 * 128 * 2048; L.W = P.wqkv + tn1 * 128; L.lw = lw; L.c = c1; L.kt0 = 0; L.init(y, lane, 2048, 0, 0, 0);
      const unsigned frec = (FL_P0 * 256 + (lane < 16 ? tm1 * 16 + lane : lane < 28 ? ((lane - 16) / 3) * 48 + ((lane - 16) % 3) * 16 + tn1 : tn1)) * 128;
      if (wid == 4) TS(31);
      if (lw == 0) dma4<true>(0u * lane, P.ws + WS_CTL, lds0 + S1_FLG);
      load_all<C1, false, (1u << (PF + 3)) | (1u << 31), 0x8005u | W1M | (1u << PF) | (1u << (PF + 3)) | (1u << PR)>(L, lds0 + R1,
        [&](int s) {
          if (s < 0) { if (wid == 4) TS(30); return; }
          if (lw == 0) {
            const u32x4 v = *(volatile LAS u32x4*)(smem + S1_CSR + lane * 16);
            if (!__all(v[0] == y.tag && v[1] == y.tag && v[2] == y.tag && v[3] == y.tag)) {
#pragma unroll 1
              for (int k = 0; k < 4; ++k) poll(y, frec + 4 * k, 64, lane);
            }
          }
        },
        [&](int s) {
          if (s == 0) y.tag = __builtin_amdgcn_readfirstlane(*(volatile LAS unsigned*)(smem + S1_FLG)) + 1u;
          if (s == 2) dma<false>(lane * 16, P.wphi + tn1 * 4096 + lw * 1024, lds0 + S1_WP + lw * 1024);
          if (((W1M >> s) & 1u) && lw == __builtin_popcount(W1M & ((1u << s) - 1u))) dma4<false>(min(lane, 55) * 128, P.w1 + bid * 28672 + lw * 7168, lds0 + S1_DUM);
          if (s == PF && lw == 0) dma<true>(frec, P.ws + WS_FLG, lds0 + S1_CSR);
          if (s == 15 && lw == 1) dma4<false>(lane * 128, P.wo + bid * 8192, lds0 + S1_DUM);
          if (s == PF + 3) {
            if (lw == 3) dma<true>(lane * 16, P.ws + WS_ST1 + tm1 * 1024, lds0 + S1_ST);
#pragma unroll
            for (int i = 0; i < 3; ++i)
              dma<true>((lane >> 4) * 12288 + (lane & 15) * 16, P.ws + WS_CSQ + lw * 49152 + i * 4096 + tn1 * 256, lds0 + S1_CSR + i * 4096 + lw * 1024);
          }
          if (s == PR) {
            if (lt < 192) {
              const LAS float* rawp = (const LAS float*)(smem + S1_CSR) + (lt >> 6) * 1024 + (lt & 63);
              float t = 0.f;
#pragma unroll
              for (int i = 0; i < 16; ++i) t += rawp[i * 64];
              ((LAS float*)(smem + S1_CSF))[lt] = t;
            }
            wait_lgkm0();
          }
        }, DBG(0));
    }
    if (wid == 4) TS(16);
    {
      Ld<C3> L; L.A = P.ws; L.W = P.wo + tn3 * 128; L.lw = lw; L.c = c3; L.kt0 = 0; L.init(y, lane, 0, 0, 0, 9);
      if (bid >= 224 && bid < 224 + NPAGE) {
        if (lw == 1) {
          const unsigned po = bid == 224 ? WS_PRB : (bid - 224) << 21;
          unsigned tmin[2] = {~0u, ~0u};
#pragma unroll 1
          for (int k = 0; k < NPROBE; ++k) {
            const unsigned long long t0 = __builtin_amdgcn_s_memtime();
            if (lane < 8) st16(y.r, (u32x4){0u, 0u, 0u, 0u}, lane * 16, po + (k & 1) * 8192);
            wait_vm<0>();
            const unsigned dt = (unsigned)(__builtin_amdgcn_s_memtime() - t0);
            if (k & 1) tmin[1] = min(tmin[1], dt); else tmin[0] = min(tmin[0], dt);
          }
          if (lane == 0) st4(y.r, (y.tag << 1) | (((unsigned)(tmin[1] < tmin[0]) ^ (unsigned)(bid >> 2)) & 1u), 0, WS_FLG + (FL_CS * 256 + bid) * 128 + 4);
        }
        bar_loaders(y, lw);
      }
      load_preW<C3>(L, lds0 + R3);
      if (lw >= 2 && bid < 224) {
        u32x4 v[16];
        colsum_load<16>(v, P.w1 + (size_t)((bid / 56) * 256 + (lw - 2) * 128) * (FF * 2) + (bid % 56) * 128, FF * 2, lane);
        colsum_finish<16>(v, y.r, WS_CS1 + (((bid / 56) * 2 + lw - 2) * FF + (bid % 56) * 64) * 4, lane);
      }
      publish(y, FL_CS, bid, lane);
      lds_wait(y, 24 + FL_KV, 4u);
      Gate G; G.off = (FL_KV * 256 + m16 * 16 + tn1) * 128; G.mask = 0; G.word = 8;
      const unsigned pbo = WS_FLG + (FL_CS * 256 + 224 + (lane & 31)) * 128 + 4;
      unsigned pbv = 0;
      kv_sum(y, G, smem, 2 * tn1 + 1, 1, lt, lw == 0, lw == 1, lane, [&](int) { if (lw == 0) pbv = __builtin_amdgcn_raw_buffer_load_b32(y.r, pbo, 0, 16); });
      if (lw == 0) {
        const unsigned long long t0 = __builtin_amdgcn_s_memrealtime();
        while (!__all(lane >= NPAGE || (pbv >> 1) == (y.tag & 0x7fffffffu))) {
          if (__builtin_amdgcn_s_memrealtime() - t0 > 4000000ull) { if (lane == 0) st4(y.r, y.tag, 0, WS_CTL + 128); break; }
          pbv = __builtin_amdgcn_raw_buffer_load_b32(y.r, pbo, 0, 16);
        }
        const unsigned raw = (unsigned)__ballot(pbv & 1u), odd = __builtin_popcount((raw ^ 0xaaaaaaaau) & 0x3fffffdu);
        *(volatile LAS unsigned*)(y.w + 13) = odd <= 6 ? 0xaaaaaaaau : odd >= 19 ? 0x55555555u : raw;
      }
      if (wid == 4) TS(17);
      wait_lgkm0(); bar();
      y.pb = __builtin_amdgcn_readfirstlane(*(volatile LAS unsigned*)(y.w + 13));
      L.A += near(y, WS_O + tm3 * 524288, tm3); L.G.off = rec(y, FL_O, tm3 * 16 + m16);
      if (wid == 4) TS(18);
      load_all<C3, true, 0u, 0u>(L, lds0 + R3, nop, nop, DBG(1));
    }
    if (wid == 4) TS(19);
    {
      Ld<C4> L; L.A = P.ws + near(y, WS_H + tm1 * 524288, tm1); L.W = P.w1 + ch0 * 128; L.lw = lw; L.c = c1; L.kt0 = 0;
      L.init(y, lane, 128, par ? -64 : 384, (lane & 16) ? rec(y, FL_CS, (m16 >> 2) * 56 + base7 + 3 * par + (lane & 3)) : rec(y, FL_H, tm1 * 16 + m16), 10);
      load_preW<C4>(L, lds0 + R4, 0, E4);
      lds_wait(y, 24 + FL_H, 4u);
      load_preW<C4>(L, lds0 + R4, E4);
      load_all<C4, true, 0u, 0xa000u | (W2AT == 0 ? 0x550u : W2AT > 0 ? 1u << (W2AT & 31) : 0u)>(L, lds0 + R4, nop,
        [&](int s) {
          if (W2AT == 0 ? (s < 12 && lw == ((s >> 1) & 3)) : s == W2AT) dma4<false>(min(lane, 55) * 128, P.w2 + bid * 28672 + lw * 7168, lds0 + S4_DUM);
          if (s == 13) {
            gate(y, L.G, ~0u, lw == 0, lane);
#pragma unroll
            for (int i = 0; i < 4; ++i) dma<true>(lane * 16, P.ws + near(y, WS_ST2 + tm1 * 32768, tm1) + (lw >> 1) * 16384 + ((lw & 1) * 4 + i) * 1024, lds0 + S4_STR + (lw * 4 + i) * 1024);
#pragma unroll
            for (int i = 0; i < 2; ++i) dma<true>(lane * 16, P.ws + WS_CS1 + ((lw * 2 + i) * FF + (base7 + 3 * par) * 64) * 4, lds0 + S4_CSR + (lw * 2 + i) * 1024);
          }
          if (s == 15) {
            const LAS float* rawp = (const LAS float*)(smem + S4_CSR) + lt;
            float t = 0.f;
#pragma unroll
            for (int i = 0; i < 8; ++i) t += rawp[i * 256];
            ((LAS float*)(smem + S4_CSF))[lt] = t;
            wait_lgkm0();
          }
          if (s == 15 && lw < 2) {
            f32x2 st[16]; float mu = 0.f, m2 = 0.f;
#pragma unroll
            for (int i = 0; i < 16; ++i) { st[i] = *(const LAS f32x2*)(smem + S4_STR + i * 1024 + lt * 8); mu += st[i][0]; }
            mu *= (1.f / 16.f);
#pragma unroll
            for (int i = 0; i < 16; ++i) { const float d = st[i][0] - mu; m2 += st[i][1] + 64.f * d * d; }
            *(LAS f32x2*)(smem + S4_STF + lt * 8) = (f32x2){mu, __builtin_amdgcn_rsqf(m2 * (1.f / D) + 1e-5f)};
            wait_lgkm0();
          }
        }, DBG(2));
    }
    {
      Ld<C5> L; L.A = P.ws + near(y, WS_F + tm5 * 2097152, tm5); L.W = P.w2 + tn5 * 256; L.lw = lw; L.c = c5; L.kt0 = 28 * kh; L.init(y, lane, 128, 0, rec(y, FL_F, tm5 * 16 + 8 * kh + ((lane >> 1) & 7)) + (lane & 1) * 4, 11);
      load_preW<C5>(L, lds0 + R5);

      load_all<C5, true, 0u, 0u>(L, lds0 + R5, nop, nop, DBG(3));
    }
    if (wid == 4) { TS(23); TS_FLUSH(); }
    return;
  }

  const int wm = wid >> 1, wn = wid & 1;
  if (wid == 0 && lane < 32) y.w[lane] = 0u;
  {
    f32x4 acc[24];
    Adr ad;
    ad.v[0] = lds0 + R1 + (wm * 64 + m16) * 128 + ((g ^ (m16 >> 1)) << 4); ad.v[1] = lds0 + R1 + (wm * 64 + m16) * 128 + ((g ^ (m16 >> 1) ^ 4) << 4);
    ad.v[2] = lds0 + R1 + 16384 + wlane(lane, 2 * wn); ad.v[3] = lds0 + R1 + 16384 + wlane(lane, 2 * wn + 1); ad.v[4] = 0; ad.v[5] = 0;
    u32x4 px[4], pw[8];
    const int prow = bid * 8 + wid * 2 + (lane >> 5);
    consume<C1, 1, RPG1, PH0, PH1, PH2>(acc, ad, DBG4, [&](int s) {
      if (s < 0) {
        if (wid == 0) TS(1);
        y.tag = __builtin_amdgcn_readfirstlane(*(volatile LAS unsigned*)(smem + S1_FLG)) + 1u;
      }
      if (s == 0) {
#pragma unroll
        for (int i = 0; i < 4; ++i) px[i] = *(const u32x4*)(P.x + (size_t)prow * 2048 + ((lane & 31) + 32 * i) * 16);
        colsum_load<8>(pw, P.wqkv + (size_t)((min(bid, 191) / 48) * 256 + wid * 64) * (3 * D * 2) + (bid % 48) * 128, 3 * D * 2, lane);
      }
      if (s == 1) {
#pragma unroll
        for (int i = 0; i < 4; ++i) asm volatile("" : "+v"(px[i]));
#pragma unroll
        for (int i = 0; i < 8; ++i) asm volatile("" : "+v"(pw[i]));
        const unsigned one2 = opaque(0x3f803f80u);
        float sm = 0.f, sq = 0.f;
#pragma unroll
        for (int i = 0; i < 4; ++i)
#pragma unroll
          for (int j = 0; j < 4; ++j) { sm = dot2(px[i][j], one2, sm); sq = dot2(px[i][j], px[i][j], sq); }
        const float mean = xsum16(row16_sum(sm)) * (1.f / D);
        const float var = fmaxf(xsum16(row16_sum(sq)) * (1.f / D) - mean * mean, 0.f);
        if ((lane & 31) == 0) st8(y.r, (u32x2){bu(mean), bu(__builtin_amdgcn_rsqf(var + 1e-5f))}, prow * 8, WS_ST1);
        if (bid < 192) colsum_finish<8>(pw, y.r, WS_CSQ + (((bid / 48) * 4 + wid) * 3 * D + (bid % 48) * 64) * 4, lane);
      }
      if (s == 2) { wait_vm<0>(); if (lane == 0) st4(y.r, y.tag, 0, WS_FLG + (FL_P0 * 256 + bid) * 128 + wid * 4); }
    });
    if (wid == 0) TS(2);
    const int head = 2 * tn1 + wn;
    f32x4 cs[4]; float csv[2];
#pragma unroll
    for (int n = 0; n < 4; ++n) cs[n] = *(const LAS f32x4*)(smem + S1_CSF + ((n >> 1) * 64 + wn * 32 + (n & 1) * 16 + 4 * g) * 4);
#pragma unroll
    for (int n = 0; n < 2; ++n) csv[n] = *(const LAS float*)(smem + S1_CSF + (128 + wn * 32 + n * 16 + m16) * 4);
    u32x4 wp[2];
#pragma unroll
    for (int F = 0; F < 2; ++F) { lds_ptr a0 = smem + S1_WP + wn * 2048 + (4 * g + (m16 >> 2)) * 64 + F * 32 + (lane & 3) * 8; wp[F] = lds_tr2(a0, a0 + 1024); }
    u32x4 qb[4], vb[2][2];
    f32x4 p[4][2];
    const f32x4 zero = {0.f, 0.f, 0.f, 0.f};
    {
      f32x4 vv[4][2];
#pragma unroll
      for (int m = 0; m < 4; ++m) {
        const f32x2 st = *(const LAS f32x2*)(smem + S1_ST + (wm * 64 + m * 16 + m16) * 8);
        const f32x4 s0 = *(const LAS f32x4*)(smem + S1_ST + (wm * 64 + m * 16 + 4 * g) * 8), s1 = *(const LAS f32x4*)(smem + S1_ST + (wm * 64 + m * 16 + 4 * g) * 8 + 16);
        const f32x4 mu = {s0[0], s0[2], s1[0], s1[2]}, rs = {s0[1], s0[3], s1[1], s1[3]};
        qb[m] = pack8((acc[m * 6] - cs[0] * st[0]) * st[1], (acc[m * 6 + 1] - cs[1] * st[0]) * st[1]);
        const u32x4 kb = pack8((acc[m * 6 + 2] - cs[2] * st[0]) * st[1], (acc[m * 6 + 3] - cs[3] * st[0]) * st[1]);
        p[m][0] = mfma(wp[0], kb, zero); p[m][1] = mfma(wp[1], kb, zero);
        vv[m][0] = (acc[m * 6 + 4] - mu * csv[0]) * rs; vv[m][1] = (acc[m * 6 + 5] - mu * csv[1]) * rs;
      }
#pragma unroll
      for (int u = 0; u < 2; ++u) { vb[u][0] = pack8(vv[2 * u][0], vv[2 * u + 1][0]); vb[u][1] = pack8(vv[2 * u][1], vv[2 * u + 1][1]); }
    }
    u32x4 pp[4], pn[4];
    if (wid == 0) TS(26);
    dual_softmax4(p, pp, pn);
    if (wid == 0) TS(27);
    {
      lds_ptr own = smem + R3 + wid * C3::STAGE, oth = smem + R3 + (wid ^ 2) * C3::STAGE;
#pragma unroll
      for (int m = 0; m < 4; ++m) {
        lds_ptr w = own + (16 * m + m16) * 136 + 8 * g;
        *(LAS u32x2*)(w) = (u32x2){pp[m][0], pp[m][1]}; *(LAS u32x2*)(w + 32) = (u32x2){pp[m][2], pp[m][3]};
        *(LAS u32x2*)(w + 64) = (u32x2){pn[m][0], pn[m][1]}; *(LAS u32x2*)(w + 96) = (u32x2){pn[m][2], pn[m][3]};
      }
#pragma unroll
      for (int i = 0; i < 4; ++i) *(LAS u32x4*)(own + 8704 + i * 1024 + lane * 16) = vb[i >> 1][i & 1];
      wait_lgkm0();
      volatile LAS unsigned* xf = y.w + 16;
      xf[wid] = 1u;
      f32x4 kv[2][2] = {{zero, zero}, {zero, zero}}, z[2] = {zero, zero};
      const u32x4 ones = {0x3f803f80u, 0x3f803f80u, 0x3f803f80u, 0x3f803f80u};
      const unsigned ro = (4 * g + (m16 >> 2)) * 136 + wm * 64 + (lane & 3) * 8;
#pragma unroll
      for (int u = 0; u < 2; ++u)
#pragma unroll
        for (int F = 0; F < 2; ++F) {
          const u32x4 pa = lds_tr2(own + ro + u * 32 * 136 + F * 32, own + ro + u * 32 * 136 + F * 32 + 16 * 136);
          kv[F][0] = mfma(pa, vb[u][0], kv[F][0]); kv[F][1] = mfma(pa, vb[u][1], kv[F][1]); z[F] = mfma(pa, ones, z[F]);
        }
      while (xf[wid ^ 2] == 0u) __builtin_amdgcn_s_sleep(0);
      asm volatile("" ::: "memory");
#pragma unroll
      for (int u = 0; u < 2; ++u) {
        const u32x4 v0 = lds128(oth + 8704 + u * 2048 + lane * 16), v1 = lds128(oth + 8704 + u * 2048 + 1024 + lane * 16);
#pragma unroll
        for (int F = 0; F < 2; ++F) {
          const u32x4 pa = lds_tr2(oth + ro + u * 32 * 136 + F * 32, oth + ro + u * 32 * 136 + F * 32 + 16 * 136);
          kv[F][0] = mfma(pa, v0, kv[F][0]); kv[F][1] = mfma(pa, v1, kv[F][1]); z[F] = mfma(pa, ones, z[F]);
        }
      }
      const unsigned kd = WS_KVP + (head * 16 + tm1) * 4096 + wm * 2048;
      st16(y.r, pack8(kv[0][0], kv[1][0]), lane * 16, kd);
      st16(y.r, pack8(kv[0][1], kv[1][1]), lane * 16, kd + 1024);
      if (m16 == 0) {
        st16(y.r, __builtin_bit_cast(u32x4, z[0]), g * 32, WS_ZP + (head * 16 + tm1) * 256 + wm * 128);
        st16(y.r, __builtin_bit_cast(u32x4, z[1]), g * 32 + 16, WS_ZP + (head * 16 + tm1) * 256 + wm * 128);
      }
      if (wid == 0) TS(25);
      publish(y, FL_KV, tm1 * 16 + tn1, lane);
    }
    if (wid == 0) TS(3);
#pragma unroll
    for (int m = 0; m < 4; ++m) { p[m][0] = mfma(wp[0], qb[m], zero); p[m][1] = mfma(wp[1], qb[m], zero); }
    dual_softmax4(p, pp, pn);
    if (wid == 0) TS(4);
    { Gate G; G.off = 0; G.mask = 0; G.word = 8; kv_sum(y, G, smem, 2 * tn1, 0, threadIdx.x, false, wid == 0, lane, [](int) {}); }
    wait_lgkm0(); bar();
    if (wid == 0) TS(5);
    y.pb = __builtin_amdgcn_readfirstlane(*(volatile LAS unsigned*)(y.w + 13));
    u32x4 ka[2][2], za[2];
#pragma unroll
    for (int ks = 0; ks < 2; ++ks) {
      ka[ks][0] = lds128(smem + S1_KV + wn * 4096 + ks * 2048 + lane * 16); ka[ks][1] = lds128(smem + S1_KV + wn * 4096 + ks * 2048 + 1024 + lane * 16);
      za[ks] = lds128(smem + S1_KV + 8192 + wn * 128 + ks * 64 + g * 16);
    }
#pragma unroll
    for (int m = 0; m < 4; ++m) {
      f32x4 n0 = mfma(ka[0][0], pp[m], zero), n1 = mfma(ka[0][1], pp[m], zero), dn = mfma(za[0], pp[m], zero);
      n0 = mfma(ka[1][0], pn[m], n0); n1 = mfma(ka[1][1], pn[m], n1); dn = mfma(za[1], pn[m], dn);
      const float rd = __builtin_amdgcn_rcpf(dn[0] + 1e-6f);
      st16(y.r, pack8(n0 * rd, n1 * rd), lane * 16, near(y, WS_O + (tm1 * 16 + tn1) * 32768, tm1) + wm * 16384 + (m * 2 + wn) * 1024);
    }
    publish(y, FL_O, tm1 * 16 + tn1, lane);
    if (wid == 0) TS(6);
  }
  {
    u32x4 xr[2][2];
#pragma unroll
    for (int m = 0; m < 2; ++m)
#pragma unroll
      for (int Q = 0; Q < 2; ++Q)
        xr[m][Q] = *(const u32x4*)(P.x + (size_t)(tm3 * 128 + wid * 32 + m * 16 + m16) * 2048 + tn3 * 128 + Q * 64 + (g & 1) * 32 + (g >> 1) * 16);
    f32x4 acc[8];
    Adr ad; ad.v[0] = lds0 + R3 + wid * 4096 + lane * 16; ad.v[1] = 0;
#pragma unroll
    for (int n = 0; n < 4; ++n) ad.v[2 + n] = lds0 + R3 + 16384 + wlane(lane, n);
    consume_s<C3, 3, 2>(acc, ad, [&](int) { if (wid == 0) TS(7); });
    if (wid == 0) TS(8);
#pragma unroll
    for (int m = 0; m < 2; ++m) {
      float hv[16];
#pragma unroll
      for (int Q = 0; Q < 2; ++Q) {
        auto r0 = __builtin_amdgcn_permlane16_swap(xr[m][Q][0], xr[m][Q][2], false, false);
        auto r1 = __builtin_amdgcn_permlane16_swap(xr[m][Q][1], xr[m][Q][3], false, false);
        const f32x4 a = acc[m * 4 + 2 * Q], b = acc[m * 4 + 2 * Q + 1];
        const u32x4 hp = {pk2(a[0] + bflo(r0[0]), a[1] + bfhi(r0[0])), pk2(a[2] + bflo(r1[0]), a[3] + bfhi(r1[0])),
                          pk2(b[0] + bflo(r0[1]), b[1] + bfhi(r0[1])), pk2(b[2] + bflo(r1[1]), b[3] + bfhi(r1[1]))};
        st16(y.r, hp, lane * 16, near(y, WS_H + (tm3 * 16 + tn3) * 32768, tm3) + wm * 16384 + wn * 4096 + (m * 2 + Q) * 1024);
#pragma unroll
        for (int j = 0; j < 4; ++j) { hv[Q * 8 + 2 * j] = bflo(hp[j]); hv[Q * 8 + 2 * j + 1] = bfhi(hp[j]); }
      }
      float s = 0.f;
#pragma unroll
      for (int j = 0; j < 16; ++j) s += hv[j];
      s = xsum32(xsum16(s)) * (1.f / 64.f);
      float q = 0.f;
#pragma unroll
      for (int j = 0; j < 16; ++j) { const float d = hv[j] - s; q += d * d; }
      q = xsum32(xsum16(q));
      if (g == 0) st8(y.r, (u32x2){bu(s), bu(q)}, m16 * 8, near(y, WS_ST2 + tm3 * 32768, tm3) + (tn3 >> 3) * 16384 + (tn3 & 7) * 1024 + (wid * 32 + m * 16) * 8);
    }
    if (wid == 0) TS(29);
    publish(y, FL_H, tm3 * 16 + tn3, lane);
    if (wid == 0) TS(9);
  }
  {
    f32x4 acc[28];
    Adr ad;
    ad.v[0] = lds0 + R4 + wm * 8192 + wn * 4096 + lane * 16; ad.v[1] = lds0 + R4 + wm * 8192 + (wn ^ 1) * 4096 + lane * 16;
    ad.v[2] = lds0 + R4 + 16384 + wlane(lane, wn); ad.v[3] = lds0 + R4 + 16384 + wlane(lane, 2 + wn);
    ad.v[4] = lds0 + R4 + 16384 + 6144 + hlane(lane, 0); ad.v[5] = lds0 + R4 + 16384 + 6144 + hlane(lane, 1);
    consume<C4, 4, RPG4, 0, 0, 0>(acc, ad, DBG4, [&](int s) { if (s < 0 && wid == 0) TS(10); });
    if (wid == 0) TS(11);
    f32x4 cs[8];
#pragma unroll
    for (int n = 0; n < 6; ++n) cs[n] = *(const LAS f32x4*)(smem + S4_CSF + ((par + (n >> 1)) * 64 + (2 * (n & 1) + wn) * 16 + 4 * g) * 4);
#pragma unroll
    for (int n = 0; n < 2; ++n) cs[6 + n] = *(const LAS f32x4*)(smem + S4_CSF + ((par ? 32 : 192) + n * 16 + 4 * g) * 4);
    const f32x4 zero = {0.f, 0.f, 0.f, 0.f};
    f32x2 st[4];
#pragma unroll
    for (int m = 0; m < 4; ++m) st[m] = *(const LAS f32x2*)(smem + S4_STF + ((wm * 4 + ((m + 2 * wn) & 3)) * 16 + m16) * 8);
#pragma unroll
    for (int i = 0; i < 4; ++i) {
#pragma unroll
      for (int m = 0; m < (i < 3 ? 4 : 2); ++m) {
        const unsigned fd = near(y, WS_F + tm1 * 2097152, tm1) + wm * 16384 + ((m + 2 * wn) & 3) * 2048 + (i < 3 ? (ch0 + i) * 32768 + wn * 1024 : (base7 + 3) * 32768 + par * 1024);
        const int a = i < 3 ? m * 6 + 2 * i : 24 + 2 * m;
        st16(y.r, pack8(__builtin_elementwise_max((acc[a] - cs[2 * i] * st[m][0]) * st[m][1], zero),
                        __builtin_elementwise_max((acc[a + 1] - cs[2 * i + 1] * st[m][0]) * st[m][1], zero)), lane * 16, fd);
        SB();
      }
      if (i == FAT) {
        wait_vm<FAT == 3 ? 10 : 4 * FAT>();
        if (lane == 0 && __hip_atomic_fetch_add(y.w + FL_F, 1u, __ATOMIC_RELAXED, __HIP_MEMORY_SCOPE_WORKGROUP) == 3u) st4(y.r, y.tag, 0, WS_FLG + rec(y, FL_F, tm1 * 16 + tn1));
        SB();
      }
    }
    if (wid == 0) TS(12);
  }
  {
    f32x4 acc[16];
    Adr ad;
    ad.v[0] = lds0 + R5 + wm * 8192 + kh * 4096 + lane * 16; ad.v[1] = lds0 + R5 + wm * 8192 + (kh ^ 1) * 4096 + lane * 16;
#pragma unroll
    for (int n = 0; n < 4; ++n) ad.v[2 + n] = lds0 + R5 + 16384 + wn * 2048 + wlane(lane, n);
    u32x4 hr[2][2];
    consume_s<C5, 5, 1>(acc, ad, [&](int s) {
      if (s < 0) {
        if (wid == 0) TS(13);
        wait_vm<0>();
        if (lane == 0 && __hip_atomic_fetch_add(y.w + 20, 1u, __ATOMIC_RELAXED, __HIP_MEMORY_SCOPE_WORKGROUP) == 3u) st4(y.r, y.tag, 0, WS_FLG + rec(y, FL_F, tm1 * 16 + tn1) + 4);
#pragma unroll
        for (int m = 0; m < 2; ++m)
#pragma unroll
          for (int Q = 0; Q < 2; ++Q) hr[m][Q] = ld16(y.r, lane * 16, near(y, WS_H + (tm5 * 16 + 2 * tn5 + wn) * 32768, tm5) + wm * 16384 + ((2 * kh + m) * 2 + Q) * 1024);
      }
    });
    if (wid == 0) TS(14);
    const int tile = tm5 * 8 + tn5;
#pragma unroll
    for (int i = 0; i < 4; ++i) st16(y.r, pack8(acc[8 + 2 * i], acc[9 + 2 * i]), lane * 16, near(y, WS_O + (tile * 2 + kh) * 32768, tm5) + wm * 16384 + wn * 4096 + i * 1024);
    wait_vm<0>();
    if (lane == 0) st4(y.r, y.tag, 0, WS_FLG + rec(y, FL_X + wm, tile * 2 + kh) + wn * 64);
    poll(y, rec(y, FL_X + wm, tile * 2 + (kh ^ 1)) + wn * 64, 1, lane);
    if (wid == 0) TS(15);
    const rsrc_t ro = __builtin_amdgcn_make_buffer_rsrc(P.out, 0, 2048 * 2048, 0x00020000);
    u32x4 xv[4];
#pragma unroll
    for (int i = 0; i < 4; ++i) xv[i] = ld16(y.r, lane * 16, near(y, WS_O + (tile * 2 + (kh ^ 1)) * 32768, tm5) + wm * 16384 + wn * 4096 + i * 1024);
#pragma unroll
    for (int m = 0; m < 2; ++m)
#pragma unroll
      for (int Q = 0; Q < 2; ++Q) {
        const u32x4 xx = xv[m * 2 + Q];
        const f32x4 a = acc[m * 4 + 2 * Q] + (f32x4){bflo(xx[0]), bfhi(xx[0]), bflo(xx[1]), bfhi(xx[1])}, b = acc[m * 4 + 2 * Q + 1] + (f32x4){bflo(xx[2]), bfhi(xx[2]), bflo(xx[3]), bfhi(xx[3])};
        const u32x4 hh = hr[m][Q];
        const unsigned a0 = pk2(a[0] + bflo(hh[0]), a[1] + bfhi(hh[0])), a1 = pk2(a[2] + bflo(hh[1]), a[3] + bfhi(hh[1]));
        const unsigned b0 = pk2(b[0] + bflo(hh[2]), b[1] + bfhi(hh[2])), b1 = pk2(b[2] + bflo(hh[3]), b[3] + bfhi(hh[3]));
        auto r0 = __builtin_amdgcn_permlane16_swap(a0, b0, false, false);
        auto r1 = __builtin_amdgcn_permlane16_swap(a1, b1, false, false);
        __builtin_amdgcn_raw_buffer_store_b128((u32x4){r0[0], r1[0], r0[1], r1[1]}, ro, (tm5 * 128 + wm * 64 + (2 * kh + m) * 16 + m16) * 2048 + tn5 * 256 + wn * 128 + Q * 64 + (g & 1) * 32 + (g >> 1) * 16, 0, 16);
      }
    { const unsigned long long lim = 10000ull + ((bid == 37 && wid == 0) ? (tx - tstart) : 0ull); while (__builtin_amdgcn_s_memrealtime() - tstart < lim) __builtin_amdgcn_s_sleep(1); }
    if (bid == 0 && threadIdx.x == 0) st4(y.r, y.tag, 0, WS_CTL);
    if (wid == 0) { TS(24); TS_FLUSH(); }
  }
}

extern "C" void kernel_launch(void* const* d_in, const int* in_sizes, int n_in, void* d_out, int out_size, void* d_ws, size_t ws_size,
                              hipStream_t stream) {
  Params p;
  p.x = (const char*)d_in[0]; p.wqkv = (const char*)d_in[1]; p.wphi = (const char*)d_in[2]; p.wo = (const char*)d_in[3];
  p.w1 = (const char*)d_in[5]; p.w2 = (const char*)d_in[6]; p.ws = (char*)(((size_t)d_ws + 0x1fffff) & ~(size_t)0x1fffff); p.out = (char*)d_out;
  (void)hipFuncSetAttribute((const void*)fused, hipFuncAttributeMaxDynamicSharedMemorySize, LDS_BYTES);
  fused<<<256, 512, LDS_BYTES, stream>>>(p);
}
```

```cpp
#include <hip/hip_runtime.h>
#include <hip/hip_bf16.h>

#define LAS __attribute__((address_space(3)))
#define DI __device__ __forceinline__
typedef __bf16 bf16x8 __attribute__((ext_vector_type(8)));
typedef __bf16 bf16x2 __attribute__((ext_vector_type(2)));
typedef short s16x4 __attribute__((ext_vector_type(4)));
typedef float f32x2 __attribute__((ext_vector_type(2)));
typedef float f32x4 __attribute__((ext_vector_type(4)));
typedef unsigned u32x2 __attribute__((ext_vector_type(2)));
typedef unsigned u32x4 __attribute__((ext_vector_type(4)));
typedef LAS char* lds_ptr;
typedef __amdgpu_buffer_rsrc_t rsrc_t;

#define UNR 1
constexpr int D = 1024, FF = 3584;
constexpr unsigned WS_CTL = 0;
constexpr unsigned WS_FLG = 4096;
constexpr unsigned WS_ST1 = 320u << 10;
constexpr unsigned WS_CSQ = 384u << 10;
constexpr unsigned WS_CS1 = 576u << 10;
constexpr unsigned WS_ZP = 704u << 10;
constexpr unsigned WS_ST2 = 1024u << 10;
constexpr unsigned WS_FL2 = 1536u << 10;
constexpr unsigned WS_PRB = 1920u << 10;
constexpr unsigned WS_KVP = 2u << 20;
constexpr unsigned WS_O = 4u << 20;
constexpr unsigned WS_H = 12u << 20;
constexpr unsigned WS_F = 20u << 20;
constexpr unsigned WS_END = 52u << 20;
constexpr int NPAGE = 26;
enum { FL_P0 = 0, FL_KV = 1, FL_CS = 2, FL_O = 3, FL_H = 4, FL_F = 5, FL_X = 6   };


struct Params { const char* x; const char* wqkv; const char* wphi; const char* wo; const char* w1; const char* w2; char* ws; char* out; };

DI unsigned pk2(float a, float b) { f32x2 v = {a, b}; return __builtin_bit_cast(unsigned, __builtin_convertvector(v, bf16x2)); }
DI float bflo(unsigned u) { return __uint_as_float(u << 16); }
DI float bfhi(unsigned u) { return __uint_as_float(u & 0xffff0000u); }
DI u32x4 pack8(const f32x4& a, const f32x4& b) { return (u32x4){pk2(a[0], a[1]), pk2(a[2], a[3]), pk2(b[0], b[1]), pk2(b[2], b[3])}; }
DI bf16x8 asbf(const u32x4& v) { return __builtin_bit_cast(bf16x8, v); }
#define SB() __builtin_amdgcn_sched_barrier(0)
DI void bar() { asm volatile("" ::: "memory"); __builtin_amdgcn_s_barrier(); asm volatile("" ::: "memory"); }
DI void wait_lgkm0() { __builtin_amdgcn_s_waitcnt(0xC07F); asm volatile("" ::: "memory"); }
template <int N> DI void wait_vm() { asm volatile("s_waitcnt vmcnt(%0)" ::"i"(N) : "memory"); }
template <bool SC1> DI void dma(unsigned voff, const char* sbase, unsigned lds) {
  if (SC1) asm volatile("s_mov_b32 m0, %2\n\ts_nop 3\n\tglobal_load_lds_dwordx4 %0, %1 sc1" ::"v"(voff), "s"(sbase), "s"(lds) : "memory");
  else asm volatile("s_mov_b32 m0, %2\n\ts_nop 3\n\tglobal_load_lds_dwordx4 %0, %1" ::"v"(voff), "s"(sbase), "s"(lds) : "memory");
}
template <bool SC1> DI void dma4(unsigned voff, const char* sbase, unsigned lds) {
  if (SC1) asm volatile("s_mov_b32 m0, %2\n\ts_nop 3\n\tglobal_load_lds_dword %0, %1 sc1" ::"v"(voff), "s"(sbase), "s"(lds) : "memory");
  else asm volatile("s_mov_b32 m0, %2\n\ts_nop 3\n\tglobal_load_lds_dword %0, %1" ::"v"(voff), "s"(sbase), "s"(lds) : "memory");
}
DI u32x4 lds128(lds_ptr p) { return *(const LAS u32x4*)p; }
DI u32x4 lds_tr2(lds_ptr p0, lds_ptr p1) {
  s16x4 a = __builtin_amdgcn_ds_read_tr16_b64_v4i16((LAS s16x4*)p0);
  s16x4 b = __builtin_amdgcn_ds_read_tr16_b64_v4i16((LAS s16x4*)p1);
  return __builtin_bit_cast(u32x4, __builtin_shufflevector(a, b, 0, 1, 2, 3, 4, 5, 6, 7));
}
DI u32x2 lds_tr(lds_ptr p) { return __builtin_bit_cast(u32x2, __builtin_amdgcn_ds_read_tr16_b64_v4i16((LAS s16x4*)p)); }
DI f32x4 mfma(const u32x4& a, const u32x4& b, const f32x4& c) { return __builtin_amdgcn_mfma_f32_16x16x32_bf16(asbf(a), asbf(b), c, 0, 0, 0); }
DI void mfma_ip(f32x4& acc, const u32x4& a, const u32x4& b) { asm volatile("v_mfma_f32_16x16x32_bf16 %0, %1, %2, %0" : "+v"(acc) : "v"(a), "v"(b)); }
DI u32x4 ld16(rsrc_t r, unsigned voff, unsigned soff) { return __builtin_amdgcn_raw_buffer_load_b128(r, voff, soff, 16); }
DI void st16(rsrc_t r, const u32x4& v, unsigned voff, unsigned soff) { __builtin_amdgcn_raw_buffer_store_b128(v, r, voff + soff, 0, 16);
}
DI void st8(rsrc_t r, const u32x2& v, unsigned voff, unsigned soff) { __builtin_amdgcn_raw_buffer_store_b64(v, r, voff, soff, 16); }
DI void st4(rsrc_t r, unsigned v, unsigned voff, unsigned soff) { __builtin_amdgcn_raw_buffer_store_b32(v, r, voff, soff, 16); }
DI float fb(unsigned u) { return __uint_as_float(u); }
DI unsigned bu(float f) { return __float_as_uint(f); }
DI float xsum16(float x) { auto r = __builtin_amdgcn_permlane16_swap(bu(x), bu(x), false, false); return fb(r[0]) + fb(r[1]); }
DI float xsum32(float x) { auto r = __builtin_amdgcn_permlane32_swap(bu(x), bu(x), false, false); return fb(r[0]) + fb(r[1]); }
template <int CTRL> DI float dpp(float x) { return __builtin_bit_cast(float, __builtin_amdgcn_update_dpp(0, __builtin_bit_cast(int, x), CTRL, 0xf, 0xf, true)); }
DI float row16_sum(float x) { x += dpp<0xB1>(x); x += dpp<0x4E>(x); x += dpp<0x141>(x); x += dpp<0x140>(x); return x; }

struct Sync { rsrc_t r; unsigned tag, pb; LAS unsigned* w; };
DI unsigned near(const Sync& y, unsigned off, int tm) { return off + ((((y.pb >> (off >> 21)) ^ (tm >> 3)) & 1u) << 13); }
DI unsigned rec(const Sync& y, int phase, int idx) {
  return phase < FL_O ? (phase * 256 + idx) * 128 : near(y, WS_FL2 + (phase - FL_O) * 65536 + (idx >> 6) * 16384 + (idx & 63) * 128, idx >> 4) - WS_FLG;
}
DI void publish(const Sync& y, int phase, int idx, int lane) {
  if (lane == 0) __hip_atomic_fetch_add(y.w + 24 + phase, 1u, __ATOMIC_RELAXED, __HIP_MEMORY_SCOPE_WORKGROUP);
  wait_vm<0>();
  if (lane == 0 && __hip_atomic_fetch_add(y.w + phase, 1u, __ATOMIC_RELAXED, __HIP_MEMORY_SCOPE_WORKGROUP) == 3u) st4(y.r, y.tag, 0, WS_FLG + rec(y, phase, idx));
}
DI void poll(const Sync& y, unsigned off, int n, int lane) {
  const unsigned long long t0 = __builtin_amdgcn_s_memrealtime();
  for (unsigned it = 0;; ++it) {
    const unsigned v = __builtin_amdgcn_raw_buffer_load_b32(y.r, WS_FLG + off, 0, 16);
    if (__all(lane >= n || v == y.tag)) break;
    if ((it & 63) == 63) {
      if (__builtin_amdgcn_readfirstlane(__builtin_amdgcn_raw_buffer_load_b32(y.r, 0, WS_CTL + 128, 16)) == y.tag) break;
      if (__builtin_amdgcn_s_memrealtime() - t0 > 4000000ull) { if (lane == 0) st4(y.r, y.tag, 0, WS_CTL + 128); break; }
    }
    __builtin_amdgcn_s_sleep(2);
  }
  asm volatile("" ::: "memory");
}
DI void lds_wait(const Sync& y, int word, unsigned v);
DI void bar_loaders(const Sync& y, int lw) { if (lw == 1) *(volatile LAS unsigned*)(y.w + 14) = 1u; else lds_wait(y, 14, 1u); }
DI void lds_wait(const Sync& y, int word, unsigned v) {
  volatile LAS unsigned* w = y.w + word;
  while (*w < v) __builtin_amdgcn_s_sleep(1);
  asm volatile("" ::: "memory");
}
struct Gate { unsigned off, mask; int word; };
DI void gate_slow(const Sync& y, Gate& G, unsigned need, bool poller, int lane, int cnt = 0) {
  volatile LAS unsigned* w = y.w + G.word;
  if (poller) {
    const unsigned long long t0 = __builtin_amdgcn_s_memrealtime();
    for (unsigned it = 0;; ++it) {
      G.mask = (unsigned)__ballot(__builtin_amdgcn_raw_buffer_load_b32(y.r, WS_FLG + G.off, 0, 16) == y.tag);
      if ((it & 63) == 63) {
        const bool late = __builtin_amdgcn_s_memrealtime() - t0 > 4000000ull;
        if (late && lane == 0) st4(y.r, y.tag, 0, WS_CTL + 128);
        if (late || __builtin_amdgcn_readfirstlane(__builtin_amdgcn_raw_buffer_load_b32(y.r, 0, WS_CTL + 128, 16)) == y.tag) G.mask = ~0u;
      }
      *w = G.mask;
      if ((G.mask & need) == need && __builtin_popcount(G.mask & 0xffffu) >= cnt) break;
      __builtin_amdgcn_s_sleep(1);
    }
  } else {
    for (;;) { G.mask = __builtin_amdgcn_readfirstlane(*w); if ((G.mask & need) == need && __builtin_popcount(G.mask & 0xffffu) >= cnt) break; __builtin_amdgcn_s_sleep(1); }
  }
  asm volatile("" ::: "memory");
}
DI void gate(const Sync& y, Gate& G, unsigned need, bool poller, int lane) { if (__builtin_expect((G.mask & need) != need, 0)) gate_slow(y, G, need, poller, lane); }

template <int NK, bool SPREAD> DI int kmap(int t, int c) {
  const int u = t - 1, q = u & 3, b4 = u & ~3, body = (int)(u < NK - 4);
  const int lead = ((int)(q == c) & body & (int)(u >= 0)) | (int)(SPREAD && t == 0), head = SPREAD ? 0 : -(int)(u < 4);
  const int non = ((b4 + ((c + 1 + q - ((int)(q > c) & body)) & 3)) & ~head) | ((t - (int)(u > c)) & head);
  return non ^ (((b4 + 4 + c) ^ non) & -lead);
}
#define LAG3 1
#define LAG5 1
enum { ORD_NAT = 0, ORD_PAIR = 1, ORD_F = 2 };
template <int NSUB_, bool HALF_, int NSLOT_, int NK_, int ORD_, int WSTRIDE_, bool XROW_, int LAG_> struct Cfg {
  static constexpr int NSUB = NSUB_, NSLOT = NSLOT_, NK = NK_, ORD = ORD_, WSTRIDE = WSTRIDE_;
  static constexpr bool HALF = HALF_, XROW = XROW_;
  static constexpr int LAG = LAG_;
  static constexpr int STAGE = 16384 + NSUB * 8192 + (HALF ? 4096 : 0), PW = 4 + 2 * NSUB + (HALF ? 1 : 0), RING = STAGE * NSLOT;
  static constexpr int WB = NSUB * 2048 + (HALF ? 1024 : 0);
};
typedef Cfg<3, false, 3, 16, ORD_NAT, 3 * D * 2, true, 0> C1;
typedef Cfg<1, false, 5, 16, ORD_PAIR, D * 2, false, LAG3> C3;
typedef Cfg<3, true, 3, 16, ORD_PAIR, FF * 2, false, 0> C4;
typedef Cfg<2, false, 4, 28, ORD_F, D * 2, false, LAG5> C5;

#define GLDS "global_load_lds_dwordx4 "
template <class C> struct Ld {
  const char* A; const char* W; int lw, c, kt0; unsigned vA[4], vW[7];
  const char* sA; const char* sW; unsigned need;
  Gate G; const Sync* y; int lane_;
  static constexpr int C0 = C::WB > 4096 ? 4096 : 0;
  DI void init(const Sync& y_, int lane, int substep, int halfrel, unsigned goff, int gword) {
    y = &y_; lane_ = lane; G.off = goff; G.mask = 0; G.word = gword;
#pragma unroll
    for (int i = 0; i < 4; ++i) vA[i] = C::XROW ? (lane >> 3) * 2048 + ((((lane & 7) ^ (lane >> 4)) << 4) ^ ((i & 1) * 64)) + i * (16384 - 1024) : lane * 16;
    const int rl = (C::ORD == ORD_NAT) ? 8 * (lane >> 5) + ((lane >> 3) & 3) : (lane >> 3);
    const unsigned w0 = rl * C::WSTRIDE + (((lane & 7) ^ (((lane >> 4) & 3) << 1)) << 4) + 4096;
#pragma unroll
    for (int q = 0; q < 2 * C::NSUB; ++q) vW[q] = w0 + (q & 1) * ((C::ORD == ORD_NAT ? 16 : 8) * C::WSTRIDE) + (q >> 1) * substep - (q * 1024 - C0);
    if (C::HALF) vW[6] = (lane >> 2) * C::WSTRIDE + (((lane & 3) ^ (((lane >> 4) & 1) << 1)) << 4) + 4096 + halfrel - (6144 - C0);
    A += C::XROW ? lw * 32 * 2048 : (lw >> 1) * 16384 + (lw & 1) * 4096;
    W -= 4096;
  }
  DI void prep(int s) {
    int kt = kmap<C::NK, C::XROW>(s, c);
    const int kb = lw >> 1, t = lw & 1;
    int ur;
    need = 1u << kt;
    if (C::ORD == ORD_NAT) ur = 32 * kb + 4 * t;
    else if (C::ORD == ORD_F) {
      const int p = kt & 7, q = kt >> 3, sp = -(int)(q == 3);
      need = ((1u << (2 * p + (int)(q > 0))) & ~sp) | ((0xau << (4 * (p & 3))) & sp);
      kt = kt0 + (((7 * (p >> 1) + 4 * (p & 1) + q) & ~sp) | ((7 * p + 3) & sp));
      ur = (16 * lw & sp) | ((16 * kb + 32 * t) & ~sp);
    }
    else ur = 16 * lw;
    sA = A + (unsigned)(kt * (C::XROW ? 128 : 32768));
    sW = W + (unsigned)((kt * 64 + ur) * C::WSTRIDE);
  }
  DI void issueA(unsigned base) {
    if (!C::XROW) gate(*y, G, need, lw == 0, lane_);
    const unsigned m = base + lw * 4096;
    if (C::XROW) asm volatile("s_mov_b32 m0, %5\n\ts_nop 3\n\t" GLDS "%0, %4\n\t" GLDS "%1, %4 offset:1024\n\t" GLDS "%2, %4 offset:2048\n\t" GLDS "%3, %4 offset:3072"
                              ::"v"(vA[0]), "v"(vA[1]), "v"(vA[2]), "v"(vA[3]), "s"(sA), "s"(m) : "memory");
    else asm volatile("s_mov_b32 m0, %2\n\ts_nop 3\n\t" GLDS "%0, %1 sc1\n\t" GLDS "%0, %1 offset:1024 sc1\n\t" GLDS "%0, %1 offset:2048 sc1\n\t" GLDS "%0, %1 offset:3072 sc1"
                      ::"v"(vA[0]), "s"(sA), "s"(m) : "memory");
  }
  DI void issueW(unsigned base) const {
    const unsigned m = base + 16384 + lw * C::WB + C0;
    if (C::NSUB == 1) asm volatile("s_mov_b32 m0, %3\n\ts_nop 3\n\t" GLDS "%0, %2\n\t" GLDS "%1, %2 offset:1024" ::"v"(vW[0]), "v"(vW[1]), "s"(sW), "s"(m) : "memory");
    if (C::NSUB == 2) asm volatile("s_mov_b32 m0, %5\n\ts_nop 3\n\t" GLDS "%0, %4\n\t" GLDS "%1, %4 offset:1024\n\t" GLDS "%2, %4 offset:2048\n\t" GLDS "%3, %4 offset:3072"
                                   ::"v"(vW[0]), "v"(vW[1]), "v"(vW[2]), "v"(vW[3]), "s"(sW), "s"(m) : "memory");
    if (C::NSUB == 3) asm volatile("s_mov_b32 m0, %7\n\ts_nop 3\n\t" GLDS "%0, %6 offset:-4096\n\t" GLDS "%1, %6 offset:-3072\n\t" GLDS "%2, %6 offset:-2048\n\t" GLDS "%3, %6 offset:-1024\n\t"
                                   GLDS "%4, %6\n\t" GLDS "%5, %6 offset:1024"
                                   ::"v"(vW[0]), "v"(vW[1]), "v"(vW[2]), "v"(vW[3]), "v"(vW[4]), "v"(vW[5]), "s"(sW), "s"(m) : "memory");
    if (C::HALF) asm volatile(GLDS "%0, %1 offset:2048" ::"v"(vW[6]), "s"(sW) : "memory");
  }
};
template <class C> DI void load_preW(Ld<C>& L, unsigned ring, int s0 = 0, int s1 = C::NSLOT) {
#pragma unroll 1
  for (int s = s0; s < s1; ++s) { L.prep(s); L.issueW(ring + s * C::STAGE); }
}
template <class C, bool SPLIT> constexpr int vm_allow(int S) {
  const int N = C::NSLOT, behind = S - 1 - C::LAG > 0 ? S - 1 - C::LAG : 0;
  const int ramp = S + 1 < N - 1 ? S + 1 : N - 1;
  const int last = ramp + behind < C::NK - 1 ? ramp + behind : C::NK - 1;
  return (SPLIT && S < N) ? 4 * (ramp - S) + C::PW * (last - ramp) : C::PW * (last - S);
}
template <class C, bool SPLIT, unsigned POSTM, int S, int SE, class Post> DI void load_fixed(Ld<C>& L, unsigned ring, const Post& post) {
  if constexpr (S < SE) {
    static_assert(C::NSLOT <= C::LAG + 4, "the ring must be full before the first refill");
    wait_vm<vm_allow<C, SPLIT>(S)>();
    bar();
    if constexpr (S + 2 < C::NSLOT) {
      if (!SPLIT) L.issueW(ring + (S + 2) * C::STAGE);
      L.issueA(ring + (S + 2) * C::STAGE);
      L.prep(S + 3);
    }
    constexpr int T = S - 1 - C::LAG + C::NSLOT;
    if constexpr (S - 1 - C::LAG >= 0 && T < C::NK) {
      L.issueW(ring + (T % C::NSLOT) * C::STAGE); L.issueA(ring + (T % C::NSLOT) * C::STAGE);
      if constexpr (T + 1 < C::NK) L.prep(T + 1);
    }
    if constexpr ((POSTM >> S) & 1u) post(S);
    load_fixed<C, SPLIT, POSTM, S + 1, SE>(L, ring, post);
  }
}
template <class C, bool SPLIT, unsigned PREM, unsigned POSTM, class Pre, class Post>
DI void load_all(Ld<C>& L, unsigned ring, const Pre& pre, const Post& post) {
  constexpr int N = C::NSLOT, S0 = N - 1 > 2 + C::LAG ? N - 1 : 2 + C::LAG, S1 = C::NK - N + C::LAG;
#pragma unroll 1
  for (int s = 0; s < 2; ++s) { L.prep(s); if (!SPLIT) L.issueW(ring + s * C::STAGE); L.issueA(ring + s * C::STAGE); }
  L.prep(2);
  load_fixed<C, SPLIT, POSTM, 0, S0>(L, ring, post);
  static_assert(vm_allow<C, SPLIT>(S0) == (N - 2 - C::LAG) * C::PW && vm_allow<C, SPLIT>(S1) == (N - 2 - C::LAG) * C::PW, "steady count");
  unsigned b = ring + ((S0 - 1 - C::LAG) % N) * C::STAGE;
#pragma unroll 1
  for (int s = S0; s <= S1; ++s) {
    wait_vm<(N - 2 - C::LAG) * C::PW>();
    if (PREM && ((PREM >> s) & 1u)) pre(s);
    bar();
    L.issueW(b);
    L.issueA(b);
    L.prep(min(s - C::LAG + N, C::NK - 1));
    b = (b == ring + (N - 1) * C::STAGE) ? ring : b + C::STAGE;
    if (POSTM && ((POSTM >> s) & 1u)) post(s);
  }
  load_fixed<C, SPLIT, POSTM, S1 + 1, C::NK>(L, ring, post);
  bar();
}

template <int MT, int NWF> struct Fr { u32x4 a[MT]; u32x2 w[NWF][2]; };
DI u32x4 cat(const u32x2 (&w)[2]) { return (u32x4){w[0][0], w[0][1], w[1][0], w[1][1]}; }
struct Adr { unsigned v[6]; };
template <int KIND> struct Shape;
template <> struct Shape<1> { static constexpr int MT = 4, NWF = 6, NMF = 24, NACC = 24, WB = 6144; static constexpr unsigned USE = 0x0f; };
template <> struct Shape<3> { static constexpr int MT = 2, NWF = 4, NMF = 8, NACC = 8, WB = 2048; static constexpr unsigned USE = 0x3d; };
template <> struct Shape<4> { static constexpr int MT = 4, NWF = 8, NMF = 28, NACC = 28, WB = 7168; static constexpr unsigned USE = 0x3f; };
template <> struct Shape<5> { static constexpr int MT = 4, NWF = 4, NMF = 16, NACC = 16, WB = 4096; static constexpr unsigned USE = 0x3f; };
DI lds_ptr lp(unsigned a) { return (lds_ptr)(size_t)a; }
template <int KIND, int J> DI void rd1(Fr<Shape<KIND>::MT, Shape<KIND>::NWF>& f, const Adr& ad, int kb) {
  constexpr int NWF = Shape<KIND>::NWF, MT = Shape<KIND>::MT, WB = Shape<KIND>::WB;
  if constexpr (J < 2 * NWF) {
    constexpr int n = J >> 1, t = J & 1;
    if constexpr (KIND == 4 && n >= 6) f.w[n][t] = lds_tr(lp(ad.v[4 + (n - 6)]) + (2 * kb + t) * WB);
    else if constexpr (KIND == 1 || KIND == 4) f.w[n][t] = lds_tr(lp(ad.v[2 + (n & 1)]) + (n >> 1) * 2048 + (2 * kb + t) * WB);
    else f.w[n][t] = lds_tr(lp(ad.v[2 + n]) + (2 * kb + t) * WB);
  } else if constexpr (J < 2 * NWF + MT) {
    constexpr int m = J - 2 * NWF;
    if constexpr (KIND == 1) f.a[m] = lds128(lp(ad.v[kb]) + m * 2048);
    else f.a[m] = lds128(lp(ad.v[m >> 1]) + (m & 1) * 2048 + kb * 1024);
  }
}
template <int KIND, int I> DI void mm1(f32x4 (&acc)[Shape<KIND>::NACC], const Fr<Shape<KIND>::MT, Shape<KIND>::NWF>& f) {
  if constexpr (KIND == 1) { constexpr int m = I / 6, n = I % 6; if constexpr (n < 4) mfma_ip(acc[I], cat(f.w[n]), f.a[m]); else mfma_ip(acc[I], f.a[m], cat(f.w[n])); }
  else if constexpr (KIND == 4) { if constexpr (I < 24) mfma_ip(acc[I], cat(f.w[I % 6]), f.a[I / 6]); else mfma_ip(acc[I], cat(f.w[6 + ((I - 24) & 1)]), f.a[(I - 24) >> 1]); }
  else mfma_ip(acc[I], cat(f.w[I % 4]), f.a[I / 4]);
}
template <int KIND, int RPG, bool READ, bool MOVE, int I = 0>
DI void step(f32x4 (&acc)[Shape<KIND>::NACC], const Fr<Shape<KIND>::MT, Shape<KIND>::NWF>& cur, Fr<Shape<KIND>::MT, Shape<KIND>::NWF>& nxt, Adr& ad, int kb, int delta) {
  constexpr int NMF = Shape<KIND>::NMF, G0 = (2 * Shape<KIND>::NWF + Shape<KIND>::MT + RPG - 1) / RPG;
  if constexpr (I < NMF) {
    if constexpr (READ) {
      rd1<KIND, I * RPG>(nxt, ad, kb);
      if constexpr (RPG > 1) rd1<KIND, I * RPG + 1>(nxt, ad, kb);
    }
    if constexpr (MOVE && I >= G0) {
      constexpr int per = (6 + (NMF - G0) - 1) / (NMF - G0);
#pragma unroll
      for (int k = (I - G0) * per; k < (I - G0 + 1) * per && k < 6; ++k)
        if ((Shape<KIND>::USE >> k) & 1u) { ad.v[k] += delta; asm volatile("" : "+v"(ad.v[k])); }
    }
    mm1<KIND, I>(acc, cur);
    SB();
    step<KIND, RPG, READ, MOVE, I + 1>(acc, cur, nxt, ad, kb, delta);
  }
}
template <int KIND, int J = 0> DI void rdall(Fr<Shape<KIND>::MT, Shape<KIND>::NWF>& f, const Adr& ad, int kb) {
  if constexpr (J < 2 * Shape<KIND>::NWF + Shape<KIND>::MT) { rd1<KIND, J>(f, ad, kb); rdall<KIND, J + 1>(f, ad, kb); }
}
template <class C, int KIND, int RPG, int CNT>
DI void stages(f32x4 (&acc)[Shape<KIND>::NACC], Fr<Shape<KIND>::MT, Shape<KIND>::NWF>& f0, Fr<Shape<KIND>::MT, Shape<KIND>::NWF>& f1, Adr& ad, int& slot) {
#pragma unroll UNR
  for (int s = 0; s < CNT; ++s) {
    const int delta = (slot + 1 == C::NSLOT) ? -(C::NSLOT - 1) * C::STAGE : C::STAGE;
    slot = (slot + 1 == C::NSLOT) ? 0 : slot + 1;
    SB();
    step<KIND, RPG, true, true>(acc, f0, f1, ad, 1, delta);
    if (!C::LAG) wait_lgkm0();
    bar();
    SB();
    step<KIND, RPG, true, false>(acc, f1, f0, ad, 0, 0);
  }
}
template <class C, int KIND, int RPG, int H0, int H1, int H2, class Hook> DI void consume(f32x4 (&acc)[Shape<KIND>::NACC], Adr& ad, const Hook& hook) {
  Fr<Shape<KIND>::MT, Shape<KIND>::NWF> f0, f1;
#pragma unroll
  for (int i = 0; i < Shape<KIND>::NACC; ++i) { acc[i] = (f32x4){0.f, 0.f, 0.f, 0.f}; asm volatile("" : "+v"(acc[i])); }
  bar();
  rdall<KIND>(f0, ad, 0);
  hook(-1);
  wait_lgkm0();
  int slot = 0;
  if constexpr (H1 > 0) {
    if constexpr (H0 > 0) stages<C, KIND, RPG, H0>(acc, f0, f1, ad, slot);
    hook(0);
    stages<C, KIND, RPG, H1 - H0>(acc, f0, f1, ad, slot);
    hook(1);
    stages<C, KIND, RPG, H2 - H1>(acc, f0, f1, ad, slot);
    hook(2);
  }
  stages<C, KIND, RPG, C::NK - 1 - H2>(acc, f0, f1, ad, slot);
  SB();
  step<KIND, RPG, true, false>(acc, f0, f1, ad, 1, 0);
  wait_lgkm0();
  bar();
  SB();
  step<KIND, RPG, false, false>(acc, f1, f0, ad, 0, 0);
  asm volatile("s_nop 7\n\ts_nop 3");
  SB();
}
template <class C, int KIND, int RPG, class Hook> DI void consume_s(f32x4 (&acc)[Shape<KIND>::NACC], Adr& ad, const Hook& hook) {
  Fr<Shape<KIND>::MT, Shape<KIND>::NWF> f0, f1, g0, g1;
#pragma unroll
  for (int i = 0; i < Shape<KIND>::NACC; ++i) { acc[i] = (f32x4){0.f, 0.f, 0.f, 0.f}; asm volatile("" : "+v"(acc[i])); }
  bar();
  rdall<KIND>(f0, ad, 0); rdall<KIND>(f1, ad, 1);
  hook(-1);
#pragma unroll
  for (int k = 0; k < 6; ++k) ad.v[k] += C::STAGE;
  int slot = 1;
#pragma unroll 1
  for (int s = 0; s < C::NK - 2; s += 2) {
    int delta = (slot + 1 == C::NSLOT) ? -(C::NSLOT - 1) * C::STAGE : C::STAGE;
    slot = (slot + 1 == C::NSLOT) ? 0 : slot + 1;
    if (!C::LAG) wait_lgkm0();
    bar(); SB();
    step<KIND, RPG, true, false>(acc, f0, g0, ad, 0, 0);
    step<KIND, RPG, true, true>(acc, f1, g1, ad, 1, delta);
    delta = (slot + 1 == C::NSLOT) ? -(C::NSLOT - 1) * C::STAGE : C::STAGE;
    slot = (slot + 1 == C::NSLOT) ? 0 : slot + 1;
    if (!C::LAG) wait_lgkm0();
    bar(); SB();
    step<KIND, RPG, true, false>(acc, g0, f0, ad, 0, 0);
    step<KIND, RPG, true, true>(acc, g1, f1, ad, 1, delta);
  }
  if (!C::LAG) wait_lgkm0();
  bar(); SB();
  step<KIND, RPG, true, false>(acc, f0, g0, ad, 0, 0);
  step<KIND, RPG, true, false>(acc, f1, g1, ad, 1, 0);
  wait_lgkm0(); bar(); SB();
  step<KIND, RPG, false, false>(acc, g0, f0, ad, 0, 0);
  step<KIND, RPG, false, false>(acc, g1, f1, ad, 1, 0);
  asm volatile("s_nop 7\n\ts_nop 3");
  SB();
}
DI unsigned wlane(int lane, int nt) {
  const int g = lane >> 4, q = (lane >> 2) & 3, p = lane & 3, sw = (2 * g + (q >> 1)) & 3;
  return (4 * g + q) * 128 + ((nt ^ sw) << 5) + p * 8;
}
DI unsigned hlane(int lane, int nt) {
  const int g = lane >> 4, q = (lane >> 2) & 3, p = lane & 3;
  return (4 * g + q) * 64 + ((nt ^ (g & 1)) << 5) + p * 8;
}

template <int OP> DI float op2(float a, float b) { return OP == 0 ? a + b : OP == 1 ? __builtin_elementwise_maximum(a, b) : __builtin_elementwise_minimum(a, b); }
template <int OP> DI float red4(float x0, float x1, float x2, float x3) {
  auto a = __builtin_amdgcn_permlane16_swap(bu(x0), bu(x1), false, false);
  auto b = __builtin_amdgcn_permlane16_swap(bu(x2), bu(x3), false, false);
  const float y0 = op2<OP>(fb(a[0]), fb(a[1])), y1 = op2<OP>(fb(b[0]), fb(b[1]));
  auto c = __builtin_amdgcn_permlane32_swap(bu(y0), bu(y1), false, false);
  return op2<OP>(fb(c[0]), fb(c[1]));
}
DI void spread(float z, float (&o)[4]) {
  auto a = __builtin_amdgcn_permlane32_swap(bu(z), bu(z), false, false);
  auto b = __builtin_amdgcn_permlane16_swap(a[0], a[0], false, false);
  auto c = __builtin_amdgcn_permlane16_swap(a[1], a[1], false, false);
  o[0] = fb(b[0]); o[1] = fb(b[1]); o[2] = fb(c[0]); o[3] = fb(c[1]);
}
DI void dual_softmax4(const f32x4 (&p)[4][2], u32x4 (&pp)[4], u32x4 (&pn)[4]) {
  constexpr float L2E = 1.4426950408889634f;
  float mx[4], mn[4];
#pragma unroll
  for (int m = 0; m < 4; ++m) {
    mx[m] = op2<1>(op2<1>(op2<1>(op2<1>(p[m][0][0], p[m][0][1]), p[m][0][2]), op2<1>(op2<1>(p[m][0][3], p[m][1][0]), p[m][1][1])), op2<1>(p[m][1][2], p[m][1][3]));
    mn[m] = op2<2>(op2<2>(op2<2>(op2<2>(p[m][0][0], p[m][0][1]), p[m][0][2]), op2<2>(op2<2>(p[m][0][3], p[m][1][0]), p[m][1][1])), op2<2>(p[m][1][2], p[m][1][3]));
  }
  spread(-L2E * red4<1>(mx[0], mx[1], mx[2], mx[3]), mx);
  spread(L2E * red4<2>(mn[0], mn[1], mn[2], mn[3]), mn);
  f32x4 ep[4][2], en[4][2];
  float sp[4], sn[4];
#pragma unroll
  for (int m = 0; m < 4; ++m) {
#pragma unroll
    for (int F = 0; F < 2; ++F) {
      const f32x4 a = p[m][F] * L2E + mx[m], b = p[m][F] * (-L2E) + mn[m];
#pragma unroll
      for (int r = 0; r < 4; ++r) { ep[m][F][r] = __builtin_amdgcn_exp2f(a[r]); en[m][F][r] = __builtin_amdgcn_exp2f(b[r]); }
    }
    const f32x4 a = ep[m][0] + ep[m][1], b = en[m][0] + en[m][1];
    sp[m] = (a[0] + a[1]) + (a[2] + a[3]); sn[m] = (b[0] + b[1]) + (b[2] + b[3]);
  }
  spread(__builtin_amdgcn_rcpf(red4<0>(sp[0], sp[1], sp[2], sp[3])), sp);
  spread(__builtin_amdgcn_rcpf(red4<0>(sn[0], sn[1], sn[2], sn[3])), sn);
#pragma unroll
  for (int m = 0; m < 4; ++m) { pp[m] = pack8(ep[m][0] * sp[m], ep[m][1] * sp[m]); pn[m] = pack8(en[m][0] * sn[m], en[m][1] * sn[m]); }
}

DI float dot2(unsigned a, unsigned b, float c) { return __builtin_amdgcn_fdot2_f32_bf16(__builtin_bit_cast(bf16x2, a), __builtin_bit_cast(bf16x2, b), c, false); }
DI unsigned opaque(unsigned c) { asm volatile("" : "+s"(c)); return c; }
template <int NL> DI void colsum_load(u32x4 (&v)[NL], const char* w, int stride, int lane) {
#pragma unroll
  for (int i = 0; i < NL; ++i) v[i] = *(const u32x4*)(w + (size_t)(8 * i + (lane >> 3)) * stride + (lane & 7) * 16);
}
template <int NL> DI void colsum_finish(const u32x4 (&v)[NL], rsrc_t r, unsigned dst, int lane) {
  f32x2 s2[4];
#pragma unroll
  for (int j = 0; j < 4; ++j) s2[j] = (f32x2){bflo(v[0][j]), bfhi(v[0][j])};
#pragma unroll
  for (int i = 1; i < NL; ++i)
#pragma unroll
    for (int j = 0; j < 4; ++j) s2[j] += (f32x2){bflo(v[i][j]), bfhi(v[i][j])};
  const float s[8] = {s2[0][0], s2[0][1], s2[1][0], s2[1][1], s2[2][0], s2[2][1], s2[3][0], s2[3][1]};
  float t[4], u[2];
#pragma unroll
  for (int j = 0; j < 4; ++j) { auto q = __builtin_amdgcn_permlane32_swap(bu(s[j]), bu(s[j + 4]), false, false); t[j] = fb(q[0]) + fb(q[1]); }
#pragma unroll
  for (int j = 0; j < 2; ++j) { auto q = __builtin_amdgcn_permlane16_swap(bu(t[j]), bu(t[j + 2]), false, false); u[j] = fb(q[0]) + fb(q[1]); }
  const bool b3 = lane & 8;
  const float tot = (b3 ? u[1] : u[0]) + dpp<0x128>(b3 ? u[0] : u[1]);
  st4(r, bu(tot), ((lane & 7) * 8 + ((lane >> 5) & 1) * 4 + ((lane >> 4) & 1) * 2 + ((lane >> 3) & 1)) * 4, dst);
}

template <int XN, int TM, int TN> DI void tile_of(int bid, int& tm, int& tn, int& c) {
  const int x = bid & 7, r = bid >> 3, li = r % TM, lj = r / TM;
  tm = (x / XN) * TM + li; tn = (x % XN) * TN + lj; c = (li + lj) & 3;
}

#define RPG1 1
#define RPG4 1
#define RPG5 1
#define W2AT 0
#define PH0 2
#define PH1 6
#define PH2 7
#define PF 10
#define W1M 0u
constexpr int PR = PF + 6 > 15 ? 15 : PF + 6;
#define NPROBE 4
#define FAT 1
#define E4 0
constexpr int R1 = 40704, R3 = 40704, R4 = 0, R5 = 0, SYNCW = 163584;
constexpr int S1_ST = 0, S1_CSR = 1024, S1_CSF = 13312, S1_FLG = 14080, S1_WP = 14336, S1_KV = 18432, S1_DUM = 26880;
constexpr int S4_STR = C4::RING, S4_CSR = S4_STR + 16384, S4_STF = S4_CSR + 8192, S4_CSF = S4_STF + 1024, S4_DUM = S4_CSF + 1024;
constexpr int LDS_BYTES = 163840, CODE_BYTES = 54 * 1024;
static_assert(S4_DUM + 256 <= SYNCW && S1_KV + 8448 <= R1 && R1 + C1::RING <= SYNCW && R3 + C3::RING <= SYNCW && C5::RING <= S4_STR, "lds");

template <class St> DI void kv_sum(const Sync& y, Gate& G, lds_ptr smem, int head, int h, int t, bool poller, bool doz, int lane, const St& stamp) {
  u32x4 v[16], zz[4];
  unsigned have = 0;
  f32x2 s2[4] = {{0.f, 0.f}, {0.f, 0.f}, {0.f, 0.f}, {0.f, 0.f}};
#pragma unroll 1
  for (int round = poller ? 2 : 0; round < 3; ++round) {
    gate_slow(y, G, 0u, poller, lane, round == 0 ? 0 : round == 1 ? 13 : 16);
    stamp(round);
    const unsigned fresh = G.mask & ~have;
    have = G.mask;
#pragma unroll
    for (int r = 0; r < 16; ++r) if ((fresh >> r) & 1u) v[r] = ld16(y.r, t * 16, WS_KVP + (head * 16 + r) * 4096);
    if (round == 2 && doz) {
#pragma unroll
      for (int i = 0; i < 4; ++i) zz[i] = ld16(y.r, lane * 16, WS_ZP + head * 4096 + i * 1024);
    }
  }
#pragma unroll
  for (int r = 0; r < 16; ++r)
#pragma unroll
    for (int j = 0; j < 4; ++j) s2[j] += (f32x2){bflo(v[r][j]), bfhi(v[r][j])};
  const float s[8] = {s2[0][0], s2[0][1], s2[1][0], s2[1][1], s2[2][0], s2[2][1], s2[3][0], s2[3][1]};
  *(LAS u32x4*)(smem + S1_KV + h * 4096 + t * 16) = (u32x4){pk2(s[0], s[1]), pk2(s[2], s[3]), pk2(s[4], s[5]), pk2(s[6], s[7])};
  if (doz) {
    f32x4 z = (__builtin_bit_cast(f32x4, zz[0]) + __builtin_bit_cast(f32x4, zz[1])) + (__builtin_bit_cast(f32x4, zz[2]) + __builtin_bit_cast(f32x4, zz[3]));
#pragma unroll
    for (int j = 0; j < 4; ++j) z[j] = xsum32(xsum16(z[j]));
    if (lane < 16) *(LAS u32x2*)(smem + S1_KV + 8192 + h * 128 + lane * 8) = (u32x2){pk2(z[0], z[1]), pk2(z[2], z[3])};
  }
}

__global__ __launch_bounds__(512, 2) void fused(Params P) {
  extern __shared__ __attribute__((aligned(16))) char smem_[];
  lds_ptr smem = (lds_ptr)smem_;
  const unsigned lds0 = (unsigned)(size_t)smem;
  const int wid = __builtin_amdgcn_readfirstlane(threadIdx.x >> 6), lane = threadIdx.x & 63, bid = blockIdx.x;
  {
    const char* pc; asm volatile("s_getpc_b64 %0" : "=s"(pc));
    const unsigned co = ((bid >> 3) & 7) * 8192 + threadIdx.x * 128;
    if (threadIdx.x < 64 && co < CODE_BYTES) dma4<false>(co, pc, lds0 + S1_DUM);
    SB();
  }
  int tm1, tn1, c1, tm3, tn3, c3;
  tile_of<4, 8, 4>(bid, tm1, tn1, c1);
  tile_of<2, 4, 8>(bid, tm3, tn3, c3);
  const int kh = bid & 1, tm5 = ((bid & 7) >> 1) * 4 + ((bid >> 3) & 3), tn5 = bid >> 5, c5 = ((bid >> 3) + tn5) & 3;
  const int par = tn1 & 1, base7 = 7 * (tn1 >> 1), ch0 = base7 + 4 * par;
  const int g = lane >> 4, m16 = lane & 15;
  Sync y;
  y.r = __builtin_amdgcn_make_buffer_rsrc(P.ws, 0, WS_END, 0x00020000);
  y.w = (LAS unsigned*)(smem + SYNCW); y.pb = 0;

  if (wid >= 4) {
    const int lw = wid - 4, lt = threadIdx.x - 256;
    const auto nop = [](int) {};
    {
      Ld<C1> L; L.A = P.x + (size_t)tm1 * 128 * 2048; L.W = P.wqkv + tn1 * 128; L.lw = lw; L.c = c1; L.kt0 = 0; L.init(y, lane, 2048, 0, 0, 0);
      const unsigned frec = (FL_P0 * 256 + (lane < 16 ? tm1 * 16 + lane : lane < 28 ? ((lane - 16) / 3) * 48 + ((lane - 16) % 3) * 16 + tn1 : tn1)) * 128;
      if (lw == 0) dma4<true>(0u * lane, P.ws + WS_CTL, lds0 + S1_FLG);
      load_all<C1, false, (1u << (PF + 3)), 0x8005u | W1M | (1u << PF) | (1u << (PF + 3)) | (1u << PR)>(L, lds0 + R1,
        [&](int s) {
          if (lw == 0) {
            const u32x4 v = *(volatile LAS u32x4*)(smem + S1_CSR + lane * 16);
            if (!__all(v[0] == y.tag && v[1] == y.tag && v[2] == y.tag && v[3] == y.tag)) {
#pragma unroll 1
              for (int k = 0; k < 4; ++k) poll(y, frec + 4 * k, 64, lane);
            }
          }
        },
        [&](int s) {
          if (s == 0) y.tag = __builtin_amdgcn_readfirstlane(*(volatile LAS unsigned*)(smem + S1_FLG)) + 1u;
          if (s == 2) dma<false>(lane * 16, P.wphi + tn1 * 4096 + lw * 1024, lds0 + S1_WP + lw * 1024);
          if (((W1M >> s) & 1u) && lw == __builtin_popcount(W1M & ((1u << s) - 1u))) dma4<false>(min(lane, 55) * 128, P.w1 + bid * 28672 + lw * 7168, lds0 + S1_DUM);
          if (s == PF && lw == 0) dma<true>(frec, P.ws + WS_FLG, lds0 + S1_CSR);
          if (s == 15 && lw == 1) dma4<false>(lane * 128, P.wo + bid * 8192, lds0 + S1_DUM);
          if (s == PF + 3) {
            if (lw == 3) dma<true>(lane * 16, P.ws + WS_ST1 + tm1 * 1024, lds0 + S1_ST);
#pragma unroll
            for (int i = 0; i < 3; ++i)
              dma<true>((lane >> 4) * 12288 + (lane & 15) * 16, P.ws + WS_CSQ + lw * 49152 + i * 4096 + tn1 * 256, lds0 + S1_CSR + i * 4096 + lw * 1024);
          }
          if (s == PR) {
            if (lt < 192) {
              const LAS float* rawp = (const LAS float*)(smem + S1_CSR) + (lt >> 6) * 1024 + (lt & 63);
              float t = 0.f;
#pragma unroll
              for (int i = 0; i < 16; ++i) t += rawp[i * 64];
              ((LAS float*)(smem + S1_CSF))[lt] = t;
            }
            wait_lgkm0();
          }
        });
    }
    {
      Ld<C3> L; L.A = P.ws; L.W = P.wo + tn3 * 128; L.lw = lw; L.c = c3; L.kt0 = 0; L.init(y, lane, 0, 0, 0, 9);
      if (bid >= 224 && bid < 224 + NPAGE) {
        if (lw == 1) {
          const unsigned po = bid == 224 ? WS_PRB : (bid - 224) << 21;
          unsigned tmin[2] = {~0u, ~0u};
#pragma unroll 1
          for (int k = 0; k < NPROBE; ++k) {
            const unsigned long long t0 = __builtin_amdgcn_s_memtime();
            if (lane < 8) st16(y.r, (u32x4){0u, 0u, 0u, 0u}, lane * 16, po + (k & 1) * 8192);
            wait_vm<0>();
            const unsigned dt = (unsigned)(__builtin_amdgcn_s_memtime() - t0);
            if (k & 1) tmin[1] = min(tmin[1], dt); else tmin[0] = min(tmin[0], dt);
          }
          if (lane == 0) st4(y.r, (y.tag << 1) | (((unsigned)(tmin[1] < tmin[0]) ^ (unsigned)(bid >> 2)) & 1u), 0, WS_FLG + (FL_CS * 256 + bid) * 128 + 4);
        }
        bar_loaders(y, lw);
      }
      load_preW<C3>(L, lds0 + R3);
      if (lw >= 2 && bid < 224) {
        u32x4 v[16];
        colsum_load<16>(v, P.w1 + (size_t)((bid / 56) * 256 + (lw - 2) * 128) * (FF * 2) + (bid % 56) * 128, FF * 2, lane);
        colsum_finish<16>(v, y.r, WS_CS1 + (((bid / 56) * 2 + lw - 2) * FF + (bid % 56) * 64) * 4, lane);
      }
      publish(y, FL_CS, bid, lane);
      lds_wait(y, 24 + FL_KV, 4u);
      Gate G; G.off = (FL_KV * 256 + m16 * 16 + tn1) * 128; G.mask = 0; G.word = 8;
      const unsigned pbo = WS_FLG + (FL_CS * 256 + 224 + (lane & 31)) * 128 + 4;
      unsigned pbv = 0;
      kv_sum(y, G, smem, 2 * tn1 + 1, 1, lt, lw == 0, lw == 1, lane, [&](int) { if (lw == 0) pbv = __builtin_amdgcn_raw_buffer_load_b32(y.r, pbo, 0, 16); });
      if (lw == 0) {
        const unsigned long long t0 = __builtin_amdgcn_s_memrealtime();
        while (!__all(lane >= NPAGE || (pbv >> 1) == (y.tag & 0x7fffffffu))) {
          if (__builtin_amdgcn_s_memrealtime() - t0 > 4000000ull) { if (lane == 0) st4(y.r, y.tag, 0, WS_CTL + 128); break; }
          pbv = __builtin_amdgcn_raw_buffer_load_b32(y.r, pbo, 0, 16);
        }
        const unsigned raw = (unsigned)__ballot(pbv & 1u), odd = __builtin_popcount((raw ^ 0xaaaaaaaau) & 0x3fffffdu);
        *(volatile LAS unsigned*)(y.w + 13) = odd <= 6 ? 0xaaaaaaaau : odd >= 19 ? 0x55555555u : raw;
      }
      wait_lgkm0(); bar();
      y.pb = __builtin_amdgcn_readfirstlane(*(volatile LAS unsigned*)(y.w + 13));
      L.A += near(y, WS_O + tm3 * 524288, tm3); L.G.off = rec(y, FL_O, tm3 * 16 + m16);
      load_all<C3, true, 0u, 0u>(L, lds0 + R3, nop, nop);
    }
    {
      Ld<C4> L; L.A = P.ws + near(y, WS_H + tm1 * 524288, tm1); L.W = P.w1 + ch0 * 128; L.lw = lw; L.c = c1; L.kt0 = 0;
      L.init(y, lane, 128, par ? -64 : 384, (lane & 16) ? rec(y, FL_CS, (m16 >> 2) * 56 + base7 + 3 * par + (lane & 3)) : rec(y, FL_H, tm1 * 16 + m16), 10);
      load_preW<C4>(L, lds0 + R4, 0, E4);
      lds_wait(y, 24 + FL_H, 4u);
      load_preW<C4>(L, lds0 + R4, E4);
      load_all<C4, true, 0u, 0xa000u | (W2AT == 0 ? 0x550u : W2AT > 0 ? 1u << (W2AT & 31) : 0u)>(L, lds0 + R4, nop,
        [&](int s) {
          if (W2AT == 0 ? (s < 12 && lw == ((s >> 1) & 3)) : s == W2AT) dma4<false>(min(lane, 55) * 128, P.w2 + bid * 28672 + lw * 7168, lds0 + S4_DUM);
          if (s == 13) {
            gate(y, L.G, ~0u, lw == 0, lane);
#pragma unroll
            for (int i = 0; i < 4; ++i) dma<true>(lane * 16, P.ws + near(y, WS_ST2 + tm1 * 32768, tm1) + (lw >> 1) * 16384 + ((lw & 1) * 4 + i) * 1024, lds0 + S4_STR + (lw * 4 + i) * 1024);
#pragma unroll
            for (int i = 0; i < 2; ++i) dma<true>(lane * 16, P.ws + WS_CS1 + ((lw * 2 + i) * FF + (base7 + 3 * par) * 64) * 4, lds0 + S4_CSR + (lw * 2 + i) * 1024);
          }
          if (s == 15) {
            const LAS float* rawp = (const LAS float*)(smem + S4_CSR) + lt;
            float t = 0.f;
#pragma unroll
            for (int i = 0; i < 8; ++i) t += rawp[i * 256];
            ((LAS float*)(smem + S4_CSF))[lt] = t;
            wait_lgkm0();
          }
          if (s == 15 && lw < 2) {
            f32x2 st[16]; float mu = 0.f, m2 = 0.f;
#pragma unroll
            for (int i = 0; i < 16; ++i) { st[i] = *(const LAS f32x2*)(smem + S4_STR + i * 1024 + lt * 8); mu += st[i][0]; }
            mu *= (1.f / 16.f);
#pragma unroll
            for (int i = 0; i < 16; ++i) { const float d = st[i][0] - mu; m2 += st[i][1] + 64.f * d * d; }
            *(LAS f32x2*)(smem + S4_STF + lt * 8) = (f32x2){mu, __builtin_amdgcn_rsqf(m2 * (1.f / D) + 1e-5f)};
            wait_lgkm0();
          }
        });
    }
    {
      Ld<C5> L; L.A = P.ws + near(y, WS_F + tm5 * 2097152, tm5); L.W = P.w2 + tn5 * 256; L.lw = lw; L.c = c5; L.kt0 = 28 * kh; L.init(y, lane, 128, 0, rec(y, FL_F, tm5 * 16 + 8 * kh + ((lane >> 1) & 7)) + (lane & 1) * 4, 11);
      load_preW<C5>(L, lds0 + R5);

      load_all<C5, true, 0u, 0u>(L, lds0 + R5, nop, nop);
    }
    return;
  }

  const int wm = wid >> 1, wn = wid & 1;
  __builtin_amdgcn_s_setprio(1);
  if (wid == 0 && lane < 32) y.w[lane] = 0u;
  {
    f32x4 acc[24];
    Adr ad;
    ad.v[0] = lds0 + R1 + (wm * 64 + m16) * 128 + ((g ^ (m16 >> 1)) << 4); ad.v[1] = lds0 + R1 + (wm * 64 + m16) * 128 + ((g ^ (m16 >> 1) ^ 4) << 4);
    ad.v[2] = lds0 + R1 + 16384 + wlane(lane, 2 * wn); ad.v[3] = lds0 + R1 + 16384 + wlane(lane, 2 * wn + 1); ad.v[4] = 0; ad.v[5] = 0;
    u32x4 px[4], pw[8];
    const int prow = bid * 8 + wid * 2 + (lane >> 5);
    consume<C1, 1, RPG1, PH0, PH1, PH2>(acc, ad, [&](int s) {
      if (s < 0) {
        y.tag = __builtin_amdgcn_readfirstlane(*(volatile LAS unsigned*)(smem + S1_FLG)) + 1u;
      }
      if (s == 0) {
#pragma unroll
        for (int i = 0; i < 4; ++i) px[i] = *(const u32x4*)(P.x + (size_t)prow * 2048 + ((lane & 31) + 32 * i) * 16);
        colsum_load<8>(pw, P.wqkv + (size_t)((min(bid, 191) / 48) * 256 + wid * 64) * (3 * D * 2) + (bid % 48) * 128, 3 * D * 2, lane);
      }
      if (s == 1) {
#pragma unroll
        for (int i = 0; i < 4; ++i) asm volatile("" : "+v"(px[i]));
#pragma unroll
        for (int i = 0; i < 8; ++i) asm volatile("" : "+v"(pw[i]));
        const unsigned one2 = opaque(0x3f803f80u);
        float sm = 0.f, sq = 0.f;
#pragma unroll
        for (int i = 0; i < 4; ++i)
#pragma unroll
          for (int j = 0; j < 4; ++j) { sm = dot2(px[i][j], one2, sm); sq = dot2(px[i][j], px[i][j], sq); }
        const float mean = xsum16(row16_sum(sm)) * (1.f / D);
        const float var = fmaxf(xsum16(row16_sum(sq)) * (1.f / D) - mean * mean, 0.f);
        if ((lane & 31) == 0) st8(y.r, (u32x2){bu(mean), bu(__builtin_amdgcn_rsqf(var + 1e-5f))}, prow * 8, WS_ST1);
        if (bid < 192) colsum_finish<8>(pw, y.r, WS_CSQ + (((bid / 48) * 4 + wid) * 3 * D + (bid % 48) * 64) * 4, lane);
      }
      if (s == 2) { wait_vm<0>(); if (lane == 0) st4(y.r, y.tag, 0, WS_FLG + (FL_P0 * 256 + bid) * 128 + wid * 4); }
    });
    const int head = 2 * tn1 + wn;
    f32x4 cs[4]; float csv[2];
#pragma unroll
    for (int n = 0; n < 4; ++n) cs[n] = *(const LAS f32x4*)(smem + S1_CSF + ((n >> 1) * 64 + wn * 32 + (n & 1) * 16 + 4 * g) * 4);
#pragma unroll
    for (int n = 0; n < 2; ++n) csv[n] = *(const LAS float*)(smem + S1_CSF + (128 + wn * 32 + n * 16 + m16) * 4);
    u32x4 wp[2];
#pragma unroll
    for (int F = 0; F < 2; ++F) { lds_ptr a0 = smem + S1_WP + wn * 2048 + (4 * g + (m16 >> 2)) * 64 + F * 32 + (lane & 3) * 8; wp[F] = lds_tr2(a0, a0 + 1024); }
    u32x4 qb[4], vb[2][2];
    f32x4 p[4][2];
    const f32x4 zero = {0.f, 0.f, 0.f, 0.f};
    {
      f32x4 vv[4][2];
#pragma unroll
      for (int m = 0; m < 4; ++m) {
        const f32x2 st = *(const LAS f32x2*)(smem + S1_ST + (wm * 64 + m * 16 + m16) * 8);
        const f32x4 s0 = *(const LAS f32x4*)(smem + S1_ST + (wm * 64 + m * 16 + 4 * g) * 8), s1 = *(const LAS f32x4*)(smem + S1_ST + (wm * 64 + m * 16 + 4 * g) * 8 + 16);
        const f32x4 mu = {s0[0], s0[2], s1[0], s1[2]}, rs = {s0[1], s0[3], s1[1], s1[3]};
        qb[m] = pack8((acc[m * 6] - cs[0] * st[0]) * st[1], (acc[m * 6 + 1] - cs[1] * st[0]) * st[1]);
        const u32x4 kb = pack8((acc[m * 6 + 2] - cs[2] * st[0]) * st[1], (acc[m * 6 + 3] - cs[3] * st[0]) * st[1]);
        p[m][0] = mfma(wp[0], kb, zero); p[m][1] = mfma(wp[1], kb, zero);
        vv[m][0] = (acc[m * 6 + 4] - mu * csv[0]) * rs; vv[m][1] = (acc[m * 6 + 5] - mu * csv[1]) * rs;
      }
#pragma unroll
      for (int u = 0; u < 2; ++u) { vb[u][0] = pack8(vv[2 * u][0], vv[2 * u + 1][0]); vb[u][1] = pack8(vv[2 * u][1], vv[2 * u + 1][1]); }
    }
    u32x4 pp[4], pn[4];
    dual_softmax4(p, pp, pn);
    {
      lds_ptr own = smem + R3 + wid * C3::STAGE, oth = smem + R3 + (wid ^ 2) * C3::STAGE;
#pragma unroll
      for (int m = 0; m < 4; ++m) {
        lds_ptr w = own + (16 * m + m16) * 136 + 8 * g;
        *(LAS u32x2*)(w) = (u32x2){pp[m][0], pp[m][1]}; *(LAS u32x2*)(w + 32) = (u32x2){pp[m][2], pp[m][3]};
        *(LAS u32x2*)(w + 64) = (u32x2){pn[m][0], pn[m][1]}; *(LAS u32x2*)(w + 96) = (u32x2){pn[m][2], pn[m][3]};
      }
#pragma unroll
      for (int i = 0; i < 4; ++i) *(LAS u32x4*)(own + 8704 + i * 1024 + lane * 16) = vb[i >> 1][i & 1];
      wait_lgkm0();
      volatile LAS unsigned* xf = y.w + 16;
      xf[wid] = 1u;
      f32x4 kv[2][2] = {{zero, zero}, {zero, zero}}, z[2] = {zero, zero};
      const u32x4 ones = {0x3f803f80u, 0x3f803f80u, 0x3f803f80u, 0x3f803f80u};
      const unsigned ro = (4 * g + (m16 >> 2)) * 136 + wm * 64 + (lane & 3) * 8;
#pragma unroll
      for (int u = 0; u < 2; ++u)
#pragma unroll
        for (int F = 0; F < 2; ++F) {
          const u32x4 pa = lds_tr2(own + ro + u * 32 * 136 + F * 32, own + ro + u * 32 * 136 + F * 32 + 16 * 136);
          kv[F][0] = mfma(pa, vb[u][0], kv[F][0]); kv[F][1] = mfma(pa, vb[u][1], kv[F][1]); z[F] = mfma(pa, ones, z[F]);
        }
      while (xf[wid ^ 2] == 0u) __builtin_amdgcn_s_sleep(0);
      asm volatile("" ::: "memory");
#pragma unroll
      for (int u = 0; u < 2; ++u) {
        const u32x4 v0 = lds128(oth + 8704 + u * 2048 + lane * 16), v1 = lds128(oth + 8704 + u * 2048 + 1024 + lane * 16);
#pragma unroll
        for (int F = 0; F < 2; ++F) {
          const u32x4 pa = lds_tr2(oth + ro + u * 32 * 136 + F * 32, oth + ro + u * 32 * 136 + F * 32 + 16 * 136);
          kv[F][0] = mfma(pa, v0, kv[F][0]); kv[F][1] = mfma(pa, v1, kv[F][1]); z[F] = mfma(pa, ones, z[F]);
        }
      }
      const unsigned kd = WS_KVP + (head * 16 + tm1) * 4096 + wm * 2048;
      st16(y.r, pack8(kv[0][0], kv[1][0]), lane * 16, kd);
      st16(y.r, pack8(kv[0][1], kv[1][1]), lane * 16, kd + 1024);
      if (m16 == 0) {
        st16(y.r, __builtin_bit_cast(u32x4, z[0]), g * 32, WS_ZP + (head * 16 + tm1) * 256 + wm * 128);
        st16(y.r, __builtin_bit_cast(u32x4, z[1]), g * 32 + 16, WS_ZP + (head * 16 + tm1) * 256 + wm * 128);
      }
      publish(y, FL_KV, tm1 * 16 + tn1, lane);
    }
#pragma unroll
    for (int m = 0; m < 4; ++m) { p[m][0] = mfma(wp[0], qb[m], zero); p[m][1] = mfma(wp[1], qb[m], zero); }
    dual_softmax4(p, pp, pn);
    { Gate G; G.off = 0; G.mask = 0; G.word = 8; kv_sum(y, G, smem, 2 * tn1, 0, threadIdx.x, false, wid == 0, lane, [](int) {}); }
    wait_lgkm0(); bar();
    y.pb = __builtin_amdgcn_readfirstlane(*(volatile LAS unsigned*)(y.w + 13));
    u32x4 ka[2][2], za[2];
#pragma unroll
    for (int ks = 0; ks < 2; ++ks) {
      ka[ks][0] = lds128(smem + S1_KV + wn * 4096 + ks * 2048 + lane * 16); ka[ks][1] = lds128(smem + S1_KV + wn * 4096 + ks * 2048 + 1024 + lane * 16);
      za[ks] = lds128(smem + S1_KV + 8192 + wn * 128 + ks * 64 + g * 16);
    }
#pragma unroll
    for (int m = 0; m < 4; ++m) {
      f32x4 n0 = mfma(ka[0][0], pp[m], zero), n1 = mfma(ka[0][1], pp[m], zero), dn = mfma(za[0], pp[m], zero);
      n0 = mfma(ka[1][0], pn[m], n0); n1 = mfma(ka[1][1], pn[m], n1); dn = mfma(za[1], pn[m], dn);
      const float rd = __builtin_amdgcn_rcpf(dn[0] + 1e-6f);
      st16(y.r, pack8(n0 * rd, n1 * rd), lane * 16, near(y, WS_O + (tm1 * 16 + tn1) * 32768, tm1) + wm * 16384 + (m * 2 + wn) * 1024);
    }
    publish(y, FL_O, tm1 * 16 + tn1, lane);
  }
  {
    u32x4 xr[2][2];
#pragma unroll
    for (int m = 0; m < 2; ++m)
#pragma unroll
      for (int Q = 0; Q < 2; ++Q)
        xr[m][Q] = *(const u32x4*)(P.x + (size_t)(tm3 * 128 + wid * 32 + m * 16 + m16) * 2048 + tn3 * 128 + Q * 64 + (g & 1) * 32 + (g >> 1) * 16);
    f32x4 acc[8];
    Adr ad; ad.v[0] = lds0 + R3 + wid * 4096 + lane * 16; ad.v[1] = 0;
#pragma unroll
    for (int n = 0; n < 4; ++n) ad.v[2 + n] = lds0 + R3 + 16384 + wlane(lane, n);
    consume_s<C3, 3, 2>(acc, ad, [](int) {});
#pragma unroll
    for (int m = 0; m < 2; ++m) {
      float hv[16];
#pragma unroll
      for (int Q = 0; Q < 2; ++Q) {
        auto r0 = __builtin_amdgcn_permlane16_swap(xr[m][Q][0], xr[m][Q][2], false, false);
        auto r1 = __builtin_amdgcn_permlane16_swap(xr[m][Q][1], xr[m][Q][3], false, false);
        const f32x4 a = acc[m * 4 + 2 * Q], b = acc[m * 4 + 2 * Q + 1];
        const u32x4 hp = {pk2(a[0] + bflo(r0[0]), a[1] + bfhi(r0[0])), pk2(a[2] + bflo(r1[0]), a[3] + bfhi(r1[0])),
                          pk2(b[0] + bflo(r0[1]), b[1] + bfhi(r0[1])), pk2(b[2] + bflo(r1[1]), b[3] + bfhi(r1[1]))};
        st16(y.r, hp, lane * 16, near(y, WS_H + (tm3 * 16 + tn3) * 32768, tm3) + wm * 16384 + wn * 4096 + (m * 2 + Q) * 1024);
#pragma unroll
        for (int j = 0; j < 4; ++j) { hv[Q * 8 + 2 * j] = bflo(hp[j]); hv[Q * 8 + 2 * j + 1] = bfhi(hp[j]); }
      }
      float s = 0.f;
#pragma unroll
      for (int j = 0; j < 16; ++j) s += hv[j];
      s = xsum32(xsum16(s)) * (1.f / 64.f);
      float q = 0.f;
#pragma unroll
      for (int j = 0; j < 16; ++j) { const float d = hv[j] - s; q += d * d; }
      q = xsum32(xsum16(q));
      if (g == 0) st8(y.r, (u32x2){bu(s), bu(q)}, m16 * 8, near(y, WS_ST2 + tm3 * 32768, tm3) + (tn3 >> 3) * 16384 + (tn3 & 7) * 1024 + (wid * 32 + m * 16) * 8);
    }
    publish(y, FL_H, tm3 * 16 + tn3, lane);
  }
  {
    f32x4 acc[28];
    Adr ad;
    ad.v[0] = lds0 + R4 + wm * 8192 + wn * 4096 + lane * 16; ad.v[1] = lds0 + R4 + wm * 8192 + (wn ^ 1) * 4096 + lane * 16;
    ad.v[2] = lds0 + R4 + 16384 + wlane(lane, wn); ad.v[3] = lds0 + R4 + 16384 + wlane(lane, 2 + wn);
    ad.v[4] = lds0 + R4 + 16384 + 6144 + hlane(lane, 0); ad.v[5] = lds0 + R4 + 16384 + 6144 + hlane(lane, 1);
    consume<C4, 4, RPG4, 0, 0, 0>(acc, ad, [](int) {});
    f32x4 cs[8];
#pragma unroll
    for (int n = 0; n < 6; ++n) cs[n] = *(const LAS f32x4*)(smem + S4_CSF + ((par + (n >> 1)) * 64 + (2 * (n & 1) + wn) * 16 + 4 * g) * 4);
#pragma unroll
    for (int n = 0; n < 2; ++n) cs[6 + n] = *(const LAS f32x4*)(smem + S4_CSF + ((par ? 32 : 192) + n * 16 + 4 * g) * 4);
    const f32x4 zero = {0.f, 0.f, 0.f, 0.f};
    f32x2 st[4];
#pragma unroll
    for (int m = 0; m < 4; ++m) st[m] = *(const LAS f32x2*)(smem + S4_STF + ((wm * 4 + ((m + 2 * wn) & 3)) * 16 + m16) * 8);
#pragma unroll
    for (int i = 0; i < 4; ++i) {
#pragma unroll
      for (int m = 0; m < (i < 3 ? 4 : 2); ++m) {
        const unsigned fd = near(y, WS_F + tm1 * 2097152, tm1) + wm * 16384 + ((m + 2 * wn) & 3) * 2048 + (i < 3 ? (ch0 + i) * 32768 + wn * 1024 : (base7 + 3) * 32768 + par * 1024);
        const int a = i < 3 ? m * 6 + 2 * i : 24 + 2 * m;
        st16(y.r, pack8(__builtin_elementwise_max((acc[a] - cs[2 * i] * st[m][0]) * st[m][1], zero),
                        __builtin_elementwise_max((acc[a + 1] - cs[2 * i + 1] * st[m][0]) * st[m][1], zero)), lane * 16, fd);
        SB();
      }
      if (i == FAT) {
        wait_vm<FAT == 3 ? 10 : 4 * FAT>();
        if (lane == 0 && __hip_atomic_fetch_add(y.w + FL_F, 1u, __ATOMIC_RELAXED, __HIP_MEMORY_SCOPE_WORKGROUP) == 3u) st4(y.r, y.tag, 0, WS_FLG + rec(y, FL_F, tm1 * 16 + tn1));
        SB();
      }
    }
  }
  {
    f32x4 acc[16];
    Adr ad;
    ad.v[0] = lds0 + R5 + wm * 8192 + kh * 4096 + lane * 16; ad.v[1] = lds0 + R5 + wm * 8192 + (kh ^ 1) * 4096 + lane * 16;
#pragma unroll
    for (int n = 0; n < 4; ++n) ad.v[2 + n] = lds0 + R5 + 16384 + wn * 2048 + wlane(lane, n);
    u32x4 hr[2][2];
    consume_s<C5, 5, 1>(acc, ad, [&](int s) {
      if (s < 0) {
        wait_vm<0>();
        if (lane == 0 && __hip_atomic_fetch_add(y.w + 20, 1u, __ATOMIC_RELAXED, __HIP_MEMORY_SCOPE_WORKGROUP) == 3u) st4(y.r, y.tag, 0, WS_FLG + rec(y, FL_F, tm1 * 16 + tn1) + 4);
#pragma unroll
        for (int m = 0; m < 2; ++m)
#pragma unroll
          for (int Q = 0; Q < 2; ++Q) hr[m][Q] = ld16(y.r, lane * 16, near(y, WS_H + (tm5 * 16 + 2 * tn5 + wn) * 32768, tm5) + wm * 16384 + ((2 * kh + m) * 2 + Q) * 1024);
      }
    });
    const int tile = tm5 * 8 + tn5;
#pragma unroll
    for (int i = 0; i < 4; ++i) st16(y.r, pack8(acc[8 + 2 * i], acc[9 + 2 * i]), lane * 16, near(y, WS_O + (tile * 2 + kh) * 32768, tm5) + wm * 16384 + wn * 4096 + i * 1024);
    wait_vm<0>();
    if (lane == 0) st4(y.r, y.tag, 0, WS_FLG + rec(y, FL_X + wm, tile * 2 + kh) + wn * 64);
    poll(y, rec(y, FL_X + wm, tile * 2 + (kh ^ 1)) + wn * 64, 1, lane);
    const rsrc_t ro = __builtin_amdgcn_make_buffer_rsrc(P.out, 0, 2048 * 2048, 0x00020000);
    u32x4 xv[4];
#pragma unroll
    for (int i = 0; i < 4; ++i) xv[i] = ld16(y.r, lane * 16, near(y, WS_O + (tile * 2 + (kh ^ 1)) * 32768, tm5) + wm * 16384 + wn * 4096 + i * 1024);
#pragma unroll
    for (int m = 0; m < 2; ++m)
#pragma unroll
      for (int Q = 0; Q < 2; ++Q) {
        const u32x4 xx = xv[m * 2 + Q];
        const f32x4 a = acc[m * 4 + 2 * Q] + (f32x4){bflo(xx[0]), bfhi(xx[0]), bflo(xx[1]), bfhi(xx[1])}, b = acc[m * 4 + 2 * Q + 1] + (f32x4){bflo(xx[2]), bfhi(xx[2]), bflo(xx[3]), bfhi(xx[3])};
        const u32x4 hh = hr[m][Q];
        const unsigned a0 = pk2(a[0] + bflo(hh[0]), a[1] + bfhi(hh[0])), a1 = pk2(a[2] + bflo(hh[1]), a[3] + bfhi(hh[1]));
        const unsigned b0 = pk2(b[0] + bflo(hh[2]), b[1] + bfhi(hh[2])), b1 = pk2(b[2] + bflo(hh[3]), b[3] + bfhi(hh[3]));
        auto r0 = __builtin_amdgcn_permlane16_swap(a0, b0, false, false);
        auto r1 = __builtin_amdgcn_permlane16_swap(a1, b1, false, false);
        __builtin_amdgcn_raw_buffer_store_b128((u32x4){r0[0], r1[0], r0[1], r1[1]}, ro, (tm5 * 128 + wm * 64 + (2 * kh + m) * 16 + m16) * 2048 + tn5 * 256 + wn * 128 + Q * 64 + (g & 1) * 32 + (g >> 1) * 16, 0, 16);
      }
    if (bid == 0 && threadIdx.x == 0) st4(y.r, y.tag, 0, WS_CTL);
  }
}

extern "C" void kernel_launch(void* const* d_in, const int* in_sizes, int n_in, void* d_out, int out_size, void* d_ws, size_t ws_size,
                              hipStream_t stream) {
  Params p;
  p.x = (const char*)d_in[0]; p.wqkv = (const char*)d_in[1]; p.wphi = (const char*)d_in[2]; p.wo = (const char*)d_in[3];
  p.w1 = (const char*)d_in[5]; p.w2 = (const char*)d_in[6]; p.ws = (char*)(((size_t)d_ws + 0x1fffff) & ~(size_t)0x1fffff); p.out = (char*)d_out;
  (void)hipFuncSetAttribute((const void*)fused, hipFuncAttributeMaxDynamicSharedMemorySize, LDS_BYTES);
  fused<<<256, 512, LDS_BYTES, stream>>>(p);
}
```

```cpp
#include <hip/hip_runtime.h>
#include <hip/hip_bf16.h>

#define LAS __attribute__((address_space(3)))
#define DI __device__ __forceinline__
typedef __bf16 bf16x8 __attribute__((ext_vector_type(8)));
typedef __bf16 bf16x2 __attribute__((ext_vector_type(2)));
typedef short s16x4 __attribute__((ext_vector_type(4)));
typedef float f32x2 __attribute__((ext_vector_type(2)));
typedef float f32x4 __attribute__((ext_vector_type(4)));
typedef unsigned u32x2 __attribute__((ext_vector_type(2)));
typedef unsigned u32x4 __attribute__((ext_vector_type(4)));
typedef LAS char* lds_ptr;
typedef __amdgpu_buffer_rsrc_t rsrc_t;

constexpr int D = 1024, FF = 3584;
constexpr unsigned WS_CTL = 0;
constexpr unsigned WS_FLG = 4096;
constexpr unsigned WS_ST1 = 320u << 10;
constexpr unsigned WS_CSQ = 384u << 10;
constexpr unsigned WS_CS1 = 576u << 10;
constexpr unsigned WS_ZP = 704u << 10;
constexpr unsigned WS_ST2 = 1024u << 10;
constexpr unsigned WS_FL2 = 1536u << 10;
constexpr unsigned WS_PRB = 1920u << 10;
constexpr unsigned WS_KVP = 2u << 20;
constexpr unsigned WS_O = 4u << 20;
constexpr unsigned WS_H = 12u << 20;
constexpr unsigned WS_F = 20u << 20;
constexpr unsigned WS_END = 52u << 20;
constexpr int NPAGE = 26;
enum { FL_P0 = 0, FL_KV = 1, FL_CS = 2, FL_O = 3, FL_H = 4, FL_F = 5, FL_X = 6   };


struct Params { const char* x; const char* wqkv; const char* wphi; const char* wo; const char* w1; const char* w2; char* ws; char* out; };

DI unsigned pk2(float a, float b) { f32x2 v = {a, b}; return __builtin_bit_cast(unsigned, __builtin_convertvector(v, bf16x2)); }
DI float bflo(unsigned u) { return __uint_as_float(u << 16); }
DI float bfhi(unsigned u) { return __uint_as_float(u & 0xffff0000u); }
DI u32x4 pack8(const f32x4& a, const f32x4& b) { return (u32x4){pk2(a[0], a[1]), pk2(a[2], a[3]), pk2(b[0], b[1]), pk2(b[2], b[3])}; }
DI bf16x8 asbf(const u32x4& v) { return __builtin_bit_cast(bf16x8, v); }
#define SB() __builtin_amdgcn_sched_barrier(0)
DI void bar() { asm volatile("" ::: "memory"); __builtin_amdgcn_s_barrier(); asm volatile("" ::: "memory"); }
DI void wait_lgkm0() { __builtin_amdgcn_s_waitcnt(0xC07F); asm volatile("" ::: "memory"); }
template <int N> DI void wait_vm() { asm volatile("s_waitcnt vmcnt(%0)" ::"i"(N) : "memory"); }
template <bool SC1> DI void dma(unsigned voff, const char* sbase, unsigned lds) {
  if (SC1) asm volatile("s_mov_b32 m0, %2\n\ts_nop 3\n\tglobal_load_lds_dwordx4 %0, %1 sc1" ::"v"(voff), "s"(sbase), "s"(lds) : "memory");
  else asm volatile("s_mov_b32 m0, %2\n\ts_nop 3\n\tglobal_load_lds_dwordx4 %0, %1" ::"v"(voff), "s"(sbase), "s"(lds) : "memory");
}
template <bool SC1> DI void dma4(unsigned voff, const char* sbase, unsigned lds) {
  if (SC1) asm volatile("s_mov_b32 m0, %2\n\ts_nop 3\n\tglobal_load_lds_dword %0, %1 sc1" ::"v"(voff), "s"(sbase), "s"(lds) : "memory");
  else asm volatile("s_mov_b32 m0, %2\n\ts_nop 3\n\tglobal_load_lds_dword %0, %1" ::"v"(voff), "s"(sbase), "s"(lds) : "memory");
}
DI u32x4 lds128(lds_ptr p) { return *(const LAS u32x4*)p; }
DI u32x4 lds_tr2(lds_ptr p0, lds_ptr p1) {
  s16x4 a = __builtin_amdgcn_ds_read_tr16_b64_v4i16((LAS s16x4*)p0);
  s16x4 b = __builtin_amdgcn_ds_read_tr16_b64_v4i16((LAS s16x4*)p1);
  return __builtin_bit_cast(u32x4, __builtin_shufflevector(a, b, 0, 1, 2, 3, 4, 5, 6, 7));
}
DI u32x2 lds_tr(lds_ptr p) { return __builtin_bit_cast(u32x2, __builtin_amdgcn_ds_read_tr16_b64_v4i16((LAS s16x4*)p)); }
DI f32x4 mfma(const u32x4& a, const u32x4& b, const f32x4& c) { return __builtin_amdgcn_mfma_f32_16x16x32_bf16(asbf(a), asbf(b), c, 0, 0, 0); }
DI void mfma_ip(f32x4& acc, const u32x4& a, const u32x4& b) { asm volatile("v_mfma_f32_16x16x32_bf16 %0, %1, %2, %0" : "+v"(acc) : "v"(a), "v"(b)); }
DI u32x4 ld16(rsrc_t r, unsigned voff, unsigned soff) { return __builtin_amdgcn_raw_buffer_load_b128(r, voff, soff, 16); }
DI void st16(rsrc_t r, const u32x4& v, unsigned voff, unsigned soff) { __builtin_amdgcn_raw_buffer_store_b128(v, r, voff + soff, 0, 16); }
DI void st8(rsrc_t r, const u32x2& v, unsigned voff, unsigned soff) { __builtin_amdgcn_raw_buffer_store_b64(v, r, voff, soff, 16); }
DI void st4(rsrc_t r, unsigned v, unsigned voff, unsigned soff) { __builtin_amdgcn_raw_buffer_store_b32(v, r, voff, soff, 16); }
DI float fb(unsigned u) { return __uint_as_float(u); }
DI unsigned bu(float f) { return __float_as_uint(f); }
DI float xsum16(float x) { auto r = __builtin_amdgcn_permlane16_swap(bu(x), bu(x), false, false); return fb(r[0]) + fb(r[1]); }
DI float xsum32(float x) { auto r = __builtin_amdgcn_permlane32_swap(bu(x), bu(x), false, false); return fb(r[0]) + fb(r[1]); }
template <int CTRL> DI float dpp(float x) { return __builtin_bit_cast(float, __builtin_amdgcn_update_dpp(0, __builtin_bit_cast(int, x), CTRL, 0xf, 0xf, true)); }
DI float row16_sum(float x) { x += dpp<0xB1>(x); x += dpp<0x4E>(x); x += dpp<0x141>(x); x += dpp<0x140>(x); return x; }

struct Sync { rsrc_t r; unsigned tag, pb; LAS unsigned* w; };
DI unsigned near(const Sync& y, unsigned off, int tm) { return off + ((((y.pb >> (off >> 21)) ^ (tm >> 3)) & 1u) << 13); }
DI unsigned rec(const Sync& y, int phase, int idx) {
  return phase < FL_O ? (phase * 256 + idx) * 128 : near(y, WS_FL2 + (phase - FL_O) * 65536 + (idx >> 6) * 16384 + (idx & 63) * 128, idx >> 4) - WS_FLG;
}
DI void publish(const Sync& y, int phase, int idx, int lane) {
  if (lane == 0) __hip_atomic_fetch_add(y.w + 24 + phase, 1u, __ATOMIC_RELAXED, __HIP_MEMORY_SCOPE_WORKGROUP);
  wait_vm<0>();
  if (lane == 0 && __hip_atomic_fetch_add(y.w + phase, 1u, __ATOMIC_RELAXED, __HIP_MEMORY_SCOPE_WORKGROUP) == 3u) st4(y.r, y.tag, 0, WS_FLG + rec(y, phase, idx));
}
DI void poll(const Sync& y, unsigned off, int n, int lane) {
  const unsigned long long t0 = __builtin_amdgcn_s_memrealtime();
  for (unsigned it = 0;; ++it) {
    const unsigned v = __builtin_amdgcn_raw_buffer_load_b32(y.r, WS_FLG + off, 0, 16);
    if (__all(lane >= n || v == y.tag)) break;
    if ((it & 63) == 63) {
      if (__builtin_amdgcn_readfirstlane(__builtin_amdgcn_raw_buffer_load_b32(y.r, 0, WS_CTL + 128, 16)) == y.tag) break;
      if (__builtin_amdgcn_s_memrealtime() - t0 > 4000000ull) { if (lane == 0) st4(y.r, y.tag, 0, WS_CTL + 128); break; }
    }
    __builtin_amdgcn_s_sleep(2);
  }
  asm volatile("" ::: "memory");
}
DI void lds_wait(const Sync& y, int word, unsigned v) {
  volatile LAS unsigned* w = y.w + word;
  while (*w < v) __builtin_amdgcn_s_sleep(1);
  asm volatile("" ::: "memory");
}
struct Gate { unsigned off, mask; int word; };
DI void gate_slow(const Sync& y, Gate& G, unsigned need, bool poller, int lane, int cnt = 0) {
  volatile LAS unsigned* w = y.w + G.word;
  if (poller) {
    const unsigned long long t0 = __builtin_amdgcn_s_memrealtime();
    for (unsigned it = 0;; ++it) {
      G.mask = (unsigned)__ballot(__builtin_amdgcn_raw_buffer_load_b32(y.r, WS_FLG + G.off, 0, 16) == y.tag);
      if ((it & 63) == 63) {
        const bool late = __builtin_amdgcn_s_memrealtime() - t0 > 4000000ull;
        if (late && lane == 0) st4(y.r, y.tag, 0, WS_CTL + 128);
        if (late || __builtin_amdgcn_readfirstlane(__builtin_amdgcn_raw_buffer_load_b32(y.r, 0, WS_CTL + 128, 16)) == y.tag) G.mask = ~0u;
      }
      *w = G.mask;
      if ((G.mask & need) == need && __builtin_popcount(G.mask & 0xffffu) >= cnt) break;
      __builtin_amdgcn_s_sleep(1);
    }
  } else {
    for (;;) { G.mask = __builtin_amdgcn_readfirstlane(*w); if ((G.mask & need) == need && __builtin_popcount(G.mask & 0xffffu) >= cnt) break; __builtin_amdgcn_s_sleep(1); }
  }
  asm volatile("" ::: "memory");
}
DI void gate(const Sync& y, Gate& G, unsigned need, bool poller, int lane) { if (__builtin_expect((G.mask & need) != need, 0)) gate_slow(y, G, need, poller, lane); }

template <int NK, bool SPREAD> DI int kmap(int t, int c) {
  const int u = t - 1, q = u & 3, b4 = u & ~3, body = (int)(u < NK - 4);
  const int lead = ((int)(q == c) & body & (int)(u >= 0)) | (int)(SPREAD && t == 0), head = SPREAD ? 0 : -(int)(u < 4);
  const int non = ((b4 + ((c + 1 + q - ((int)(q > c) & body)) & 3)) & ~head) | ((t - (int)(u > c)) & head);
  return non ^ (((b4 + 4 + c) ^ non) & -lead);
}
enum { ORD_NAT = 0, ORD_PAIR = 1, ORD_F = 2 };
template <int NSUB_, bool HALF_, int NSLOT_, int NK_, int ORD_, int WSTRIDE_, bool XROW_, int LAG_> struct Cfg {
  static constexpr int NSUB = NSUB_, NSLOT = NSLOT_, NK = NK_, ORD = ORD_, WSTRIDE = WSTRIDE_;
  static constexpr bool HALF = HALF_, XROW = XROW_;
  static constexpr int LAG = LAG_;
  static constexpr int STAGE = 16384 + NSUB * 8192 + (HALF ? 4096 : 0), PW = 4 + 2 * NSUB + (HALF ? 1 : 0), RING = STAGE * NSLOT;
  static constexpr int WB = NSUB * 2048 + (HALF ? 1024 : 0);
};
typedef Cfg<3, false, 3, 16, ORD_NAT, 3 * D * 2, true, 0> C1;
typedef Cfg<1, false, 5, 16, ORD_PAIR, D * 2, false, 1> C3;
typedef Cfg<3, true, 3, 16, ORD_PAIR, FF * 2, false, 0> C4;
typedef Cfg<2, false, 4, 28, ORD_F, D * 2, false, 1> C5;

#define GLDS "global_load_lds_dwordx4 "
template <class C> struct Ld {
  const char* A; const char* W; int lw, c, kt0; unsigned vA[4], vW[7];
  const char* sA; const char* sW; unsigned need;
  Gate G; const Sync* y; int lane_;
  static constexpr int C0 = C::WB > 4096 ? 4096 : 0;
  DI void init(const Sync& y_, int lane, int substep, int halfrel, unsigned goff, int gword) {
    y = &y_; lane_ = lane; G.off = goff; G.mask = 0; G.word = gword;
#pragma unroll
    for (int i = 0; i < 4; ++i) vA[i] = C::XROW ? (lane >> 3) * 2048 + ((((lane & 7) ^ (lane >> 4)) << 4) ^ ((i & 1) * 64)) + i * (16384 - 1024) : lane * 16;
    const int rl = (C::ORD == ORD_NAT) ? 8 * (lane >> 5) + ((lane >> 3) & 3) : (lane >> 3);
    const unsigned w0 = rl * C::WSTRIDE + (((lane & 7) ^ (((lane >> 4) & 3) << 1)) << 4) + 4096;
#pragma unroll
    for (int q = 0; q < 2 * C::NSUB; ++q) vW[q] = w0 + (q & 1) * ((C::ORD == ORD_NAT ? 16 : 8) * C::WSTRIDE) + (q >> 1) * substep - (q * 1024 - C0);
    if (C::HALF) vW[6] = (lane >> 2) * C::WSTRIDE + (((lane & 3) ^ (((lane >> 4) & 1) << 1)) << 4) + 4096 + halfrel - (6144 - C0);
    A += C::XROW ? lw * 32 * 2048 : (lw >> 1) * 16384 + (lw & 1) * 4096;
    W -= 4096;
  }
  DI void prep(int s) {
    int kt = kmap<C::NK, C::XROW>(s, c);
    const int kb = lw >> 1, t = lw & 1;
    int ur;
    need = 1u << kt;
    if (C::ORD == ORD_NAT) ur = 32 * kb + 4 * t;
    else if (C::ORD == ORD_F) {
      const int p = kt & 7, q = kt >> 3, sp = -(int)(q == 3);
      need = ((1u << (2 * p + (int)(q > 0))) & ~sp) | ((0xau << (4 * (p & 3))) & sp);
      kt = kt0 + (((7 * (p >> 1) + 4 * (p & 1) + q) & ~sp) | ((7 * p + 3) & sp));
      ur = (16 * lw & sp) | ((16 * kb + 32 * t) & ~sp);
    }
    else ur = 16 * lw;
    sA = A + (unsigned)(kt * (C::XROW ? 128 : 32768));
    sW = W + (unsigned)((kt * 64 + ur) * C::WSTRIDE);
  }
  DI void issueA(unsigned base) {
    if (!C::XROW) gate(*y, G, need, lw == 0, lane_);
    const unsigned m = base + lw * 4096;
    if (C::XROW) asm volatile("s_mov_b32 m0, %5\n\ts_nop 3\n\t" GLDS "%0, %4\n\t" GLDS "%1, %4 offset:1024\n\t" GLDS "%2, %4 offset:2048\n\t" GLDS "%3, %4 offset:3072"
                              ::"v"(vA[0]), "v"(vA[1]), "v"(vA[2]), "v"(vA[3]), "s"(sA), "s"(m) : "memory");
    else asm volatile("s_mov_b32 m0, %2\n\ts_nop 3\n\t" GLDS "%0, %1 sc1\n\t" GLDS "%0, %1 offset:1024 sc1\n\t" GLDS "%0, %1 offset:2048 sc1\n\t" GLDS "%0, %1 offset:3072 sc1"
                      ::"v"(vA[0]), "s"(sA), "s"(m) : "memory");
  }
  DI void issueW(unsigned base) const {
    const unsigned m = base + 16384 + lw * C::WB + C0;
    if (C::NSUB == 1) asm volatile("s_mov_b32 m0, %3\n\ts_nop 3\n\t" GLDS "%0, %2\n\t" GLDS "%1, %2 offset:1024" ::"v"(vW[0]), "v"(vW[1]), "s"(sW), "s"(m) : "memory");
    if (C::NSUB == 2) asm volatile("s_mov_b32 m0, %5\n\ts_nop 3\n\t" GLDS "%0, %4\n\t" GLDS "%1, %4 offset:1024\n\t" GLDS "%2, %4 offset:2048\n\t" GLDS "%3, %4 offset:3072"
                                   ::"v"(vW[0]), "v"(vW[1]), "v"(vW[2]), "v"(vW[3]), "s"(sW), "s"(m) : "memory");
    if (C::NSUB == 3) asm volatile("s_mov_b32 m0, %7\n\ts_nop 3\n\t" GLDS "%0, %6 offset:-4096\n\t" GLDS "%1, %6 offset:-3072\n\t" GLDS "%2, %6 offset:-2048\n\t" GLDS "%3, %6 offset:-1024\n\t"
                                   GLDS "%4, %6\n\t" GLDS "%5, %6 offset:1024"
                                   ::"v"(vW[0]), "v"(vW[1]), "v"(vW[2]), "v"(vW[3]), "v"(vW[4]), "v"(vW[5]), "s"(sW), "s"(m) : "memory");
    if (C::HALF) asm volatile(GLDS "%0, %1 offset:2048" ::"v"(vW[6]), "s"(sW) : "memory");
  }
};
template <class C> DI void load_preW(Ld<C>& L, unsigned ring) {
#pragma unroll 1
  for (int s = 0; s < C::NSLOT; ++s) { L.prep(s); L.issueW(ring + s * C::STAGE); }
}
template <class C, bool SPLIT> constexpr int vm_allow(int S) {
  const int N = C::NSLOT, behind = S - 1 - C::LAG > 0 ? S - 1 - C::LAG : 0;
  const int ramp = S + 1 < N - 1 ? S + 1 : N - 1;
  const int last = ramp + behind < C::NK - 1 ? ramp + behind : C::NK - 1;
  return (SPLIT && S < N) ? 4 * (ramp - S) + C::PW * (last - ramp) : C::PW * (last - S);
}
template <class C, bool SPLIT, unsigned POSTM, int S, int SE, class Post> DI void load_fixed(Ld<C>& L, unsigned ring, const Post& post) {
  if constexpr (S < SE) {
    static_assert(C::NSLOT <= C::LAG + 4, "the ring must be full before the first refill");
    wait_vm<vm_allow<C, SPLIT>(S)>();
    bar();
    if constexpr (S + 2 < C::NSLOT) {
      if (!SPLIT) L.issueW(ring + (S + 2) * C::STAGE);
      L.issueA(ring + (S + 2) * C::STAGE);
      L.prep(S + 3);
    }
    constexpr int T = S - 1 - C::LAG + C::NSLOT;
    if constexpr (S - 1 - C::LAG >= 0 && T < C::NK) {
      L.issueW(ring + (T % C::NSLOT) * C::STAGE); L.issueA(ring + (T % C::NSLOT) * C::STAGE);
      if constexpr (T + 1 < C::NK) L.prep(T + 1);
    }
    if constexpr ((POSTM >> S) & 1u) post(S);
    load_fixed<C, SPLIT, POSTM, S + 1, SE>(L, ring, post);
  }
}
template <class C, bool SPLIT, unsigned PREM, unsigned POSTM, class Pre, class Post>
DI void load_all(Ld<C>& L, unsigned ring, const Pre& pre, const Post& post) {
  constexpr int N = C::NSLOT, S0 = N - 1 > 2 + C::LAG ? N - 1 : 2 + C::LAG, S1 = C::NK - N + C::LAG;
#pragma unroll 1
  for (int s = 0; s < 2; ++s) { L.prep(s); if (!SPLIT) L.issueW(ring + s * C::STAGE); L.issueA(ring + s * C::STAGE); }
  L.prep(2);
  load_fixed<C, SPLIT, POSTM, 0, S0>(L, ring, post);
  static_assert(vm_allow<C, SPLIT>(S0) == (N - 2 - C::LAG) * C::PW && vm_allow<C, SPLIT>(S1) == (N - 2 - C::LAG) * C::PW, "steady count");
  unsigned b = ring + ((S0 - 1 - C::LAG) % N) * C::STAGE;
#pragma unroll 1
  for (int s = S0; s <= S1; ++s) {
    wait_vm<(N - 2 - C::LAG) * C::PW>();
    if (PREM && ((PREM >> s) & 1u)) pre(s);
    bar();
    L.issueW(b);
    L.issueA(b);
    L.prep(min(s - C::LAG + N, C::NK - 1));
    b = (b == ring + (N - 1) * C::STAGE) ? ring : b + C::STAGE;
    if (POSTM && ((POSTM >> s) & 1u)) post(s);
  }
  load_fixed<C, SPLIT, POSTM, S1 + 1, C::NK>(L, ring, post);
  bar();
}

template <int MT, int NWF> struct Fr { u32x4 a[MT]; u32x2 w[NWF][2]; };
DI u32x4 cat(const u32x2 (&w)[2]) { return (u32x4){w[0][0], w[0][1], w[1][0], w[1][1]}; }
struct Adr { unsigned v[6]; };
template <int KIND> struct Shape;
template <> struct Shape<1> { static constexpr int MT = 4, NWF = 6, NMF = 24, NACC = 24, WB = 6144; static constexpr unsigned USE = 0x0f; };
template <> struct Shape<3> { static constexpr int MT = 2, NWF = 4, NMF = 8, NACC = 8, WB = 2048; static constexpr unsigned USE = 0x3d; };
template <> struct Shape<4> { static constexpr int MT = 4, NWF = 8, NMF = 28, NACC = 28, WB = 7168; static constexpr unsigned USE = 0x3f; };
template <> struct Shape<5> { static constexpr int MT = 4, NWF = 4, NMF = 16, NACC = 16, WB = 4096; static constexpr unsigned USE = 0x3f; };
DI lds_ptr lp(unsigned a) { return (lds_ptr)(size_t)a; }
template <int KIND, int J> DI void rd1(Fr<Shape<KIND>::MT, Shape<KIND>::NWF>& f, const Adr& ad, int kb) {
  constexpr int NWF = Shape<KIND>::NWF, MT = Shape<KIND>::MT, WB = Shape<KIND>::WB;
  if constexpr (J < 2 * NWF) {
    constexpr int n = J >> 1, t = J & 1;
    if constexpr (KIND == 4 && n >= 6) f.w[n][t] = lds_tr(lp(ad.v[4 + (n - 6)]) + (2 * kb + t) * WB);
    else if constexpr (KIND == 1 || KIND == 4) f.w[n][t] = lds_tr(lp(ad.v[2 + (n & 1)]) + (n >> 1) * 2048 + (2 * kb + t) * WB);
    else f.w[n][t] = lds_tr(lp(ad.v[2 + n]) + (2 * kb + t) * WB);
  } else if constexpr (J < 2 * NWF + MT) {
    constexpr int m = J - 2 * NWF;
    if constexpr (KIND == 1) f.a[m] = lds128(lp(ad.v[kb]) + m * 2048);
    else f.a[m] = lds128(lp(ad.v[m >> 1]) + (m & 1) * 2048 + kb * 1024);
  }
}
template <int KIND, int I> DI void mm1(f32x4 (&acc)[Shape<KIND>::NACC], const Fr<Shape<KIND>::MT, Shape<KIND>::NWF>& f) {
  if constexpr (KIND == 1) { constexpr int m = I / 6, n = I % 6; if constexpr (n < 4) mfma_ip(acc[I], cat(f.w[n]), f.a[m]); else mfma_ip(acc[I], f.a[m], cat(f.w[n])); }
  else if constexpr (KIND == 4) { if constexpr (I < 24) mfma_ip(acc[I], cat(f.w[I % 6]), f.a[I / 6]); else mfma_ip(acc[I], cat(f.w[6 + ((I - 24) & 1)]), f.a[(I - 24) >> 1]); }
  else mfma_ip(acc[I], cat(f.w[I % 4]), f.a[I / 4]);
}
template <int KIND, int RPG, bool READ, bool MOVE, int I = 0>
DI void step(f32x4 (&acc)[Shape<KIND>::NACC], const Fr<Shape<KIND>::MT, Shape<KIND>::NWF>& cur, Fr<Shape<KIND>::MT, Shape<KIND>::NWF>& nxt, Adr& ad, int kb, int delta) {
  constexpr int NMF = Shape<KIND>::NMF, G0 = (2 * Shape<KIND>::NWF + Shape<KIND>::MT + RPG - 1) / RPG;
  if constexpr (I < NMF) {
    if constexpr (READ) {
      rd1<KIND, I * RPG>(nxt, ad, kb);
      if constexpr (RPG > 1) rd1<KIND, I * RPG + 1>(nxt, ad, kb);
    }
    if constexpr (MOVE && I >= G0) {
      constexpr int per = (6 + (NMF - G0) - 1) / (NMF - G0);
#pragma unroll
      for (int k = (I - G0) * per; k < (I - G0 + 1) * per && k < 6; ++k)
        if ((Shape<KIND>::USE >> k) & 1u) { ad.v[k] += delta; asm volatile("" : "+v"(ad.v[k])); }
    }
    mm1<KIND, I>(acc, cur);
    SB();
    step<KIND, RPG, READ, MOVE, I + 1>(acc, cur, nxt, ad, kb, delta);
  }
}
template <int KIND, int J = 0> DI void rdall(Fr<Shape<KIND>::MT, Shape<KIND>::NWF>& f, const Adr& ad, int kb) {
  if constexpr (J < 2 * Shape<KIND>::NWF + Shape<KIND>::MT) { rd1<KIND, J>(f, ad, kb); rdall<KIND, J + 1>(f, ad, kb); }
}
template <class C, int KIND, int RPG, int CNT>
DI void stages(f32x4 (&acc)[Shape<KIND>::NACC], Fr<Shape<KIND>::MT, Shape<KIND>::NWF>& f0, Fr<Shape<KIND>::MT, Shape<KIND>::NWF>& f1, Adr& ad, int& slot) {
#pragma unroll 1
  for (int s = 0; s < CNT; ++s) {
    const int delta = (slot + 1 == C::NSLOT) ? -(C::NSLOT - 1) * C::STAGE : C::STAGE;
    slot = (slot + 1 == C::NSLOT) ? 0 : slot + 1;
    SB();
    step<KIND, RPG, true, true>(acc, f0, f1, ad, 1, delta);
    if (!C::LAG) wait_lgkm0();
    bar();
    SB();
    step<KIND, RPG, true, false>(acc, f1, f0, ad, 0, 0);
  }
}
template <class C, int KIND, int RPG, int H0, int H1, int H2, class Hook> DI void consume(f32x4 (&acc)[Shape<KIND>::NACC], Adr& ad, const Hook& hook) {
  Fr<Shape<KIND>::MT, Shape<KIND>::NWF> f0, f1;
#pragma unroll
  for (int i = 0; i < Shape<KIND>::NACC; ++i) { acc[i] = (f32x4){0.f, 0.f, 0.f, 0.f}; asm volatile("" : "+v"(acc[i])); }
  bar();
  rdall<KIND>(f0, ad, 0);
  hook(-1);
  wait_lgkm0();
  int slot = 0;
  if constexpr (H1 > 0) {
    if constexpr (H0 > 0) stages<C, KIND, RPG, H0>(acc, f0, f1, ad, slot);
    hook(0);
    stages<C, KIND, RPG, H1 - H0>(acc, f0, f1, ad, slot);
    hook(1);
    stages<C, KIND, RPG, H2 - H1>(acc, f0, f1, ad, slot);
    hook(2);
  }
  stages<C, KIND, RPG, C::NK - 1 - H2>(acc, f0, f1, ad, slot);
  SB();
  step<KIND, RPG, true, false>(acc, f0, f1, ad, 1, 0);
  wait_lgkm0();
  bar();
  SB();
  step<KIND, RPG, false, false>(acc, f1, f0, ad, 0, 0);
  asm volatile("s_nop 7\n\ts_nop 3");
  SB();
}
template <class C, int KIND, int RPG, class Hook> DI void consume_s(f32x4 (&acc)[Shape<KIND>::NACC], Adr& ad, const Hook& hook) {
  Fr<Shape<KIND>::MT, Shape<KIND>::NWF> f0, f1, g0, g1;
#pragma unroll
  for (int i = 0; i < Shape<KIND>::NACC; ++i) { acc[i] = (f32x4){0.f, 0.f, 0.f, 0.f}; asm volatile("" : "+v"(acc[i])); }
  bar();
  rdall<KIND>(f0, ad, 0); rdall<KIND>(f1, ad, 1);
  hook(-1);
#pragma unroll
  for (int k = 0; k < 6; ++k) ad.v[k] += C::STAGE;
  int slot = 1;
#pragma unroll 1
  for (int s = 0; s < C::NK - 2; s += 2) {
    int delta = (slot + 1 == C::NSLOT) ? -(C::NSLOT - 1) * C::STAGE : C::STAGE;
    slot = (slot + 1 == C::NSLOT) ? 0 : slot + 1;
    if (!C::LAG) wait_lgkm0();
    bar(); SB();
    step<KIND, RPG, true, false>(acc, f0, g0, ad, 0, 0);
    step<KIND, RPG, true, true>(acc, f1, g1, ad, 1, delta);
    delta = (slot + 1 == C::NSLOT) ? -(C::NSLOT - 1) * C::STAGE : C::STAGE;
    slot = (slot + 1 == C::NSLOT) ? 0 : slot + 1;
    if (!C::LAG) wait_lgkm0();
    bar(); SB();
    step<KIND, RPG, true, false>(acc, g0, f0, ad, 0, 0);
    step<KIND, RPG, true, true>(acc, g1, f1, ad, 1, delta);
  }
  if (!C::LAG) wait_lgkm0();
  bar(); SB();
  step<KIND, RPG, true, false>(acc, f0, g0, ad, 0, 0);
  step<KIND, RPG, true, false>(acc, f1, g1, ad, 1, 0);
  wait_lgkm0(); bar(); SB();
  step<KIND, RPG, false, false>(acc, g0, f0, ad, 0, 0);
  step<KIND, RPG, false, false>(acc, g1, f1, ad, 1, 0);
  asm volatile("s_nop 7\n\ts_nop 3");
  SB();
}
DI unsigned wlane(int lane, int nt) {
  const int g = lane >> 4, q = (lane >> 2) & 3, p = lane & 3, sw = (2 * g + (q >> 1)) & 3;
  return (4 * g + q) * 128 + ((nt ^ sw) << 5) + p * 8;
}
DI unsigned hlane(int lane, int nt) {
  const int g = lane >> 4, q = (lane >> 2) & 3, p = lane & 3;
  return (4 * g + q) * 64 + ((nt ^ (g & 1)) << 5) + p * 8;
}

template <int OP> DI float op2(float a, float b) { return OP == 0 ? a + b : OP == 1 ? __builtin_elementwise_maximum(a, b) : __builtin_elementwise_minimum(a, b); }
template <int OP> DI float red4(float x0, float x1, float x2, float x3) {
  auto a = __builtin_amdgcn_permlane16_swap(bu(x0), bu(x1), false, false);
  auto b = __builtin_amdgcn_permlane16_swap(bu(x2), bu(x3), false, false);
  const float y0 = op2<OP>(fb(a[0]), fb(a[1])), y1 = op2<OP>(fb(b[0]), fb(b[1]));
  auto c = __builtin_amdgcn_permlane32_swap(bu(y0), bu(y1), false, false);
  return op2<OP>(fb(c[0]), fb(c[1]));
}
DI void spread(float z, float (&o)[4]) {
  auto a = __builtin_amdgcn_permlane32_swap(bu(z), bu(z), false, false);
  auto b = __builtin_amdgcn_permlane16_swap(a[0], a[0], false, false);
  auto c = __builtin_amdgcn_permlane16_swap(a[1], a[1], false, false);
  o[0] = fb(b[0]); o[1] = fb(b[1]); o[2] = fb(c[0]); o[3] = fb(c[1]);
}
DI void dual_softmax4(const f32x4 (&p)[4][2], u32x4 (&pp)[4], u32x4 (&pn)[4]) {
  constexpr float L2E = 1.4426950408889634f;
  float mx[4], mn[4];
#pragma unroll
  for (int m = 0; m < 4; ++m) {
    mx[m] = op2<1>(op2<1>(op2<1>(op2<1>(p[m][0][0], p[m][0][1]), p[m][0][2]), op2<1>(op2<1>(p[m][0][3], p[m][1][0]), p[m][1][1])), op2<1>(p[m][1][2], p[m][1][3]));
    mn[m] = op2<2>(op2<2>(op2<2>(op2<2>(p[m][0][0], p[m][0][1]), p[m][0][2]), op2<2>(op2<2>(p[m][0][3], p[m][1][0]), p[m][1][1])), op2<2>(p[m][1][2], p[m][1][3]));
  }
  spread(-L2E * red4<1>(mx[0], mx[1], mx[2], mx[3]), mx);
  spread(L2E * red4<2>(mn[0], mn[1], mn[2], mn[3]), mn);
  f32x4 ep[4][2], en[4][2];
  float sp[4], sn[4];
#pragma unroll
  for (int m = 0; m < 4; ++m) {
#pragma unroll
    for (int F = 0; F < 2; ++F) {
      const f32x4 a = p[m][F] * L2E + mx[m], b = p[m][F] * (-L2E) + mn[m];
#pragma unroll
      for (int r = 0; r < 4; ++r) { ep[m][F][r] = __builtin_amdgcn_exp2f(a[r]); en[m][F][r] = __builtin_amdgcn_exp2f(b[r]); }
    }
    const f32x4 a = ep[m][0] + ep[m][1], b = en[m][0] + en[m][1];
    sp[m] = (a[0] + a[1]) + (a[2] + a[3]); sn[m] = (b[0] + b[1]) + (b[2] + b[3]);
  }
  spread(__builtin_amdgcn_rcpf(red4<0>(sp[0], sp[1], sp[2], sp[3])), sp);
  spread(__builtin_amdgcn_rcpf(red4<0>(sn[0], sn[1], sn[2], sn[3])), sn);
#pragma unroll
  for (int m = 0; m < 4; ++m) { pp[m] = pack8(ep[m][0] * sp[m], ep[m][1] * sp[m]); pn[m] = pack8(en[m][0] * sn[m], en[m][1] * sn[m]); }
}

DI float dot2(unsigned a, unsigned b, float c) { return __builtin_amdgcn_fdot2_f32_bf16(__builtin_bit_cast(bf16x2, a), __builtin_bit_cast(bf16x2, b), c, false); }
DI unsigned opaque(unsigned c) { asm volatile("" : "+s"(c)); return c; }
template <int NL> DI void colsum_load(u32x4 (&v)[NL], const char* w, int stride, int lane) {
#pragma unroll
  for (int i = 0; i < NL; ++i) v[i] = *(const u32x4*)(w + (size_t)(8 * i + (lane >> 3)) * stride + (lane & 7) * 16);
}
template <int NL> DI void colsum_finish(const u32x4 (&v)[NL], rsrc_t r, unsigned dst, int lane) {
  f32x2 s2[4];
#pragma unroll
  for (int j = 0; j < 4; ++j) s2[j] = (f32x2){bflo(v[0][j]), bfhi(v[0][j])};
#pragma unroll
  for (int i = 1; i < NL; ++i)
#pragma unroll
    for (int j = 0; j < 4; ++j) s2[j] += (f32x2){bflo(v[i][j]), bfhi(v[i][j])};
  const float s[8] = {s2[0][0], s2[0][1], s2[1][0], s2[1][1], s2[2][0], s2[2][1], s2[3][0], s2[3][1]};
  float t[4], u[2];
#pragma unroll
  for (int j = 0; j < 4; ++j) { auto q = __builtin_amdgcn_permlane32_swap(bu(s[j]), bu(s[j + 4]), false, false); t[j] = fb(q[0]) + fb(q[1]); }
#pragma unroll
  for (int j = 0; j < 2; ++j) { auto q = __builtin_amdgcn_permlane16_swap(bu(t[j]), bu(t[j + 2]), false, false); u[j] = fb(q[0]) + fb(q[1]); }
  const bool b3 = lane & 8;
  const float tot = (b3 ? u[1] : u[0]) + dpp<0x128>(b3 ? u[0] : u[1]);
  st4(r, bu(tot), ((lane & 7) * 8 + ((lane >> 5) & 1) * 4 + ((lane >> 4) & 1) * 2 + ((lane >> 3) & 1)) * 4, dst);
}

template <int XN, int TM, int TN> DI void tile_of(int bid, int& tm, int& tn, int& c) {
  const int x = bid & 7, r = bid >> 3, li = r % TM, lj = r / TM;
  tm = (x / XN) * TM + li; tn = (x % XN) * TN + lj; c = (li + lj) & 3;
}

constexpr int PH0 = 2, PH1 = 6, PH2 = 7, PF = 10, PR = 15, NPROBE = 4;
constexpr int R1 = 40704, R3 = 40704, R4 = 0, R5 = 0, SYNCW = 163584;
constexpr int S1_ST = 0, S1_CSR = 1024, S1_CSF = 13312, S1_FLG = 14080, S1_WP = 14336, S1_KV = 18432, S1_DUM = 26880;
constexpr int S4_STR = C4::RING, S4_CSR = S4_STR + 16384, S4_STF = S4_CSR + 8192, S4_CSF = S4_STF + 1024, S4_DUM = S4_CSF + 1024;
constexpr int LDS_BYTES = 163840, CODE_BYTES = 54 * 1024;
static_assert(S4_DUM + 256 <= SYNCW && S1_KV + 8448 <= R1 && R1 + C1::RING <= SYNCW && R3 + C3::RING <= SYNCW && C5::RING <= S4_STR, "lds");

template <class St> DI void kv_sum(const Sync& y, Gate& G, lds_ptr smem, int head, int h, int t, bool poller, bool doz, int lane, const St& onround) {
  u32x4 v[16], zz[4];
  unsigned have = 0;
  f32x2 s2[4] = {{0.f, 0.f}, {0.f, 0.f}, {0.f, 0.f}, {0.f, 0.f}};
#pragma unroll 1
  for (int round = poller ? 2 : 0; round < 3; ++round) {
    gate_slow(y, G, 0u, poller, lane, round == 0 ? 0 : round == 1 ? 13 : 16);
    onround(round);
    const unsigned fresh = G.mask & ~have;
    have = G.mask;
#pragma unroll
    for (int r = 0; r < 16; ++r) if ((fresh >> r) & 1u) v[r] = ld16(y.r, t * 16, WS_KVP + (head * 16 + r) * 4096);
    if (round == 2 && doz) {
#pragma unroll
      for (int i = 0; i < 4; ++i) zz[i] = ld16(y.r, lane * 16, WS_ZP + head * 4096 + i * 1024);
    }
  }
#pragma unroll
  for (int r = 0; r < 16; ++r)
#pragma unroll
    for (int j = 0; j < 4; ++j) s2[j] += (f32x2){bflo(v[r][j]), bfhi(v[r][j])};
  const float s[8] = {s2[0][0], s2[0][1], s2[1][0], s2[1][1], s2[2][0], s2[2][1], s2[3][0], s2[3][1]};
  *(LAS u32x4*)(smem + S1_KV + h * 4096 + t * 16) = (u32x4){pk2(s[0], s[1]), pk2(s[2], s[3]), pk2(s[4], s[5]), pk2(s[6], s[7])};
  if (doz) {
    f32x4 z = (__builtin_bit_cast(f32x4, zz[0]) + __builtin_bit_cast(f32x4, zz[1])) + (__builtin_bit_cast(f32x4, zz[2]) + __builtin_bit_cast(f32x4, zz[3]));
#pragma unroll
    for (int j = 0; j < 4; ++j) z[j] = xsum32(xsum16(z[j]));
    if (lane < 16) *(LAS u32x2*)(smem + S1_KV + 8192 + h * 128 + lane * 8) = (u32x2){pk2(z[0], z[1]), pk2(z[2], z[3])};
  }
}

__global__ __launch_bounds__(512, 2) void fused(Params P) {
  extern __shared__ __attribute__((aligned(16))) char smem_[];
  lds_ptr smem = (lds_ptr)smem_;
  const unsigned lds0 = (unsigned)(size_t)smem;
  const int wid = __builtin_amdgcn_readfirstlane(threadIdx.x >> 6), lane = threadIdx.x & 63, bid = blockIdx.x;
  {
    const char* pc; asm volatile("s_getpc_b64 %0" : "=s"(pc));
    const unsigned co = ((bid >> 3) & 7) * 8192 + threadIdx.x * 128;
    if (threadIdx.x < 64 && co < CODE_BYTES) dma4<false>(co, pc, lds0 + S1_DUM);
    SB();
  }
  int tm1, tn1, c1, tm3, tn3, c3;
  tile_of<4, 8, 4>(bid, tm1, tn1, c1);
  tile_of<2, 4, 8>(bid, tm3, tn3, c3);
  const int kh = bid & 1, tm5 = ((bid & 7) >> 1) * 4 + ((bid >> 3) & 3), tn5 = bid >> 5, c5 = ((bid >> 3) + tn5) & 3;
  const int par = tn1 & 1, base7 = 7 * (tn1 >> 1), ch0 = base7 + 4 * par;
  const int g = lane >> 4, m16 = lane & 15;
  Sync y;
  y.r = __builtin_amdgcn_make_buffer_rsrc(P.ws, 0, WS_END, 0x00020000);
  y.w = (LAS unsigned*)(smem + SYNCW); y.pb = 0;

  if (wid >= 4) {
    const int lw = wid - 4, lt = threadIdx.x - 256;
    const auto nop = [](int) {};
    {
      Ld<C1> L; L.A = P.x + (size_t)tm1 * 128 * 2048; L.W = P.wqkv + tn1 * 128; L.lw = lw; L.c = c1; L.kt0 = 0; L.init(y, lane, 2048, 0, 0, 0);
      const unsigned frec = (FL_P0 * 256 + (lane < 16 ? tm1 * 16 + lane : lane < 28 ? ((lane - 16) / 3) * 48 + ((lane - 16) % 3) * 16 + tn1 : tn1)) * 128;
      if (lw == 0) dma4<true>(0u * lane, P.ws + WS_CTL, lds0 + S1_FLG);
      load_all<C1, false, (1u << (PF + 3)), 0x8005u | (1u << PF) | (1u << (PF + 3)) | (1u << PR)>(L, lds0 + R1,
        [&](int s) {
          if (lw == 0) {
            const u32x4 v = *(volatile LAS u32x4*)(smem + S1_CSR + lane * 16);
            if (!__all(v[0] == y.tag && v[1] == y.tag && v[2] == y.tag && v[3] == y.tag)) {
#pragma unroll 1
              for (int k = 0; k < 4; ++k) poll(y, frec + 4 * k, 64, lane);
            }
          }
        },
        [&](int s) {
          if (s == 0) y.tag = __builtin_amdgcn_readfirstlane(*(volatile LAS unsigned*)(smem + S1_FLG)) + 1u;
          if (s == 2) dma<false>(lane * 16, P.wphi + tn1 * 4096 + lw * 1024, lds0 + S1_WP + lw * 1024);
          if (s == PF && lw == 0) dma<true>(frec, P.ws + WS_FLG, lds0 + S1_CSR);
          if (s == 15 && lw == 1) dma4<false>(lane * 128, P.wo + bid * 8192, lds0 + S1_DUM);
          if (s == PF + 3) {
            if (lw == 3) dma<true>(lane * 16, P.ws + WS_ST1 + tm1 * 1024, lds0 + S1_ST);
#pragma unroll
            for (int i = 0; i < 3; ++i)
              dma<true>((lane >> 4) * 12288 + (lane & 15) * 16, P.ws + WS_CSQ + lw * 49152 + i * 4096 + tn1 * 256, lds0 + S1_CSR + i * 4096 + lw * 1024);
          }
          if (s == PR) {
            if (lt < 192) {
              const LAS float* rawp = (const LAS float*)(smem + S1_CSR) + (lt >> 6) * 1024 + (lt & 63);
              float t = 0.f;
#pragma unroll
              for (int i = 0; i < 16; ++i) t += rawp[i * 64];
              ((LAS float*)(smem + S1_CSF))[lt] = t;
            }
            wait_lgkm0();
          }
        });
    }
    {
      Ld<C3> L; L.A = P.ws; L.W = P.wo + tn3 * 128; L.lw = lw; L.c = c3; L.kt0 = 0; L.init(y, lane, 0, 0, 0, 9);
      if (bid >= 224 && bid < 224 + NPAGE) {
        if (lw == 1) {
          const unsigned po = bid == 224 ? WS_PRB : (bid - 224) << 21;
          unsigned tmin[2] = {~0u, ~0u};
#pragma unroll 1
          for (int k = 0; k < NPROBE; ++k) {
            const unsigned long long t0 = __builtin_amdgcn_s_memtime();
            if (lane < 8) st16(y.r, (u32x4){0u, 0u, 0u, 0u}, lane * 16, po + (k & 1) * 8192);
            wait_vm<0>();
            const unsigned dt = (unsigned)(__builtin_amdgcn_s_memtime() - t0);
            if (k & 1) tmin[1] = min(tmin[1], dt); else tmin[0] = min(tmin[0], dt);
          }
          if (lane == 0) st4(y.r, (y.tag << 1) | (((unsigned)(tmin[1] < tmin[0]) ^ (unsigned)(bid >> 2)) & 1u), 0, WS_FLG + (FL_CS * 256 + bid) * 128 + 4);
        }
        if (lw == 1) *(volatile LAS unsigned*)(y.w + 14) = 1u; else lds_wait(y, 14, 1u);
      }
      load_preW<C3>(L, lds0 + R3);
      if (lw >= 2 && bid < 224) {
        u32x4 v[16];
        colsum_load<16>(v, P.w1 + (size_t)((bid / 56) * 256 + (lw - 2) * 128) * (FF * 2) + (bid % 56) * 128, FF * 2, lane);
        colsum_finish<16>(v, y.r, WS_CS1 + (((bid / 56) * 2 + lw - 2) * FF + (bid % 56) * 64) * 4, lane);
      }
      publish(y, FL_CS, bid, lane);
      lds_wait(y, 24 + FL_KV, 4u);
      Gate G; G.off = (FL_KV * 256 + m16 * 16 + tn1) * 128; G.mask = 0; G.word = 8;
      const unsigned pbo = WS_FLG + (FL_CS * 256 + 224 + (lane & 31)) * 128 + 4;
      unsigned pbv = 0;
      kv_sum(y, G, smem, 2 * tn1 + 1, 1, lt, lw == 0, lw == 1, lane, [&](int) { if (lw == 0) pbv = __builtin_amdgcn_raw_buffer_load_b32(y.r, pbo, 0, 16); });
      if (lw == 0) {
        const unsigned long long t0 = __builtin_amdgcn_s_memrealtime();
        while (!__all(lane >= NPAGE || (pbv >> 1) == (y.tag & 0x7fffffffu))) {
          if (__builtin_amdgcn_s_memrealtime() - t0 > 4000000ull) { if (lane == 0) st4(y.r, y.tag, 0, WS_CTL + 128); break; }
          pbv = __builtin_amdgcn_raw_buffer_load_b32(y.r, pbo, 0, 16);
        }
        const unsigned raw = (unsigned)__ballot(pbv & 1u), odd = __builtin_popcount((raw ^ 0xaaaaaaaau) & 0x3fffffdu);
        *(volatile LAS unsigned*)(y.w + 13) = odd <= 6 ? 0xaaaaaaaau : odd >= 19 ? 0x55555555u : raw;
      }
      wait_lgkm0(); bar();
      y.pb = __builtin_amdgcn_readfirstlane(*(volatile LAS unsigned*)(y.w + 13));
      L.A += near(y, WS_O + tm3 * 524288, tm3); L.G.off = rec(y, FL_O, tm3 * 16 + m16);
      load_all<C3, true, 0u, 0u>(L, lds0 + R3, nop, nop);
    }
    {
      Ld<C4> L; L.A = P.ws + near(y, WS_H + tm1 * 524288, tm1); L.W = P.w1 + ch0 * 128; L.lw = lw; L.c = c1; L.kt0 = 0;
      L.init(y, lane, 128, par ? -64 : 384, (lane & 16) ? rec(y, FL_CS, (m16 >> 2) * 56 + base7 + 3 * par + (lane & 3)) : rec(y, FL_H, tm1 * 16 + m16), 10);
      lds_wait(y, 24 + FL_H, 4u);
      load_preW<C4>(L, lds0 + R4);
      load_all<C4, true, 0u, 0xa550u>(L, lds0 + R4, nop,
        [&](int s) {
          if (s < 12 && lw == ((s >> 1) & 3)) dma4<false>(min(lane, 55) * 128, P.w2 + bid * 28672 + lw * 7168, lds0 + S4_DUM);
          if (s == 13) {
            gate(y, L.G, ~0u, lw == 0, lane);
#pragma unroll
            for (int i = 0; i < 4; ++i) dma<true>(lane * 16, P.ws + near(y, WS_ST2 + tm1 * 32768, tm1) + (lw >> 1) * 16384 + ((lw & 1) * 4 + i) * 1024, lds0 + S4_STR + (lw * 4 + i) * 1024);
#pragma unroll
            for (int i = 0; i < 2; ++i) dma<true>(lane * 16, P.ws + WS_CS1 + ((lw * 2 + i) * FF + (base7 + 3 * par) * 64) * 4, lds0 + S4_CSR + (lw * 2 + i) * 1024);
          }
          if (s == 15) {
            const LAS float* rawp = (const LAS float*)(smem + S4_CSR) + lt;
            float t = 0.f;
#pragma unroll
            for (int i = 0; i < 8; ++i) t += rawp[i * 256];
            ((LAS float*)(smem + S4_CSF))[lt] = t;
            wait_lgkm0();
          }
          if (s == 15 && lw < 2) {
            f32x2 st[16]; float mu = 0.f, m2 = 0.f;
#pragma unroll
            for (int i = 0; i < 16; ++i) { st[i] = *(const LAS f32x2*)(smem + S4_STR + i * 1024 + lt * 8); mu += st[i][0]; }
            mu *= (1.f / 16.f);
#pragma unroll
            for (int i = 0; i < 16; ++i) { const float d = st[i][0] - mu; m2 += st[i][1] + 64.f * d * d; }
            *(LAS f32x2*)(smem + S4_STF + lt * 8) = (f32x2){mu, __builtin_amdgcn_rsqf(m2 * (1.f / D) + 1e-5f)};
            wait_lgkm0();
          }
        });
    }
    {
      Ld<C5> L; L.A = P.ws + near(y, WS_F + tm5 * 2097152, tm5); L.W = P.w2 + tn5 * 256; L.lw = lw; L.c = c5; L.kt0 = 28 * kh; L.init(y, lane, 128, 0, rec(y, FL_F, tm5 * 16 + 8 * kh + ((lane >> 1) & 7)) + (lane & 1) * 4, 11);
      load_preW<C5>(L, lds0 + R5);
      load_all<C5, true, 0u, 0u>(L, lds0 + R5, nop, nop);
    }
    return;
  }

  const int wm = wid >> 1, wn = wid & 1;
  __builtin_amdgcn_s_setprio(1);
  if (wid == 0 && lane < 32) y.w[lane] = 0u;
  {
    f32x4 acc[24];
    Adr ad;
    ad.v[0] = lds0 + R1 + (wm * 64 + m16) * 128 + ((g ^ (m16 >> 1)) << 4); ad.v[1] = lds0 + R1 + (wm * 64 + m16) * 128 + ((g ^ (m16 >> 1) ^ 4) << 4);
    ad.v[2] = lds0 + R1 + 16384 + wlane(lane, 2 * wn); ad.v[3] = lds0 + R1 + 16384 + wlane(lane, 2 * wn + 1); ad.v[4] = 0; ad.v[5] = 0;
    u32x4 px[4], pw[8];
    const int prow = bid * 8 + wid * 2 + (lane >> 5);
    consume<C1, 1, 1, PH0, PH1, PH2>(acc, ad, [&](int s) {
      if (s < 0) {
        y.tag = __builtin_amdgcn_readfirstlane(*(volatile LAS unsigned*)(smem + S1_FLG)) + 1u;
      }
      if (s == 0) {
#pragma unroll
        for (int i = 0; i < 4; ++i) px[i] = *(const u32x4*)(P.x + (size_t)prow * 2048 + ((lane & 31) + 32 * i) * 16);
        colsum_load<8>(pw, P.wqkv + (size_t)((min(bid, 191) / 48) * 256 + wid * 64) * (3 * D * 2) + (bid % 48) * 128, 3 * D * 2, lane);
      }
      if (s == 1) {
#pragma unroll
        for (int i = 0; i < 4; ++i) asm volatile("" : "+v"(px[i]));
#pragma unroll
        for (int i = 0; i < 8; ++i) asm volatile("" : "+v"(pw[i]));
        const unsigned one2 = opaque(0x3f803f80u);
        float sm = 0.f, sq = 0.f;
#pragma unroll
        for (int i = 0; i < 4; ++i)
#pragma unroll
          for (int j = 0; j < 4; ++j) { sm = dot2(px[i][j], one2, sm); sq = dot2(px[i][j], px[i][j], sq); }
        const float mean = xsum16(row16_sum(sm)) * (1.f / D);
        const float var = fmaxf(xsum16(row16_sum(sq)) * (1.f / D) - mean * mean, 0.f);
        if ((lane & 31) == 0) st8(y.r, (u32x2){bu(mean), bu(__builtin_amdgcn_rsqf(var + 1e-5f))}, prow * 8, WS_ST1);
        if (bid < 192) colsum_finish<8>(pw, y.r, WS_CSQ + (((bid / 48) * 4 + wid) * 3 * D + (bid % 48) * 64) * 4, lane);
      }
      if (s == 2) { wait_vm<0>(); if (lane == 0) st4(y.r, y.tag, 0, WS_FLG + (FL_P0 * 256 + bid) * 128 + wid * 4); }
    });
    const int head = 2 * tn1 + wn;
    f32x4 cs[4]; float csv[2];
#pragma unroll
    for (int n = 0; n < 4; ++n) cs[n] = *(const LAS f32x4*)(smem + S1_CSF + ((n >> 1) * 64 + wn * 32 + (n & 1) * 16 + 4 * g) * 4);
#pragma unroll
    for (int n = 0; n < 2; ++n) csv[n] = *(const LAS float*)(smem + S1_CSF + (128 + wn * 32 + n * 16 + m16) * 4);
    u32x4 wp[2];
#pragma unroll
    for (int F = 0; F < 2; ++F) { lds_ptr a0 = smem + S1_WP + wn * 2048 + (4 * g + (m16 >> 2)) * 64 + F * 32 + (lane & 3) * 8; wp[F] = lds_tr2(a0, a0 + 1024); }
    u32x4 qb[4], vb[2][2];
    f32x4 p[4][2];
    const f32x4 zero = {0.f, 0.f, 0.f, 0.f};
    {
      f32x4 vv[4][2];
#pragma unroll
      for (int m = 0; m < 4; ++m) {
        const f32x2 st = *(const LAS f32x2*)(smem + S1_ST + (wm * 64 + m * 16 + m16) * 8);
        const f32x4 s0 = *(const LAS f32x4*)(smem + S1_ST + (wm * 64 + m * 16 + 4 * g) * 8), s1 = *(const LAS f32x4*)(smem + S1_ST + (wm * 64 + m * 16 + 4 * g) * 8 + 16);
        const f32x4 mu = {s0[0], s0[2], s1[0], s1[2]}, rs = {s0[1], s0[3], s1[1], s1[3]};
        qb[m] = pack8((acc[m * 6] - cs[0] * st[0]) * st[1], (acc[m * 6 + 1] - cs[1] * st[0]) * st[1]);
        const u32x4 kb = pack8((acc[m * 6 + 2] - cs[2] * st[0]) * st[1], (acc[m * 6 + 3] - cs[3] * st[0]) * st[1]);
        p[m][0] = mfma(wp[0], kb, zero); p[m][1] = mfma(wp[1], kb, zero);
        vv[m][0] = (acc[m * 6 + 4] - mu * csv[0]) * rs; vv[m][1] = (acc[m * 6 + 5] - mu * csv[1]) * rs;
      }
#pragma unroll
      for (int u = 0; u < 2; ++u) { vb[u][0] = pack8(vv[2 * u][0], vv[2 * u + 1][0]); vb[u][1] = pack8(vv[2 * u][1], vv[2 * u + 1][1]); }
    }
    u32x4 pp[4], pn[4];
    dual_softmax4(p, pp, pn);
    {
      lds_ptr own = smem + R3 + wid * C3::STAGE, oth = smem + R3 + (wid ^ 2) * C3::STAGE;
#pragma unroll
      for (int m = 0; m < 4; ++m) {
        lds_ptr w = own + (16 * m + m16) * 136 + 8 * g;
        *(LAS u32x2*)(w) = (u32x2){pp[m][0], pp[m][1]}; *(LAS u32x2*)(w + 32) = (u32x2){pp[m][2], pp[m][3]};
        *(LAS u32x2*)(w + 64) = (u32x2){pn[m][0], pn[m][1]}; *(LAS u32x2*)(w + 96) = (u32x2){pn[m][2], pn[m][3]};
      }
#pragma unroll
      for (int i = 0; i < 4; ++i) *(LAS u32x4*)(own + 8704 + i * 1024 + lane * 16) = vb[i >> 1][i & 1];
      wait_lgkm0();
      volatile LAS unsigned* xf = y.w + 16;
      xf[wid] = 1u;
      f32x4 kv[2][2] = {{zero, zero}, {zero, zero}}, z[2] = {zero, zero};
      const u32x4 ones = {0x3f803f80u, 0x3f803f80u, 0x3f803f80u, 0x3f803f80u};
      const unsigned ro = (4 * g + (m16 >> 2)) * 136 + wm * 64 + (lane & 3) * 8;
#pragma unroll
      for (int u = 0; u < 2; ++u)
#pragma unroll
        for (int F = 0; F < 2; ++F) {
          const u32x4 pa = lds_tr2(own + ro + u * 32 * 136 + F * 32, own + ro + u * 32 * 136 + F * 32 + 16 * 136);
          kv[F][0] = mfma(pa, vb[u][0], kv[F][0]); kv[F][1] = mfma(pa, vb[u][1], kv[F][1]); z[F] = mfma(pa, ones, z[F]);
        }
      while (xf[wid ^ 2] == 0u) __builtin_amdgcn_s_sleep(0);
      asm volatile("" ::: "memory");
#pragma unroll
      for (int u = 0; u < 2; ++u) {
        const u32x4 v0 = lds128(oth + 8704 + u * 2048 + lane * 16), v1 = lds128(oth + 8704 + u * 2048 + 1024 + lane * 16);
#pragma unroll
        for (int F = 0; F < 2; ++F) {
          const u32x4 pa = lds_tr2(oth + ro + u * 32 * 136 + F * 32, oth + ro + u * 32 * 136 + F * 32 + 16 * 136);
          kv[F][0] = mfma(pa, v0, kv[F][0]); kv[F][1] = mfma(pa, v1, kv[F][1]); z[F] = mfma(pa, ones, z[F]);
        }
      }
      const unsigned kd = WS_KVP + (head * 16 + tm1) * 4096 + wm * 2048;
      st16(y.r, pack8(kv[0][0], kv[1][0]), lane * 16, kd);
      st16(y.r, pack8(kv[0][1], kv[1][1]), lane * 16, kd + 1024);
      if (m16 == 0) {
        st16(y.r, __builtin_bit_cast(u32x4, z[0]), g * 32, WS_ZP + (head * 16 + tm1) * 256 + wm * 128);
        st16(y.r, __builtin_bit_cast(u32x4, z[1]), g * 32 + 16, WS_ZP + (head * 16 + tm1) * 256 + wm * 128);
      }
      publish(y, FL_KV, tm1 * 16 + tn1, lane);
    }
#pragma unroll
    for (int m = 0; m < 4; ++m) { p[m][0] = mfma(wp[0], qb[m], zero); p[m][1] = mfma(wp[1], qb[m], zero); }
    dual_softmax4(p, pp, pn);
    { Gate G; G.off = 0; G.mask = 0; G.word = 8; kv_sum(y, G, smem, 2 * tn1, 0, threadIdx.x, false, wid == 0, lane, [](int) {}); }
    wait_lgkm0(); bar();
    y.pb = __builtin_amdgcn_readfirstlane(*(volatile LAS unsigned*)(y.w + 13));
    u32x4 ka[2][2], za[2];
#pragma unroll
    for (int ks = 0; ks < 2; ++ks) {
      ka[ks][0] = lds128(smem + S1_KV + wn * 4096 + ks * 2048 + lane * 16); ka[ks][1] = lds128(smem + S1_KV + wn * 4096 + ks * 2048 + 1024 + lane * 16);
      za[ks] = lds128(smem + S1_KV + 8192 + wn * 128 + ks * 64 + g * 16);
    }
#pragma unroll
    for (int m = 0; m < 4; ++m) {
      f32x4 n0 = mfma(ka[0][0], pp[m], zero), n1 = mfma(ka[0][1], pp[m], zero), dn = mfma(za[0], pp[m], zero);
      n0 = mfma(ka[1][0], pn[m], n0); n1 = mfma(ka[1][1], pn[m], n1); dn = mfma(za[1], pn[m], dn);
      const float rd = __builtin_amdgcn_rcpf(dn[0] + 1e-6f);
      st16(y.r, pack8(n0 * rd, n1 * rd), lane * 16, near(y, WS_O + (tm1 * 16 + tn1) * 32768, tm1) + wm * 16384 + (m * 2 + wn) * 1024);
    }
    publish(y, FL_O, tm1 * 16 + tn1, lane);
  }
  {
    u32x4 xr[2][2];
#pragma unroll
    for (int m = 0; m < 2; ++m)
#pragma unroll
      for (int Q = 0; Q < 2; ++Q)
        xr[m][Q] = *(const u32x4*)(P.x + (size_t)(tm3 * 128 + wid * 32 + m * 16 + m16) * 2048 + tn3 * 128 + Q * 64 + (g & 1) * 32 + (g >> 1) * 16);
    f32x4 acc[8];
    Adr ad; ad.v[0] = lds0 + R3 + wid * 4096 + lane * 16; ad.v[1] = 0;
#pragma unroll
    for (int n = 0; n < 4; ++n) ad.v[2 + n] = lds0 + R3 + 16384 + wlane(lane, n);
    consume_s<C3, 3, 2>(acc, ad, [](int) {});
#pragma unroll
    for (int m = 0; m < 2; ++m) {
      float hv[16];
#pragma unroll
      for (int Q = 0; Q < 2; ++Q) {
        auto r0 = __builtin_amdgcn_permlane16_swap(xr[m][Q][0], xr[m][Q][2], false, false);
        auto r1 = __builtin_amdgcn_permlane16_swap(xr[m][Q][1], xr[m][Q][3], false, false);
        const f32x4 a = acc[m * 4 + 2 * Q], b = acc[m * 4 + 2 * Q + 1];
        const u32x4 hp = {pk2(a[0] + bflo(r0[0]), a[1] + bfhi(r0[0])), pk2(a[2] + bflo(r1[0]), a[3] + bfhi(r1[0])),
                          pk2(b[0] + bflo(r0[1]), b[1] + bfhi(r0[1])), pk2(b[2] + bflo(r1[1]), b[3] + bfhi(r1[1]))};
        st16(y.r, hp, lane * 16, near(y, WS_H + (tm3 * 16 + tn3) * 32768, tm3) + wm * 16384 + wn * 4096 + (m * 2 + Q) * 1024);
#pragma unroll
        for (int j = 0; j < 4; ++j) { hv[Q * 8 + 2 * j] = bflo(hp[j]); hv[Q * 8 + 2 * j + 1] = bfhi(hp[j]); }
      }
      float s = 0.f;
#pragma unroll
      for (int j = 0; j < 16; ++j) s += hv[j];
      s = xsum32(xsum16(s)) * (1.f / 64.f);
      float q = 0.f;
#pragma unroll
      for (int j = 0; j < 16; ++j) { const float d = hv[j] - s; q += d * d; }
      q = xsum32(xsum16(q));
      if (g == 0) st8(y.r, (u32x2){bu(s), bu(q)}, m16 * 8, near(y, WS_ST2 + tm3 * 32768, tm3) + (tn3 >> 3) * 16384 + (tn3 & 7) * 1024 + (wid * 32 + m * 16) * 8);
    }
    publish(y, FL_H, tm3 * 16 + tn3, lane);
  }
  {
    f32x4 acc[28];
    Adr ad;
    ad.v[0] = lds0 + R4 + wm * 8192 + wn * 4096 + lane * 16; ad.v[1] = lds0 + R4 + wm * 8192 + (wn ^ 1) * 4096 + lane * 16;
    ad.v[2] = lds0 + R4 + 16384 + wlane(lane, wn); ad.v[3] = lds0 + R4 + 16384 + wlane(lane, 2 + wn);
    ad.v[4] = lds0 + R4 + 16384 + 6144 + hlane(lane, 0); ad.v[5] = lds0 + R4 + 16384 + 6144 + hlane(lane, 1);
    consume<C4, 4, 1, 0, 0, 0>(acc, ad, [](int) {});
    f32x4 cs[8];
#pragma unroll
    for (int n = 0; n < 6; ++n) cs[n] = *(const LAS f32x4*)(smem + S4_CSF + ((par + (n >> 1)) * 64 + (2 * (n & 1) + wn) * 16 + 4 * g) * 4);
#pragma unroll
    for (int n = 0; n < 2; ++n) cs[6 + n] = *(const LAS f32x4*)(smem + S4_CSF + ((par ? 32 : 192) + n * 16 + 4 * g) * 4);
    const f32x4 zero = {0.f, 0.f, 0.f, 0.f};
    f32x2 st[4];
#pragma unroll
    for (int m = 0; m < 4; ++m) st[m] = *(const LAS f32x2*)(smem + S4_STF + ((wm * 4 + ((m + 2 * wn) & 3)) * 16 + m16) * 8);
#pragma unroll
    for (int i = 0; i < 4; ++i) {
#pragma unroll
      for (int m = 0; m < (i < 3 ? 4 : 2); ++m) {
        const unsigned fd = near(y, WS_F + tm1 * 2097152, tm1) + wm * 16384 + ((m + 2 * wn) & 3) * 2048 + (i < 3 ? (ch0 + i) * 32768 + wn * 1024 : (base7 + 3) * 32768 + par * 1024);
        const int a = i < 3 ? m * 6 + 2 * i : 24 + 2 * m;
        st16(y.r, pack8(__builtin_elementwise_max((acc[a] - cs[2 * i] * st[m][0]) * st[m][1], zero),
                        __builtin_elementwise_max((acc[a + 1] - cs[2 * i + 1] * st[m][0]) * st[m][1], zero)), lane * 16, fd);
        SB();
      }
      if (i == 1) {
        wait_vm<4>();
        if (lane == 0 && __hip_atomic_fetch_add(y.w + FL_F, 1u, __ATOMIC_RELAXED, __HIP_MEMORY_SCOPE_WORKGROUP) == 3u) st4(y.r, y.tag, 0, WS_FLG + rec(y, FL_F, tm1 * 16 + tn1));
        SB();
      }
    }
  }
  {
    f32x4 acc[16];
    Adr ad;
    ad.v[0] = lds0 + R5 + wm * 8192 + kh * 4096 + lane * 16; ad.v[1] = lds0 + R5 + wm * 8192 + (kh ^ 1) * 4096 + lane * 16;
#pragma unroll
    for (int n = 0; n < 4; ++n) ad.v[2 + n] = lds0 + R5 + 16384 + wn * 2048 + wlane(lane, n);
    u32x4 hr[2][2];
    consume_s<C5, 5, 1>(acc, ad, [&](int s) {
      if (s < 0) {
        wait_vm<0>();
        if (lane == 0 && __hip_atomic_fetch_add(y.w + 20, 1u, __ATOMIC_RELAXED, __HIP_MEMORY_SCOPE_WORKGROUP) == 3u) st4(y.r, y.tag, 0, WS_FLG + rec(y, FL_F, tm1 * 16 + tn1) + 4);
#pragma unroll
        for (int m = 0; m < 2; ++m)
#pragma unroll
          for (int Q = 0; Q < 2; ++Q) hr[m][Q] = ld16(y.r, lane * 16, near(y, WS_H + (tm5 * 16 + 2 * tn5 + wn) * 32768, tm5) + wm * 16384 + ((2 * kh + m) * 2 + Q) * 1024);
      }
    });
    const int tile = tm5 * 8 + tn5;
#pragma unroll
    for (int i = 0; i < 4; ++i) st16(y.r, pack8(acc[8 + 2 * i], acc[9 + 2 * i]), lane * 16, near(y, WS_O + (tile * 2 + kh) * 32768, tm5) + wm * 16384 + wn * 4096 + i * 1024);
    wait_vm<0>();
    if (lane == 0) st4(y.r, y.tag, 0, WS_FLG + rec(y, FL_X + wm, tile * 2 + kh) + wn * 64);
    poll(y, rec(y, FL_X + wm, tile * 2 + (kh ^ 1)) + wn * 64, 1, lane);
    const rsrc_t ro = __builtin_amdgcn_make_buffer_rsrc(P.out, 0, 2048 * 2048, 0x00020000);
    u32x4 xv[4];
#pragma unroll
    for (int i = 0; i < 4; ++i) xv[i] = ld16(y.r, lane * 16, near(y, WS_O + (tile * 2 + (kh ^ 1)) * 32768, tm5) + wm * 16384 + wn * 4096 + i * 1024);
#pragma unroll
    for (int m = 0; m < 2; ++m)
#pragma unroll
      for (int Q = 0; Q < 2; ++Q) {
        const u32x4 xx = xv[m * 2 + Q];
        const f32x4 a = acc[m * 4 + 2 * Q] + (f32x4){bflo(xx[0]), bfhi(xx[0]), bflo(xx[1]), bfhi(xx[1])}, b = acc[m * 4 + 2 * Q + 1] + (f32x4){bflo(xx[2]), bfhi(xx[2]), bflo(xx[3]), bfhi(xx[3])};
        const u32x4 hh = hr[m][Q];
        const unsigned a0 = pk2(a[0] + bflo(hh[0]), a[1] + bfhi(hh[0])), a1 = pk2(a[2] + bflo(hh[1]), a[3] + bfhi(hh[1]));
        const unsigned b0 = pk2(b[0] + bflo(hh[2]), b[1] + bfhi(hh[2])), b1 = pk2(b[2] + bflo(hh[3]), b[3] + bfhi(hh[3]));
        auto r0 = __builtin_amdgcn_permlane16_swap(a0, b0, false, false);
        auto r1 = __builtin_amdgcn_permlane16_swap(a1, b1, false, false);
        __builtin_amdgcn_raw_buffer_store_b128((u32x4){r0[0], r1[0], r0[1], r1[1]}, ro, (tm5 * 128 + wm * 64 + (2 * kh + m) * 16 + m16) * 2048 + tn5 * 256 + wn * 128 + Q * 64 + (g & 1) * 32 + (g >> 1) * 16, 0, 16);
      }
    if (bid == 0 && threadIdx.x == 0) st4(y.r, y.tag, 0, WS_CTL);
  }
}

extern "C" void kernel_launch(void* const* d_in, const int* in_sizes, int n_in, void* d_out, int out_size, void* d_ws, size_t ws_size,
                              hipStream_t stream) {
  Params p;
  p.x = (const char*)d_in[0]; p.wqkv = (const char*)d_in[1]; p.wphi = (const char*)d_in[2]; p.wo = (const char*)d_in[3];
  p.w1 = (const char*)d_in[5]; p.w2 = (const char*)d_in[6]; p.ws = (char*)(((size_t)d_ws + 0x1fffff) & ~(size_t)0x1fffff); p.out = (char*)d_out;
  (void)hipFuncSetAttribute((const void*)fused, hipFuncAttributeMaxDynamicSharedMemorySize, LDS_BYTES);
  fused<<<256, 512, LDS_BYTES, stream>>>(p);
}
```

```cpp
#include <hip/hip_runtime.h>
#include <hip/hip_bf16.h>

#define LAS __attribute__((address_space(3)))
#define DI __device__ __forceinline__
typedef __bf16 bf16x8 __attribute__((ext_vector_type(8)));
typedef __bf16 bf16x2 __attribute__((ext_vector_type(2)));
typedef short s16x4 __attribute__((ext_vector_type(4)));
typedef float f32x2 __attribute__((ext_vector_type(2)));
typedef float f32x4 __attribute__((ext_vector_type(4)));
typedef unsigned u32x2 __attribute__((ext_vector_type(2)));
typedef unsigned u32x4 __attribute__((ext_vector_type(4)));
typedef LAS char* lds_ptr;
typedef __amdgpu_buffer_rsrc_t rsrc_t;

constexpr int D = 1024, FF = 3584;
constexpr unsigned WS_CTL = 0;
constexpr unsigned WS_FLG = 4096;
constexpr unsigned WS_ST1 = 320u << 10;
constexpr unsigned WS_CSQ = 384u << 10;
constexpr unsigned WS_CS1 = 576u << 10;
constexpr unsigned WS_ZP = 704u << 10;
constexpr unsigned WS_ST2 = 1024u << 10;
constexpr unsigned WS_FL2 = 1536u << 10;
constexpr unsigned WS_PRB = 1920u << 10;
constexpr unsigned WS_KVP = 2u << 20;
constexpr unsigned WS_O = 4u << 20;
constexpr unsigned WS_H = 12u << 20;
constexpr unsigned WS_F = 20u << 20;
constexpr unsigned WS_END = 52u << 20;
constexpr int NPAGE = 26;
enum { FL_P0 = 0, FL_KV = 1, FL_CS = 2, FL_O = 3, FL_H = 4, FL_F = 5, FL_X = 6   };


struct Params { const char* x; const char* wqkv; const char* wphi; const char* wo; const char* w1; const char* w2; char* ws; char* out; };

DI unsigned pk2(float a, float b) { f32x2 v = {a, b}; return __builtin_bit_cast(unsigned, __builtin_convertvector(v, bf16x2)); }
DI float bflo(unsigned u) { return __uint_as_float(u << 16); }
DI float bfhi(unsigned u) { return __uint_as_float(u & 0xffff0000u); }
DI float rb(float x) { return bflo(pk2(x, 0.f)); }
DI f32x2 ln_consts(float mean, float var) { return (f32x2){rb(mean), __builtin_amdgcn_rcpf(rb(__builtin_amdgcn_sqrtf(rb(rb(var) + 1e-5f))))}; }
DI u32x4 pack8(const f32x4& a, const f32x4& b) { return (u32x4){pk2(a[0], a[1]), pk2(a[2], a[3]), pk2(b[0], b[1]), pk2(b[2], b[3])}; }
DI bf16x8 asbf(const u32x4& v) { return __builtin_bit_cast(bf16x8, v); }
#define SB() __builtin_amdgcn_sched_barrier(0)
DI void bar() { asm volatile("" ::: "memory"); __builtin_amdgcn_s_barrier(); asm volatile("" ::: "memory"); }
DI void wait_lgkm0() { __builtin_amdgcn_s_waitcnt(0xC07F); asm volatile("" ::: "memory"); }
template <int N> DI void wait_vm() { asm volatile("s_waitcnt vmcnt(%0)" ::"i"(N) : "memory"); }
template <bool SC1> DI void dma(unsigned voff, const char* sbase, unsigned lds) {
  if (SC1) asm volatile("s_mov_b32 m0, %2\n\ts_nop 3\n\tglobal_load_lds_dwordx4 %0, %1 sc1" ::"v"(voff), "s"(sbase), "s"(lds) : "memory");
  else asm volatile("s_mov_b32 m0, %2\n\ts_nop 3\n\tglobal_load_lds_dwordx4 %0, %1" ::"v"(voff), "s"(sbase), "s"(lds) : "memory");
}
template <bool SC1> DI void dma4(unsigned voff, const char* sbase, unsigned lds) {
  if (SC1) asm volatile("s_mov_b32 m0, %2\n\ts_nop 3\n\tglobal_load_lds_dword %0, %1 sc1" ::"v"(voff), "s"(sbase), "s"(lds) : "memory");
  else asm volatile("s_mov_b32 m0, %2\n\ts_nop 3\n\tglobal_load_lds_dword %0, %1" ::"v"(voff), "s"(sbase), "s"(lds) : "memory");
}
DI u32x4 lds128(lds_ptr p) { return *(const LAS u32x4*)p; }
DI u32x4 lds_tr2(lds_ptr p0, lds_ptr p1) {
  s16x4 a = __builtin_amdgcn_ds_read_tr16_b64_v4i16((LAS s16x4*)p0);
  s16x4 b = __builtin_amdgcn_ds_read_tr16_b64_v4i16((LAS s16x4*)p1);
  return __builtin_bit_cast(u32x4, __builtin_shufflevector(a, b, 0, 1, 2, 3, 4, 5, 6, 7));
}
DI u32x2 lds_tr(lds_ptr p) { return __builtin_bit_cast(u32x2, __builtin_amdgcn_ds_read_tr16_b64_v4i16((LAS s16x4*)p)); }
DI f32x4 mfma(const u32x4& a, const u32x4& b, const f32x4& c) { return __builtin_amdgcn_mfma_f32_16x16x32_bf16(asbf(a), asbf(b), c, 0, 0, 0); }
DI void mfma_ip(f32x4& acc, const u32x4& a, const u32x4& b) { asm volatile("v_mfma_f32_16x16x32_bf16 %0, %1, %2, %0" : "+v"(acc) : "v"(a), "v"(b)); }
DI u32x4 ld16(rsrc_t r, unsigned voff, unsigned soff) { return __builtin_amdgcn_raw_buffer_load_b128(r, voff, soff, 16); }
DI void st16(rsrc_t r, const u32x4& v, unsigned voff, unsigned soff) { __builtin_amdgcn_raw_buffer_store_b128(v, r, voff + soff, 0, 16); }
DI void st8(rsrc_t r, const u32x2& v, unsigned voff, unsigned soff) { __builtin_amdgcn_raw_buffer_store_b64(v, r, voff, soff, 16); }
DI void st4(rsrc_t r, unsigned v, unsigned voff, unsigned soff) { __builtin_amdgcn_raw_buffer_store_b32(v, r, voff, soff, 16); }
DI float fb(unsigned u) { return __uint_as_float(u); }
DI unsigned bu(float f) { return __float_as_uint(f); }
DI float xsum16(float x) { auto r = __builtin_amdgcn_permlane16_swap(bu(x), bu(x), false, false); return fb(r[0]) + fb(r[1]); }
DI float xsum32(float x) { auto r = __builtin_amdgcn_permlane32_swap(bu(x), bu(x), false, false); return fb(r[0]) + fb(r[1]); }
template <int CTRL> DI float dpp(float x) { return __builtin_bit_cast(float, __builtin_amdgcn_update_dpp(0, __builtin_bit_cast(int, x), CTRL, 0xf, 0xf, true)); }
DI float row16_sum(float x) { x += dpp<0xB1>(x); x += dpp<0x4E>(x); x += dpp<0x141>(x); x += dpp<0x140>(x); return x; }

struct Sync { rsrc_t r; unsigned tag, pb; LAS unsigned* w; };
DI unsigned near(const Sync& y, unsigned off, int tm) { return off + ((((y.pb >> (off >> 21)) ^ (tm >> 3)) & 1u) << 13); }
DI unsigned rec(const Sync& y, int phase, int idx) {
  return phase < FL_O ? (phase * 256 + idx) * 128 : near(y, WS_FL2 + (phase - FL_O) * 65536 + (idx >> 6) * 16384 + (idx & 63) * 128, idx >> 4) - WS_FLG;
}
DI void publish(const Sync& y, int phase, int idx, int lane) {
  if (lane == 0) __hip_atomic_fetch_add(y.w + 24 + phase, 1u, __ATOMIC_RELAXED, __HIP_MEMORY_SCOPE_WORKGROUP);
  wait_vm<0>();
  if (lane == 0 && __hip_atomic_fetch_add(y.w + phase, 1u, __ATOMIC_RELAXED, __HIP_MEMORY_SCOPE_WORKGROUP) == 3u) st4(y.r, y.tag, 0, WS_FLG + rec(y, phase, idx));
}
DI void poll(const Sync& y, unsigned off, int n, int lane) {
  const unsigned long long t0 = __builtin_amdgcn_s_memrealtime();
  for (unsigned it = 0;; ++it) {
    const unsigned v = __builtin_amdgcn_raw_buffer_load_b32(y.r, WS_FLG + off, 0, 16);
    if (__all(lane >= n || v == y.tag)) break;
    if ((it & 63) == 63) {
      if (__builtin_amdgcn_readfirstlane(__builtin_amdgcn_raw_buffer_load_b32(y.r, 0, WS_CTL + 128, 16)) == y.tag) break;
      if (__builtin_amdgcn_s_memrealtime() - t0 > 4000000ull) { if (lane == 0) st4(y.r, y.tag, 0, WS_CTL + 128); break; }
    }
    __builtin_amdgcn_s_sleep(2);
  }
  asm volatile("" ::: "memory");
}
DI void lds_wait(const Sync& y, int word, unsigned v) {
  volatile LAS unsigned* w = y.w + word;
  while (*w < v) __builtin_amdgcn_s_sleep(1);
  asm volatile("" ::: "memory");
}
struct Gate { unsigned off, mask; int word; };
DI void gate_slow(const Sync& y, Gate& G, unsigned need, bool poller, int lane, int cnt = 0) {
  volatile LAS unsigned* w = y.w + G.word;
  if (poller) {
    const unsigned long long t0 = __builtin_amdgcn_s_memrealtime();
    for (unsigned it = 0;; ++it) {
      G.mask = (unsigned)__ballot(__builtin_amdgcn_raw_buffer_load_b32(y.r, WS_FLG + G.off, 0, 16) == y.tag);
      if ((it & 63) == 63) {
        const bool late = __builtin_amdgcn_s_memrealtime() - t0 > 4000000ull;
        if (late && lane == 0) st4(y.r, y.tag, 0, WS_CTL + 128);
        if (late || __builtin_amdgcn_readfirstlane(__builtin_amdgcn_raw_buffer_load_b32(y.r, 0, WS_CTL + 128, 16)) == y.tag) G.mask = ~0u;
      }
      *w = G.mask;
      if ((G.mask & need) == need && __builtin_popcount(G.mask & 0xffffu) >= cnt) break;
      __builtin_amdgcn_s_sleep(1);
    }
  } else {
    for (;;) { G.mask = __builtin_amdgcn_readfirstlane(*w); if ((G.mask & need) == need && __builtin_popcount(G.mask & 0xffffu) >= cnt) break; __builtin_amdgcn_s_sleep(1); }
  }
  asm volatile("" ::: "memory");
}
DI void gate(const Sync& y, Gate& G, unsigned need, bool poller, int lane) { if (__builtin_expect((G.mask & need) != need, 0)) gate_slow(y, G, need, poller, lane); }

template <int NK, bool SPREAD> DI int kmap(int t, int c) {
  const int u = t - 1, q = u & 3, b4 = u & ~3, body = (int)(u < NK - 4);
  const int lead = ((int)(q == c) & body & (int)(u >= 0)) | (int)(SPREAD && t == 0), head = SPREAD ? 0 : -(int)(u < 4);
  const int non = ((b4 + ((c + 1 + q - ((int)(q > c) & body)) & 3)) & ~head) | ((t - (int)(u > c)) & head);
  return non ^ (((b4 + 4 + c) ^ non) & -lead);
}
enum { ORD_NAT = 0, ORD_PAIR = 1, ORD_F = 2 };
template <int NSUB_, bool HALF_, int NSLOT_, int NK_, int ORD_, int WSTRIDE_, bool XROW_, int LAG_> struct Cfg {
  static constexpr int NSUB = NSUB_, NSLOT = NSLOT_, NK = NK_, ORD = ORD_, WSTRIDE = WSTRIDE_;
  static constexpr bool HALF = HALF_, XROW = XROW_;
  static constexpr int LAG = LAG_;
  static constexpr int STAGE = 16384 + NSUB * 8192 + (HALF ? 4096 : 0), PW = 4 + 2 * NSUB + (HALF ? 1 : 0), RING = STAGE * NSLOT;
  static constexpr int WB = NSUB * 2048 + (HALF ? 1024 : 0);
};
typedef Cfg<3, false, 3, 16, ORD_NAT, 3 * D * 2, true, 0> C1;
typedef Cfg<1, false, 5, 16, ORD_PAIR, D * 2, false, 1> C3;
typedef Cfg<3, true, 3, 16, ORD_PAIR, FF * 2, false, 0> C4;
typedef Cfg<2, false, 4, 28, ORD_F, D * 2, false, 1> C5;

#define GLDS "global_load_lds_dwordx4 "
template <class C> struct Ld {
  const char* A; const char* W; int lw, c, kt0; unsigned vA[4], vW[7];
  const char* sA; const char* sW; unsigned need;
  Gate G; const Sync* y; int lane_;
  static constexpr int C0 = C::WB > 4096 ? 4096 : 0;
  DI void init(const Sync& y_, int lane, int substep, int halfrel, unsigned goff, int gword) {
    y = &y_; lane_ = lane; G.off = goff; G.mask = 0; G.word = gword;
#pragma unroll
    for (int i = 0; i < 4; ++i) vA[i] = C::XROW ? (lane >> 3) * 2048 + ((((lane & 7) ^ (lane >> 4)) << 4) ^ ((i & 1) * 64)) + i * (16384 - 1024) : lane * 16;
    const int rl = (C::ORD == ORD_NAT) ? 8 * (lane >> 5) + ((lane >> 3) & 3) : (lane >> 3);
    const unsigned w0 = rl * C::WSTRIDE + (((lane & 7) ^ (((lane >> 4) & 3) << 1)) << 4) + 4096;
#pragma unroll
    for (int q = 0; q < 2 * C::NSUB; ++q) vW[q] = w0 + (q & 1) * ((C::ORD == ORD_NAT ? 16 : 8) * C::WSTRIDE) + (q >> 1) * substep - (q * 1024 - C0);
    if (C::HALF) vW[6] = (lane >> 2) * C::WSTRIDE + (((lane & 3) ^ (((lane >> 4) & 1) << 1)) << 4) + 4096 + halfrel - (6144 - C0);
    A += C::XROW ? lw * 32 * 2048 : (lw >> 1) * 16384 + (lw & 1) * 4096;
    W -= 4096;
  }
  DI void prep(int s) {
    int kt = kmap<C::NK, C::XROW>(s, c);
    const int kb = lw >> 1, t = lw & 1;
    int ur;
    need = 1u << kt;
    if (C::ORD == ORD_NAT) ur = 32 * kb + 4 * t;
    else if (C::ORD == ORD_F) {
      const int p = kt & 7, q = kt >> 3, sp = -(int)(q == 3);
      need = ((1u << (2 * p + (int)(q > 0))) & ~sp) | ((0xau << (4 * (p & 3))) & sp);
      kt = kt0 + (((7 * (p >> 1) + 4 * (p & 1) + q) & ~sp) | ((7 * p + 3) & sp));
      ur = (16 * lw & sp) | ((16 * kb + 32 * t) & ~sp);
    }
    else ur = 16 * lw;
    sA = A + (unsigned)(kt * (C::XROW ? 128 : 32768));
    sW = W + (unsigned)((kt * 64 + ur) * C::WSTRIDE);
  }
  DI void issueA(unsigned base) {
    if (!C::XROW) gate(*y, G, need, lw == 0, lane_);
    const unsigned m = base + lw * 4096;
    if (C::XROW) asm volatile("s_mov_b32 m0, %5\n\ts_nop 3\n\t" GLDS "%0, %4\n\t" GLDS "%1, %4 offset:1024\n\t" GLDS "%2, %4 offset:2048\n\t" GLDS "%3, %4 offset:3072"
                              ::"v"(vA[0]), "v"(vA[1]), "v"(vA[2]), "v"(vA[3]), "s"(sA), "s"(m) : "memory");
    else asm volatile("s_mov_b32 m0, %2\n\ts_nop 3\n\t" GLDS "%0, %1 sc1\n\t" GLDS "%0, %1 offset:1024 sc1\n\t" GLDS "%0, %1 offset:2048 sc1\n\t" GLDS "%0, %1 offset:3072 sc1"
                      ::"v"(vA[0]), "s"(sA), "s"(m) : "memory");
  }
  DI void issueW(unsigned base) const {
    const unsigned m = base + 16384 + lw * C::WB + C0;
    if (C::NSUB == 1) asm volatile("s_mov_b32 m0, %3\n\ts_nop 3\n\t" GLDS "%0, %2\n\t" GLDS "%1, %2 offset:1024" ::"v"(vW[0]), "v"(vW[1]), "s"(sW), "s"(m) : "memory");
    if (C::NSUB == 2) asm volatile("s_mov_b32 m0, %5\n\ts_nop 3\n\t" GLDS "%0, %4\n\t" GLDS "%1, %4 offset:1024\n\t" GLDS "%2, %4 offset:2048\n\t" GLDS "%3, %4 offset:3072"
                                   ::"v"(vW[0]), "v"(vW[1]), "v"(vW[2]), "v"(vW[3]), "s"(sW), "s"(m) : "memory");
    if (C::NSUB == 3) asm volatile("s_mov_b32 m0, %7\n\ts_nop 3\n\t" GLDS "%0, %6 offset:-4096\n\t" GLDS "%1, %6 offset:-3072\n\t" GLDS "%2, %6 offset:-2048\n\t" GLDS "%3, %6 offset:-1024\n\t"
                                   GLDS "%4, %6\n\t" GLDS "%5, %6 offset:1024"
                                   ::"v"(vW[0]), "v"(vW[1]), "v"(vW[2]), "v"(vW[3]), "v"(vW[4]), "v"(vW[5]), "s"(sW), "s"(m) : "memory");
    if (C::HALF) asm volatile(GLDS "%0, %1 offset:2048" ::"v"(vW[6]), "s"(sW) : "memory");
  }
};
template <class C> DI void load_preW(Ld<C>& L, unsigned ring) {
#pragma unroll 1
  for (int s = 0; s < C::NSLOT; ++s) { L.prep(s); L.issueW(ring + s * C::STAGE); }
}
template <class C, bool SPLIT> constexpr int vm_allow(int S) {
  const int N = C::NSLOT, behind = S - 1 - C::LAG > 0 ? S - 1 - C::LAG : 0;
  const int ramp = S + 1 < N - 1 ? S + 1 : N - 1;
  const int last = ramp + behind < C::NK - 1 ? ramp + behind : C::NK - 1;
  return (SPLIT && S < N) ? 4 * (ramp - S) + C::PW * (last - ramp) : C::PW * (last - S);
}
template <class C, bool SPLIT, unsigned POSTM, int S, int SE, class Post> DI void load_fixed(Ld<C>& L, unsigned ring, const Post& post) {
  if constexpr (S < SE) {
    static_assert(C::NSLOT <= C::LAG + 4, "the ring must be full before the first refill");
    wait_vm<vm_allow<C, SPLIT>(S)>();
    bar();
    if constexpr (S + 2 < C::NSLOT) {
      if (!SPLIT) L.issueW(ring + (S + 2) * C::STAGE);
      L.issueA(ring + (S + 2) * C::STAGE);
      L.prep(S + 3);
    }
    constexpr int T = S - 1 - C::LAG + C::NSLOT;
    if constexpr (S - 1 - C::LAG >= 0 && T < C::NK) {
      L.issueW(ring + (T % C::NSLOT) * C::STAGE); L.issueA(ring + (T % C::NSLOT) * C::STAGE);
      if constexpr (T + 1 < C::NK) L.prep(T + 1);
    }
    if constexpr ((POSTM >> S) & 1u) post(S);
    load_fixed<C, SPLIT, POSTM, S + 1, SE>(L, ring, post);
  }
}
template <class C, bool SPLIT, unsigned PREM, unsigned POSTM, class Pre, class Post>
DI void load_all(Ld<C>& L, unsigned ring, const Pre& pre, const Post& post) {
  constexpr int N = C::NSLOT, S0 = N - 1 > 2 + C::LAG ? N - 1 : 2 + C::LAG, S1 = C::NK - N + C::LAG;
#pragma unroll 1
  for (int s = 0; s < 2; ++s) { L.prep(s); if (!SPLIT) L.issueW(ring + s * C::STAGE); L.issueA(ring + s * C::STAGE); }
  L.prep(2);
  load_fixed<C, SPLIT, POSTM, 0, S0>(L, ring, post);
  static_assert(vm_allow<C, SPLIT>(S0) == (N - 2 - C::LAG) * C::PW && vm_allow<C, SPLIT>(S1) == (N - 2 - C::LAG) * C::PW, "steady count");
  unsigned b = ring + ((S0 - 1 - C::LAG) % N) * C::STAGE;
#pragma unroll 1
  for (int s = S0; s <= S1; ++s) {
    wait_vm<(N - 2 - C::LAG) * C::PW>();
    if (PREM && ((PREM >> s) & 1u)) pre(s);
    bar();
    L.issueW(b);
    L.issueA(b);
    L.prep(min(s - C::LAG + N, C::NK - 1));
    b = (b == ring + (N - 1) * C::STAGE) ? ring : b + C::STAGE;
    if (POSTM && ((POSTM >> s) & 1u)) post(s);
  }
  load_fixed<C, SPLIT, POSTM, S1 + 1, C::NK>(L, ring, post);
  bar();
}

template <int MT, int NWF> struct Fr { u32x4 a[MT]; u32x2 w[NWF][2]; };
DI u32x4 cat(const u32x2 (&w)[2]) { return (u32x4){w[0][0], w[0][1], w[1][0], w[1][1]}; }
struct Adr { unsigned v[6]; };
template <int KIND> struct Shape;
template <> struct Shape<1> { static constexpr int MT = 4, NWF = 6, NMF = 24, NACC = 24, WB = 6144; static constexpr unsigned USE = 0x0f; };
template <> struct Shape<3> { static constexpr int MT = 2, NWF = 4, NMF = 8, NACC = 8, WB = 2048; static constexpr unsigned USE = 0x3d; };
template <> struct Shape<4> { static constexpr int MT = 4, NWF = 8, NMF = 28, NACC = 28, WB = 7168; static constexpr unsigned USE = 0x3f; };
template <> struct Shape<5> { static constexpr int MT = 4, NWF = 4, NMF = 16, NACC = 16, WB = 4096; static constexpr unsigned USE = 0x3f; };
DI lds_ptr lp(unsigned a) { return (lds_ptr)(size_t)a; }
template <int KIND, int J> DI void rd1(Fr<Shape<KIND>::MT, Shape<KIND>::NWF>& f, const Adr& ad, int kb) {
  constexpr int NWF = Shape<KIND>::NWF, MT = Shape<KIND>::MT, WB = Shape<KIND>::WB;
  if constexpr (J < 2 * NWF) {
    constexpr int n = J >> 1, t = J & 1;
    if constexpr (KIND == 4 && n >= 6) f.w[n][t] = lds_tr(lp(ad.v[4 + (n - 6)]) + (2 * kb + t) * WB);
    else if constexpr (KIND == 1 || KIND == 4) f.w[n][t] = lds_tr(lp(ad.v[2 + (n & 1)]) + (n >> 1) * 2048 + (2 * kb + t) * WB);
    else f.w[n][t] = lds_tr(lp(ad.v[2 + n]) + (2 * kb + t) * WB);
  } else if constexpr (J < 2 * NWF + MT) {
    constexpr int m = J - 2 * NWF;
    if constexpr (KIND == 1) f.a[m] = lds128(lp(ad.v[kb]) + m * 2048);
    else f.a[m] = lds128(lp(ad.v[m >> 1]) + (m & 1) * 2048 + kb * 1024);
  }
}
template <int KIND, int I> DI void mm1(f32x4 (&acc)[Shape<KIND>::NACC], const Fr<Shape<KIND>::MT, Shape<KIND>::NWF>& f) {
  if constexpr (KIND == 1) { constexpr int m = I / 6, n = I % 6; if constexpr (n < 4) mfma_ip(acc[I], cat(f.w[n]), f.a[m]); else mfma_ip(acc[I], f.a[m], cat(f.w[n])); }
  else if constexpr (KIND == 4) { if constexpr (I < 24) mfma_ip(acc[I], cat(f.w[I % 6]), f.a[I / 6]); else mfma_ip(acc[I], cat(f.w[6 + ((I - 24) & 1)]), f.a[(I - 24) >> 1]); }
  else mfma_ip(acc[I], cat(f.w[I % 4]), f.a[I / 4]);
}
template <int KIND, int RPG, bool READ, bool MOVE, int I = 0>
DI void step(f32x4 (&acc)[Shape<KIND>::NACC], const Fr<Shape<KIND>::MT, Shape<KIND>::NWF>& cur, Fr<Shape<KIND>::MT, Shape<KIND>::NWF>& nxt, Adr& ad, int kb, int delta) {
  constexpr int NMF = Shape<KIND>::NMF, G0 = (2 * Shape<KIND>::NWF + Shape<KIND>::MT + RPG - 1) / RPG;
  if constexpr (I < NMF) {
    if constexpr (READ) {
      rd1<KIND, I * RPG>(nxt, ad, kb);
      if constexpr (RPG > 1) rd1<KIND, I * RPG + 1>(nxt, ad, kb);
    }
    if constexpr (MOVE && I >= G0) {
      constexpr int per = (6 + (NMF - G0) - 1) / (NMF - G0);
#pragma unroll
      for (int k = (I - G0) * per; k < (I - G0 + 1) * per && k < 6; ++k)
        if ((Shape<KIND>::USE >> k) & 1u) { ad.v[k] += delta; asm volatile("" : "+v"(ad.v[k])); }
    }
    mm1<KIND, I>(acc, cur);
    SB();
    step<KIND, RPG, READ, MOVE, I + 1>(acc, cur, nxt, ad, kb, delta);
  }
}
template <int KIND, int J = 0> DI void rdall(Fr<Shape<KIND>::MT, Shape<KIND>::NWF>& f, const Adr& ad, int kb) {
  if constexpr (J < 2 * Shape<KIND>::NWF + Shape<KIND>::MT) { rd1<KIND, J>(f, ad, kb); rdall<KIND, J + 1>(f, ad, kb); }
}
template <class C, int KIND, int RPG, int CNT>
DI void stages(f32x4 (&acc)[Shape<KIND>::NACC], Fr<Shape<KIND>::MT, Shape<KIND>::NWF>& f0, Fr<Shape<KIND>::MT, Shape<KIND>::NWF>& f1, Adr& ad, int& slot) {
#pragma unroll 1
  for (int s = 0; s < CNT; ++s) {
    const int delta = (slot + 1 == C::NSLOT) ? -(C::NSLOT - 1) * C::STAGE : C::STAGE;
    slot = (slot + 1 == C::NSLOT) ? 0 : slot + 1;
    SB();
    step<KIND, RPG, true, true>(acc, f0, f1, ad, 1, delta);
    if (!C::LAG) wait_lgkm0();
    bar();
    SB();
    step<KIND, RPG, true, false>(acc, f1, f0, ad, 0, 0);
  }
}
template <class C, int KIND, int RPG, int H0, int H1, int H2, class Hook> DI void consume(f32x4 (&acc)[Shape<KIND>::NACC], Adr& ad, const Hook& hook) {
  Fr<Shape<KIND>::MT, Shape<KIND>::NWF> f0, f1;
#pragma unroll
  for (int i = 0; i < Shape<KIND>::NACC; ++i) { acc[i] = (f32x4){0.f, 0.f, 0.f, 0.f}; asm volatile("" : "+v"(acc[i])); }
  bar();
  rdall<KIND>(f0, ad, 0);
  hook(-1);
  wait_lgkm0();
  int slot = 0;
  if constexpr (H1 > 0) {
    if constexpr (H0 > 0) stages<C, KIND, RPG, H0>(acc, f0, f1, ad, slot);
    hook(0);
    stages<C, KIND, RPG, H1 - H0>(acc, f0, f1, ad, slot);
    hook(1);
    stages<C, KIND, RPG, H2 - H1>(acc, f0, f1, ad, slot);
    hook(2);
  }
  stages<C, KIND, RPG, C::NK - 1 - H2>(acc, f0, f1, ad, slot);
  SB();
  step<KIND, RPG, true, false>(acc, f0, f1, ad, 1, 0);
  wait_lgkm0();
  bar();
  SB();
  step<KIND, RPG, false, false>(acc, f1, f0, ad, 0, 0);
  asm volatile("s_nop 7\n\ts_nop 3");
  SB();
}
template <class C, int KIND, int RPG, class Hook> DI void consume_s(f32x4 (&acc)[Shape<KIND>::NACC], Adr& ad, const Hook& hook) {
  Fr<Shape<KIND>::MT, Shape<KIND>::NWF> f0, f1, g0, g1;
#pragma unroll
  for (int i = 0; i < Shape<KIND>::NACC; ++i) { acc[i] = (f32x4){0.f, 0.f, 0.f, 0.f}; asm volatile("" : "+v"(acc[i])); }
  bar();
  rdall<KIND>(f0, ad, 0); rdall<KIND>(f1, ad, 1);
  hook(-1);
#pragma unroll
  for (int k = 0; k < 6; ++k) ad.v[k] += C::STAGE;
  int slot = 1;
#pragma unroll 1
  for (int s = 0; s < C::NK - 2; s += 2) {
    int delta = (slot + 1 == C::NSLOT) ? -(C::NSLOT - 1) * C::STAGE : C::STAGE;
    slot = (slot + 1 == C::NSLOT) ? 0 : slot + 1;
    if (!C::LAG) wait_lgkm0();
    bar(); SB();
    step<KIND, RPG, true, false>(acc, f0, g0, ad, 0, 0);
    step<KIND, RPG, true, true>(acc, f1, g1, ad, 1, delta);
    delta = (slot + 1 == C::NSLOT) ? -(C::NSLOT - 1) * C::STAGE : C::STAGE;
    slot = (slot + 1 == C::NSLOT) ? 0 : slot + 1;
    if (!C::LAG) wait_lgkm0();
    bar(); SB();
    step<KIND, RPG, true, false>(acc, g0, f0, ad, 0, 0);
    step<KIND, RPG, true, true>(acc, g1, f1, ad, 1, delta);
  }
  if (!C::LAG) wait_lgkm0();
  bar(); SB();
  step<KIND, RPG, true, false>(acc, f0, g0, ad, 0, 0);
  step<KIND, RPG, true, false>(acc, f1, g1, ad, 1, 0);
  wait_lgkm0(); bar(); SB();
  step<KIND, RPG, false, false>(acc, g0, f0, ad, 0, 0);
  step<KIND, RPG, false, false>(acc, g1, f1, ad, 1, 0);
  asm volatile("s_nop 7\n\ts_nop 3");
  SB();
}
DI unsigned wlane(int lane, int nt) {
  const int g = lane >> 4, q = (lane >> 2) & 3, p = lane & 3, sw = (2 * g + (q >> 1)) & 3;
  return (4 * g + q) * 128 + ((nt ^ sw) << 5) + p * 8;
}
DI unsigned hlane(int lane, int nt) {
  const int g = lane >> 4, q = (lane >> 2) & 3, p = lane & 3;
  return (4 * g + q) * 64 + ((nt ^ (g & 1)) << 5) + p * 8;
}

template <int OP> DI float op2(float a, float b) { return OP == 0 ? a + b : OP == 1 ? __builtin_elementwise_maximum(a, b) : __builtin_elementwise_minimum(a, b); }
template <int OP> DI float red4(float x0, float x1, float x2, float x3) {
  auto a = __builtin_amdgcn_permlane16_swap(bu(x0), bu(x1), false, false);
  auto b = __builtin_amdgcn_permlane16_swap(bu(x2), bu(x3), false, false);
  const float y0 = op2<OP>(fb(a[0]), fb(a[1])), y1 = op2<OP>(fb(b[0]), fb(b[1]));
  auto c = __builtin_amdgcn_permlane32_swap(bu(y0), bu(y1), false, false);
  return op2<OP>(fb(c[0]), fb(c[1]));
}
DI void spread(float z, float (&o)[4]) {
  auto a = __builtin_amdgcn_permlane32_swap(bu(z), bu(z), false, false);
  auto b = __builtin_amdgcn_permlane16_swap(a[0], a[0], false, false);
  auto c = __builtin_amdgcn_permlane16_swap(a[1], a[1], false, false);
  o[0] = fb(b[0]); o[1] = fb(b[1]); o[2] = fb(c[0]); o[3] = fb(c[1]);
}
DI void dual_softmax4(const f32x4 (&p)[4][2], u32x4 (&pp)[4], u32x4 (&pn)[4]) {
  constexpr float L2E = 1.4426950408889634f;
  float mx[4], mn[4];
#pragma unroll
  for (int m = 0; m < 4; ++m) {
    mx[m] = op2<1>(op2<1>(op2<1>(op2<1>(p[m][0][0], p[m][0][1]), p[m][0][2]), op2<1>(op2<1>(p[m][0][3], p[m][1][0]), p[m][1][1])), op2<1>(p[m][1][2], p[m][1][3]));
    mn[m] = op2<2>(op2<2>(op2<2>(op2<2>(p[m][0][0], p[m][0][1]), p[m][0][2]), op2<2>(op2<2>(p[m][0][3], p[m][1][0]), p[m][1][1])), op2<2>(p[m][1][2], p[m][1][3]));
  }
  spread(-L2E * red4<1>(mx[0], mx[1], mx[2], mx[3]), mx);
  spread(L2E * red4<2>(mn[0], mn[1], mn[2], mn[3]), mn);
  f32x4 ep[4][2], en[4][2];
  float sp[4], sn[4];
#pragma unroll
  for (int m = 0; m < 4; ++m) {
#pragma unroll
    for (int F = 0; F < 2; ++F) {
      const f32x4 a = p[m][F] * L2E + mx[m], b = p[m][F] * (-L2E) + mn[m];
#pragma unroll
      for (int r = 0; r < 4; ++r) { ep[m][F][r] = __builtin_amdgcn_exp2f(a[r]); en[m][F][r] = __builtin_amdgcn_exp2f(b[r]); }
    }
    const f32x4 a = ep[m][0] + ep[m][1], b = en[m][0] + en[m][1];
    sp[m] = (a[0] + a[1]) + (a[2] + a[3]); sn[m] = (b[0] + b[1]) + (b[2] + b[3]);
  }
  spread(__builtin_amdgcn_rcpf(red4<0>(sp[0], sp[1], sp[2], sp[3])), sp);
  spread(__builtin_amdgcn_rcpf(red4<0>(sn[0], sn[1], sn[2], sn[3])), sn);
#pragma unroll
  for (int m = 0; m < 4; ++m) { pp[m] = pack8(ep[m][0] * sp[m], ep[m][1] * sp[m]); pn[m] = pack8(en[m][0] * sn[m], en[m][1] * sn[m]); }
}

DI float dot2(unsigned a, unsigned b, float c) { return __builtin_amdgcn_fdot2_f32_bf16(__builtin_bit_cast(bf16x2, a), __builtin_bit_cast(bf16x2, b), c, false); }
DI unsigned opaque(unsigned c) { asm volatile("" : "+s"(c)); return c; }
template <int NL> DI void colsum_load(u32x4 (&v)[NL], const char* w, int stride, int lane) {
#pragma unroll
  for (int i = 0; i < NL; ++i) v[i] = *(const u32x4*)(w + (size_t)(8 * i + (lane >> 3)) * stride + (lane & 7) * 16);
}
template <int NL> DI void colsum_finish(const u32x4 (&v)[NL], rsrc_t r, unsigned dst, int lane) {
  f32x2 s2[4];
#pragma unroll
  for (int j = 0; j < 4; ++j) s2[j] = (f32x2){bflo(v[0][j]), bfhi(v[0][j])};
#pragma unroll
  for (int i = 1; i < NL; ++i)
#pragma unroll
    for (int j = 0; j < 4; ++j) s2[j] += (f32x2){bflo(v[i][j]), bfhi(v[i][j])};
  const float s[8] = {s2[0][0], s2[0][1], s2[1][0], s2[1][1], s2[2][0], s2[2][1], s2[3][0], s2[3][1]};
  float t[4], u[2];
#pragma unroll
  for (int j = 0; j < 4; ++j) { auto q = __builtin_amdgcn_permlane32_swap(bu(s[j]), bu(s[j + 4]), false, false); t[j] = fb(q[0]) + fb(q[1]); }
#pragma unroll
  for (int j = 0; j < 2; ++j) { auto q = __builtin_amdgcn_permlane16_swap(bu(t[j]), bu(t[j + 2]), false, false); u[j] = fb(q[0]) + fb(q[1]); }
  const bool b3 = lane & 8;
  const float tot = (b3 ? u[1] : u[0]) + dpp<0x128>(b3 ? u[0] : u[1]);
  st4(r, bu(tot), ((lane & 7) * 8 + ((lane >> 5) & 1) * 4 + ((lane >> 4) & 1) * 2 + ((lane >> 3) & 1)) * 4, dst);
}

template <int XN, int TM, int TN> DI void tile_of(int bid, int& tm, int& tn, int& c) {
  const int x = bid & 7, r = bid >> 3, li = r % TM, lj = r / TM;
  tm = (x / XN) * TM + li; tn = (x % XN) * TN + lj; c = (li + lj) & 3;
}

constexpr int PH0 = 2, PH1 = 6, PH2 = 7, PF = 10, PR = 15, NPROBE = 4;
constexpr int R1 = 40704, R3 = 40704, R4 = 0, R5 = 0, SYNCW = 163584;
constexpr int S1_ST = 0, S1_CSR = 1024, S1_CSF = 13312, S1_FLG = 14080, S1_WP = 14336, S1_KV = 18432, S1_DUM = 26880;
constexpr int S4_STR = C4::RING, S4_CSR = S4_STR + 16384, S4_STF = S4_CSR + 8192, S4_CSF = S4_STF + 1024, S4_DUM = S4_CSF + 1024;
constexpr int LDS_BYTES = 163840, CODE_BYTES = 54 * 1024;
static_assert(S4_DUM + 256 <= SYNCW && S1_KV + 8448 <= R1 && R1 + C1::RING <= SYNCW && R3 + C3::RING <= SYNCW && C5::RING <= S4_STR, "lds");

template <class St> DI void kv_sum(const Sync& y, Gate& G, lds_ptr smem, int head, int h, int t, bool poller, bool doz, int lane, const St& onround) {
  u32x4 v[16], zz[4];
  unsigned have = 0;
  f32x2 s2[4] = {{0.f, 0.f}, {0.f, 0.f}, {0.f, 0.f}, {0.f, 0.f}};
#pragma unroll 1
  for (int round = poller ? 2 : 0; round < 3; ++round) {
    gate_slow(y, G, 0u, poller, lane, round == 0 ? 0 : round == 1 ? 13 : 16);
    onround(round);
    const unsigned fresh = G.mask & ~have;
    have = G.mask;
#pragma unroll
    for (int r = 0; r < 16; ++r) if ((fresh >> r) & 1u) v[r] = ld16(y.r, t * 16, WS_KVP + (head * 16 + r) * 4096);
    if (round == 2 && doz) {
#pragma unroll
      for (int i = 0; i < 4; ++i) zz[i] = ld16(y.r, lane * 16, WS_ZP + head * 4096 + i * 1024);
    }
  }
#pragma unroll
  for (int r = 0; r < 16; ++r)
#pragma unroll
    for (int j = 0; j < 4; ++j) s2[j] += (f32x2){bflo(v[r][j]), bfhi(v[r][j])};
  const float s[8] = {s2[0][0], s2[0][1], s2[1][0], s2[1][1], s2[2][0], s2[2][1], s2[3][0], s2[3][1]};
  *(LAS u32x4*)(smem + S1_KV + h * 4096 + t * 16) = (u32x4){pk2(s[0], s[1]), pk2(s[2], s[3]), pk2(s[4], s[5]), pk2(s[6], s[7])};
  if (doz) {
    f32x4 z = (__builtin_bit_cast(f32x4, zz[0]) + __builtin_bit_cast(f32x4, zz[1])) + (__builtin_bit_cast(f32x4, zz[2]) + __builtin_bit_cast(f32x4, zz[3]));
#pragma unroll
    for (int j = 0; j < 4; ++j) z[j] = xsum32(xsum16(z[j]));
    if (lane < 16) *(LAS u32x2*)(smem + S1_KV + 8192 + h * 128 + lane * 8) = (u32x2){pk2(z[0], z[1]), pk2(z[2], z[3])};
  }
}

__global__ __launch_bounds__(512, 2) void fused(Params P) {
  extern __shared__ __attribute__((aligned(16))) char smem_[];
  lds_ptr smem = (lds_ptr)smem_;
  const unsigned lds0 = (unsigned)(size_t)smem;
  const int wid = __builtin_amdgcn_readfirstlane(threadIdx.x >> 6), lane = threadIdx.x & 63, bid = blockIdx.x;
  {
    const char* pc; asm volatile("s_getpc_b64 %0" : "=s"(pc));
    const unsigned co = ((bid >> 3) & 7) * 8192 + threadIdx.x * 128;
    if (threadIdx.x < 64 && co < CODE_BYTES) dma4<false>(co, pc, lds0 + S1_DUM);
    SB();
  }
  int tm1, tn1, c1, tm3, tn3, c3;
  tile_of<4, 8, 4>(bid, tm1, tn1, c1);
  tile_of<2, 4, 8>(bid, tm3, tn3, c3);
  const int kh = bid & 1, tm5 = ((bid & 7) >> 1) * 4 + ((bid >> 3) & 3), tn5 = bid >> 5, c5 = ((bid >> 3) + tn5) & 3;
  const int par = tn1 & 1, base7 = 7 * (tn1 >> 1), ch0 = base7 + 4 * par;
  const int g = lane >> 4, m16 = lane & 15;
  Sync y;
  y.r = __builtin_amdgcn_make_buffer_rsrc(P.ws, 0, WS_END, 0x00020000);
  y.w = (LAS unsigned*)(smem + SYNCW); y.pb = 0;

  if (wid >= 4) {
    const int lw = wid - 4, lt = threadIdx.x - 256;
    const auto nop = [](int) {};
    {
      Ld<C1> L; L.A = P.x + (size_t)tm1 * 128 * 2048; L.W = P.wqkv + tn1 * 128; L.lw = lw; L.c = c1; L.kt0 = 0; L.init(y, lane, 2048, 0, 0, 0);
      const unsigned frec = (FL_P0 * 256 + (lane < 16 ? tm1 * 16 + lane : lane < 28 ? ((lane - 16) / 3) * 48 + ((lane - 16) % 3) * 16 + tn1 : tn1)) * 128;
      if (lw == 0) dma4<true>(0u * lane, P.ws + WS_CTL, lds0 + S1_FLG);
      load_all<C1, false, (1u << (PF + 3)), 0x8005u | (1u << PF) | (1u << (PF + 3)) | (1u << PR)>(L, lds0 + R1,
        [&](int s) {
          if (lw == 0) {
            const u32x4 v = *(volatile LAS u32x4*)(smem + S1_CSR + lane * 16);
            if (!__all(v[0] == y.tag && v[1] == y.tag && v[2] == y.tag && v[3] == y.tag)) {
#pragma unroll 1
              for (int k = 0; k < 4; ++k) poll(y, frec + 4 * k, 64, lane);
            }
          }
        },
        [&](int s) {
          if (s == 0) y.tag = __builtin_amdgcn_readfirstlane(*(volatile LAS unsigned*)(smem + S1_FLG)) + 1u;
          if (s == 2) dma<false>(lane * 16, P.wphi + tn1 * 4096 + lw * 1024, lds0 + S1_WP + lw * 1024);
          if (s == PF && lw == 0) dma<true>(frec, P.ws + WS_FLG, lds0 + S1_CSR);
          if (s == 15 && lw == 1) dma4<false>(lane * 128, P.wo + bid * 8192, lds0 + S1_DUM);
          if (s == PF + 3) {
            if (lw == 3) dma<true>(lane * 16, P.ws + WS_ST1 + tm1 * 1024, lds0 + S1_ST);
#pragma unroll
            for (int i = 0; i < 3; ++i)
              dma<true>((lane >> 4) * 12288 + (lane & 15) * 16, P.ws + WS_CSQ + lw * 49152 + i * 4096 + tn1 * 256, lds0 + S1_CSR + i * 4096 + lw * 1024);
          }
          if (s == PR) {
            if (lt < 192) {
              const LAS float* rawp = (const LAS float*)(smem + S1_CSR) + (lt >> 6) * 1024 + (lt & 63);
              float t = 0.f;
#pragma unroll
              for (int i = 0; i < 16; ++i) t += rawp[i * 64];
              ((LAS float*)(smem + S1_CSF))[lt] = t;
            }
            wait_lgkm0();
          }
        });
    }
    {
      Ld<C3> L; L.A = P.ws; L.W = P.wo + tn3 * 128; L.lw = lw; L.c = c3; L.kt0 = 0; L.init(y, lane, 0, 0, 0, 9);
      if (bid >= 224 && bid < 224 + NPAGE) {
        if (lw == 1) {
          const unsigned po = bid < 226 ? WS_PRB + (bid - 224) * 1024 : (bid - 224) << 21;
          unsigned tmin[2] = {~0u, ~0u};
#pragma unroll 1
          for (int k = 0; k < NPROBE; ++k) {
            const unsigned long long t0 = __builtin_amdgcn_s_memtime();
            if (lane < 8) st16(y.r, (u32x4){0u, 0u, 0u, 0u}, lane * 16, po + (k & 1) * 8192);
            wait_vm<0>();
            const unsigned dt = (unsigned)(__builtin_amdgcn_s_memtime() - t0);
            if (k & 1) tmin[1] = min(tmin[1], dt); else tmin[0] = min(tmin[0], dt);
          }
          if (lane == 0) st4(y.r, (y.tag << 1) | (((unsigned)(tmin[1] < tmin[0]) ^ (unsigned)(bid >> 2)) & 1u), 0, WS_FLG + (FL_CS * 256 + bid) * 128 + 4);
        }
        if (lw == 1) *(volatile LAS unsigned*)(y.w + 14) = 1u; else lds_wait(y, 14, 1u);
      }
      load_preW<C3>(L, lds0 + R3);
      if (lw >= 2 && bid < 224) {
        u32x4 v[16];
        colsum_load<16>(v, P.w1 + (size_t)((bid / 56) * 256 + (lw - 2) * 128) * (FF * 2) + (bid % 56) * 128, FF * 2, lane);
        colsum_finish<16>(v, y.r, WS_CS1 + (((bid / 56) * 2 + lw - 2) * FF + (bid % 56) * 64) * 4, lane);
      }
      publish(y, FL_CS, bid, lane);
      lds_wait(y, 24 + FL_KV, 4u);
      Gate G; G.off = (FL_KV * 256 + m16 * 16 + tn1) * 128; G.mask = 0; G.word = 8;
      const unsigned pbo = WS_FLG + (FL_CS * 256 + 224 + (lane & 31)) * 128 + 4;
      unsigned pbv = 0;
      kv_sum(y, G, smem, 2 * tn1 + 1, 1, lt, lw == 0, lw == 1, lane, [&](int) { if (lw == 0) pbv = __builtin_amdgcn_raw_buffer_load_b32(y.r, pbo, 0, 16); });
      if (lw == 0) {
        const unsigned long long t0 = __builtin_amdgcn_s_memrealtime();
        while (!__all(lane >= NPAGE || (pbv >> 1) == (y.tag & 0x7fffffffu))) {
          if (__builtin_amdgcn_s_memrealtime() - t0 > 4000000ull) { if (lane == 0) st4(y.r, y.tag, 0, WS_CTL + 128); break; }
          pbv = __builtin_amdgcn_raw_buffer_load_b32(y.r, pbo, 0, 16);
        }
        const unsigned raw = (unsigned)__ballot(pbv & 1u), odd = __builtin_popcount((raw ^ 0xaaaaaaaau) & 0x3fffffdu);
        *(volatile LAS unsigned*)(y.w + 13) = odd <= 6 ? 0xaaaaaaaau : odd >= 19 ? 0x55555555u : raw;
      }
      wait_lgkm0(); bar();
      y.pb = __builtin_amdgcn_readfirstlane(*(volatile LAS unsigned*)(y.w + 13));
      L.A += near(y, WS_O + tm3 * 524288, tm3); L.G.off = rec(y, FL_O, tm3 * 16 + m16);
      load_all<C3, true, 0u, 0u>(L, lds0 + R3, nop, nop);
    }
    {
      Ld<C4> L; L.A = P.ws + near(y, WS_H + tm1 * 524288, tm1); L.W = P.w1 + ch0 * 128; L.lw = lw; L.c = c1; L.kt0 = 0;
      L.init(y, lane, 128, par ? -64 : 384, (lane & 16) ? rec(y, FL_CS, (m16 >> 2) * 56 + base7 + 3 * par + (lane & 3)) : rec(y, FL_H, tm1 * 16 + m16), 10);
      lds_wait(y, 24 + FL_H, 4u);
      load_preW<C4>(L, lds0 + R4);
      load_all<C4, true, 0u, 0xa550u>(L, lds0 + R4, nop,
        [&](int s) {
          if (s < 12 && lw == ((s >> 1) & 3)) dma4<false>(min(lane, 55) * 128, P.w2 + bid * 28672 + lw * 7168, lds0 + S4_DUM);
          if (s == 13) {
            gate(y, L.G, ~0u, lw == 0, lane);
#pragma unroll
            for (int i = 0; i < 4; ++i) dma<true>(lane * 16, P.ws + near(y, WS_ST2 + tm1 * 32768, tm1) + (lw >> 1) * 16384 + ((lw & 1) * 4 + i) * 1024, lds0 + S4_STR + (lw * 4 + i) * 1024);
#pragma unroll
            for (int i = 0; i < 2; ++i) dma<true>(lane * 16, P.ws + WS_CS1 + ((lw * 2 + i) * FF + (base7 + 3 * par) * 64) * 4, lds0 + S4_CSR + (lw * 2 + i) * 1024);
          }
          if (s == 15) {
            const LAS float* rawp = (const LAS float*)(smem + S4_CSR) + lt;
            float t = 0.f;
#pragma unroll
            for (int i = 0; i < 8; ++i) t += rawp[i * 256];
            ((LAS float*)(smem + S4_CSF))[lt] = t;
            wait_lgkm0();
          }
          if (s == 15 && lw < 2) {
            f32x2 st[16]; float mu = 0.f, m2 = 0.f;
#pragma unroll
            for (int i = 0; i < 16; ++i) { st[i] = *(const LAS f32x2*)(smem + S4_STR + i * 1024 + lt * 8); mu += st[i][0]; }
            mu *= (1.f / 16.f);
#pragma unroll
            for (int i = 0; i < 16; ++i) { const float d = st[i][0] - mu; m2 += st[i][1] + 64.f * d * d; }
            *(LAS f32x2*)(smem + S4_STF + lt * 8) = ln_consts(mu, m2 * (1.f / D));
            wait_lgkm0();
          }
        });
    }
    {
      Ld<C5> L; L.A = P.ws + near(y, WS_F + tm5 * 2097152, tm5); L.W = P.w2 + tn5 * 256; L.lw = lw; L.c = c5; L.kt0 = 28 * kh; L.init(y, lane, 128, 0, rec(y, FL_F, tm5 * 16 + 8 * kh + ((lane >> 1) & 7)) + (lane & 1) * 4, 11);
      load_preW<C5>(L, lds0 + R5);
      load_all<C5, true, 0u, 0u>(L, lds0 + R5, nop, nop);
    }
    return;
  }

  const int wm = wid >> 1, wn = wid & 1;
  __builtin_amdgcn_s_setprio(1);
  if (wid == 0 && lane < 32) y.w[lane] = 0u;
  {
    f32x4 acc[24];
    Adr ad;
    ad.v[0] = lds0 + R1 + (wm * 64 + m16) * 128 + ((g ^ (m16 >> 1)) << 4); ad.v[1] = lds0 + R1 + (wm * 64 + m16) * 128 + ((g ^ (m16 >> 1) ^ 4) << 4);
    ad.v[2] = lds0 + R1 + 16384 + wlane(lane, 2 * wn); ad.v[3] = lds0 + R1 + 16384 + wlane(lane, 2 * wn + 1); ad.v[4] = 0; ad.v[5] = 0;
    u32x4 px[4], pw[8];
    const int prow = bid * 8 + wid * 2 + (lane >> 5);
    consume<C1, 1, 1, PH0, PH1, PH2>(acc, ad, [&](int s) {
      if (s < 0) {
        y.tag = __builtin_amdgcn_readfirstlane(*(volatile LAS unsigned*)(smem + S1_FLG)) + 1u;
      }
      if (s == 0) {
#pragma unroll
        for (int i = 0; i < 4; ++i) px[i] = *(const u32x4*)(P.x + (size_t)prow * 2048 + ((lane & 31) + 32 * i) * 16);
        colsum_load<8>(pw, P.wqkv + (size_t)((min(bid, 191) / 48) * 256 + wid * 64) * (3 * D * 2) + (bid % 48) * 128, 3 * D * 2, lane);
      }
      if (s == 1) {
#pragma unroll
        for (int i = 0; i < 4; ++i) asm volatile("" : "+v"(px[i]));
#pragma unroll
        for (int i = 0; i < 8; ++i) asm volatile("" : "+v"(pw[i]));
        const unsigned one2 = opaque(0x3f803f80u);
        float sm = 0.f;
#pragma unroll
        for (int i = 0; i < 4; ++i)
#pragma unroll
          for (int j = 0; j < 4; ++j) sm = dot2(px[i][j], one2, sm);
        const float mean = xsum16(row16_sum(sm)) * (1.f / D);
        f32x2 sq = {0.f, 0.f};
#pragma unroll
        for (int i = 0; i < 4; ++i)
#pragma unroll
          for (int j = 0; j < 4; ++j) { const f32x2 d = (f32x2){bflo(px[i][j]), bfhi(px[i][j])} - mean; sq = __builtin_elementwise_fma(d, d, sq); }
        const float var = xsum16(row16_sum(sq[0] + sq[1])) * (1.f / D);
        if ((lane & 31) == 0) st8(y.r, __builtin_bit_cast(u32x2, ln_consts(mean, var)), prow * 8, WS_ST1);
        if (bid < 192) colsum_finish<8>(pw, y.r, WS_CSQ + (((bid / 48) * 4 + wid) * 3 * D + (bid % 48) * 64) * 4, lane);
      }
      if (s == 2) { wait_vm<0>(); if (lane == 0) st4(y.r, y.tag, 0, WS_FLG + (FL_P0 * 256 + bid) * 128 + wid * 4); }
    });
    const int head = 2 * tn1 + wn;
    f32x4 cs[4]; float csv[2];
#pragma unroll
    for (int n = 0; n < 4; ++n) cs[n] = *(const LAS f32x4*)(smem + S1_CSF + ((n >> 1) * 64 + wn * 32 + (n & 1) * 16 + 4 * g) * 4);
#pragma unroll
    for (int n = 0; n < 2; ++n) csv[n] = *(const LAS float*)(smem + S1_CSF + (128 + wn * 32 + n * 16 + m16) * 4);
    u32x4 wp[2];
#pragma unroll
    for (int F = 0; F < 2; ++F) { lds_ptr a0 = smem + S1_WP + wn * 2048 + (4 * g + (m16 >> 2)) * 64 + F * 32 + (lane & 3) * 8; wp[F] = lds_tr2(a0, a0 + 1024); }
    u32x4 qb[4], vb[2][2];
    f32x4 p[4][2];
    const f32x4 zero = {0.f, 0.f, 0.f, 0.f};
    {
      f32x4 vv[4][2];
#pragma unroll
      for (int m = 0; m < 4; ++m) {
        const f32x2 st = *(const LAS f32x2*)(smem + S1_ST + (wm * 64 + m * 16 + m16) * 8);
        const f32x4 s0 = *(const LAS f32x4*)(smem + S1_ST + (wm * 64 + m * 16 + 4 * g) * 8), s1 = *(const LAS f32x4*)(smem + S1_ST + (wm * 64 + m * 16 + 4 * g) * 8 + 16);
        const f32x4 mu = {s0[0], s0[2], s1[0], s1[2]}, rs = {s0[1], s0[3], s1[1], s1[3]};
        qb[m] = pack8((acc[m * 6] - cs[0] * st[0]) * st[1], (acc[m * 6 + 1] - cs[1] * st[0]) * st[1]);
        const u32x4 kb = pack8((acc[m * 6 + 2] - cs[2] * st[0]) * st[1], (acc[m * 6 + 3] - cs[3] * st[0]) * st[1]);
        p[m][0] = mfma(wp[0], kb, zero); p[m][1] = mfma(wp[1], kb, zero);
        vv[m][0] = (acc[m * 6 + 4] - mu * csv[0]) * rs; vv[m][1] = (acc[m * 6 + 5] - mu * csv[1]) * rs;
      }
#pragma unroll
      for (int u = 0; u < 2; ++u) { vb[u][0] = pack8(vv[2 * u][0], vv[2 * u + 1][0]); vb[u][1] = pack8(vv[2 * u][1], vv[2 * u + 1][1]); }
    }
    u32x4 pp[4], pn[4];
    dual_softmax4(p, pp, pn);
    {
      lds_ptr own = smem + R3 + wid * C3::STAGE, oth = smem + R3 + (wid ^ 2) * C3::STAGE;
#pragma unroll
      for (int m = 0; m < 4; ++m) {
        lds_ptr w = own + (16 * m + m16) * 136 + 8 * g;
        *(LAS u32x2*)(w) = (u32x2){pp[m][0], pp[m][1]}; *(LAS u32x2*)(w + 32) = (u32x2){pp[m][2], pp[m][3]};
        *(LAS u32x2*)(w + 64) = (u32x2){pn[m][0], pn[m][1]}; *(LAS u32x2*)(w + 96) = (u32x2){pn[m][2], pn[m][3]};
      }
#pragma unroll
      for (int i = 0; i < 4; ++i) *(LAS u32x4*)(own + 8704 + i * 1024 + lane * 16) = vb[i >> 1][i & 1];
      wait_lgkm0();
      volatile LAS unsigned* xf = y.w + 16;
      xf[wid] = 1u;
      f32x4 kv[2][2] = {{zero, zero}, {zero, zero}}, z[2] = {zero, zero};
      const u32x4 ones = {0x3f803f80u, 0x3f803f80u, 0x3f803f80u, 0x3f803f80u};
      const unsigned ro = (4 * g + (m16 >> 2)) * 136 + wm * 64 + (lane & 3) * 8;
#pragma unroll
      for (int u = 0; u < 2; ++u)
#pragma unroll
        for (int F = 0; F < 2; ++F) {
          const u32x4 pa = lds_tr2(own + ro + u * 32 * 136 + F * 32, own + ro + u * 32 * 136 + F * 32 + 16 * 136);
          kv[F][0] = mfma(pa, vb[u][0], kv[F][0]); kv[F][1] = mfma(pa, vb[u][1], kv[F][1]); z[F] = mfma(pa, ones, z[F]);
        }
      while (xf[wid ^ 2] == 0u) __builtin_amdgcn_s_sleep(0);
      asm volatile("" ::: "memory");
#pragma unroll
      for (int u = 0; u < 2; ++u) {
        const u32x4 v0 = lds128(oth + 8704 + u * 2048 + lane * 16), v1 = lds128(oth + 8704 + u * 2048 + 1024 + lane * 16);
#pragma unroll
        for (int F = 0; F < 2; ++F) {
          const u32x4 pa = lds_tr2(oth + ro + u * 32 * 136 + F * 32, oth + ro + u * 32 * 136 + F * 32 + 16 * 136);
          kv[F][0] = mfma(pa, v0, kv[F][0]); kv[F][1] = mfma(pa, v1, kv[F][1]); z[F] = mfma(pa, ones, z[F]);
        }
      }
      const unsigned kd = WS_KVP + (head * 16 + tm1) * 4096 + wm * 2048;
      st16(y.r, pack8(kv[0][0], kv[1][0]), lane * 16, kd);
      st16(y.r, pack8(kv[0][1], kv[1][1]), lane * 16, kd + 1024);
      if (m16 == 0) {
        st16(y.r, __builtin_bit_cast(u32x4, z[0]), g * 32, WS_ZP + (head * 16 + tm1) * 256 + wm * 128);
        st16(y.r, __builtin_bit_cast(u32x4, z[1]), g * 32 + 16, WS_ZP + (head * 16 + tm1) * 256 + wm * 128);
      }
      publish(y, FL_KV, tm1 * 16 + tn1, lane);
    }
#pragma unroll
    for (int m = 0; m < 4; ++m) { p[m][0] = mfma(wp[0], qb[m], zero); p[m][1] = mfma(wp[1], qb[m], zero); }
    dual_softmax4(p, pp, pn);
    { Gate G; G.off = 0; G.mask = 0; G.word = 8; kv_sum(y, G, smem, 2 * tn1, 0, threadIdx.x, false, wid == 0, lane, [](int) {}); }
    wait_lgkm0(); bar();
    y.pb = __builtin_amdgcn_readfirstlane(*(volatile LAS unsigned*)(y.w + 13));
    u32x4 ka[2][2], za[2];
#pragma unroll
    for (int ks = 0; ks < 2; ++ks) {
      ka[ks][0] = lds128(smem + S1_KV + wn * 4096 + ks * 2048 + lane * 16); ka[ks][1] = lds128(smem + S1_KV + wn * 4096 + ks * 2048 + 1024 + lane * 16);
      za[ks] = lds128(smem + S1_KV + 8192 + wn * 128 + ks * 64 + g * 16);
    }
#pragma unroll
    for (int m = 0; m < 4; ++m) {
      f32x4 n0 = mfma(ka[0][0], pp[m], zero), n1 = mfma(ka[0][1], pp[m], zero), dn = mfma(za[0], pp[m], zero);
      n0 = mfma(ka[1][0], pn[m], n0); n1 = mfma(ka[1][1], pn[m], n1); dn = mfma(za[1], pn[m], dn);
      const float rd = __builtin_amdgcn_rcpf(dn[0] + 1e-6f);
      st16(y.r, pack8(n0 * rd, n1 * rd), lane * 16, near(y, WS_O + (tm1 * 16 + tn1) * 32768, tm1) + wm * 16384 + (m * 2 + wn) * 1024);
    }
    publish(y, FL_O, tm1 * 16 + tn1, lane);
  }
  {
    u32x4 xr[2][2];
#pragma unroll
    for (int m = 0; m < 2; ++m)
#pragma unroll
      for (int Q = 0; Q < 2; ++Q)
        xr[m][Q] = *(const u32x4*)(P.x + (size_t)(tm3 * 128 + wid * 32 + m * 16 + m16) * 2048 + tn3 * 128 + Q * 64 + (g & 1) * 32 + (g >> 1) * 16);
    f32x4 acc[8];
    Adr ad; ad.v[0] = lds0 + R3 + wid * 4096 + lane * 16; ad.v[1] = 0;
#pragma unroll
    for (int n = 0; n < 4; ++n) ad.v[2 + n] = lds0 + R3 + 16384 + wlane(lane, n);
    consume_s<C3, 3, 2>(acc, ad, [](int) {});
#pragma unroll
    for (int m = 0; m < 2; ++m) {
      float hv[16];
#pragma unroll
      for (int Q = 0; Q < 2; ++Q) {
        auto r0 = __builtin_amdgcn_permlane16_swap(xr[m][Q][0], xr[m][Q][2], false, false);
        auto r1 = __builtin_amdgcn_permlane16_swap(xr[m][Q][1], xr[m][Q][3], false, false);
        const f32x4 a = acc[m * 4 + 2 * Q], b = acc[m * 4 + 2 * Q + 1];
        const u32x4 hp = {pk2(a[0] + bflo(r0[0]), a[1] + bfhi(r0[0])), pk2(a[2] + bflo(r1[0]), a[3] + bfhi(r1[0])),
                          pk2(b[0] + bflo(r0[1]), b[1] + bfhi(r0[1])), pk2(b[2] + bflo(r1[1]), b[3] + bfhi(r1[1]))};
        st16(y.r, hp, lane * 16, near(y, WS_H + (tm3 * 16 + tn3) * 32768, tm3) + wm * 16384 + wn * 4096 + (m * 2 + Q) * 1024);
#pragma unroll
        for (int j = 0; j < 4; ++j) { hv[Q * 8 + 2 * j] = bflo(hp[j]); hv[Q * 8 + 2 * j + 1] = bfhi(hp[j]); }
      }
      float s = 0.f;
#pragma unroll
      for (int j = 0; j < 16; ++j) s += hv[j];
      s = xsum32(xsum16(s)) * (1.f / 64.f);
      float q = 0.f;
#pragma unroll
      for (int j = 0; j < 16; ++j) { const float d = hv[j] - s; q += d * d; }
      q = xsum32(xsum16(q));
      if (g == 0) st8(y.r, (u32x2){bu(s), bu(q)}, m16 * 8, near(y, WS_ST2 + tm3 * 32768, tm3) + (tn3 >> 3) * 16384 + (tn3 & 7) * 1024 + (wid * 32 + m * 16) * 8);
    }
    publish(y, FL_H, tm3 * 16 + tn3, lane);
  }
  {
    f32x4 acc[28];
    Adr ad;
    ad.v[0] = lds0 + R4 + wm * 8192 + wn * 4096 + lane * 16; ad.v[1] = lds0 + R4 + wm * 8192 + (wn ^ 1) * 4096 + lane * 16;
    ad.v[2] = lds0 + R4 + 16384 + wlane(lane, wn); ad.v[3] = lds0 + R4 + 16384 + wlane(lane, 2 + wn);
    ad.v[4] = lds0 + R4 + 16384 + 6144 + hlane(lane, 0); ad.v[5] = lds0 + R4 + 16384 + 6144 + hlane(lane, 1);
    consume<C4, 4, 1, 0, 0, 0>(acc, ad, [](int) {});
    f32x4 cs[8];
#pragma unroll
    for (int n = 0; n < 6; ++n) cs[n] = *(const LAS f32x4*)(smem + S4_CSF + ((par + (n >> 1)) * 64 + (2 * (n & 1) + wn) * 16 + 4 * g) * 4);
#pragma unroll
    for (int n = 0; n < 2; ++n) cs[6 + n] = *(const LAS f32x4*)(smem + S4_CSF + ((par ? 32 : 192) + n * 16 + 4 * g) * 4);
    const f32x4 zero = {0.f, 0.f, 0.f, 0.f};
    f32x2 st[4];
#pragma unroll
    for (int m = 0; m < 4; ++m) st[m] = *(const LAS f32x2*)(smem + S4_STF + ((wm * 4 + ((m + 2 * wn) & 3)) * 16 + m16) * 8);
#pragma unroll
    for (int i = 0; i < 4; ++i) {
#pragma unroll
      for (int m = 0; m < (i < 3 ? 4 : 2); ++m) {
        const unsigned fd = near(y, WS_F + tm1 * 2097152, tm1) + wm * 16384 + ((m + 2 * wn) & 3) * 2048 + (i < 3 ? (ch0 + i) * 32768 + wn * 1024 : (base7 + 3) * 32768 + par * 1024);
        const int a = i < 3 ? m * 6 + 2 * i : 24 + 2 * m;
        st16(y.r, pack8(__builtin_elementwise_max((acc[a] - cs[2 * i] * st[m][0]) * st[m][1], zero),
                        __builtin_elementwise_max((acc[a + 1] - cs[2 * i + 1] * st[m][0]) * st[m][1], zero)), lane * 16, fd);
        SB();
      }
      if (i == 1) {
        wait_vm<4>();
        if (lane == 0 && __hip_atomic_fetch_add(y.w + FL_F, 1u, __ATOMIC_RELAXED, __HIP_MEMORY_SCOPE_WORKGROUP) == 3u) st4(y.r, y.tag, 0, WS_FLG + rec(y, FL_F, tm1 * 16 + tn1));
        SB();
      }
    }
  }
  {
    f32x4 acc[16];
    Adr ad;
    ad.v[0] = lds0 + R5 + wm * 8192 + kh * 4096 + lane * 16; ad.v[1] = lds0 + R5 + wm * 8192 + (kh ^ 1) * 4096 + lane * 16;
#pragma unroll
    for (int n = 0; n < 4; ++n) ad.v[2 + n] = lds0 + R5 + 16384 + wn * 2048 + wlane(lane, n);
    u32x4 hr[2][2];
    consume_s<C5, 5, 1>(acc, ad, [&](int s) {
      if (s < 0) {
        wait_vm<0>();
        if (lane == 0 && __hip_atomic_fetch_add(y.w + 20, 1u, __ATOMIC_RELAXED, __HIP_MEMORY_SCOPE_WORKGROUP) == 3u) st4(y.r, y.tag, 0, WS_FLG + rec(y, FL_F, tm1 * 16 + tn1) + 4);
#pragma unroll
        for (int m = 0; m < 2; ++m)
#pragma unroll
          for (int Q = 0; Q < 2; ++Q) hr[m][Q] = ld16(y.r, lane * 16, near(y, WS_H + (tm5 * 16 + 2 * tn5 + wn) * 32768, tm5) + wm * 16384 + ((2 * kh + m) * 2 + Q) * 1024);
      }
    });
    const int tile = tm5 * 8 + tn5;
#pragma unroll
    for (int i = 0; i < 4; ++i) st16(y.r, pack8(acc[8 + 2 * i], acc[9 + 2 * i]), lane * 16, near(y, WS_O + (tile * 2 + kh) * 32768, tm5) + wm * 16384 + wn * 4096 + i * 1024);
    wait_vm<0>();
    if (lane == 0) st4(y.r, y.tag, 0, WS_FLG + rec(y, FL_X + wm, tile * 2 + kh) + wn * 64);
    poll(y, rec(y, FL_X + wm, tile * 2 + (kh ^ 1)) + wn * 64, 1, lane);
    const rsrc_t ro = __builtin_amdgcn_make_buffer_rsrc(P.out, 0, 2048 * 2048, 0x00020000);
    u32x4 xv[4];
#pragma unroll
    for (int i = 0; i < 4; ++i) xv[i] = ld16(y.r, lane * 16, near(y, WS_O + (tile * 2 + (kh ^ 1)) * 32768, tm5) + wm * 16384 + wn * 4096 + i * 1024);
#pragma unroll
    for (int m = 0; m < 2; ++m)
#pragma unroll
      for (int Q = 0; Q < 2; ++Q) {
        const u32x4 xx = xv[m * 2 + Q];
        const f32x4 a = acc[m * 4 + 2 * Q] + (f32x4){bflo(xx[0]), bfhi(xx[0]), bflo(xx[1]), bfhi(xx[1])}, b = acc[m * 4 + 2 * Q + 1] + (f32x4){bflo(xx[2]), bfhi(xx[2]), bflo(xx[3]), bfhi(xx[3])};
        const u32x4 hh = hr[m][Q];
        const unsigned a0 = pk2(a[0] + bflo(hh[0]), a[1] + bfhi(hh[0])), a1 = pk2(a[2] + bflo(hh[1]), a[3] + bfhi(hh[1]));
        const unsigned b0 = pk2(b[0] + bflo(hh[2]), b[1] + bfhi(hh[2])), b1 = pk2(b[2] + bflo(hh[3]), b[3] + bfhi(hh[3]));
        auto r0 = __builtin_amdgcn_permlane16_swap(a0, b0, false, false);
        auto r1 = __builtin_amdgcn_permlane16_swap(a1, b1, false, false);
        __builtin_amdgcn_raw_buffer_store_b128((u32x4){r0[0], r1[0], r0[1], r1[1]}, ro, (tm5 * 128 + wm * 64 + (2 * kh + m) * 16 + m16) * 2048 + tn5 * 256 + wn * 128 + Q * 64 + (g & 1) * 32 + (g >> 1) * 16, 0, 16);
      }
    if (bid == 0 && threadIdx.x == 0) st4(y.r, y.tag, 0, WS_CTL);
  }
}

extern "C" void kernel_launch(void* const* d_in, const int* in_sizes, int n_in, void* d_out, int out_size, void* d_ws, size_t ws_size,
                              hipStream_t stream) {
  Params p;
  p.x = (const char*)d_in[0]; p.wqkv = (const char*)d_in[1]; p.wphi = (const char*)d_in[2]; p.wo = (const char*)d_in[3];
  p.w1 = (const char*)d_in[5]; p.w2 = (const char*)d_in[6]; p.ws = (char*)(((size_t)d_ws + 0x1fffff) & ~(size_t)0x1fffff); p.out = (char*)d_out;
  (void)hipFuncSetAttribute((const void*)fused, hipFuncAttributeMaxDynamicSharedMemorySize, LDS_BYTES);
  fused<<<256, 512, LDS_BYTES, stream>>>(p);
}
```
